```python
import torch

def _softmax(x, axis=-1):
    return torch.softmax(x, dim=axis)
def _gelu(x):
    return torch.nn.functional.gelu(x, approximate="tanh")
def _silu(x):
    return torch.nn.functional.silu(x)
def _sigmoid(x):
    return torch.sigmoid(x)
def _layernorm(x, axis=-1, eps=1e-5):
    m = x.mean(dim=axis, keepdim=True)
    v = x.var(dim=axis, keepdim=True, unbiased=False)
    return (x - m) / torch.sqrt(v + eps)
def _scatter_add(tgt, idx, src, axis=0):
    return tgt.clone().index_add_(axis, idx, src)
def _segment_sum(src, seg, n):
    out = torch.zeros((n,) + src.shape[1:], dtype=src.dtype)
    return out.index_add_(0, seg, src)
def _pad(x, pw):
    flat = tuple(v for pair in reversed(pw) for v in pair)
    return torch.nn.functional.pad(x, flat)

kernel_name = 'interleave_layers__sliding_local_dilated__ssm_conv_h3__prompt'

def setup_inputs():

    _w = (lambda a: (lambda t: t.to(torch.bfloat16) if t.is_floating_point() else t)(torch.as_tensor(a)))
    def _A_setup():
        B, S, D = 1, 2048, 512
        return {
            "x": _w((lambda *s: torch.randn(*s, dtype=torch.bfloat16) * 0.05)(B, S, D)),
            "wqkv": _w((lambda *s: torch.randn(*s, dtype=torch.bfloat16) * 0.05)(D, 3 * D)),
            "wo": _w((lambda *s: torch.randn(*s, dtype=torch.bfloat16) * 0.05)(D, D)),
        }
    _A_in = _A_setup()

    def _B_setup():
        B, S, D = 1, 2048, 512
        ks = 4
        return {
            "q": _w((lambda *s: torch.randn(*s, dtype=torch.bfloat16) * 0.05)(B, S, D)),
            "k": _w((lambda *s: torch.randn(*s, dtype=torch.bfloat16) * 0.4)(B, S, D)),
            "v": _w((lambda *s: torch.randn(*s, dtype=torch.bfloat16) * 0.4)(B, S, D)),
            "shift_k": _w((lambda *s: torch.randn(*s, dtype=torch.bfloat16) * 0.4)(D, ks) * 0.5),
            "ssm_k": _w((lambda *s: torch.randn(*s, dtype=torch.bfloat16) * 0.8)(D, S) * (1.0 / S ** 0.5)),
            "ssm_D": _w((lambda *s: torch.randn(*s, dtype=torch.bfloat16) * 0.4)(D)),
        }
    _B_in = _B_setup()

    return {
        "A_x": _A_in["x"],
        "A_wqkv": _A_in["wqkv"],
        "A_wo": _A_in["wo"],
        "B_q": _B_in["q"],
        "B_k": _B_in["k"],
        "B_v": _B_in["v"],
        "B_shift_k": _B_in["shift_k"],
        "B_ssm_k": _B_in["ssm_k"],
        "B_ssm_D": _B_in["ssm_D"],
    }

def reference(A_x, A_wqkv, A_wo, B_q, B_k, B_v, B_shift_k, B_ssm_k, B_ssm_D):
    def _A_ref(x, wqkv, wo):
        B, S, D = x.shape
        H = 32; Dh = D // H
        W, dil = 8, 2
        qkv = (lambda x, *d: x.permute(*d))((x @ wqkv).reshape(B, S, 3, H, Dh), 2, 0, 3, 1, 4)
        q, k, v = qkv[0], qkv[1], qkv[2]
        scale = Dh ** -0.5
        pos = torch.arange(S)
        off = torch.arange(W) * dil
        idx = pos[:, None] - off[None, :]
        mask = idx >= 0
        idx_c = torch.where(mask, idx, 0).reshape(-1)
        kg = k[:, :, idx_c, :].reshape(B, H, S, W, Dh)
        vg = v[:, :, idx_c, :].reshape(B, H, S, W, Dh)
        logits = torch.einsum("bhsd,bhswd->bhsw", q, kg) * scale
        logits = torch.where(mask[None, None, :, :], logits, -1e9)
        attn = _softmax(logits, axis=-1)
        o = torch.einsum("bhsw,bhswd->bhsd", attn, vg)
        return (lambda x, *d: x.permute(*d))(o, 0, 2, 1, 3).reshape(B, S, D) @ wo

    def _B_ref(q, k, v, shift_k, ssm_k, ssm_D):
        B, S, D = q.shape
        ks = shift_k.shape[-1]
        k_pad = _pad(k, ((0, 0), (ks - 1, 0), (0, 0)))
        k_shift = torch.zeros_like(k)
        for t in range(ks):
            k_shift = k_shift + k_pad[:, t:t + S, :] * shift_k[:, ks - 1 - t][None, None, :]
        kv = k_shift * v
        L = 2 * S
        kv_pad = _pad(kv, ((0, 0), (0, S), (0, 0)))
        kf_pad = _pad(ssm_k, ((0, 0), (0, S)))
        kv_f = (lambda x, axes=(-2,-1): torch.fft.rfft2(x.float(), dim=axes))(kv_pad[:, None, :, :], axes=(1, 2))
        kf_f = (lambda x, axes=(-2,-1): torch.fft.rfft2(x.float(), dim=axes))(kf_pad[:, None, :], axes=(1, 2))
        kf_f = (lambda x, *d: x.permute(*d))(kf_f, 1, 2, 0)
        y_f = kv_f * kf_f[None, :, :, :]
        y = (lambda x, s, axes=(-2,-1): torch.fft.irfft2(x, s=s, dim=axes).to(torch.bfloat16))(y_f, (1, L), axes=(1, 2))[:, 0, :S, :]
        y = y + kv * ssm_D[None, None, :]
        return q * y

    x = A_x
    x = x + _A_ref(_layernorm(x, axis=-1), A_wqkv, A_wo)
    x = x + _B_ref(_layernorm(x, axis=-1), B_k, B_v, B_shift_k, B_ssm_k, B_ssm_D)
    x = x + _A_ref(_layernorm(x, axis=-1), A_wqkv, A_wo)
    x = x + _B_ref(_layernorm(x, axis=-1), B_k, B_v, B_shift_k, B_ssm_k, B_ssm_D)
    return x
```

```cpp
#include <hip/hip_runtime.h>
#include <hip/hip_bf16.h>
#include <stdint.h>

typedef unsigned short u16;
__device__ __forceinline__ float bf2f(u16 x) { return __uint_as_float(((unsigned)x) << 16); }
__device__ __forceinline__ u16 f2bf(float f) { __hip_bfloat16 b = __float2bfloat16(f); return *reinterpret_cast<u16*>(&b); }
__device__ __forceinline__ float rbf(float f) { return bf2f(f2bf(f)); }

constexpr int S = 2048, D = 512, H = 32, DH = 16;

__global__ void k_kv(const u16* k, const u16* v, const u16* sk, u16* u) {
    int i = blockIdx.x * blockDim.x + threadIdx.x; int t = i / D, d = i % D;
    float acc = 0.f;
    for (int j = 0; j < 4; ++j) { int tt = t + j - 3; float kk = tt >= 0 ? bf2f(k[tt * D + d]) : 0.f;
        acc = rbf(acc + rbf(kk * bf2f(sk[d * 4 + 3 - j]))); }
    u[i] = f2bf(acc * bf2f(v[i]));
}
__global__ void k_conv(const u16* u, const u16* h, const u16* Dv, u16* Y) {
    int i = blockIdx.x * blockDim.x + threadIdx.x; int t = i / D, d = i % D;
    float acc = 0.f;
    for (int s = 0; s <= t; ++s) acc += bf2f(h[d * S + t - s]) * bf2f(u[s * D + d]);
    float y = rbf(acc);
    Y[i] = f2bf(y + rbf(bf2f(u[i]) * bf2f(Dv[d])));
}
__device__ void ln_row(const u16* x, float* out8, int lane) {
    float v[8]; float s = 0.f;
    for (int j = 0; j < 8; ++j) { v[j] = bf2f(x[lane * 8 + j]); s += v[j]; }
    for (int m = 32; m >= 1; m >>= 1) s += __shfl_xor(s, m);
    float mean = s / D; float q = 0.f;
    for (int j = 0; j < 8; ++j) { float dlt = v[j] - mean; q += dlt * dlt; }
    for (int m = 32; m >= 1; m >>= 1) q += __shfl_xor(q, m);
    float var = rbf(q / D); float mb = rbf(mean);
    float den = rbf(sqrtf(rbf(var + 1e-5f)));
    for (int j = 0; j < 8; ++j) out8[j] = rbf(rbf(v[j] - mb) / den);
}
__global__ void k_ln(const u16* x, u16* o) {
    int row = blockIdx.x, lane = threadIdx.x; float r[8]; ln_row(x + row * D, r, lane);
    for (int j = 0; j < 8; ++j) o[row * D + lane * 8 + j] = f2bf(r[j]);
}
__global__ void k_lny(const u16* x, const u16* Y, u16* o) {
    int row = blockIdx.x, lane = threadIdx.x; float r[8]; ln_row(x + row * D, r, lane);
    for (int j = 0; j < 8; ++j) { int i = row * D + lane * 8 + j; o[i] = f2bf(bf2f(x[i]) + rbf(r[j] * bf2f(Y[i]))); }
}
__global__ void k_gemm(const u16* A, const u16* B, const u16* res, u16* C, int N, int K) {
    int n = blockIdx.x * blockDim.x + threadIdx.x, m = blockIdx.y;
    float acc = 0.f;
    for (int k = 0; k < K; ++k) acc += bf2f(A[m * K + k]) * bf2f(B[k * N + n]);
    float r = rbf(acc);
    if (res) r = bf2f(res[m * N + n]) + r;
    C[m * N + n] = f2bf(r);
}
__global__ void k_attn(const u16* qkv, u16* o) {
    int i = blockIdx.x * blockDim.x + threadIdx.x; int t = i / H, h = i % H;
    float lg[8]; float mx = -1e30f;
    for (int w = 0; w < 8; ++w) { int s = t - 2 * w; float a = 0.f;
        if (s >= 0) { for (int d = 0; d < DH; ++d) a += bf2f(qkv[t * 1536 + h * 16 + d]) * bf2f(qkv[s * 1536 + 512 + h * 16 + d]); a = rbf(rbf(a) * 0.25f); }
        else a = rbf(-1e9f);
        lg[w] = a; mx = fmaxf(mx, a); }
    float sum = 0.f; for (int w = 0; w < 8; ++w) { lg[w] = expf(lg[w] - mx); sum += lg[w]; }
    for (int w = 0; w < 8; ++w) lg[w] = rbf(lg[w] / sum);
    for (int d = 0; d < DH; ++d) { float a = 0.f;
        for (int w = 0; w < 8; ++w) { int s = t - 2 * w; if (s < 0) s = 0; a += lg[w] * bf2f(qkv[s * 1536 + 1024 + h * 16 + d]); }
        o[t * D + h * 16 + d] = f2bf(a); }
}

extern "C" void kernel_launch(void* const* d_in, const int* in_sizes, int n_in,
                              void* d_out, int out_size, void* d_ws, size_t ws_size,
                              hipStream_t stream) {
    const u16* x = (const u16*)d_in[0]; const u16* wqkv = (const u16*)d_in[1]; const u16* wo = (const u16*)d_in[2];
    const u16* bk = (const u16*)d_in[4]; const u16* bv = (const u16*)d_in[5]; const u16* sk = (const u16*)d_in[6];
    const u16* hk = (const u16*)d_in[7]; const u16* Dv = (const u16*)d_in[8];
    u16* ws = (u16*)d_ws; const size_t SD = (size_t)S * D;
    u16 *u = ws, *Y = ws + SD, *ln = ws + 2 * SD, *qkv = ws + 3 * SD, *o = ws + 6 * SD, *x1 = ws + 7 * SD, *x2 = ws + 8 * SD, *x3 = ws + 9 * SD;
    u16* out = (u16*)d_out;
    k_kv<<<SD / 256, 256, 0, stream>>>(bk, bv, sk, u);
    k_conv<<<SD / 256, 256, 0, stream>>>(u, hk, Dv, Y);
    k_ln<<<S, 64, 0, stream>>>(x, ln);
    k_gemm<<<dim3(1536 / 256, S), 256, 0, stream>>>(ln, wqkv, nullptr, qkv, 1536, 512);
    k_attn<<<S * H / 256, 256, 0, stream>>>(qkv, o);
    k_gemm<<<dim3(512 / 256, S), 256, 0, stream>>>(o, wo, x, x1, 512, 512);
    k_lny<<<S, 64, 0, stream>>>(x1, Y, x2);
    k_ln<<<S, 64, 0, stream>>>(x2, ln);
    k_gemm<<<dim3(1536 / 256, S), 256, 0, stream>>>(ln, wqkv, nullptr, qkv, 1536, 512);
    k_attn<<<S * H / 256, 256, 0, stream>>>(qkv, o);
    k_gemm<<<dim3(512 / 256, S), 256, 0, stream>>>(o, wo, x2, x3, 512, 512);
    k_lny<<<S, 64, 0, stream>>>(x3, Y, out);
}
```

```cpp
#include <hip/hip_runtime.h>
#include <stdint.h>

typedef unsigned short u16;
typedef unsigned u32;
typedef unsigned long long u64;
typedef short s16x4 __attribute__((ext_vector_type(4)));
typedef short s16x8 __attribute__((ext_vector_type(8)));
typedef __bf16 bf16x2 __attribute__((ext_vector_type(2)));
typedef float f32x2 __attribute__((ext_vector_type(2)));
typedef float f32x4 __attribute__((ext_vector_type(4)));
typedef u32 u32x2 __attribute__((ext_vector_type(2)));
typedef u32 u32x4 __attribute__((ext_vector_type(4)));
#define LAS __attribute__((address_space(3)))
#define DEV __device__ __forceinline__
typedef __amdgpu_buffer_rsrc_t rsrc_t;

constexpr u32 WS_EPOCH = 0;
constexpr u32 WS_FLAGS = 4096;
constexpr u32 WS_ST1 = 8192;
constexpr u32 WS_ST2 = WS_ST1 + 262144;
constexpr u32 WS_ST3 = WS_ST2 + 262144;
constexpr u32 WS_UT = 1u << 20;
constexpr u32 WS_O1 = WS_UT + (2u << 20);
constexpr u32 WS_O2 = WS_O1 + (2u << 20);
constexpr u32 WS_Y2 = WS_O2 + (2u << 20);
constexpr u32 WS_X2 = WS_Y2 + (2u << 20);
constexpr u32 WS_END = WS_X2 + (2u << 20);

constexpr u32 L_SLOT = 34816;
constexpr u32 L_KV = 4 * L_SLOT;
constexpr u32 L_K = L_KV, L_V = L_KV + 9216;
constexpr u32 L_SK = L_KV + 17408;
constexpr u32 L_WO = 0;
constexpr u32 L_CX = 65536;
constexpr u32 L_HC = L_CX;
constexpr u32 L_UR = L_CX + 69632;
constexpr u32 L_WX = L_CX + 32768;
constexpr u32 L_EX = 147456;
constexpr u32 L_TAB = L_EX + 12288;
constexpr u32 L_TOTAL = 160 * 1024;

constexpr int SPIN_CAP = 1 << 14;
constexpr u32 SENT = 0xFFFFFFFFu;

#define WAIT_VM(n) asm volatile("s_waitcnt vmcnt(%0)" ::"i"(n) : "memory")
#define BAR() do { asm volatile("s_waitcnt lgkmcnt(0)" ::: "memory"); __builtin_amdgcn_s_barrier(); asm volatile("" ::: "memory"); } while (0)
#define RBAR() do { asm volatile("" ::: "memory"); __builtin_amdgcn_s_barrier(); asm volatile("" ::: "memory"); } while (0)
#define CFENCE() asm volatile("" ::: "memory")
#define SB() __builtin_amdgcn_sched_barrier(0)
template <int N> struct IC { static constexpr int value = N; };

DEV f32x4 mfma32(s16x8 a, s16x8 b, f32x4 c) { return __builtin_amdgcn_mfma_f32_16x16x32_bf16(a, b, c, 0, 0, 0); }
DEV f32x4 mfma16(s16x4 a, s16x4 b, f32x4 c) { return __builtin_amdgcn_mfma_f32_16x16x16bf16_1k(a, b, c, 0, 0, 0); }
DEV float vmax(float a, float b) { float r; asm("v_max_f32 %0, %1, %2" : "=v"(r) : "v"(a), "v"(b)); return r; }
DEV u32 pk2(float lo, float hi) { f32x2 f = {lo, hi}; bf16x2 b = __builtin_convertvector(f, bf16x2); return __builtin_bit_cast(u32, b); }
DEV float bflo(u32 w) { return __uint_as_float(w << 16); }
DEV float bfhi(u32 w) { return __uint_as_float(w & 0xffff0000u); }
DEV float dot2(u32 a, u32 b, float c) { return __builtin_amdgcn_fdot2_f32_bf16(__builtin_bit_cast(bf16x2, a), __builtin_bit_cast(bf16x2, b), c, false); }
DEV u32 nosent(u32 w) { return min(w, 0xFFFFFFFEu); }
DEV float xsum(float v) {
    auto r = __builtin_amdgcn_permlane32_swap(__float_as_uint(v), __float_as_uint(v), false, false);
    v = __uint_as_float(r[0]) + __uint_as_float(r[1]);
    auto s = __builtin_amdgcn_permlane16_swap(__float_as_uint(v), __float_as_uint(v), false, false);
    return __uint_as_float(s[0]) + __uint_as_float(s[1]);
}
DEV float xmax(float v) {
    auto r = __builtin_amdgcn_permlane32_swap(__float_as_uint(v), __float_as_uint(v), false, false);
    v = vmax(__uint_as_float(r[0]), __uint_as_float(r[1]));
    auto s = __builtin_amdgcn_permlane16_swap(__float_as_uint(v), __float_as_uint(v), false, false);
    return vmax(__uint_as_float(s[0]), __uint_as_float(s[1]));
}
DEV int opq(int v) { asm volatile("" : "+v"(v)); return v; }
DEV s16x8 cat8(s16x4 a, s16x4 b) { return __builtin_shufflevector(a, b, 0, 1, 2, 3, 4, 5, 6, 7); }
DEV s16x4 pack4(f32x4 v) { u32x2 w = {pk2(v[0], v[1]), pk2(v[2], v[3])}; return __builtin_bit_cast(s16x4, w); }
DEV u64 mix64(u64 z) { z += 0x9E3779B97F4A7C15ull; z = (z ^ (z >> 30)) * 0xBF58476D1CE4E5B9ull; z = (z ^ (z >> 27)) * 0x94D049BB133111EBull; return z ^ (z >> 31); }

#define STAMP(i) do { } while (0)

__global__ void __launch_bounds__(512, 2)
fused_kernel(const u16* __restrict__ gx, const u16* __restrict__ gwqkv, const u16* __restrict__ gwo,
             const u16* __restrict__ gbk, const u16* __restrict__ gbv, const u16* __restrict__ gsk,
             const u16* __restrict__ ghk, const u16* __restrict__ gdv, u16* __restrict__ gout, char* __restrict__ ws) {
    __shared__ __attribute__((aligned(16))) char lds_[L_TOTAL];
    LAS char* const lds = (LAS char*)lds_;
    STAMP(0);
    const int b = blockIdx.x, tid = threadIdx.x, lane = tid & 63;
    const int wave = __builtin_amdgcn_readfirstlane(tid >> 6);
    const int hd = wave & 3, role = wave >> 2;
    const int grp = (b & 7) * 4 + (b >> 6), mem = (b >> 3) & 7;
    const int T0 = grp * 64;
    const int rot = (mem + 2 * (grp & 3)) & 7;
    const int li = lane & 15, lq = lane >> 4;

    const u64 epoch = *(const u64*)(ws + WS_EPOCH);
    const u32 dw = *(const u32*)(gdv + 2 * b);
    const rsrc_t r_x = __builtin_amdgcn_make_buffer_rsrc((void*)gx, 0, 2048 * 512 * 2, 0x00020000);
    const rsrc_t r_h = __builtin_amdgcn_make_buffer_rsrc((void*)ghk, 0, 512 * 2048 * 2, 0x00020000);
    const rsrc_t r_ws = __builtin_amdgcn_make_buffer_rsrc((void*)ws, 0, WS_END, 0x00020000);
    const rsrc_t r_out = __builtin_amdgcn_make_buffer_rsrc((void*)gout, 0, 2048 * 512 * 2, 0x00020000);
    const u32 ldsbase = (u32)(size_t)lds;
    auto mkdesc = [](const void* p, u32 bytes) { const u64 a = (u64)p; return (u32x4){(u32)a, (u32)(a >> 32) & 0xffffu, bytes, 0x00020000u}; };
    const u32x4 d_x = mkdesc(gx, 2048 * 512 * 2), d_w = mkdesc(gwqkv, 512 * 1536 * 2), d_wo = mkdesc(gwo, 512 * 512 * 2);
    const u32x4 d_k = mkdesc(gbk, 2048 * 512 * 2), d_v = mkdesc(gbv, 2048 * 512 * 2), d_sk = mkdesc(gsk, 512 * 4 * 2);
    const u32x4 d_x2 = mkdesc(ws + WS_X2, 2u << 20);
    auto dma = [&](u32x4 rs, u32 ldsoff, u32 voff, u32 soff) {
        asm volatile("s_mov_b32 m0, %0\n\ts_nop 0\n\tbuffer_load_dwordx4 %1, %2, %3 offen lds"
                     :: "s"(ldsbase + ldsoff), "v"(voff), "s"(rs), "s"(soff) : "memory");
    };
    auto dma_sc1 = [&](u32x4 rs, u32 ldsoff, u32 voff, u32 soff) {
        asm volatile("s_mov_b32 m0, %0\n\ts_nop 0\n\tbuffer_load_dwordx4 %1, %2, %3 offen sc1 lds"
                     :: "s"(ldsbase + ldsoff), "v"(voff), "s"(rs), "s"(soff) : "memory");
    };
    auto dma_q = [&](u32x4 rs, u32 ldsoff, u32 voff, u32 soff) {
        asm volatile("s_mov_b32 m0, %0\n\ts_mov_b64 exec, 0xffff\n\tbuffer_load_dwordx4 %1, %2, %3 offen lds\n\ts_mov_b64 exec, -1"
                     :: "s"(ldsbase + ldsoff), "v"(voff), "s"(rs), "s"(soff) : "memory");
    };
    auto dma_q_sc1 = [&](u32x4 rs, u32 ldsoff, u32 voff, u32 soff) {
        asm volatile("s_mov_b32 m0, %0\n\ts_mov_b64 exec, 0xffff\n\tbuffer_load_dwordx4 %1, %2, %3 offen sc1 lds\n\ts_mov_b64 exec, -1"
                     :: "s"(ldsbase + ldsoff), "v"(voff), "s"(rs), "s"(soff) : "memory");
    };

    const int pr = lane >> 3, pc = lane & 7;
    const int xr0 = 8 * wave + pr, xr2 = 64 + 2 * wave + pr;
    const u32 xv0 = (u32)((T0 - 16 + xr0) * 1024 + ((pc ^ ((xr0 >> 1) & 7)) << 4));
    const u32 xv2 = (u32)((T0 - 16 + xr2) * 1024 + ((pc ^ ((xr2 >> 1) & 7)) << 4));
    const int wfw = ((xr0 >> 1) & 1) | (((xr0 >> 3) & 1) << 1);
    const u32 wv0 = (u32)(xr0 * 3072 + mem * 128 + ((((pc >> 1) ^ wfw) * 2 + (pc & 1)) << 4));
    auto issue_piece = [&](int s, int i) {
        const u32 kp = (u32)((s + rot) & 7);
        const u32 sb = (u32)(s & 3) * L_SLOT;
        if (i == 0) dma(d_x, sb + wave * 1024, xv0, kp * 128);
        else if (i == 1) dma_q(d_x, sb + 8192 + wave * 256, xv2, kp * 128);
        else { const int g = i - 2; dma(d_w, sb + 10240 + g * 8192 + wave * 1024, wv0, kp * (64 * 3072) + g * 1024); }
    };
    auto issue_stage = [&](int s) {
#pragma unroll
        for (int i = 0; i < 5; ++i) issue_piece(s, i);
    };
    const u32 wov = (u32)(xr0 * 1024 + mem * 128 + ((((pc >> 1) ^ ((pr >> 1) & 3)) * 2 + (pc & 1)) << 4));
    auto issue_wo = [&](int i) { dma(d_wo, L_WO + i * 8192 + wave * 1024, wov, i * 65536); };

    issue_stage(0);
    {
        const u32 cb = (u32)(mem * 128);
        dma(d_k, L_K + wave * 1024, (u32)((T0 - 8 + xr0) * 1024) + cb + ((pc ^ wave) << 4), 0);
        dma(d_v, L_V + wave * 1024, (u32)((T0 + xr0) * 1024) + cb + ((pc ^ wave) << 4), 0);
        if (wave == 0) dma(d_k, L_K + 8 * 1024, (u32)((T0 - 8 + 64 + pr) * 1024) + cb + (pc << 4), 0);
        else dma(d_sk, L_SK, (u32)(mem * 512 + lane * 16), 0);
    }
    issue_stage(1);
    const u64 tag = mix64(epoch);

    const u32 xa0 = (u32)(li * 128 + ((lq ^ (li >> 1)) << 4));
    const u32 xa1 = (u32)(li * 128 + (((4 + lq) ^ (li >> 1)) << 4));
    const int tq = (lane & 15) >> 2, tp = lane & 3;
    const u32 wa = (u32)((8 * lq + tq) * 128 + ((hd ^ (((tq >> 1) & 1) | ((lq & 1) << 1))) << 5) + tp * 8);
    auto trr = [&](u32 off) { return __builtin_amdgcn_ds_read_tr16_b64_v4i16((LAS s16x4*)(lds + off)); };
    auto ldsA = [&](u32 off) { return *(LAS s16x8*)(lds + off); };

    s16x8 Wq[16], Wm[16];
    f32x4 accM[5], accQ[2], csQ, csM;
    const f32x4 zero4 = {0.f, 0.f, 0.f, 0.f};
    const s16x8 ones8 = {0x3F80, 0x3F80, 0x3F80, 0x3F80, 0x3F80, 0x3F80, 0x3F80, 0x3F80};
    float st_s = 0.f, st_q = 0.f;
    const int stile = wave < 4 ? wave : 4;
    s16x8 Ab[2][5], Ao[2];
    LAS float* const mtab = (LAS float*)(lds + L_TAB);
    LAS float* const rtab = mtab + 80;
    auto put_stat = [&](int tile, float s, float q) {
        const int lane = opq(tid) & 63, li = lane & 15, lq = lane >> 4;
        const float m = s * (1.f / 512.f);
        const float var = fmaxf(q * (1.f / 512.f) - m * m, 0.f);
        if (lq == 0) { mtab[tile * 16 + li] = m; rtab[tile * 16 + li] = __builtin_amdgcn_rsqf(var + 1e-5f); }
    };
    auto qkv_step = [&](auto rc, int st, const s16x8 (&A)[5], auto fill) {
        constexpr int R = decltype(rc)::value;
#pragma unroll
        for (int mt = 0; mt < 5; ++mt) {
            const f32x4 cm = st ? accM[mt] : zero4;
            accM[mt] = R ? mfma32(A[mt], Wm[st], cm) : mfma32(Wm[st], A[mt], cm);
            if (mt == 1 + 2 * R || mt == 2 + 2 * R) { const int j = mt - 1 - 2 * R; accQ[j] = mfma32(Wq[st], A[mt], st ? accQ[j] : zero4); }
            fill(mt);
        }
    };

    LAS float* const cstab = (LAS float*)(lds + L_TAB + 1024) + wave * 32;
    u32x4 fl0, fl1, hd0, hd1;
    auto phase1 = [&](auto rc) {
        constexpr int R = decltype(rc)::value;
        u32 xa, wb;
        auto pf_addr = [&](int st) {
            const u32 sb = (u32)((st >> 1) & 3) * L_SLOT;
            xa = sb + ((st & 1) ? xa1 : xa0); wb = sb + 10240 + (st & 1) * 4096 + wa;
            asm volatile("" : "+v"(xa), "+v"(wb));
        };
        auto pf_part = [&](int st, int mt) {
            if (mt == 0) Wm[st] = cat8(trr(wb + (1 + R) * 8192), trr(wb + (1 + R) * 8192 + 512));
            Ab[st & 1][mt] = ldsA(xa + mt * 2048);
            if (mt == 1) Wq[st] = cat8(trr(wb), trr(wb + 512));
            if (mt == 2) Ao[st & 1] = ldsA(xa + stile * 2048);
        };
        auto stat_part = [&](int st, int i) { const u32x4 w = __builtin_bit_cast(u32x4, Ao[st & 1]); st_s = dot2(w[i], 0x3F803F80u, st_s); st_q = dot2(w[i], w[i], st_q); };
        STAMP(15);
        WAIT_VM(8);
        RBAR();
        STAMP(16);
        pf_addr(0);
#pragma unroll
        for (int mt = 0; mt < 5; ++mt) pf_part(0, mt);
#pragma unroll
        for (int st = 0; st < 16; ++st) {
            if ((st & 1) && st < 15) {
                const int s1 = (st >> 1) + 1;
                if (s1 == 1) WAIT_VM(5); else if (s1 == 2) WAIT_VM(6); else if (s1 == 3) WAIT_VM(11); else if (s1 == 4) WAIT_VM(10);
                else if (s1 == 5) WAIT_VM(5); else if (s1 == 6) WAIT_VM(5); else WAIT_VM(4);
                RBAR();
                STAMP(16 + s1);
                if (R == 0 && s1 == 5 && tid == 0) __builtin_amdgcn_raw_buffer_store_b64(__builtin_bit_cast(u32x2, tag), r_ws, WS_FLAGS + b * 8, 0, 16);
                CFENCE();
            }
            if (st < 15) pf_addr(st + 1);
            if (st == 15) {
#pragma unroll
                for (int i = 0; i < 4; ++i) stat_part(15, i);
                const float s = xsum(st_s), q = xsum(st_q);
                if (wave < 5) put_stat(stile, s, q);
            }
            SB();
            qkv_step(rc, st, Ab[st & 1], [&](int mt) {
                if (st < 15) pf_part(st + 1, mt);
                if (st == 0) issue_piece(2, mt);
                if (st >= 1 && st <= 10) { const int sn = ((st - 1) >> 1) + 3; if ((st - 1) & 1) { if (mt == 1) issue_piece(sn, 3); if (mt == 3) issue_piece(sn, 4); } else if (!(mt & 1)) issue_piece(sn, mt >> 1); }
                if (st >= 11 && st <= 14 && (mt & 1)) issue_wo((st - 11) * 2 + (mt >> 1));
                if (mt < 4 && st < 15) stat_part(st, mt);
                SB();
            });
            csQ = mfma32(Wq[st], ones8, st ? csQ : zero4);
            csM = R ? mfma32(ones8, Wm[st], st ? csM : zero4) : mfma32(Wm[st], ones8, st ? csM : zero4);
            SB();
            if (st == 2) {
                const int l2 = opq(tid) & 63, tg = l2 & 7, chl = l2 >> 3;
                const u32x2 skw = *(LAS u32x2*)(lds + L_SK + (8 * wave + chl) * 8);
                const u32 ka = L_K + tg * 1024 + (((wave ^ tg) & 7) << 4) + chl * 2;
                const u32 kb = L_K + tg * 1024 + (((wave ^ (tg + 1)) & 7) << 4) + chl * 2;
                float kf[11], vf[8];
#pragma unroll
                for (int i = 0; i < 11; ++i) kf[i] = bflo(*(LAS u16*)(lds + ((i + 5 < 8) ? ka : kb) + (i + 5) * 128));
#pragma unroll
                for (int i = 0; i < 8; ++i) vf[i] = bflo(*(LAS u16*)(lds + ka + (L_V - L_K) + i * 128));
                const float a0 = bflo(skw[0]), a1 = bfhi(skw[0]), a2 = bflo(skw[1]), a3 = bfhi(skw[1]);
                float ua[8];
#pragma unroll
                for (int i = 0; i < 8; ++i) ua[i] = (kf[i] * a3 + kf[i + 1] * a2 + kf[i + 2] * a1 + kf[i + 3] * a0) * vf[i];
                const u32x4 oa = {pk2(ua[7], ua[6]), pk2(ua[5], ua[4]), pk2(ua[3], ua[2]), pk2(ua[1], ua[0])};
                __builtin_amdgcn_raw_buffer_store_b128(oa, r_ws, WS_UT + (u32)((mem * 64 + 8 * wave + chl) * 4096 + (2040 - T0 - 8 * tg) * 2), 0, 16);
                CFENCE();
                SB();
            }
            if (st == 4) {
                const int l2 = opq(tid) & 63;
                const u32x4 sv = {SENT, SENT, SENT, SENT};
                const u32 tb = (u32)((4 * grp + hd) * 16384 + mem * 2048 + role * 1024 + l2 * 16);
                __builtin_amdgcn_raw_buffer_store_b128(sv, r_ws, tb, WS_O1, 16);
                __builtin_amdgcn_raw_buffer_store_b128(sv, r_ws, tb, WS_O2, 16);
                __builtin_amdgcn_raw_buffer_store_b128(sv, r_ws, tb, WS_X2, 16);
                __builtin_amdgcn_raw_buffer_store_b128(sv, r_ws, (u32)(b * 8192 + wave * 1024 + l2 * 16), WS_Y2, 16);
                const u32 so = l2 < 24 ? WS_ST1 + (u32)((l2 >> 3) * 262144 + ((4 * grp + hd) * 8 + mem) * 256 + role * 128 + (l2 & 7) * 16) : 0xFFFFFF00u;
                __builtin_amdgcn_raw_buffer_store_b128(sv, r_ws, so, 0, 16);
                CFENCE();
                SB();
            }
        }
        if (li == 0) *(LAS f32x4*)(cstab + 4 * lq) = csQ;
        if (R == 0) { if (li == 0) *(LAS f32x4*)(cstab + 16 + 4 * lq) = csM; } else if (lq == 0) cstab[16 + li] = csM[0];
    };
    if (role == 0) phase1(IC<0>{}); else phase1(IC<1>{});
    {
        const int t2 = opq(tid);
        fl0 = __builtin_amdgcn_raw_buffer_load_b128(r_ws, WS_FLAGS + (t2 & 63) * 32, 0, 16);
        fl1 = __builtin_amdgcn_raw_buffer_load_b128(r_ws, WS_FLAGS + (t2 & 63) * 32 + 16, 0, 16);
        hd0 = __builtin_amdgcn_raw_buffer_load_b128(r_h, (u32)(b * 8192 + t2 * 16), 0, 0);
        hd1 = __builtin_amdgcn_raw_buffer_load_b128(r_h, (u32)(b * 8192 + t2 * 16 + 16), 0, 0);
        SB();
    }
    BAR();
    STAMP(1);

    auto attention = [&](u32 ws_o) {
        const int lane = opq(tid) & 63, li = lane & 15, lq = lane >> 4;
        const int par = li & 1;
        const bool cur0 = (4 * lq + par) <= li, cur1 = (4 * lq + par + 2) <= li;
        const u32 pm = par ? 0xffff0000u : 0x0000ffffu;
        const u32 mC0 = cur0 ? pm : 0u, mP0 = cur0 ? 0u : pm, mC1 = cur1 ? pm : 0u, mP1 = cur1 ? 0u : pm;
        s16x4 Kt[3], Vt[3], Qt[2];
        float rq[2], mq[2];
        const u32 exw = L_EX + wave * 1536 + lane * 8, exr = L_EX + (wave ^ 4) * 1536 + lane * 8;
        const f32x4 csQ = *(LAS f32x4*)(cstab + 4 * lq);
        if (role == 0) {
            const f32x4 csM = *(LAS f32x4*)(cstab + 16 + 4 * lq);
            float m1[5], r1[5];
#pragma unroll
            for (int mt = 0; mt < 5; ++mt) { m1[mt] = mtab[mt * 16 + li]; r1[mt] = rtab[mt * 16 + li]; }
            SB();
            s16x4 Kb[5];
#pragma unroll
            for (int mt = 4; mt >= 0; --mt) {
                Kb[mt] = pack4((accM[mt] - m1[mt] * csM) * r1[mt]);
                if (mt >= 2) *(LAS s16x4*)(lds + exw + (mt - 2) * 512) = Kb[mt];
            }
#pragma unroll
            for (int j = 0; j < 3; ++j) Kt[j] = Kb[j];
#pragma unroll
            for (int j = 0; j < 2; ++j) { mq[j] = m1[1 + j]; rq[j] = r1[1 + j]; Qt[j] = pack4(accQ[j] - mq[j] * csQ); }
            BAR();
#pragma unroll
            for (int j = 0; j < 3; ++j) Vt[j] = *(LAS s16x4*)(lds + exr + j * 512);
        } else {
            const float csV = cstab[16 + li];
            f32x4 m4[5], r4[5];
#pragma unroll
            for (int mt = 0; mt < 5; ++mt) { m4[mt] = *(LAS f32x4*)(mtab + mt * 16 + 4 * lq); r4[mt] = *(LAS f32x4*)(rtab + mt * 16 + 4 * lq); }
#pragma unroll
            for (int j = 0; j < 2; ++j) { mq[j] = mtab[(3 + j) * 16 + li]; rq[j] = rtab[(3 + j) * 16 + li]; }
            SB();
            s16x4 Vb[5];
#pragma unroll
            for (int mt = 0; mt < 5; ++mt) {
                Vb[mt] = pack4((accM[mt] - m4[mt] * csV) * r4[mt]);
                if (mt < 3) *(LAS s16x4*)(lds + exw + mt * 512) = Vb[mt];
            }
#pragma unroll
            for (int j = 0; j < 3; ++j) Vt[j] = Vb[2 + j];
#pragma unroll
            for (int j = 0; j < 2; ++j) Qt[j] = pack4(accQ[j] - mq[j] * csQ);
            BAR();
#pragma unroll
            for (int j = 0; j < 3; ++j) Kt[j] = *(LAS s16x4*)(lds + exr + j * 512);
        }
        f32x4 Sc[2], Sp[2];
#pragma unroll
        for (int j = 0; j < 2; ++j) { Sc[j] = mfma16(Kt[j + 1], Qt[j], zero4); Sp[j] = mfma16(Kt[j], Qt[j], zero4); }
        float s0[2], s1[2], mx[2];
#pragma unroll
        for (int j = 0; j < 2; ++j) {
            const float sp0 = Sp[j][0], sp1 = Sp[j][1], sp2 = Sp[j][2], sp3 = Sp[j][3];
            float p0 = par ? sp1 : sp0, p1 = par ? sp3 : sp2;
            asm volatile("" : "+v"(p0), "+v"(p1));
            if (j == 0 && grp == 0 && role == 0) { p0 = -3.0e38f; p1 = -3.0e38f; }
            s0[j] = cur0 ? (par ? Sc[j][1] : Sc[j][0]) : p0; s1[j] = cur1 ? (par ? Sc[j][3] : Sc[j][2]) : p1;
            mx[j] = xmax(vmax(s0[j], s1[j]));
        }
        const u32 oo = ws_o + (u32)((4 * grp + 2 * role) * 16384 + (4 * mem + hd) * 512 + lane * 8);
#pragma unroll
        for (int j = 0; j < 2; ++j) {
            const float sc = rq[j] * (0.25f * 1.44269504088896341f);
            const float e0 = __builtin_amdgcn_exp2f((s0[j] - mx[j]) * sc), e1 = __builtin_amdgcn_exp2f((s1[j] - mx[j]) * sc);
            const u32 w0 = pk2(e0, e0), w1 = pk2(e1, e1);
            const u32x4 pw = {w0 & mC0, w1 & mC1, w0 & mP0, w1 & mP1};
            const s16x8 P = __builtin_bit_cast(s16x8, pw);
            const f32x4 O = mfma32(cat8(Vt[j + 1], Vt[j]), P, zero4);
            const f32x4 L = mfma32(ones8, P, zero4);
            const float inv = __builtin_amdgcn_rcpf(L[0]);
            const u32x2 ow = {nosent(pk2(O[0] * inv, O[1] * inv)), nosent(pk2(O[2] * inv, O[3] * inv))};
            __builtin_amdgcn_raw_buffer_store_b64(ow, r_ws, oo, j * 16384, 16);
        }
        SB();
    };
    attention(WS_O1);
    STAMP(2);

    for (int spin = 0; spin < SPIN_CAP; ++spin) {
        const u32 tl = (u32)tag, th = (u32)(tag >> 32);
        const u32 bad = (fl0[0] ^ tl) | (fl0[1] ^ th) | (fl0[2] ^ tl) | (fl0[3] ^ th) | (fl1[0] ^ tl) | (fl1[1] ^ th) | (fl1[2] ^ tl) | (fl1[3] ^ th);
        if (__all(bad == 0)) break;
        __builtin_amdgcn_s_sleep(2);
        CFENCE();
        fl0 = __builtin_amdgcn_raw_buffer_load_b128(r_ws, WS_FLAGS + lane * 32, 0, 16);
        fl1 = __builtin_amdgcn_raw_buffer_load_b128(r_ws, WS_FLAGS + lane * 32 + 16, 0, 16);
    }
    CFENCE();
    if (b == 0 && tid == 0) __builtin_amdgcn_raw_buffer_store_b64(__builtin_bit_cast(u32x2, epoch + 1), r_ws, WS_EPOCH, 0, 16);
    STAMP(3);

    const int TI = 4 * grp + hd;
    const int TOK = TI * 16 + li;
    u32x2 of[16];
    auto load_o = [&](u32 ws_o) {
        const int lane = opq(tid) & 63;
#pragma unroll
        for (int i = 0; i < 16; ++i) of[i] = __builtin_amdgcn_raw_buffer_load_b64(r_ws, ws_o + (u32)(TI * 16384 + role * 8192 + lane * 8) + (i & 7) * 512, (i >> 3) * 4096, 16);
    };
    u32x2 xres[2];

    {
        const u32x4 uu = __builtin_amdgcn_raw_buffer_load_b128(r_ws, WS_UT + (u32)(b * 8192 + tid * 16), 0, 16);
        SB();
        constexpr u32 OFFS = 0xEDA85410u;
        {
            const int j = tid & 255;
            const u32 cb = L_HC + role * 34816;
            const u32 d[8] = {hd0[0], hd0[1], hd0[2], hd0[3], hd1[0], hd1[1], hd1[2], hd1[3]};
            u32 e[7];
#pragma unroll
            for (int i = 0; i < 7; ++i) e[i] = __builtin_amdgcn_alignbit(d[i + 1], d[i], 16);
#pragma unroll
            for (int p = 0; p < 8; ++p) {
                const u32 po = (272 * p + ((OFFS >> (4 * p)) & 15)) * 16;
                const int h = p >> 1;
                const u32x4 w = (p & 1) ? (u32x4){e[h], e[h + 1], e[h + 2], e[h + 3]} : (u32x4){d[h], d[h + 1], d[h + 2], d[h + 3]};
                *(LAS u32x4*)(lds + cb + po + (j + 2) * 16) = w;
            }
            if (hd == 0) {
                const u32 h0 = __builtin_amdgcn_readfirstlane(d[0]), h1 = __builtin_amdgcn_readfirstlane(d[1]);
                const u32 h2 = __builtin_amdgcn_readfirstlane(d[2]), h3 = __builtin_amdgcn_readfirstlane(d[3]);
                if (lane < 16) {
                    const int p = lane & 7;
                    const u32 po = (272 * p + ((OFFS >> (4 * p)) & 15)) * 16;
                    const unsigned __int128 hv = ((unsigned __int128)(((u64)h3 << 32) | h2) << 64) | (((u64)h1 << 32) | h0);
                    const unsigned __int128 sv = (p == 0 || lane < 8) ? (unsigned __int128)0 : (hv << (16 * (8 - p)));
                    const u32x4 w = {(u32)sv, (u32)(sv >> 32), (u32)(sv >> 64), (u32)(sv >> 96)};
                    *(LAS u32x4*)(lds + cb + po + (lane >> 3) * 16) = w;
                }
            }
            if (hd == 1 && lane < 30) *(LAS u32x4*)(lds + L_UR + role * 4576 + 4096 + lane * 16) = (u32x4){0, 0, 0, 0};
            *(LAS u32x4*)(lds + L_UR + role * 4576 + j * 16) = uu;
        }
        BAR();
        STAMP(4);
        const int ch = (wave >> 1) & 1, e0 = (wave & 1) * 4;
        const int p = (li + 1) & 7;
        const u32 aA = L_HC + ch * 34816 + (272 * p + ((OFFS >> (4 * p)) & 15) + ((li + 1) >> 3) + lq + 4 * e0) * 16;
        const u32 aB = L_UR + ch * 4576 + (254 - 224 - 2 * li + lq + 4 * e0) * 16;
        f32x4 y[4];
        auto conv_mma = [&](auto rc) {
            constexpr int R = decltype(rc)::value;
            constexpr int NN[4] = {R ? 1 : 0, R ? 2 : 3, R ? 5 : 4, R ? 6 : 7};
            constexpr int NF = R ? 7 : 8;
#pragma unroll
            for (int e = 0; e < 4; ++e) {
                s16x8 A[8], B[8];
#pragma unroll
                for (int f = 0; f < 4; ++f) { A[f] = ldsA(aA + 64 * (8 * f + e)); B[f] = ldsA(aB + 64 * e + 512 * (7 - f)); }
#pragma unroll
                for (int f = 4; f < NF; ++f) B[f] = ldsA(aB + 64 * e + 512 * (7 - f));
                SB();
#pragma unroll
                for (int i = 3; i >= 0; --i) {
#pragma unroll
                    for (int f = 0; f <= NN[i] && f < 4; ++f) y[i] = mfma32(A[f], B[NN[i] - f], (e == 0 && f == 0) ? zero4 : y[i]);
                    if (i == 2) {
                        SB();
#pragma unroll
                        for (int f = 4; f < NF; ++f) A[f] = ldsA(aA + 64 * (8 * f + e));
                        SB();
                    }
                }
#pragma unroll
                for (int i = 2; i < 4; ++i)
#pragma unroll
                    for (int f = 4; f <= NN[i]; ++f) y[i] = mfma32(A[f], B[NN[i] - f], y[i]);
                SB();
            }
            asm volatile("" :: "v"(y[0]), "v"(y[1]), "v"(y[2]), "v"(y[3]));
            STAMP(14);
            load_o(WS_O1);
#pragma unroll
            for (int j = 0; j < 2; ++j) xres[j] = __builtin_amdgcn_raw_buffer_load_b64(r_x, (u32)(TOK * 1024 + mem * 128 + role * 64 + j * 32 + lq * 8), 0, 0);
            SB();
            BAR();
#pragma unroll
            for (int i = 0; i < 4; ++i) *(LAS f32x4*)(lds + L_HC + (hd * 8 + NN[i]) * 1024 + lane * 16) = y[i];
        };
        if (role == 0) conv_mma(IC<0>{}); else conv_mma(IC<1>{});
        BAR();
        const float D0 = bflo(dw), D1 = bfhi(dw);
        {
            const int n = wave;
            const u32 ra = L_HC + n * 1024 + lane * 16;
            const f32x4 ya = *(LAS f32x4*)(lds + ra) + *(LAS f32x4*)(lds + ra + 8192);
            const f32x4 yb = *(LAS f32x4*)(lds + ra + 16384) + *(LAS f32x4*)(lds + ra + 24576);
            const int tb = 256 * n + 16 * li + 4 * lq;
            const u32x2 ua = *(LAS u32x2*)(lds + L_UR + (2044 - tb) * 2), ub = *(LAS u32x2*)(lds + L_UR + 4576 + (2044 - tb) * 2);
            const u32x4 w = {nosent(pk2(ya[0] + bfhi(ua[1]) * D0, yb[0] + bfhi(ub[1]) * D1)), nosent(pk2(ya[1] + bflo(ua[1]) * D0, yb[1] + bflo(ub[1]) * D1)),
                             nosent(pk2(ya[2] + bfhi(ua[0]) * D0, yb[2] + bfhi(ub[0]) * D1)), nosent(pk2(ya[3] + bflo(ua[0]) * D0, yb[3] + bflo(ub[0]) * D1))};
            __builtin_amdgcn_raw_buffer_store_b128(w, r_ws, WS_Y2 + (u32)(b * 8192 + tb * 4), 0, 16);
        }
        SB();
    }
    STAMP(5);

    float xr[8];
    u32 yw[4];
    auto load_y = [&]() {
#pragma unroll
        for (int i = 0; i < 4; ++i)
            yw[i] = __builtin_amdgcn_raw_buffer_load_b32(r_ws, WS_Y2 + (u32)((32 * mem + 16 * role + 2 * lq) * 8192 + TOK * 4), (8 * (i >> 1) + (i & 1)) * 8192, 16);
    };
    auto pub_stat = [&](u32 ws_st, float s, float q) {
        const int lane = opq(tid) & 63, li = lane & 15, lq = lane >> 4;
        s = xsum(s); q = xsum(q);
        if (lq == 0) { const u32x2 sv = {nosent(__float_as_uint(s)), nosent(__float_as_uint(q))};
            __builtin_amdgcn_raw_buffer_store_b64(sv, r_ws, ws_st + (u32)((TI * 8 + mem) * 256 + role * 128 + li * 8), 0, 16); }
    };
    auto get_stat = [&](u32 ws_st, int ti, float& ts, float& tqq) {
        const int lane = opq(tid) & 63, li = lane & 15, lq = lane >> 4;
        u32x2 a[4];
        for (int spin = 0; spin < SPIN_CAP; ++spin) {
            CFENCE();
#pragma unroll
            for (int i = 0; i < 4; ++i) a[i] = __builtin_amdgcn_raw_buffer_load_b64(r_ws, ws_st + (u32)(ti * 2048 + lq * 512 + li * 8) + i * 128, 0, 16);
            if (__all(max(max(max(a[0][0], a[0][1]), max(a[1][0], a[1][1])), max(max(a[2][0], a[2][1]), max(a[3][0], a[3][1]))) != SENT)) break;
            __builtin_amdgcn_s_sleep(2);
        }
        ts = xsum((__uint_as_float(a[0][0]) + __uint_as_float(a[1][0])) + (__uint_as_float(a[2][0]) + __uint_as_float(a[3][0])));
        tqq = xsum((__uint_as_float(a[0][1]) + __uint_as_float(a[1][1])) + (__uint_as_float(a[2][1]) + __uint_as_float(a[3][1])));
    };
    auto wo_phase = [&](u32 ws_o, u32 ws_st, float& mean, float& rstd, auto pdc, auto after_pub) {
        constexpr int PD = decltype(pdc)::value;
        u32 woa[4];
        { const int l2 = opq(tid) & 63, lq = l2 >> 4, tq = (l2 & 15) >> 2, tp = l2 & 3;
#pragma unroll
          for (int j = 0; j < 4; ++j) woa[j] = L_WO + (u32)(role * 32768 + (4 * lq + tq) * 128 + (((j ^ (2 * role)) ^ (((lq & 1) << 1) | (tq >> 1))) << 5) + tp * 8); }
        s16x8 F[8][4];
#pragma unroll
        for (int ks = 0; ks < PD; ++ks)
#pragma unroll
            for (int n = 0; n < 4; ++n) F[ks][n] = cat8(trr(woa[n] + ks * 4096), trr(woa[n] + ks * 4096 + 2048));
        SB();
        for (int spin = 0; spin < 64; ++spin) {
            u32 m0 = 0, m1 = 0;
#pragma unroll
            for (int i = 0; i < 16; i += 2) { m0 = max(max(m0, of[i][0]), of[i][1]); m1 = max(max(m1, of[i + 1][0]), of[i + 1][1]); }
            if (__all(max(m0, m1) != SENT)) break;
            for (int sp2 = 0; sp2 < SPIN_CAP; ++sp2) {
                CFENCE();
                __builtin_amdgcn_s_sleep(2);
                const u32 pv = __builtin_amdgcn_raw_buffer_load_b32(r_ws, ws_o + (u32)(TI * 16384 + role * 8192 + (opq(tid) & 15) * 512 + 508), 0, 16);
                if (__all(pv != SENT)) break;
            }
            CFENCE();
            load_o(ws_o);
        }
        STAMP(24);
        f32x4 acc[4];
#pragma unroll
        for (int ks = 0; ks < 8; ++ks) {
            if (ks + PD < 8) {
#pragma unroll
                for (int n = 0; n < 4; ++n) F[ks + PD][n] = cat8(trr(woa[n] + (ks + PD) * 4096), trr(woa[n] + (ks + PD) * 4096 + 2048));
            }
            SB();
#pragma unroll
            for (int n = 3; n >= 0; --n) acc[n] = mfma32(F[ks][n], __builtin_bit_cast(s16x8, (u32x4){of[2 * ks][0], of[2 * ks][1], of[2 * ks + 1][0], of[2 * ks + 1][1]}), ks ? acc[n] : zero4);
            SB();
        }
        const int lane = opq(tid) & 63;
        *(LAS f32x4*)(lds + L_WX + wave * 2048 + lane * 16) = acc[2];
        *(LAS f32x4*)(lds + L_WX + wave * 2048 + 1024 + lane * 16) = acc[3];
        BAR();
        STAMP(25);
        float s = 0.f, q = 0.f;
#pragma unroll
        for (int j = 0; j < 2; ++j) {
            const f32x4 t = acc[j] + *(LAS f32x4*)(lds + L_WX + (wave ^ 4) * 2048 + j * 1024 + lane * 16);
            const u32 w0 = pk2(xr[4 * j] + t[0], xr[4 * j + 1] + t[1]), w1 = pk2(xr[4 * j + 2] + t[2], xr[4 * j + 3] + t[3]);
            xr[4 * j] = bflo(w0); xr[4 * j + 1] = bfhi(w0); xr[4 * j + 2] = bflo(w1); xr[4 * j + 3] = bfhi(w1);
            s = dot2(w0, 0x3F803F80u, s); q = dot2(w0, w0, q); s = dot2(w1, 0x3F803F80u, s); q = dot2(w1, w1, q);
        }
        pub_stat(ws_st, s, q);
        after_pub();
        WAIT_VM(0);
        STAMP(26);
        float ts, tqq;
        get_stat(ws_st, TI, ts, tqq);
        mean = ts * (1.f / 512.f);
        rstd = __builtin_amdgcn_rsqf(fmaxf(tqq * (1.f / 512.f) - mean * mean, 0.f) + 1e-5f);
    };
#pragma unroll
    for (int j = 0; j < 2; ++j) { xr[4 * j] = bflo(xres[j][0]); xr[4 * j + 1] = bfhi(xres[j][0]); xr[4 * j + 2] = bflo(xres[j][1]); xr[4 * j + 3] = bfhi(xres[j][1]); }
    float mean, rstd;
    wo_phase(WS_O1, WS_ST1, mean, rstd, IC<2>{}, [&]() { CFENCE(); load_y(); CFENCE(); });
    STAMP(6);
    for (int spin = 0; spin < SPIN_CAP; ++spin) {
        if (__all(max(max(yw[0], yw[1]), max(yw[2], yw[3])) != SENT)) break;
        __builtin_amdgcn_s_sleep(2);
        CFENCE();
        load_y();
    }
    auto gate = [&](float mean, float rstd, u32 (&w)[4]) {
#pragma unroll
        for (int i = 0; i < 4; ++i) {
            const float a = xr[2 * i], c = xr[2 * i + 1];
            w[i] = pk2(a + (a - mean) * rstd * bflo(yw[i]), c + (c - mean) * rstd * bfhi(yw[i]));
        }
    };
    auto regroup = [&](const u32 (&w)[4]) {
        auto r0 = __builtin_amdgcn_permlane16_swap(w[0], w[2], false, false);
        auto r1 = __builtin_amdgcn_permlane16_swap(w[1], w[3], false, false);
        return (u32x4){r0[0], r1[0], r0[1], r1[1]};
    };
    const int ochunk = 4 * role + 2 * (lq & 1) + (lq >> 1);
    {
        u32 w[4];
        gate(mean, rstd, w);
        float s = 0.f, q = 0.f;
#pragma unroll
        for (int i = 0; i < 4; ++i) {
            w[i] = nosent(w[i]);
            xr[2 * i] = bflo(w[i]); xr[2 * i + 1] = bfhi(w[i]);
            s = dot2(w[i], 0x3F803F80u, s); q = dot2(w[i], w[i], q);
        }
        __builtin_amdgcn_raw_buffer_store_b128(regroup(w), r_ws, WS_X2 + (u32)((TI * 8 + mem) * 2048 + li * 128 + ((ochunk ^ (li >> 1)) << 4)), 0, 16);
        pub_stat(WS_ST2, s, q);
        WAIT_VM(0);
    }
    STAMP(7);

    {
        if (wave < 5) {
            const int ti = 4 * grp - 1 + wave;
            float ts = 0.f, tqq = 0.f;
            if (ti >= 0) get_stat(WS_ST2, ti, ts, tqq);
            put_stat(wave, ts, tqq);
        }
        BAR();
        STAMP(8);
        const int l3 = opq(tid) & 63;
        const u32 g0 = (u32)((4 * grp - 1 + (wave >> 1)) * 16384 + (wave & 1) * 1024 + l3 * 16);
        const u32 g2 = (u32)((4 * grp + 3) * 16384 + wave * 256 + l3 * 16);
        auto x2_piece = [&](int i) {
            const int s = i >> 1;
            const u32 kp = (u32)((s + rot) & 7), sb = L_CX + s * 10240;
            if (!(i & 1)) dma_sc1(d_x2, sb + wave * 1024, g0, kp * 2048);
            else dma_q_sc1(d_x2, sb + 8192 + wave * 256, g2, kp * 2048);
        };
        const int l2 = opq(tid) & 63;
        const u32 xb0 = (u32)((l2 & 15) * 128 + (((l2 >> 4) ^ ((l2 & 15) >> 1)) << 4)), xb1 = xb0 ^ 64;
        auto gemm2 = [&](auto rc) {
            u32 xa;
            auto pf_addr = [&](int st) { xa = L_CX + (st >> 1) * 10240 + ((st & 1) ? xb1 : xb0); asm volatile("" : "+v"(xa)); };
            for (int attempt = 0; attempt < 64; ++attempt) {
#pragma unroll
                for (int i = 0; i < 8; ++i) x2_piece(i);
                u32 mx0 = 0, mx1 = 0;
                WAIT_VM(4);
                RBAR();
                STAMP(13);
                pf_addr(0);
#pragma unroll
                for (int mt = 0; mt < 5; ++mt) Ab[0][mt] = ldsA(xa + mt * 2048);
#pragma unroll
                for (int st = 0; st < 16; ++st) {
                    if ((st & 3) == 3 && st < 15) {
                        if (st == 3) WAIT_VM(8); else if (st == 7) WAIT_VM(4); else WAIT_VM(0);
                        RBAR();
                    }
                    if (st < 15) pf_addr(st + 1);
                    SB();
                    qkv_step(rc, st, Ab[st & 1], [&](int mt) {
                        if (st < 15) Ab[(st + 1) & 1][mt] = ldsA(xa + mt * 2048);
                        if (st < 2 && mt < 4) x2_piece(8 + st * 4 + mt);
                        const u32x4 aw = __builtin_bit_cast(u32x4, Ab[st & 1][mt]);
                        mx0 = max(max(mx0, aw[0]), aw[1]); mx1 = max(max(mx1, aw[2]), aw[3]);
                        SB();
                    });
                }
                if (__all(max(mx0, mx1) != SENT)) break;
                BAR();
            }
        };
        if (role == 0) gemm2(IC<0>{}); else gemm2(IC<1>{});
    }
    STAMP(9);
    attention(WS_O2);
    STAMP(27);
    WAIT_VM(0);
    STAMP(10);
    load_o(WS_O2);
    SB();
    wo_phase(WS_O2, WS_ST3, mean, rstd, IC<8>{}, [&]() {});
    STAMP(11);
    {
        u32 w[4];
        gate(mean, rstd, w);
        const int lane = opq(tid) & 63, li = lane & 15, lq = lane >> 4;
        __builtin_amdgcn_raw_buffer_store_b128(regroup(w), r_out, (u32)((TI * 16 + li) * 1024 + mem * 128 + (4 * role + 2 * (lq & 1) + (lq >> 1)) * 16), 0, 0);
    }
}

extern "C" void kernel_launch(void* const* d_in, const int* in_sizes, int n_in,
                              void* d_out, int out_size, void* d_ws, size_t ws_size,
                              hipStream_t stream) {
    fused_kernel<<<256, 512, 0, stream>>>((const u16*)d_in[0], (const u16*)d_in[1], (const u16*)d_in[2],
                                          (const u16*)d_in[4], (const u16*)d_in[5], (const u16*)d_in[6],
                                          (const u16*)d_in[7], (const u16*)d_in[8], (u16*)d_out, (char*)d_ws);
}
```

```cpp
#include <hip/hip_runtime.h>
#include <stdint.h>

typedef unsigned short u16;
typedef unsigned u32;
typedef unsigned long long u64;
typedef short s16x4 __attribute__((ext_vector_type(4)));
typedef short s16x8 __attribute__((ext_vector_type(8)));
typedef __bf16 bf16x2 __attribute__((ext_vector_type(2)));
typedef float f32x2 __attribute__((ext_vector_type(2)));
typedef float f32x4 __attribute__((ext_vector_type(4)));
typedef u32 u32x2 __attribute__((ext_vector_type(2)));
typedef u32 u32x4 __attribute__((ext_vector_type(4)));
#define LAS __attribute__((address_space(3)))
#define DEV __device__ __forceinline__
typedef __amdgpu_buffer_rsrc_t rsrc_t;

constexpr u32 WS_EPOCH = 0;
constexpr u32 WS_F1 = 32768;
constexpr u32 WS_F2 = 65536;
constexpr u32 WS_ST0 = 98304;
constexpr u32 WS_ST1 = 131072;
constexpr u32 WS_ST2 = WS_ST1 + 262144;
constexpr u32 WS_ST3 = WS_ST2 + 262144;
constexpr u32 WS_UT = 1u << 20;
constexpr u32 WS_O1 = WS_UT + (2u << 20);
constexpr u32 WS_O2 = WS_O1 + (2u << 20);
constexpr u32 WS_Y2 = WS_O2 + (2u << 20);
constexpr u32 WS_X2 = WS_Y2 + (2u << 20);
constexpr u32 WS_END = WS_X2 + (2u << 20);

constexpr u32 L_SLOT = 34816;
constexpr u32 L_KV = 4 * L_SLOT;
constexpr u32 L_K = L_KV, L_V = L_KV + 9216;
constexpr u32 L_SK = L_KV + 17408;
constexpr u32 L_WO = 0;
constexpr u32 L_CX = 65536;
constexpr u32 L_HC = L_CX;
constexpr u32 L_UR = L_CX + 69632;
constexpr u32 L_WX = L_CX + 40960;
constexpr u32 L_EX = 147456;
constexpr u32 L_TAB = L_EX + 12288;
constexpr u32 L_CS = L_TAB + 1024;
constexpr u32 L_FLG = L_TAB + 2048;
constexpr u32 L_DUMMY = L_FLG + 1024;
constexpr u32 L_TOTAL = 160 * 1024;
static_assert(L_DUMMY + 1024 <= L_TOTAL, "LDS map");

constexpr int SPIN_CAP = 1 << 14;
constexpr u32 SENT = 0xFFFFFFFFu;

#define WAIT_VM(n) asm volatile("s_waitcnt vmcnt(%0)" ::"i"(n) : "memory")
#define BAR() do { asm volatile("s_waitcnt lgkmcnt(0)" ::: "memory"); __builtin_amdgcn_s_barrier(); asm volatile("" ::: "memory"); } while (0)
#define RBAR() do { asm volatile("" ::: "memory"); __builtin_amdgcn_s_barrier(); asm volatile("" ::: "memory"); } while (0)
#define CFENCE() asm volatile("" ::: "memory")
#define SB() __builtin_amdgcn_sched_barrier(0)
template <int N> struct IC { static constexpr int value = N; };
template <int I, int N, class F> DEV void sfor(F&& f) { if constexpr (I < N) { f(IC<I>{}); sfor<I + 1, N>(f); } }

DEV f32x4 mfma32(s16x8 a, s16x8 b, f32x4 c) { return __builtin_amdgcn_mfma_f32_16x16x32_bf16(a, b, c, 0, 0, 0); }
DEV f32x4 mfma16(s16x4 a, s16x4 b, f32x4 c) { return __builtin_amdgcn_mfma_f32_16x16x16bf16_1k(a, b, c, 0, 0, 0); }
DEV float vmax(float a, float b) { float r; asm("v_max_f32 %0, %1, %2" : "=v"(r) : "v"(a), "v"(b)); return r; }
DEV u32 pk2(float lo, float hi) { f32x2 f = {lo, hi}; bf16x2 b = __builtin_convertvector(f, bf16x2); return __builtin_bit_cast(u32, b); }
DEV float bflo(u32 w) { return __uint_as_float(w << 16); }
DEV float bfhi(u32 w) { return __uint_as_float(w & 0xffff0000u); }
DEV float dot2(u32 a, u32 b, float c) { return __builtin_amdgcn_fdot2_f32_bf16(__builtin_bit_cast(bf16x2, a), __builtin_bit_cast(bf16x2, b), c, false); }
DEV u32 nosent(u32 w) { return min(w, 0xFFFFFFFEu); }
DEV float xsum(float v) {
    auto r = __builtin_amdgcn_permlane32_swap(__float_as_uint(v), __float_as_uint(v), false, false);
    v = __uint_as_float(r[0]) + __uint_as_float(r[1]);
    auto s = __builtin_amdgcn_permlane16_swap(__float_as_uint(v), __float_as_uint(v), false, false);
    return __uint_as_float(s[0]) + __uint_as_float(s[1]);
}
DEV float xmax(float v) {
    auto r = __builtin_amdgcn_permlane32_swap(__float_as_uint(v), __float_as_uint(v), false, false);
    v = vmax(__uint_as_float(r[0]), __uint_as_float(r[1]));
    auto s = __builtin_amdgcn_permlane16_swap(__float_as_uint(v), __float_as_uint(v), false, false);
    return vmax(__uint_as_float(s[0]), __uint_as_float(s[1]));
}
DEV float rowsum16(float v) {
    v += __builtin_bit_cast(float, __builtin_amdgcn_update_dpp(0, __builtin_bit_cast(int, v), 0xB1, 0xf, 0xf, true));
    v += __builtin_bit_cast(float, __builtin_amdgcn_update_dpp(0, __builtin_bit_cast(int, v), 0x4E, 0xf, 0xf, true));
    v += __builtin_bit_cast(float, __builtin_amdgcn_update_dpp(0, __builtin_bit_cast(int, v), 0x141, 0xf, 0xf, true));
    v += __builtin_bit_cast(float, __builtin_amdgcn_update_dpp(0, __builtin_bit_cast(int, v), 0x140, 0xf, 0xf, true));
    return v;
}
DEV float bcast_row0(float v) { return __builtin_bit_cast(float, __builtin_amdgcn_update_dpp(0, __builtin_bit_cast(int, v), 0x150, 0xf, 0xf, true)); }
template <int OFF> DEV void ald2o(u32x2& d, u32 voff, u32x4 rs) { asm volatile("buffer_load_dwordx2 %0, %1, %2, 0 offen offset:%3 sc1" : "=v"(d) : "v"(voff), "s"(rs), "i"(OFF) : "memory"); }
template <class T> DEV void pin(T& v) { asm volatile("" : "+v"(v)); }
DEV int opq(int v) { asm volatile("" : "+v"(v)); return v; }
DEV s16x8 cat8(s16x4 a, s16x4 b) { return __builtin_shufflevector(a, b, 0, 1, 2, 3, 4, 5, 6, 7); }
DEV s16x4 pack4(f32x4 v) { u32x2 w = {pk2(v[0], v[1]), pk2(v[2], v[3])}; return __builtin_bit_cast(s16x4, w); }
DEV u64 mix64(u64 z) { z += 0x9E3779B97F4A7C15ull; z = (z ^ (z >> 30)) * 0xBF58476D1CE4E5B9ull; z = (z ^ (z >> 27)) * 0x94D049BB133111EBull; return z ^ (z >> 31); }

struct ConvSched {
    int n[4], f[18], i[18], slot[64];
    constexpr ConvSched(int R) : n{}, f{}, i{}, slot{} {
        constexpr int LEAD = 9;
        const int N0[4] = {0, 3, 4, 7};
        for (int k = 0; k < 4; ++k) n[k] = N0[k] ^ R;
        int c = 0;
        for (int ff = 0; ff < 8; ++ff) for (int k = 3; k >= 0; --k) if (ff <= n[k]) { f[c] = ff; i[c] = k; ++c; }
        for (int k = 0; k < 64; ++k) slot[k] = 1000;
        for (int p = 71; p >= 0; --p) {
            const int e = p / 18, ff = f[p % 18], g = n[i[p % 18]] - ff, s = p - LEAD < -1 ? -1 : p - LEAD;
            slot[e * 16 + ff] = s; slot[e * 16 + 8 + g] = s;
        }
    }
};

#define STAMP(i) do { } while (0)
#define CSTAMP(i) do { } while (0)

struct Args { const u16 *gx, *gwqkv, *gwo, *gbk, *gbv, *gsk, *ghk, *gdv; u16* gout; char* ws; };

template <int R>
DEV void body(const Args& A_, LAS char* const lds, const int wave) {
    const u16* gx = A_.gx; char* ws = A_.ws;
    STAMP(0);
    const int b = blockIdx.x, tid = threadIdx.x, lane = tid & 63;
    const int hd = wave & 3;
    const int grp = (b & 7) * 4 + (b >> 6), mem = (b >> 3) & 7;
    const int T0 = grp * 64;
    const int rot = (mem + 2 * (grp & 3)) & 7;

    u64 epoch;
    asm volatile("s_load_dwordx2 %0, %1, 0x0" : "=s"(epoch) : "s"(ws));
    const u32 dw = *(const u32*)(A_.gdv + 2 * b);
    const rsrc_t r_x = __builtin_amdgcn_make_buffer_rsrc((void*)gx, 0, 2048 * 512 * 2, 0x00020000);
    const rsrc_t r_ws = __builtin_amdgcn_make_buffer_rsrc((void*)ws, 0, WS_END, 0x00020000);
    const rsrc_t r_out = __builtin_amdgcn_make_buffer_rsrc((void*)A_.gout, 0, 2048 * 512 * 2, 0x00020000);
    const u32 ldsbase = (u32)(size_t)lds;
    auto mkdesc = [](const void* p, u32 bytes) { const u64 a = (u64)p; return (u32x4){(u32)a, (u32)(a >> 32) & 0xffffu, bytes, 0x00020000u}; };
    const u32x4 d_x = mkdesc(gx, 2048 * 512 * 2), d_w = mkdesc(A_.gwqkv, 512 * 1536 * 2), d_wo = mkdesc(A_.gwo, 512 * 512 * 2);
    const u32x4 d_k = mkdesc(A_.gbk, 2048 * 512 * 2), d_v = mkdesc(A_.gbv, 2048 * 512 * 2), d_sk = mkdesc(A_.gsk, 512 * 4 * 2);
    const u32x4 d_h = mkdesc(A_.ghk, 512 * 2048 * 2), d_x2 = mkdesc(ws + WS_X2, 2u << 20), d_ws = mkdesc(ws, WS_END);
    const u32 wb1k = ldsbase + wave * 1024, wb256 = ldsbase + wave * 256;
#define DMA_ASM(pre, mods, post) asm volatile("s_add_u32 m0, %0, %4\n\t" pre "buffer_load_dwordx4 %1, %2, %3 offen " mods "lds" post \
                     :: "s"(sbase), "v"(voff), "s"(rs), "s"(soff), "i"(IMM) : "memory", "scc")
    const u32 xmlo = wave == 0 ? ~0xFFu : ~0u;
    auto dma = [&](auto kind, auto imm, u32x4 rs, u32 sbase, u32 voff, u32 soff) {
        constexpr int K = decltype(kind)::value; constexpr int IMM = decltype(imm)::value;
        if constexpr (K == 0) DMA_ASM("s_nop 0\n\t", "", "");
        else if constexpr (K == 1) DMA_ASM("s_mov_b64 exec, 0xffff\n\t", "", "\n\ts_mov_b64 exec, -1");
        else if constexpr (K == 2) {
            const u32 ml = xmlo;
            asm volatile("s_add_u32 m0, %0, %4\n\ts_mov_b32 exec_lo, %5\n\tbuffer_load_dwordx4 %1, %2, %3 offen lds\n\ts_mov_b32 exec_lo, -1"
                         :: "s"(sbase), "v"(voff), "s"(rs), "s"(soff), "i"(IMM), "s"(ml) : "memory", "scc");
        }
        else if constexpr (K == 4) DMA_ASM("s_nop 0\n\t", "sc1 ", "");
        else DMA_ASM("s_mov_b64 exec, 0xffff\n\t", "sc1 ", "\n\ts_mov_b64 exec, -1");
    };
    auto ald4 = [&](u32x4& d, u32x4 rs, u32 voff) { asm volatile("buffer_load_dwordx4 %0, %1, %2, 0 offen" : "=v"(d) : "v"(voff), "s"(rs) : "memory"); };
    auto ald4s = [&](u32x4& d, u32 voff) { asm volatile("buffer_load_dwordx4 %0, %1, %2, 0 offen sc1" : "=v"(d) : "v"(voff), "s"(d_ws) : "memory"); };
    auto ald2s = [&](u32x2& d, u32 voff) { asm volatile("buffer_load_dwordx2 %0, %1, %2, 0 offen sc1" : "=v"(d) : "v"(voff), "s"(d_ws) : "memory"); };

    const int pr = lane >> 3, pc = lane & 7;
    const int xr0 = 8 * wave + pr, xr2 = 64 + 2 * wave + pr;
    const u32 xv0 = (u32)((T0 - 16 + xr0) * 1024 + ((pc ^ ((xr0 >> 1) & 7)) << 4));
    const u32 xv2 = (u32)((T0 - 16 + xr2) * 1024 + ((pc ^ ((xr2 >> 1) & 7)) << 4));
    const int wfw = ((xr0 >> 1) & 1) | (((xr0 >> 3) & 1) << 1);
    const u32 wv0 = (u32)(xr0 * 3072 + mem * 128 + ((((pc >> 1) ^ wfw) * 2 + (pc & 1)) << 4));
    auto issue_piece = [&](auto sc, auto ic) {
        constexpr int s = decltype(sc)::value, i = decltype(ic)::value;
        const u32 kp = (u32)((s + rot) & 7);
        constexpr int sb = (s & 3) * L_SLOT;
        if constexpr (i == 0) dma(IC<R ? 0 : 2>{}, IC<sb>{}, d_x, wb1k, xv0, kp * 128);
        else if constexpr (i == 1) dma(IC<1>{}, IC<sb + 8192>{}, d_x, wb256, xv2, kp * 128);
        else dma(IC<0>{}, IC<sb + 10240 + (i - 2) * 8192>{}, d_w, wb1k, wv0, kp * (64 * 3072) + (i - 2) * 1024);
    };
    auto issue_stage = [&](auto sc) { issue_piece(sc, IC<0>{}); issue_piece(sc, IC<1>{}); issue_piece(sc, IC<2>{}); issue_piece(sc, IC<3>{}); issue_piece(sc, IC<4>{}); };
    auto issue_wo = [&](auto ic) {
        constexpr int i = decltype(ic)::value;
        const int l2 = opq(tid) & 63, pr = l2 >> 3, pc = l2 & 7;
        const u32 wov = (u32)((8 * wave + pr) * 1024 + mem * 128 + ((((pc >> 1) ^ ((pr >> 1) & 3)) * 2 + (pc & 1)) << 4));
        const u32 j = (u32)((i + 2 * grp) & 7);
        dma(IC<0>{}, IC<L_WO>{}, d_wo, wb1k + j * 8192, wov, j * 65536);
    };
    auto issue_kv = [&](int i) {
        const int l2 = opq(tid) & 63, pr = l2 >> 3, pc = l2 & 7;
        const u32 cb = (u32)(mem * 128);
        if (i == 0) dma(IC<0>{}, IC<L_K>{}, d_k, wb1k, (u32)((T0 - 8 + 8 * wave + pr) * 1024) + cb + ((pc ^ wave) << 4), 0);
        else if (i == 1) dma(IC<0>{}, IC<L_V>{}, d_v, wb1k, (u32)((T0 + 8 * wave + pr) * 1024) + cb + ((pc ^ wave) << 4), 0);
        else if (R == 0) dma(IC<0>{}, IC<L_K + 8192>{}, d_k, ldsbase, (u32)((T0 - 8 + 64 + pr) * 1024) + cb + (pc << 4), 0);
        else dma(IC<0>{}, IC<L_SK>{}, d_sk, ldsbase, (u32)(mem * 512 + l2 * 16), 0);
    };

    const int srow = T0 + 8 * mem + wave;
    u32x4 xrow;
    ald4(xrow, d_x, (u32)(srow * 1024 + lane * 16));
    sfor<2, 5>([&](auto ic) { issue_piece(IC<0>{}, ic); });
    sfor<2, 5>([&](auto ic) { issue_piece(IC<1>{}, ic); });
    asm volatile("s_waitcnt lgkmcnt(0)" : "+s"(epoch));
    const u64 tag = mix64(epoch);
    const u32 tl = (u32)tag, th = (u32)(tag >> 32);
    if (R == 1 && wave == 7 && lane < 32) *(LAS u32x4*)(lds + (lane >> 3) * L_SLOT + (lane & 7) * 16) = (u32x4){0x3F803F80u, 0x3F803F80u, 0x3F803F80u, 0x3F803F80u};
    {
        WAIT_VM(6);
        pin(xrow);
        float s = 0.f, q = 0.f;
#pragma unroll
        for (int i = 0; i < 4; ++i) { s = dot2(xrow[i], 0x3F803F80u, s); q = dot2(xrow[i], xrow[i], q); }
        s = rowsum16(s); q = rowsum16(q);
        s = xsum(s); q = xsum(q);
        const float m = s * (1.f / 512.f);
        const float r = __builtin_amdgcn_rsqf(fmaxf(q * (1.f / 512.f) - m * m, 0.f) + 1e-5f);
        const u32x4 sv = {__float_as_uint(m), tl, __float_as_uint(r), th};
        __builtin_amdgcn_raw_buffer_store_b128(sv, r_ws, lane == 0 ? WS_ST0 + (u32)(srow * 16) : 0xFFFFFF00u, 0, 16);
        CFENCE();
    }
    issue_piece(IC<0>{}, IC<0>{}); issue_piece(IC<0>{}, IC<1>{}); issue_piece(IC<1>{}, IC<0>{}); issue_piece(IC<1>{}, IC<1>{});
    issue_kv(0); issue_kv(1); issue_kv(2);

    auto trr = [&](u32 off) { return __builtin_amdgcn_ds_read_tr16_b64_v4i16((LAS s16x4*)(lds + off)); };
    auto ldsA = [&](u32 off) { return *(LAS s16x8*)(lds + off); };
    auto flag_ok = [&](const u32x2& f) { return __all(f[0] == tl && f[1] == th); };
    auto spin_flags = [&](u32x2& f, u32 off) {
        _Pragma("unroll 1") for (int spin = 0; spin < SPIN_CAP; ++spin) {
            if (flag_ok(f)) break;
            __builtin_amdgcn_s_sleep(2);
            asm volatile("buffer_load_dwordx2 %0, %1, %2, 0 offen sc1\n\ts_waitcnt vmcnt(0)" : "=&v"(f) : "v"(off), "s"(d_ws) : "memory");
        }
    };

    s16x8 Wq[16], Wm[16];
    f32x4 accM[5], accQ[2], csQ;
    const f32x4 zero4 = {0.f, 0.f, 0.f, 0.f};
    const s16x8 ones8 = {0x3F80, 0x3F80, 0x3F80, 0x3F80, 0x3F80, 0x3F80, 0x3F80, 0x3F80};
    s16x8 Ab[3][5];
    auto qkv_step = [&](int st, const s16x8 (&A)[5], auto fill) {
        sfor<0, 5>([&](auto mc) {
            constexpr int mt = decltype(mc)::value;
            const f32x4 cm = st ? accM[mt] : zero4;
            accM[mt] = R ? mfma32(A[mt], Wm[st], cm) : mfma32(Wm[st], A[mt], cm);
            if constexpr (mt == 1 + 2 * R || mt == 2 + 2 * R) { constexpr int j = mt - 1 - 2 * R; accQ[j] = mfma32(Wq[st], A[mt], st ? accQ[j] : zero4); }
            fill(mc);
        });
    };

    u32x4 hd0, hd1;
    u32x2 flg;
    {
        const int li = lane & 15, lq = lane >> 4, tq = li >> 2, tp = lane & 3;
        const u32 xa0 = (u32)(li * 128 + ((lq ^ (li >> 1)) << 4));
        const u32 wa = (u32)((8 * lq + tq) * 128 + ((hd ^ (((tq >> 1) & 1) | ((lq & 1) << 1))) << 5) + tp * 8);
        u32 xa, wb;
        u32x4 s0v;
        auto pf_addr = [&](int st) {
            const u32 sb = (u32)((st >> 1) & 3) * L_SLOT;
            xa = sb + (xa0 ^ ((st & 1) * 64)); wb = sb + 10240 + (st & 1) * 4096 + wa;
            asm volatile("" : "+v"(xa), "+v"(wb));
        };
        auto pf_part = [&](int st, int mt) {
            if (mt == 0) Wm[st] = cat8(trr(wb + (1 + R) * 8192), trr(wb + (1 + R) * 8192 + 512));
            Ab[st & 1][mt] = ldsA(xa + mt * 2048);
            if (mt == 1) Wq[st] = cat8(trr(wb), trr(wb + 512));
        };
        STAMP(15);
        WAIT_VM(5);
        BAR();
        STAMP(16);
        pf_addr(0);
#pragma unroll
        for (int mt = 0; mt < 5; ++mt) pf_part(0, mt);
        sfor<0, 16>([&](auto stc) {
            constexpr int st = decltype(stc)::value;
            if constexpr ((st & 1) && st < 15) {
                constexpr int s1 = (st >> 1) + 1;
                constexpr int WN[8] = {0, 8, 5, 6, 6, 5, 6, 4};
                WAIT_VM(WN[s1]);
                if constexpr (s1 == 7) {
                    asm volatile("" : "+v"(s0v));
                    if (R == 0 || wave == 4) {
                        const int l2 = opq(tid) & 63;
                        if (T0 - 16 + wave * 16 >= 0) {
                            _Pragma("unroll 1") for (int spin = 0; spin < SPIN_CAP; ++spin) {
                                if (__all(s0v[1] == tl && s0v[3] == th)) break;
                                __builtin_amdgcn_s_sleep(2);
                                asm volatile("buffer_load_dwordx4 %0, %1, %2, 0 offen sc1\n\ts_waitcnt vmcnt(0)" : "=&v"(s0v) : "v"(WS_ST0 + (u32)((T0 - 16 + wave * 16 + (l2 & 15)) * 16)), "s"(d_ws) : "memory");
                            }
                        } else { s0v[0] = 0u; s0v[2] = 0x3F800000u; }
                        if (l2 < 16) { *(LAS u32*)(lds + L_TAB + wave * 64 + l2 * 4) = s0v[0]; *(LAS u32*)(lds + L_TAB + 320 + wave * 64 + l2 * 4) = s0v[2]; }
                        asm volatile("s_waitcnt lgkmcnt(0)" ::: "memory");
                    }
                }
                RBAR();
                STAMP(16 + s1);
                if constexpr (s1 == 5) {
                    if (R == 0 && tid == 0) __builtin_amdgcn_raw_buffer_store_b64((u32x2){tl, th}, r_ws, WS_F1 + (mem * 32 + grp) * 128, 0, 16);
                    CFENCE();
                    const int row = T0 - 16 + wave * 16 + (opq(tid) & 15);
                    ald4s(s0v, WS_ST0 + (u32)(max(row, 0) * 16));
                }
            }
            if (st < 15) pf_addr(st + 1);
            SB();
            qkv_step(st, Ab[st & 1], [&](auto mc) {
                constexpr int mt = decltype(mc)::value;
                if (st < 15) pf_part(st + 1, mt);
                if constexpr (st == 0) issue_piece(IC<2>{}, mc);
                if constexpr (st >= 1 && st <= 10) {
                    constexpr int sn = ((st - 1) >> 1) + 3;
                    if constexpr ((st - 1) & 1) { if constexpr (mt == 1) issue_piece(IC<sn>{}, IC<3>{}); if constexpr (mt == 3) issue_piece(IC<sn>{}, IC<4>{}); }
                    else if constexpr (!(mt & 1)) issue_piece(IC<sn>{}, IC<(mt >> 1)>{});
                }
                if constexpr (st == 11 && mt == 0) { const u32 ho = (u32)(b * 8192 + opq(tid) * 16); ald4(hd0, d_h, ho); ald4(hd1, d_h, ho + 16); }
                if constexpr (st == 11 && (mt == 2 || mt == 4)) {
                    const int l2 = opq(tid) & 63;
                    dma(IC<0>{}, IC<L_DUMMY>{}, d_wo, ldsbase, (u32)((l2 >> 3) * 1024 + (l2 & 7) * 16), (u32)(((grp & 3) * 128 + wave * 16 + (mt - 2) * 4) * 1024 + mem * 128));
                }
                if constexpr (R == 1 && st == 14 && mt == 4) { if (wave == 7) ald2s(flg, WS_F1 + (u32)(((b >> 5) * 32 + (opq(tid) & 31)) * 128)); }
                SB();
            });
            if ((st & 1) == R) csQ = mfma32(Wq[st], ones8, st >= 2 ? csQ : zero4);
            SB();
            if constexpr (st == 4) {
                const int l2 = opq(tid) & 63, tg = l2 & 7, chl = l2 >> 3;
                const u32x2 skw = *(LAS u32x2*)(lds + L_SK + (8 * wave + chl) * 8);
                u32 ka = L_K + tg * 1024 + (((wave ^ tg) & 7) << 4) + chl * 2;
                u32 kb = L_K + tg * 1024 + (((wave ^ (tg + 1)) & 7) << 4) + chl * 2;
                asm volatile("" : "+v"(ka), "+v"(kb));
                u16 kh[11], vh[8];
#pragma unroll
                for (int i = 0; i < 11; ++i) kh[i] = *(LAS u16*)(lds + ((i + 5 < 8) ? ka : kb) + (i + 5) * 128);
#pragma unroll
                for (int i = 0; i < 8; ++i) vh[i] = *(LAS u16*)(lds + ka + (L_V - L_K) + i * 128);
                SB();
                const float a0 = bflo(skw[0]), a1 = bfhi(skw[0]), a2 = bflo(skw[1]), a3 = bfhi(skw[1]);
                float ua[8];
#pragma unroll
                for (int i = 0; i < 8; ++i) ua[i] = (bflo(kh[i]) * a3 + bflo(kh[i + 1]) * a2 + bflo(kh[i + 2]) * a1 + bflo(kh[i + 3]) * a0) * bflo(vh[i]);
                const u32x4 oa = {pk2(ua[7], ua[6]), pk2(ua[5], ua[4]), pk2(ua[3], ua[2]), pk2(ua[1], ua[0])};
                __builtin_amdgcn_raw_buffer_store_b128(oa, r_ws, WS_UT + (u32)((mem * 64 + 8 * wave + chl) * 4096 + (2040 - T0 - 8 * tg) * 2), 0, 16);
                CFENCE();
                SB();
            }
        });
    }
    STAMP(1);

    float m1[5], r1[5]; f32x4 m4[5], r4[5];
    auto load_tab = [&]() {
        const int l2 = opq(tid) & 63;
        u32 ta = L_TAB + (l2 & 15) * 4, tb = L_TAB + (l2 >> 4) * 16;
        asm volatile("" : "+v"(ta), "+v"(tb));
#pragma unroll
        for (int mt = 0; mt < 5; ++mt) {
            if (R == 0 || mt >= 3) { m1[mt] = *(LAS float*)(lds + ta + mt * 64); r1[mt] = *(LAS float*)(lds + ta + 320 + mt * 64); }
            if (R == 1) { m4[mt] = *(LAS f32x4*)(lds + tb + mt * 64); r4[mt] = *(LAS f32x4*)(lds + tb + 320 + mt * 64); }
        }
    };
    u32x4 uu;
    auto attention = [&](auto lc) {
        constexpr int LAYER = decltype(lc)::value;
        constexpr int SBASE = LAYER ? 32 : 28;
        const int lane = opq(tid) & 63, li = lane & 15, lq = lane >> 4;
        u32 exw = L_EX + wave * 1536 + lane * 8, exr = L_EX + (wave ^ 4) * 1536 + lane * 8, csa = L_CS + hd * 128 + lq * 16;
        asm volatile("" : "+v"(exw), "+v"(exr), "+v"(csa));
        const int par = li & 1;
        const bool cur0 = (4 * lq + par) <= li, cur1 = (4 * lq + par + 2) <= li;
        const u32 pm = par ? 0xffff0000u : 0x0000ffffu;
        u32 mC0 = cur0 ? pm : 0u, mP0 = cur0 ? 0u : pm, mC1 = cur1 ? pm : 0u, mP1 = cur1 ? 0u : pm;
        u32 oo = (LAYER ? WS_O2 : WS_O1) + (u32)((4 * grp + 2 * R) * 16384 + (4 * mem + hd) * 512 + lane * 8);
        asm volatile("" : "+v"(mC0), "+v"(mP0), "+v"(mC1), "+v"(mP1), "+v"(oo));
        f32x4 csM4; float csV;
        if constexpr (LAYER == 0) {
            load_tab();
            if (li == 0) *(LAS f32x4*)(lds + csa + R * 512) = csQ;
            if constexpr (R == 0) {
#pragma unroll
                for (int i = 0; i < 4; ++i) csM4[i] = bcast_row0(accM[0][i]);
                if (li == 0) *(LAS f32x4*)(lds + csa + 64) = csM4;
            } else {
                auto s = __builtin_amdgcn_permlane16_swap(__float_as_uint(accM[0][0]), __float_as_uint(accM[0][0]), false, false);
                auto t = __builtin_amdgcn_permlane32_swap(s[0], s[0], false, false);
                csV = __uint_as_float(t[0]);
                if (lq == 0) *(LAS float*)(lds + L_CS + 512 + hd * 128 + 64 + li * 4) = csV;
            }
        } else {
            if constexpr (R == 0) csM4 = *(LAS f32x4*)(lds + csa + 64); else csV = *(LAS float*)(lds + L_CS + 512 + hd * 128 + 64 + li * 4);
        }
        SB();
        s16x4 Kt[3], Vt[3], Qt[2];
        if constexpr (R == 0) {
#pragma unroll
            for (int mt = 4; mt >= 2; --mt) { const s16x4 k = pack4((accM[mt] - m1[mt] * csM4) * r1[mt]); *(LAS s16x4*)(lds + exw + (mt - 2) * 512) = k; if (mt == 2) Kt[2] = k; }
        } else {
#pragma unroll
            for (int mt = 0; mt < 3; ++mt) { const s16x4 v = pack4((accM[mt] - m4[mt] * csV) * r4[mt]); *(LAS s16x4*)(lds + exw + mt * 512) = v; if (mt == 2) Vt[0] = v; }
            if constexpr (LAYER == 0) {
                if (wave == 7) { asm volatile("s_waitcnt vmcnt(0)" : "+v"(flg) :: "memory"); const u32 ok = flag_ok(flg); *(LAS u32*)(lds + L_FLG) = ok; }
            }
        }
        float scq[2];
#pragma unroll
        for (int j = 0; j < 2; ++j) { scq[j] = r1[1 + 2 * R + j] * (0.25f * 1.44269504088896341f); asm volatile("" : "+v"(scq[j])); }
        STAMP(SBASE);
        BAR();
        STAMP(SBASE + 1);
        const f32x4 cq0 = *(LAS f32x4*)(lds + csa), cq1 = *(LAS f32x4*)(lds + csa + 512);
#pragma unroll
        for (int j = 0; j < 3; ++j) { const s16x4 t = *(LAS s16x4*)(lds + exr + j * 512); if (R == 0) Vt[j] = t; else Kt[j] = t; }
        u32 f1ok = 1;
        if constexpr (LAYER == 0) f1ok = *(LAS u32*)(lds + L_FLG);
        SB();
        if constexpr (R == 0) {
#pragma unroll
            for (int mt = 0; mt < 2; ++mt) { Kt[mt] = pack4((accM[mt] - m1[mt] * csM4) * r1[mt]); asm volatile("" : "+v"(Kt[mt])); }
        } else {
#pragma unroll
            for (int mt = 3; mt < 5; ++mt) { Vt[mt - 2] = pack4((accM[mt] - m4[mt] * csV) * r4[mt]); asm volatile("" : "+v"(Vt[mt - 2])); }
        }
        SB();
        const f32x4 cq = cq0 + cq1;
#pragma unroll
        for (int j = 0; j < 2; ++j) Qt[j] = pack4(accQ[j] - m1[1 + 2 * R + j] * cq);
        f32x4 Sc[2], Sp[2];
#pragma unroll
        for (int j = 0; j < 2; ++j) { Sc[j] = mfma16(Kt[j + 1], Qt[j], zero4); Sp[j] = mfma16(Kt[j], Qt[j], zero4); }
        if constexpr (LAYER == 0) {
            f1ok = __builtin_amdgcn_readfirstlane(f1ok);
            if (f1ok) ald4s(uu, WS_UT + (u32)(b * 8192 + opq(tid) * 16));
        }
        float s0[2], s1[2], mx[2];
#pragma unroll
        for (int j = 0; j < 2; ++j) {
            const float sp0 = Sp[j][0], sp1 = Sp[j][1], sp2 = Sp[j][2], sp3 = Sp[j][3], c0 = Sc[j][0], c1 = Sc[j][1], c2 = Sc[j][2], c3 = Sc[j][3];
            float p0 = par ? sp1 : sp0, p1 = par ? sp3 : sp2, q0 = par ? c1 : c0, q1 = par ? c3 : c2;
            asm volatile("" : "+v"(p0), "+v"(p1), "+v"(q0), "+v"(q1));
            if (R == 0 && j == 0 && grp == 0) { p0 = -3.0e38f; p1 = -3.0e38f; }
            s0[j] = cur0 ? q0 : p0; s1[j] = cur1 ? q1 : p1;
            mx[j] = vmax(s0[j], s1[j]);
        }
#pragma unroll
        for (int j = 0; j < 2; ++j) { auto r = __builtin_amdgcn_permlane32_swap(__float_as_uint(mx[j]), __float_as_uint(mx[j]), false, false); mx[j] = vmax(__uint_as_float(r[0]), __uint_as_float(r[1])); }
#pragma unroll
        for (int j = 0; j < 2; ++j) { auto r = __builtin_amdgcn_permlane16_swap(__float_as_uint(mx[j]), __float_as_uint(mx[j]), false, false); mx[j] = vmax(__uint_as_float(r[0]), __uint_as_float(r[1])); }
        STAMP(SBASE + 2);
        f32x4 O[2], L[2];
#pragma unroll
        for (int j = 0; j < 2; ++j) {
            const float sc = scq[j];
            const float e0 = __builtin_amdgcn_exp2f((s0[j] - mx[j]) * sc), e1 = __builtin_amdgcn_exp2f((s1[j] - mx[j]) * sc);
            const u32 w0 = pk2(e0, e0), w1 = pk2(e1, e1);
            const u32x4 pw = {w0 & mC0, w1 & mC1, w0 & mP0, w1 & mP1};
            const s16x8 P = __builtin_bit_cast(s16x8, pw);
            L[j] = mfma32(ones8, P, zero4);
            O[j] = mfma32(cat8(Vt[j + 1], Vt[j]), P, zero4);
        }
#pragma unroll
        for (int j = 0; j < 2; ++j) {
            const float inv = __builtin_amdgcn_rcpf(L[j][0]);
            const u32x2 ow = {nosent(pk2(O[j][0] * inv, O[j][1] * inv)), nosent(pk2(O[j][2] * inv, O[j][3] * inv))};
            __builtin_amdgcn_raw_buffer_store_b64(ow, r_ws, oo, j * 16384, 16);
        }
        SB();
        return f1ok;
    };
    const u32 f1ok = attention(IC<0>{});
    {
        const u32x4 sv = {SENT, SENT, SENT, SENT};
        const int t2 = opq(tid);
        const u32 tb = (u32)((4 * grp + hd) * 16384 + mem * 2048 + R * 1024 + (t2 & 63) * 16);
        __builtin_amdgcn_raw_buffer_store_b128(sv, r_ws, tb, WS_O2, 16);
        __builtin_amdgcn_raw_buffer_store_b128(sv, r_ws, tb, WS_X2, 16);
        __builtin_amdgcn_raw_buffer_store_b128(sv, r_ws, (u32)(b * 8192 + t2 * 16), WS_Y2, 16);
        CFENCE();
        SB();
    }
    STAMP(2);

    const int TI = 4 * grp + hd;
    u32x2 of[16];
    auto load_o = [&](u32 ws_o) {
        const int lane = opq(tid) & 63;
#pragma unroll
        for (int i = 0; i < 16; ++i) of[i] = __builtin_amdgcn_raw_buffer_load_b64(r_ws, ws_o + (u32)(TI * 16384 + R * 8192 + lane * 8) + (i & 7) * 512, (i >> 3) * 4096, 16);
    };
    u32 xw[8], yw[8];
    u32 woa[4];
    s16x8 F[8][4];
    auto wo_addr = [&]() {
        const int l2 = opq(tid) & 63, lq = l2 >> 4, tq = (l2 & 15) >> 2, tp = l2 & 3;
#pragma unroll
        for (int n = 0; n < 4; ++n) woa[n] = L_WO + (u32)(R * 32768 + (4 * lq + tq) * 128 + ((n ^ (((lq & 1) << 1) | (tq >> 1))) << 5) + tp * 8);
    };
    auto load_F = [&](int ks) {
#pragma unroll
        for (int n = 0; n < 4; ++n) F[ks][n] = cat8(trr(woa[n] + ks * 4096), trr(woa[n] + ks * 4096 + 2048));
    };

    {
        const int t2 = opq(tid), lane = t2 & 63, li = lane & 15, lq = lane >> 4;
        constexpr u32 OFFS = 0xEDA85410u;
        {
            WAIT_VM(5);
            pin(hd0); pin(hd1);
            const int j = t2 & 255;
            u32 cb = L_HC + R * 34816 + (j + 2) * 16;
            asm volatile("" : "+v"(cb));
            const u32 d[8] = {hd0[0], hd0[1], hd0[2], hd0[3], hd1[0], hd1[1], hd1[2], hd1[3]};
            u32 e[7];
#pragma unroll
            for (int i = 0; i < 7; ++i) e[i] = __builtin_amdgcn_alignbit(d[i + 1], d[i], 16);
#pragma unroll
            for (int p = 0; p < 8; ++p) {
                const u32 po = (272 * p + ((OFFS >> (4 * p)) & 15)) * 16;
                const int h = p >> 1;
                const u32x4 w = (p & 1) ? (u32x4){e[h], e[h + 1], e[h + 2], e[h + 3]} : (u32x4){d[h], d[h + 1], d[h + 2], d[h + 3]};
                *(LAS u32x4*)(lds + cb + po) = w;
            }
            if (hd == 0) {
                const u32 h0 = __builtin_amdgcn_readfirstlane(d[0]), h1 = __builtin_amdgcn_readfirstlane(d[1]);
                const u32 h2 = __builtin_amdgcn_readfirstlane(d[2]), h3 = __builtin_amdgcn_readfirstlane(d[3]);
                if (lane < 16) {
                    const int p = lane & 7;
                    const u32 po = (272 * p + ((OFFS >> (4 * p)) & 15)) * 16;
                    const unsigned __int128 hv = ((unsigned __int128)(((u64)h3 << 32) | h2) << 64) | (((u64)h1 << 32) | h0);
                    const unsigned __int128 sv = (p == 0 || lane < 8) ? (unsigned __int128)0 : (hv << (16 * (8 - p)));
                    const u32x4 w = {(u32)sv, (u32)(sv >> 32), (u32)(sv >> 64), (u32)(sv >> 96)};
                    *(LAS u32x4*)(lds + L_HC + R * 34816 + po + (lane >> 3) * 16) = w;
                }
            }
            if (hd == 1 && lane < 30) *(LAS u32x4*)(lds + L_UR + R * 4576 + 4096 + lane * 16) = (u32x4){0, 0, 0, 0};
            STAMP(3);
            if (!f1ok) {
                const u32 fo = WS_F1 + (u32)(((b >> 5) * 32 + 31 - hd * 8 - (lane & 7)) * 128);
                flg = (u32x2){0u, 0u};
                spin_flags(flg, fo);
                ald4s(uu, WS_UT + (u32)(b * 8192 + t2 * 16));
            }
            asm volatile("s_waitcnt vmcnt(0)" : "+v"(uu) :: "memory");
            *(LAS u32x4*)(lds + L_UR + R * 4576 + j * 16) = uu;
        }
        BAR();
        if (R == 0 && tid == 0) __builtin_amdgcn_raw_buffer_store_b64((u32x2){tl, th}, r_ws, WS_F2 + (grp * 8 + mem) * 128, 0, 16);
        STAMP(4);
        const int ch = hd >> 1, e0 = (hd & 1) * 4;
        const int p = (li + 1) & 7;
        u32 aA = L_HC + ch * 34816 + (272 * p + ((OFFS >> (4 * p)) & 15) + ((li + 1) >> 3) + lq + 4 * e0) * 16;
        u32 aB = L_UR + ch * 4576 + (254 - 224 - 2 * li + lq + 4 * e0) * 16;
        asm volatile("" : "+v"(aA), "+v"(aB));
        f32x4 y[4];
        const u32 f2own = WS_F2 + (u32)((grp * 8 + 4 * R + (lane & 3)) * 128);
        u32 ooff = WS_O1 + (u32)(TI * 16384 + R * 8192 + lane * 8);
        bool early = false;
        {
            constexpr ConvSched S(R);
            s16x8 A[4][8], B[4][8];
            auto loads_at = [&](auto pc) {
                constexpr int p = decltype(pc)::value;
                sfor<0, 64>([&](auto ic) {
                    constexpr int i = decltype(ic)::value, e = i >> 4, f = i & 7;
                    if constexpr (S.slot[i] == p) {
                        if constexpr (i & 8) B[e][f] = ldsA(aB + 64 * e + 512 * (7 - f)); else A[e][f] = ldsA(aA + 64 * (8 * f + e));
                    }
                });
            };
            loads_at(IC<-1>{});
            SB();
            sfor<0, 72>([&](auto pc) {
                constexpr int p = decltype(pc)::value, e = p / 18, f = S.f[p % 18], i = S.i[p % 18], g = S.n[i] - f;
                y[i] = mfma32(A[e][f], B[e][g], (e == 0 && f == 0) ? zero4 : y[i]);
                loads_at(pc);
                if constexpr (p < 24 && (p % 3) == 1) issue_wo(IC<(p / 3)>{});
                if constexpr (p == 36) ald2s(flg, f2own);
                if constexpr (p == 54) { WAIT_VM(0); pin(flg); early = flag_ok(flg); }
                if constexpr (p >= 55 && p < 71) {
                    if (early) ald2o<((p - 55) & 7) * 512>(of[p - 55], ooff + ((p - 55) >> 3) * 4096, d_ws);
                }
                SB();
            });
        }
        STAMP(14);
        RBAR();
        {
            constexpr int N0[4] = {0, 3, 4, 7};
            u32 ra = L_HC + hd * 8192 + lane * 16;
            asm volatile("" : "+v"(ra));
#pragma unroll
            for (int i = 0; i < 4; ++i) *(LAS f32x4*)(lds + ra + (N0[i] ^ R) * 1024) = y[i];
        }
        SB();
        if constexpr (R == 0) {
#pragma unroll
            for (int n = 0; n < 4; ++n) { const u32x2 w = __builtin_amdgcn_raw_buffer_load_b64(r_x, (u32)((TI * 16 + li) * 1024 + mem * 128 + lq * 8) + n * 32, 0, 0); xw[2 * n] = w[0]; xw[2 * n + 1] = w[1]; }
        }
        u32 f2oth = 0; u32x2 flg2;
        if constexpr (R == 1) {
            if (wave == 7) {
                const int pb = 32 * mem + (lane & 31);
                f2oth = WS_F2 + (u32)((lane < 32 ? ((pb & 7) * 4 + (pb >> 6)) * 8 + ((pb >> 3) & 7) : max(grp * 8 - 8, 0) + (lane & 15)) * 128);
                CFENCE();
                ald2s(flg2, f2oth);
            }
        }
        wo_addr();
        SB();
        STAMP(37);
        BAR();
        STAMP(38);
        {
            const float D0 = bflo(dw), D1 = bfhi(dw);
            u32 ra = L_HC + wave * 1024 + lane * 16;
            const int tb = 256 * wave + 16 * li + 4 * lq;
            u32 ru = L_UR + (2044 - tb) * 2;
            asm volatile("" : "+v"(ra), "+v"(ru));
            const f32x4 y0 = *(LAS f32x4*)(lds + ra), y1 = *(LAS f32x4*)(lds + ra + 8192), y2 = *(LAS f32x4*)(lds + ra + 16384), y3 = *(LAS f32x4*)(lds + ra + 24576);
            const u32x2 ua = *(LAS u32x2*)(lds + ru), ub = *(LAS u32x2*)(lds + ru + 4576);
            load_F(0); load_F(1);
            SB();
            const f32x4 ya = y0 + y1, yb = y2 + y3;
            const u32x4 w = {nosent(pk2(ya[0] + bfhi(ua[1]) * D0, yb[0] + bfhi(ub[1]) * D1)), nosent(pk2(ya[1] + bflo(ua[1]) * D0, yb[1] + bflo(ub[1]) * D1)),
                             nosent(pk2(ya[2] + bfhi(ua[0]) * D0, yb[2] + bfhi(ub[0]) * D1)), nosent(pk2(ya[3] + bflo(ua[0]) * D0, yb[3] + bflo(ub[0]) * D1))};
            __builtin_amdgcn_raw_buffer_store_b128(w, r_ws, WS_Y2 + (u32)(b * 8192 + tb * 4), 0, 16);
        }
        SB();
        STAMP(5);
        if (!early) {
            spin_flags(flg, f2own);
            sfor<0, 16>([&](auto ic) { constexpr int i = decltype(ic)::value;
                ald2o<(i & 7) * 512>(of[i], ooff + (i >> 3) * 4096, d_ws); });
            WAIT_VM(0);
        }
        WAIT_VM(1);
        sfor<0, 16>([&](auto ic) { pin(of[decltype(ic)::value]); });
        STAMP(40);

        f32x4 acc[4];
#pragma unroll
        for (int ks = 0; ks < 8; ++ks) {
            if (ks + 2 < 8) load_F(ks + 2);
            SB();
#pragma unroll
            for (int n = 0; n < 4; ++n) acc[n] = mfma32(F[ks][n], __builtin_bit_cast(s16x8, (u32x4){of[2 * ks][0], of[2 * ks][1], of[2 * ks + 1][0], of[2 * ks + 1][1]}), ks ? acc[n] : zero4);
            SB();
        }
        STAMP(41);
        accM[0] = acc[0]; accM[1] = acc[1]; accM[2] = acc[2]; accM[3] = acc[3];
        if constexpr (R == 1) { if (wave == 7) { asm volatile("" : "+v"(flg2)); spin_flags(flg2, f2oth); } }
    }

    auto load_y = [&]() {
        const int l2 = opq(tid) & 63;
#pragma unroll
        for (int i = 0; i < 8; ++i)
            yw[i] = __builtin_amdgcn_raw_buffer_load_b32(r_ws, WS_Y2 + (u32)((32 * mem + 2 * (l2 >> 4)) * 8192 + (TI * 16 + (l2 & 15)) * 4), (8 * (i >> 1) + (i & 1)) * 8192, 16);
    };
    auto pub_stat = [&](u32 ws_st, float s, float q) {
        const int lane = opq(tid) & 63;
        s = xsum(s); q = xsum(q);
        const u32x4 sv = {__float_as_uint(s), tl, __float_as_uint(q), th};
        __builtin_amdgcn_raw_buffer_store_b128(sv, r_ws, lane < 16 ? ws_st + (u32)((TI * 8 + mem) * 256 + lane * 16) : 0xFFFFFF00u, 0, 16);
    };
    auto stat_off = [&](u32 ws_st, int ti) { const int l2 = opq(tid) & 63; return ws_st + (u32)(ti * 2048 + (l2 >> 4) * 512 + (l2 & 15) * 16); };
    auto fin_stat = [&](u32x4& a, u32x4& c, u32 off, float& mean, float& rstd) {
        _Pragma("unroll 1") for (int spin = 0; spin < SPIN_CAP; ++spin) {
            if (__all(((a[1] ^ tl) | (a[3] ^ th) | (c[1] ^ tl) | (c[3] ^ th)) == 0)) break;
            __builtin_amdgcn_s_sleep(1);
            asm volatile("buffer_load_dwordx4 %0, %2, %3, 0 offen sc1\n\tbuffer_load_dwordx4 %1, %2, %3, 0 offen offset:256 sc1\n\ts_waitcnt vmcnt(0)"
                         : "=&v"(a), "=&v"(c) : "v"(off), "s"(d_ws) : "memory");
        }
        const float ts = xsum(__uint_as_float(a[0]) + __uint_as_float(c[0])), tq = xsum(__uint_as_float(a[2]) + __uint_as_float(c[2]));
        mean = ts * (1.f / 512.f);
        rstd = __builtin_amdgcn_rsqf(fmaxf(tq * (1.f / 512.f) - mean * mean, 0.f) + 1e-5f);
    };
    auto wo_finish = [&](auto lc) {
        constexpr int LAYER = decltype(lc)::value;
        const int lane = opq(tid) & 63, li = lane & 15, lq = lane >> 4;
        u32 wx = L_WX + hd * 4096 + lane * 16;
        asm volatile("" : "+v"(wx));
        if constexpr (R == 1) {
#pragma unroll
            for (int n = 0; n < 4; ++n) *(LAS f32x4*)(lds + wx + n * 1024) = accM[n];
        }
        BAR();
        STAMP(LAYER ? 25 : 42);
        if constexpr (R == 0) {
            f32x4 t[4];
#pragma unroll
            for (int n = 0; n < 4; ++n) t[n] = *(LAS f32x4*)(lds + wx + n * 1024);
            if constexpr (!LAYER) load_y();
            const u32 so = stat_off(LAYER ? WS_ST3 : WS_ST1, TI);
            SB();
            float s = 0.f, q = 0.f, s2 = 0.f, q2 = 0.f;
#pragma unroll
            for (int n = 0; n < 4; ++n) {
                const f32x4 v = accM[n] + t[n];
                const u32 w0 = pk2(bflo(xw[2 * n]) + v[0], bfhi(xw[2 * n]) + v[1]), w1 = pk2(bflo(xw[2 * n + 1]) + v[2], bfhi(xw[2 * n + 1]) + v[3]);
                xw[2 * n] = w0; xw[2 * n + 1] = w1;
                s = dot2(w0, 0x3F803F80u, s); q = dot2(w0, w0, q); s2 = dot2(w1, 0x3F803F80u, s2); q2 = dot2(w1, w1, q2);
            }
            pub_stat(LAYER ? WS_ST3 : WS_ST1, s + s2, q + q2);
            STAMP(LAYER ? 46 : 43);
            WAIT_VM(0);
            STAMP(LAYER ? 26 : 44);
            u32x4 a = {0, 0, 0, 0}, c = {0, 0, 0, 0};
            float mean, rstd;
            fin_stat(a, c, so, mean, rstd);
            STAMP(LAYER ? 11 : 6);
            if constexpr (!LAYER) {
                _Pragma("unroll 1") for (int spin = 0; spin < SPIN_CAP; ++spin) {
                    if (__all(max(max(max(yw[0], yw[1]), max(yw[2], yw[3])), max(max(yw[4], yw[5]), max(yw[6], yw[7]))) != SENT)) break;
                    __builtin_amdgcn_s_sleep(1);
                    CFENCE();
                    load_y();
                }
            }
            STAMP(47);
            u32 w[8];
            const float nm = -mean * rstd;
#pragma unroll
            for (int i = 0; i < 8; ++i) {
                const float a = bflo(xw[i]), c = bfhi(xw[i]);
                w[i] = pk2(a + (a * rstd + nm) * bflo(yw[i]), c + (c * rstd + nm) * bfhi(yw[i]));
                if (!LAYER) w[i] = nosent(w[i]);
            }
#pragma unroll
            for (int j = 0; j < 2; ++j) {
                auto r0 = __builtin_amdgcn_permlane16_swap(w[4 * j], w[4 * j + 2], false, false);
                auto r1 = __builtin_amdgcn_permlane16_swap(w[4 * j + 1], w[4 * j + 3], false, false);
                const u32x4 o = {r0[0], r1[0], r0[1], r1[1]};
                const int c = 4 * j + 2 * (lq & 1) + (lq >> 1);
                if constexpr (LAYER) __builtin_amdgcn_raw_buffer_store_b128(o, r_out, (u32)((TI * 16 + li) * 1024 + mem * 128 + c * 16), 0, 0);
                else __builtin_amdgcn_raw_buffer_store_b128(o, r_ws, WS_X2 + (u32)((TI * 8 + mem) * 2048 + li * 128 + ((c ^ (li >> 1)) << 4)), 0, 16);
            }
            if constexpr (!LAYER) {
                float s = 0.f, q = 0.f, s2 = 0.f, q2 = 0.f;
#pragma unroll
                for (int i = 0; i < 8; i += 2) {
                    xw[i] = w[i]; xw[i + 1] = w[i + 1];
                    s = dot2(w[i], 0x3F803F80u, s); q = dot2(w[i], w[i], q); s2 = dot2(w[i + 1], 0x3F803F80u, s2); q2 = dot2(w[i + 1], w[i + 1], q2);
                    asm volatile("" : "+v"(xw[i]), "+v"(xw[i + 1]), "+v"(yw[i]), "+v"(yw[i + 1]));
                }
                pub_stat(WS_ST2, s + s2, q + q2);
                STAMP(48);
                WAIT_VM(0);
            }
        }
    };
    wo_finish(IC<0>{});
    RBAR();
    STAMP(7);

    {
        const int l3 = opq(tid) & 63;
        const u32 g0 = (u32)((4 * grp - 1 + (wave >> 1)) * 16384 + (wave & 1) * 1024 + l3 * 16);
        const u32 g2 = (u32)((4 * grp + 3) * 16384 + wave * 256 + l3 * 16);
        auto x2_piece = [&](auto ic) {
            constexpr int i = decltype(ic)::value, s = i >> 1;
            const u32 kp = (u32)((s + rot) & 7);
            if constexpr (!(i & 1)) dma(IC<4>{}, IC<L_CX + s * 10240>{}, d_x2, wb1k, g0, kp * 2048);
            else dma(IC<5>{}, IC<L_CX + s * 10240 + 8192>{}, d_x2, wb256, g2, kp * 2048);
        };
        {
            const u32 po = (l3 & 8) ? (u32)((4 * grp + 3) * 16384 + wave * 256 + 252 + (l3 & 7) * 2048) : (u32)((4 * grp - 1 + (wave >> 1)) * 16384 + (wave & 1) * 1024 + 1020 + (l3 & 7) * 2048);
            _Pragma("unroll 1") for (int spin = 0; spin < SPIN_CAP; ++spin) {
                u32 pv;
                asm volatile("buffer_load_dword %0, %1, %2, 0 offen sc1\n\ts_waitcnt vmcnt(0)" : "=&v"(pv) : "v"(po), "s"(d_x2) : "memory");
                if (__all(pv != SENT)) break;
                __builtin_amdgcn_s_sleep(1);
            }
        }
        STAMP(39);
        u32 va = L_CX + wave * 1024 + l3 * 16, vq = L_CX + 8192 + wave * 256 + (l3 & 15) * 16;
        asm volatile("" : "+v"(va), "+v"(vq));
        u32x4 vr[4];
        auto ver_read = [&](auto cc) {
            constexpr int c = decltype(cc)::value;
            u32 a = va + c * 20480, q = vq + c * 20480;
            asm volatile("" : "+v"(a), "+v"(q) :: "memory");
            vr[0] = *(LAS u32x4*)(lds + a); vr[1] = *(LAS u32x4*)(lds + a + 10240); vr[2] = *(LAS u32x4*)(lds + q); vr[3] = *(LAS u32x4*)(lds + q + 10240);
        };
        auto ver_check = [&](auto cc) {
            constexpr int c = decltype(cc)::value;
            _Pragma("unroll 1") for (int spin = 0; spin < SPIN_CAP; ++spin) {
                const u32 m0 = max(max(max(vr[0][0], vr[0][1]), max(vr[0][2], vr[0][3])), max(max(vr[1][0], vr[1][1]), max(vr[1][2], vr[1][3])));
                const u32 m1 = max(max(max(vr[2][0], vr[2][1]), max(vr[2][2], vr[2][3])), max(max(vr[3][0], vr[3][1]), max(vr[3][2], vr[3][3])));
                if (__all(max(m0, m1) != SENT)) break;
                __builtin_amdgcn_s_sleep(1);
                sfor<4 * c, 4 * c + 4>([&](auto ic) { x2_piece(ic); });
                WAIT_VM(0);
                ver_read(cc);
            }
        };
        sfor<0, 8>([&](auto ic) { x2_piece(ic); });
        const int sti = max(4 * grp - 1 + min(wave, 4), 0);
        const u32 so = stat_off(WS_ST2, sti);
        const u32 xb0 = (u32)((l3 & 15) * 128 + (((l3 >> 4) ^ ((l3 & 15) >> 1)) << 4));
        WAIT_VM(4);
        STAMP(8);
        ver_read(IC<0>{});
        ver_check(IC<0>{});
        RBAR();
        STAMP(13);
        u32 xa;
        u32x4 sa, sc;
        auto pf_addr = [&](int st) { xa = L_CX + (st >> 1) * 10240 + (xb0 ^ ((st & 1) * 64)); asm volatile("" : "+v"(xa)); };
        pf_addr(0);
#pragma unroll
        for (int mt = 0; mt < 5; ++mt) Ab[0][mt] = ldsA(xa + mt * 2048);
        ald4s(sa, so); ald4s(sc, so + 256);
        sfor<0, 16>([&](auto stc) {
            constexpr int st = decltype(stc)::value;
            if constexpr (st == 2) { WAIT_VM(10); ver_read(IC<1>{}); }
            if constexpr (st == 3) { ver_check(IC<1>{}); RBAR(); STAMP(49); }
            if constexpr (st == 5) { WAIT_VM(4); ver_read(IC<2>{}); }
            if constexpr (st == 6) {
                ver_check(IC<2>{}); WAIT_VM(0); ver_read(IC<3>{});
                if (R == 0 || wave == 4) {
                    asm volatile("" : "+v"(sa), "+v"(sc));
                    float mean = 0.f, rstd = 1.f;
                    if (4 * grp - 1 + wave >= 0) fin_stat(sa, sc, so, mean, rstd);
                    if (l3 < 16) { *(LAS float*)(lds + L_TAB + wave * 64 + l3 * 4) = mean; *(LAS float*)(lds + L_TAB + 320 + wave * 64 + l3 * 4) = rstd; }
                }
            }
            if constexpr (st == 7) { ver_check(IC<3>{}); RBAR(); STAMP(50); CSTAMP(60); }
            if (st < 15) pf_addr(st + 1);
            if constexpr (st == 14) load_tab();
            SB();
            qkv_step(st, Ab[st & 1], [&](auto mc) {
                constexpr int mt = decltype(mc)::value;
                if (st < 15) Ab[(st + 1) & 1][mt] = ldsA(xa + mt * 2048);
                if constexpr (st < 2 && mt < 4) x2_piece(IC<8 + 4 * st + mt>{});
                SB();
            });
        });
    }
    STAMP(9);
    CSTAMP(61);
    attention(IC<1>{});
    STAMP(27);

    {
        wo_addr();
#pragma unroll
        for (int ks = 0; ks < 8; ++ks) load_F(ks);
        SB();
        WAIT_VM(0);
        STAMP(10);
        load_o(WS_O2);
        SB();
        _Pragma("unroll 1") for (int spin = 0; spin < 64; ++spin) {
            u32 m0 = 0, m1 = 0;
#pragma unroll
            for (int i = 0; i < 16; i += 2) { m0 = max(max(m0, of[i][0]), of[i][1]); m1 = max(max(m1, of[i + 1][0]), of[i + 1][1]); }
            if (__all(max(m0, m1) != SENT)) break;
            _Pragma("unroll 1") for (int sp2 = 0; sp2 < SPIN_CAP; ++sp2) {
                CFENCE();
                __builtin_amdgcn_s_sleep(1);
                const u32 pv = __builtin_amdgcn_raw_buffer_load_b32(r_ws, WS_O2 + (u32)(TI * 16384 + R * 8192 + (opq(tid) & 15) * 512 + 508), 0, 16);
                if (__all(pv != SENT)) break;
            }
            CFENCE();
            load_o(WS_O2);
        }
        STAMP(24);
#pragma unroll
        for (int ks = 0; ks < 8; ++ks)
#pragma unroll
            for (int n = 0; n < 4; ++n) accM[n] = mfma32(F[ks][n], __builtin_bit_cast(s16x8, (u32x4){of[2 * ks][0], of[2 * ks][1], of[2 * ks + 1][0], of[2 * ks + 1][1]}), ks ? accM[n] : zero4);
        SB();
        STAMP(45);
    }
    wo_finish(IC<1>{});
    if constexpr (R == 1) {
        if (b == 0 && wave == 7) {
            const int l3 = opq(tid) & 63;
#pragma unroll
            for (int i = 0; i < 4; ++i) { flg = (u32x2){0u, 0u}; spin_flags(flg, WS_F1 + (u32)((i * 64 + l3) * 128)); }
            if (l3 == 0) __builtin_amdgcn_raw_buffer_store_b64(__builtin_bit_cast(u32x2, epoch + 1), r_ws, WS_EPOCH, 0, 16);
        }
    }
}

__global__ void __launch_bounds__(512, 2)
fused_kernel(const u16* __restrict__ gx, const u16* __restrict__ gwqkv, const u16* __restrict__ gwo,
             const u16* __restrict__ gbk, const u16* __restrict__ gbv, const u16* __restrict__ gsk,
             const u16* __restrict__ ghk, const u16* __restrict__ gdv, u16* __restrict__ gout, char* __restrict__ ws) {
    __shared__ __attribute__((aligned(16))) char lds_[L_TOTAL];
    const Args a = {gx, gwqkv, gwo, gbk, gbv, gsk, ghk, gdv, gout, ws};
    const int wave = __builtin_amdgcn_readfirstlane(threadIdx.x >> 6);
    if (wave < 4) body<0>(a, (LAS char*)lds_, wave); else body<1>(a, (LAS char*)lds_, wave);
}

extern "C" void kernel_launch(void* const* d_in, const int* in_sizes, int n_in,
                              void* d_out, int out_size, void* d_ws, size_t ws_size,
                              hipStream_t stream) {
    fused_kernel<<<256, 512, 0, stream>>>((const u16*)d_in[0], (const u16*)d_in[1], (const u16*)d_in[2],
                                          (const u16*)d_in[4], (const u16*)d_in[5], (const u16*)d_in[6],
                                          (const u16*)d_in[7], (const u16*)d_in[8], (u16*)d_out, (char*)d_ws);
}
```

```cpp
#define OUT_AUX 16
#include <hip/hip_runtime.h>
#include <stdint.h>

typedef unsigned short u16;
typedef unsigned u32;
typedef unsigned long long u64;
typedef short s16x4 __attribute__((ext_vector_type(4)));
typedef short s16x8 __attribute__((ext_vector_type(8)));
typedef __bf16 bf16x2 __attribute__((ext_vector_type(2)));
typedef float f32x2 __attribute__((ext_vector_type(2)));
typedef float f32x4 __attribute__((ext_vector_type(4)));
typedef u32 u32x2 __attribute__((ext_vector_type(2)));
typedef u32 u32x4 __attribute__((ext_vector_type(4)));
#define LAS __attribute__((address_space(3)))
#define DEV __device__ __forceinline__
typedef __amdgpu_buffer_rsrc_t rsrc_t;

constexpr u32 WS_EPOCH = 0;
constexpr u32 WS_F1 = 32768;
constexpr u32 WS_F2 = 65536;
constexpr u32 WS_ST0 = 98304;
constexpr u32 WS_ST1 = 131072;
constexpr u32 WS_ST2 = WS_ST1 + 262144;
constexpr u32 WS_ST3 = WS_ST2 + 262144;
constexpr u32 WS_UT = 1u << 20;
constexpr u32 WS_O1 = WS_UT + (2u << 20);
constexpr u32 WS_O2 = WS_O1 + (2u << 20);
constexpr u32 WS_Y2 = WS_O2 + (2u << 20);
constexpr u32 WS_X2 = WS_Y2 + (2u << 20);
constexpr u32 WS_END = WS_X2 + (2u << 20);

constexpr u32 L_SLOT = 34816;
constexpr u32 L_KV = 4 * L_SLOT;
constexpr u32 L_K = L_KV, L_V = L_KV + 9216;
constexpr u32 L_SK = L_KV + 17408;
constexpr u32 L_WO = 0;
constexpr u32 L_CX = 65536;
constexpr u32 L_HC = L_CX;
constexpr u32 L_UR = L_CX + 69632;
constexpr u32 L_WX = L_CX + 40960;
constexpr u32 L_EX = 147456;
constexpr u32 L_TAB = L_EX + 12288;
constexpr u32 L_CS = L_TAB + 1024;
constexpr u32 L_FLG = L_TAB + 2048;
constexpr u32 L_DUMMY = L_FLG + 1024;
constexpr u32 L_TOTAL = 160 * 1024;
static_assert(L_DUMMY + 1024 <= L_TOTAL, "LDS map");

#define WO_POS 0
constexpr int SPIN_CAP = 1 << 14;
constexpr u32 SENT = 0xFFFFFFFFu;

#define WAIT_VM(n) asm volatile("s_waitcnt vmcnt(%0)" ::"i"(n) : "memory")
#define BAR() do { asm volatile("s_waitcnt lgkmcnt(0)" ::: "memory"); __builtin_amdgcn_s_barrier(); asm volatile("" ::: "memory"); } while (0)
#define RBAR() do { asm volatile("" ::: "memory"); __builtin_amdgcn_s_barrier(); asm volatile("" ::: "memory"); } while (0)
#define CFENCE() asm volatile("" ::: "memory")
#define SB() __builtin_amdgcn_sched_barrier(0)
template <int N> struct IC { static constexpr int value = N; };
template <int I, int N, class F> DEV void sfor(F&& f) { if constexpr (I < N) { f(IC<I>{}); sfor<I + 1, N>(f); } }

DEV f32x4 mfma32(s16x8 a, s16x8 b, f32x4 c) { return __builtin_amdgcn_mfma_f32_16x16x32_bf16(a, b, c, 0, 0, 0); }
DEV f32x4 mfma16(s16x4 a, s16x4 b, f32x4 c) { return __builtin_amdgcn_mfma_f32_16x16x16bf16_1k(a, b, c, 0, 0, 0); }
DEV float vmax(float a, float b) { float r; asm("v_max_f32 %0, %1, %2" : "=v"(r) : "v"(a), "v"(b)); return r; }
DEV u32 pk2(float lo, float hi) { f32x2 f = {lo, hi}; bf16x2 b = __builtin_convertvector(f, bf16x2); return __builtin_bit_cast(u32, b); }
DEV float bflo(u32 w) { return __uint_as_float(w << 16); }
DEV float bfhi(u32 w) { return __uint_as_float(w & 0xffff0000u); }
DEV float dot2(u32 a, u32 b, float c) { return __builtin_amdgcn_fdot2_f32_bf16(__builtin_bit_cast(bf16x2, a), __builtin_bit_cast(bf16x2, b), c, false); }
DEV u32 nosent(u32 w) { return min(w, 0xFFFFFFFEu); }
DEV float xsum(float v) {
    auto r = __builtin_amdgcn_permlane32_swap(__float_as_uint(v), __float_as_uint(v), false, false);
    v = __uint_as_float(r[0]) + __uint_as_float(r[1]);
    auto s = __builtin_amdgcn_permlane16_swap(__float_as_uint(v), __float_as_uint(v), false, false);
    return __uint_as_float(s[0]) + __uint_as_float(s[1]);
}
DEV float xmax(float v) {
    auto r = __builtin_amdgcn_permlane32_swap(__float_as_uint(v), __float_as_uint(v), false, false);
    v = vmax(__uint_as_float(r[0]), __uint_as_float(r[1]));
    auto s = __builtin_amdgcn_permlane16_swap(__float_as_uint(v), __float_as_uint(v), false, false);
    return vmax(__uint_as_float(s[0]), __uint_as_float(s[1]));
}
DEV float rowsum16(float v) {
    v += __builtin_bit_cast(float, __builtin_amdgcn_update_dpp(0, __builtin_bit_cast(int, v), 0xB1, 0xf, 0xf, true));
    v += __builtin_bit_cast(float, __builtin_amdgcn_update_dpp(0, __builtin_bit_cast(int, v), 0x4E, 0xf, 0xf, true));
    v += __builtin_bit_cast(float, __builtin_amdgcn_update_dpp(0, __builtin_bit_cast(int, v), 0x141, 0xf, 0xf, true));
    v += __builtin_bit_cast(float, __builtin_amdgcn_update_dpp(0, __builtin_bit_cast(int, v), 0x140, 0xf, 0xf, true));
    return v;
}
DEV float bcast_row0(float v) { return __builtin_bit_cast(float, __builtin_amdgcn_update_dpp(0, __builtin_bit_cast(int, v), 0x150, 0xf, 0xf, true)); }
template <int OFF> DEV void ald2o(u32x2& d, u32 voff, u32x4 rs) { asm volatile("buffer_load_dwordx2 %0, %1, %2, 0 offen offset:%3 sc1" : "=v"(d) : "v"(voff), "s"(rs), "i"(OFF) : "memory"); }
template <class T> DEV void pin(T& v) { asm volatile("" : "+v"(v)); }
DEV int opq(int v) { asm volatile("" : "+v"(v)); return v; }
DEV s16x8 cat8(s16x4 a, s16x4 b) { return __builtin_shufflevector(a, b, 0, 1, 2, 3, 4, 5, 6, 7); }
DEV s16x4 pack4(f32x4 v) { u32x2 w = {pk2(v[0], v[1]), pk2(v[2], v[3])}; return __builtin_bit_cast(s16x4, w); }
DEV u64 mix64(u64 z) { z += 0x9E3779B97F4A7C15ull; z = (z ^ (z >> 30)) * 0xBF58476D1CE4E5B9ull; z = (z ^ (z >> 27)) * 0x94D049BB133111EBull; return z ^ (z >> 31); }

struct ConvSched {
    int n[4], f[18], i[18], slot[64];
    constexpr ConvSched(int R) : n{}, f{}, i{}, slot{} {
        constexpr int LEAD = 9;
        const int N0[4] = {0, 3, 4, 7};
        for (int k = 0; k < 4; ++k) n[k] = N0[k] ^ R;
        int c = 0;
        for (int ff = 0; ff < 8; ++ff) for (int k = 3; k >= 0; --k) if (ff <= n[k]) { f[c] = ff; i[c] = k; ++c; }
        for (int k = 0; k < 64; ++k) slot[k] = 1000;
        for (int p = 71; p >= 0; --p) {
            const int e = p / 18, ff = f[p % 18], g = n[i[p % 18]] - ff, s = p - LEAD < -1 ? -1 : p - LEAD;
            slot[e * 16 + ff] = s; slot[e * 16 + 8 + g] = s;
        }
    }
};

#define STAMP(i) do { } while (0)
#define CSTAMP(i) do { } while (0)

struct Args { const u16 *gx, *gwqkv, *gwo, *gbk, *gbv, *gsk, *ghk, *gdv; u16* gout; char* ws; };

template <int R>
DEV void body(const Args& A_, LAS char* const lds, const int wave) {
    const u16* gx = A_.gx; char* ws = A_.ws;
    STAMP(0);
    const int b = blockIdx.x, tid = threadIdx.x, lane = tid & 63;
    const int hd = wave & 3;
    const int grp = (b & 7) * 4 + (b >> 6), mem = (b >> 3) & 7;
    const int T0 = grp * 64;
    const int rot = (mem + 2 * (grp & 3)) & 7;

    u64 epoch;
    asm volatile("s_load_dwordx2 %0, %1, 0x0" : "=s"(epoch) : "s"(ws));
    const u32 dw = *(const u32*)(A_.gdv + 2 * b);
    const rsrc_t r_x = __builtin_amdgcn_make_buffer_rsrc((void*)gx, 0, 2048 * 512 * 2, 0x00020000);
    const rsrc_t r_ws = __builtin_amdgcn_make_buffer_rsrc((void*)ws, 0, WS_END, 0x00020000);
    const rsrc_t r_out = __builtin_amdgcn_make_buffer_rsrc((void*)A_.gout, 0, 2048 * 512 * 2, 0x00020000);
    const u32 ldsbase = (u32)(size_t)lds;
    auto mkdesc = [](const void* p, u32 bytes) { const u64 a = (u64)p; return (u32x4){(u32)a, (u32)(a >> 32) & 0xffffu, bytes, 0x00020000u}; };
    const u32x4 d_x = mkdesc(gx, 2048 * 512 * 2), d_w = mkdesc(A_.gwqkv, 512 * 1536 * 2), d_wo = mkdesc(A_.gwo, 512 * 512 * 2);
    const u32x4 d_k = mkdesc(A_.gbk, 2048 * 512 * 2), d_v = mkdesc(A_.gbv, 2048 * 512 * 2), d_sk = mkdesc(A_.gsk, 512 * 4 * 2);
    const u32x4 d_h = mkdesc(A_.ghk, 512 * 2048 * 2), d_x2 = mkdesc(ws + WS_X2, 2u << 20), d_ws = mkdesc(ws, WS_END);
    const u32 wb1k = ldsbase + wave * 1024, wb256 = ldsbase + wave * 256;
#define DMA_ASM(pre, mods, post) asm volatile("s_add_u32 m0, %0, %4\n\t" pre "buffer_load_dwordx4 %1, %2, %3 offen " mods "lds" post \
                     :: "s"(sbase), "v"(voff), "s"(rs), "s"(soff), "i"(IMM) : "memory", "scc")
    const u32 xmlo = wave == 0 ? ~0xFFu : ~0u;
    auto dma = [&](auto kind, auto imm, u32x4 rs, u32 sbase, u32 voff, u32 soff) {
        constexpr int K = decltype(kind)::value; constexpr int IMM = decltype(imm)::value;
        if constexpr (K == 0) DMA_ASM("s_nop 0\n\t", "", "");
        else if constexpr (K == 1) DMA_ASM("s_mov_b64 exec, 0xffff\n\t", "", "\n\ts_mov_b64 exec, -1");
        else if constexpr (K == 2) {
            const u32 ml = xmlo;
            asm volatile("s_add_u32 m0, %0, %4\n\ts_mov_b32 exec_lo, %5\n\tbuffer_load_dwordx4 %1, %2, %3 offen lds\n\ts_mov_b32 exec_lo, -1"
                         :: "s"(sbase), "v"(voff), "s"(rs), "s"(soff), "i"(IMM), "s"(ml) : "memory", "scc");
        }
        else if constexpr (K == 4) DMA_ASM("s_nop 0\n\t", "sc1 ", "");
        else DMA_ASM("s_mov_b64 exec, 0xffff\n\t", "sc1 ", "\n\ts_mov_b64 exec, -1");
    };
    auto ald4 = [&](u32x4& d, u32x4 rs, u32 voff) { asm volatile("buffer_load_dwordx4 %0, %1, %2, 0 offen" : "=v"(d) : "v"(voff), "s"(rs) : "memory"); };
    auto ald4s = [&](u32x4& d, u32 voff) { asm volatile("buffer_load_dwordx4 %0, %1, %2, 0 offen sc1" : "=v"(d) : "v"(voff), "s"(d_ws) : "memory"); };
    auto ald2s = [&](u32x2& d, u32 voff) { asm volatile("buffer_load_dwordx2 %0, %1, %2, 0 offen sc1" : "=v"(d) : "v"(voff), "s"(d_ws) : "memory"); };

    const int pr = lane >> 3, pc = lane & 7;
    const int xr0 = 8 * wave + pr, xr2 = 64 + 2 * wave + pr;
    const u32 xv0 = (u32)((T0 - 16 + xr0) * 1024 + ((pc ^ ((xr0 >> 1) & 7)) << 4));
    const u32 xv2 = (u32)((T0 - 16 + xr2) * 1024 + ((pc ^ ((xr2 >> 1) & 7)) << 4));
    const int wfw = ((xr0 >> 1) & 1) | (((xr0 >> 3) & 1) << 1);
    const u32 wv0 = (u32)(xr0 * 3072 + mem * 128 + ((((pc >> 1) ^ wfw) * 2 + (pc & 1)) << 4));
    auto issue_piece = [&](auto sc, auto ic) {
        constexpr int s = decltype(sc)::value, i = decltype(ic)::value;
        const u32 kp = (u32)((s + rot) & 7);
        constexpr int sb = (s & 3) * L_SLOT;
        if constexpr (i == 0) dma(IC<R ? 0 : 2>{}, IC<sb>{}, d_x, wb1k, xv0, kp * 128);
        else if constexpr (i == 1) dma(IC<1>{}, IC<sb + 8192>{}, d_x, wb256, xv2, kp * 128);
        else dma(IC<0>{}, IC<sb + 10240 + (i - 2) * 8192>{}, d_w, wb1k, wv0, kp * (64 * 3072) + (i - 2) * 1024);
    };
    auto issue_stage = [&](auto sc) { issue_piece(sc, IC<0>{}); issue_piece(sc, IC<1>{}); issue_piece(sc, IC<2>{}); issue_piece(sc, IC<3>{}); issue_piece(sc, IC<4>{}); };
    auto issue_wo = [&](auto ic) {
        constexpr int i = decltype(ic)::value;
        const int l2 = opq(tid) & 63, pr = l2 >> 3, pc = l2 & 7;
        const u32 wov = (u32)((8 * wave + pr) * 1024 + mem * 128 + ((((pc >> 1) ^ ((pr >> 1) & 3)) * 2 + (pc & 1)) << 4));
        const u32 j = (u32)((i + 2 * grp) & 7);
        dma(IC<0>{}, IC<L_WO>{}, d_wo, wb1k + j * 8192, wov, j * 65536);
    };
    auto issue_kv = [&](int i) {
        const int l2 = opq(tid) & 63, pr = l2 >> 3, pc = l2 & 7;
        const u32 cb = (u32)(mem * 128);
        if (i == 0) dma(IC<0>{}, IC<L_K>{}, d_k, wb1k, (u32)((T0 - 8 + 8 * wave + pr) * 1024) + cb + ((pc ^ wave) << 4), 0);
        else if (i == 1) dma(IC<0>{}, IC<L_V>{}, d_v, wb1k, (u32)((T0 + 8 * wave + pr) * 1024) + cb + ((pc ^ wave) << 4), 0);
        else if (R == 0) dma(IC<0>{}, IC<L_K + 8192>{}, d_k, ldsbase, (u32)((T0 - 8 + 64 + pr) * 1024) + cb + (pc << 4), 0);
        else dma(IC<0>{}, IC<L_SK>{}, d_sk, ldsbase, (u32)(mem * 512 + l2 * 16), 0);
    };

    const int srow = T0 + 8 * mem + wave;
    u32x4 xrow;
    ald4(xrow, d_x, (u32)(srow * 1024 + lane * 16));
    sfor<2, 5>([&](auto ic) { issue_piece(IC<0>{}, ic); });
    issue_kv(0); issue_kv(1); issue_kv(2);
    asm volatile("s_waitcnt lgkmcnt(0)" : "+s"(epoch));
    const u64 tag = mix64(epoch);
    const u32 tl = (u32)tag, th = (u32)(tag >> 32);
    if (R == 1 && wave == 7 && lane < 32) *(LAS u32x4*)(lds + (lane >> 3) * L_SLOT + (lane & 7) * 16) = (u32x4){0x3F803F80u, 0x3F803F80u, 0x3F803F80u, 0x3F803F80u};
    {
        WAIT_VM(6);
        pin(xrow);
        float s = 0.f, q = 0.f;
#pragma unroll
        for (int i = 0; i < 4; ++i) { s = dot2(xrow[i], 0x3F803F80u, s); q = dot2(xrow[i], xrow[i], q); }
        s = rowsum16(s); q = rowsum16(q);
        s = xsum(s); q = xsum(q);
        const float m = s * (1.f / 512.f);
        const float r = __builtin_amdgcn_rsqf(fmaxf(q * (1.f / 512.f) - m * m, 0.f) + 1e-5f);
        const u32x4 sv = {__float_as_uint(m), tl, __float_as_uint(r), th};
        __builtin_amdgcn_raw_buffer_store_b128(sv, r_ws, lane == 0 ? WS_ST0 + (u32)(srow * 16) : 0xFFFFFF00u, 0, 16);
        CFENCE();
    }
    issue_piece(IC<0>{}, IC<0>{}); issue_piece(IC<0>{}, IC<1>{});
    sfor<2, 5>([&](auto ic) { issue_piece(IC<1>{}, ic); });
    issue_piece(IC<1>{}, IC<0>{}); issue_piece(IC<1>{}, IC<1>{});

    auto trr = [&](u32 off) { return __builtin_amdgcn_ds_read_tr16_b64_v4i16((LAS s16x4*)(lds + off)); };
    auto ldsA = [&](u32 off) { return *(LAS s16x8*)(lds + off); };
    auto flag_ok = [&](const u32x2& f) { return __all(f[0] == tl && f[1] == th); };
    auto spin_flags = [&](u32x2& f, u32 off) {
        _Pragma("unroll 1") for (int spin = 0; spin < SPIN_CAP; ++spin) {
            if (flag_ok(f)) break;
            __builtin_amdgcn_s_sleep(2);
            asm volatile("buffer_load_dwordx2 %0, %1, %2, 0 offen sc1\n\ts_waitcnt vmcnt(0)" : "=&v"(f) : "v"(off), "s"(d_ws) : "memory");
        }
    };

    s16x8 Wq[16], Wm[16];
    f32x4 accM[5], accQ[5];
    const f32x4 zero4 = {0.f, 0.f, 0.f, 0.f};
    const s16x8 ones8 = {0x3F80, 0x3F80, 0x3F80, 0x3F80, 0x3F80, 0x3F80, 0x3F80, 0x3F80};
    s16x8 Ab[3][5];
    auto qkv_step = [&](auto lc, int st, const s16x8 (&A)[5], auto fill) {
        sfor<0, 5>([&](auto mc) {
            constexpr int mt = decltype(mc)::value;
            const f32x4 cm = st ? accM[mt] : zero4;
            accM[mt] = R ? mfma32(A[mt], Wm[st], cm) : mfma32(Wm[st], A[mt], cm);
            if constexpr (R == 0 && (mt > 0 || decltype(lc)::value == 0)) accQ[mt] = mfma32(Wq[st], A[mt], st ? accQ[mt] : zero4);
            fill(mc);
        });
    };

    float m1[5], r1[5]; f32x4 m4[5], r4[5], csK, csQ;
    auto load_tab = [&](bool cs) {
        const int l2 = opq(tid) & 63;
        u32 ta = L_TAB + (R ? (l2 >> 4) * 16 : (l2 & 15) * 4);
        pin(ta);
        if (R == 0 && cs) { csK = *(LAS f32x4*)(lds + L_CS + hd * 128 + (l2 >> 4) * 16); csQ = *(LAS f32x4*)(lds + L_CS + hd * 128 + 64 + (l2 >> 4) * 16); }
#pragma unroll
        for (int mt = 0; mt < 5; ++mt) {
            if (R == 0) { m1[mt] = *(LAS float*)(lds + ta + mt * 64); r1[mt] = *(LAS float*)(lds + ta + 320 + mt * 64); }
            else { m4[mt] = *(LAS f32x4*)(lds + ta + mt * 64); r4[mt] = *(LAS f32x4*)(lds + ta + 320 + mt * 64); }
        }
    };

    u32x4 hd0, hd1;
    u32x2 flg;
    {
        const int li = lane & 15, lq = lane >> 4, tq = li >> 2, tp = lane & 3;
        const u32 xa0 = (u32)(li * 128 + ((lq ^ (li >> 1)) << 4));
        const u32 wa = (u32)((8 * lq + tq) * 128 + ((hd ^ (((tq >> 1) & 1) | ((lq & 1) << 1))) << 5) + tp * 8);
        u32 xa, wb;
        u32x4 s0v;
        auto pf_addr = [&](int st) {
            const u32 sb = (u32)((st >> 1) & 3) * L_SLOT;
            xa = sb + (xa0 ^ ((st & 1) * 64)); wb = sb + 10240 + (st & 1) * 4096 + wa;
            asm volatile("" : "+v"(xa), "+v"(wb));
        };
        auto pf_part = [&](int st, int mt) {
            if (mt == 0) Wm[st] = cat8(trr(wb + (1 + R) * 8192), trr(wb + (1 + R) * 8192 + 512));
            Ab[st & 1][mt] = ldsA(xa + mt * 2048);
            if (R == 0 && mt == 1) Wq[st] = cat8(trr(wb), trr(wb + 512));
        };
        STAMP(15);
        WAIT_VM(5);
        BAR();
        STAMP(16);
        pf_addr(0);
#pragma unroll
        for (int mt = 0; mt < 5; ++mt) pf_part(0, mt);
        sfor<0, 16>([&](auto stc) {
            constexpr int st = decltype(stc)::value;
            if constexpr ((st & 1) && st < 15) {
                constexpr int s1 = (st >> 1) + 1;
                constexpr int WN[8] = {0, 6, 6, 5, 5, 5, 6, 4};
                WAIT_VM(WN[s1]);
                if constexpr (s1 == 7) {
                    asm volatile("" : "+v"(s0v));
                    if (R == 0 || wave == 4) {
                        const int l2 = opq(tid) & 63;
                        if (T0 - 16 + wave * 16 >= 0) {
                            _Pragma("unroll 1") for (int spin = 0; spin < SPIN_CAP; ++spin) {
                                if (__all(s0v[1] == tl && s0v[3] == th)) break;
                                __builtin_amdgcn_s_sleep(2);
                                asm volatile("buffer_load_dwordx4 %0, %1, %2, 0 offen sc1\n\ts_waitcnt vmcnt(0)" : "=&v"(s0v) : "v"(WS_ST0 + (u32)((T0 - 16 + wave * 16 + (l2 & 15)) * 16)), "s"(d_ws) : "memory");
                            }
                        } else { s0v[0] = 0u; s0v[2] = 0x3F800000u; }
                        if (l2 < 16) { *(LAS u32*)(lds + L_TAB + wave * 64 + l2 * 4) = s0v[0]; *(LAS u32*)(lds + L_TAB + 320 + wave * 64 + l2 * 4) = s0v[2]; }
                        asm volatile("s_waitcnt lgkmcnt(0)" ::: "memory");
                    }
                }
                RBAR();
                STAMP(16 + s1);
                if constexpr (s1 == 3) {
                    if (R == 0 && tid == 0) __builtin_amdgcn_raw_buffer_store_b64((u32x2){tl, th}, r_ws, WS_F1 + (mem * 32 + grp) * 128, 0, 16);
                    CFENCE();
                }
                if constexpr (s1 == 5) {
                    const int row = T0 - 16 + wave * 16 + (opq(tid) & 15);
                    ald4s(s0v, WS_ST0 + (u32)(max(row, 0) * 16));
                }
            }
            if (st < 15) pf_addr(st + 1);
            if constexpr (st == 14) load_tab(false);
            SB();
            qkv_step(IC<0>{}, st, Ab[st & 1], [&](auto mc) {
                constexpr int mt = decltype(mc)::value;
                if (st < 15) pf_part(st + 1, mt);
                if constexpr (st == 0) issue_piece(IC<2>{}, mc);
                if constexpr (st >= 1 && st <= 10) {
                    constexpr int sn = ((st - 1) >> 1) + 3;
                    if constexpr ((st - 1) & 1) { if constexpr (mt == 1) issue_piece(IC<sn>{}, IC<3>{}); if constexpr (mt == 3) issue_piece(IC<sn>{}, IC<4>{}); }
                    else if constexpr (!(mt & 1)) issue_piece(IC<sn>{}, IC<(mt >> 1)>{});
                }
                if constexpr (st == 11 && mt == 0) { const u32 ho = (u32)(b * 8192 + opq(tid) * 16); ald4(hd0, d_h, ho); ald4(hd1, d_h, ho + 16); }
                if constexpr (st == 11 && (mt == 2 || mt == 4)) {
                    const int l2 = opq(tid) & 63;
                    dma(IC<0>{}, IC<L_DUMMY>{}, d_wo, ldsbase, (u32)((l2 >> 3) * 1024 + (l2 & 7) * 16), (u32)(((grp & 3) * 128 + wave * 16 + (mt - 2) * 4) * 1024 + mem * 128));
                }
                if constexpr (R == 1 && st == 14 && mt == 4) { if (wave == 7) ald2s(flg, WS_F1 + (u32)(((b >> 5) * 32 + (opq(tid) & 31)) * 128)); }
                SB();
            });
            SB();
            if constexpr (st == 0) {
                const int l2 = opq(tid) & 63, tg = l2 & 7, chl = l2 >> 3;
                const u32x2 skw = *(LAS u32x2*)(lds + L_SK + (8 * wave + chl) * 8);
                u32 ka = L_K + tg * 1024 + (((wave ^ tg) & 7) << 4) + chl * 2;
                u32 kb = L_K + tg * 1024 + (((wave ^ (tg + 1)) & 7) << 4) + chl * 2;
                asm volatile("" : "+v"(ka), "+v"(kb));
                u16 kh[11], vh[8];
#pragma unroll
                for (int i = 0; i < 11; ++i) kh[i] = *(LAS u16*)(lds + ((i + 5 < 8) ? ka : kb) + (i + 5) * 128);
#pragma unroll
                for (int i = 0; i < 8; ++i) vh[i] = *(LAS u16*)(lds + ka + (L_V - L_K) + i * 128);
                SB();
                const float a0 = bflo(skw[0]), a1 = bfhi(skw[0]), a2 = bflo(skw[1]), a3 = bfhi(skw[1]);
                float ua[8];
#pragma unroll
                for (int i = 0; i < 8; ++i) ua[i] = (bflo(kh[i]) * a3 + bflo(kh[i + 1]) * a2 + bflo(kh[i + 2]) * a1 + bflo(kh[i + 3]) * a0) * bflo(vh[i]);
                const u32x4 oa = {pk2(ua[7], ua[6]), pk2(ua[5], ua[4]), pk2(ua[3], ua[2]), pk2(ua[1], ua[0])};
                __builtin_amdgcn_raw_buffer_store_b128(oa, r_ws, WS_UT + (u32)((mem * 64 + 8 * wave + chl) * 4096 + (2040 - T0 - 8 * tg) * 2), 0, 16);
                CFENCE();
                SB();
            }
        });
    }
    STAMP(R ? 1 : 53);

    u32x4 uu;
    float csV;
    auto attention = [&](auto lc) {
        constexpr int LAYER = decltype(lc)::value;
        constexpr int SBASE = LAYER ? 32 : 28;
        const int lane = opq(tid) & 63, li = lane & 15, lq = lane >> 4;
        u32 ex = L_EX + hd * 2048 + lane * 8;
        pin(ex);
        u32 f1ok = 1;
        if constexpr (R == 0) {
            if constexpr (LAYER == 0) {
#pragma unroll
                for (int i = 0; i < 4; ++i) { csK[i] = bcast_row0(accM[0][i]); csQ[i] = bcast_row0(accQ[0][i]); }
                if (li == 0) { *(LAS f32x4*)(lds + L_CS + hd * 128 + lq * 16) = csK; *(LAS f32x4*)(lds + L_CS + hd * 128 + 64 + lq * 16) = csQ; }
            }
            const int par = li & 1;
            const bool cur0 = (4 * lq + par) <= li, cur1 = (4 * lq + par + 2) <= li;
            s16x4 Kb[5], Qb[5];
#pragma unroll
            for (int mt = 0; mt < 5; ++mt) {
                Kb[mt] = pack4((accM[mt] - m1[mt] * csK) * r1[mt]);
                if (mt) Qb[mt] = pack4(accQ[mt] - m1[mt] * csQ);
            }
            f32x4 Sc[5], Sp[5];
#pragma unroll
            for (int mt = 1; mt < 5; ++mt) { Sc[mt] = mfma16(Kb[mt], Qb[mt], zero4); Sp[mt] = mfma16(Kb[mt - 1], Qb[mt], zero4); }
            float s0[5], s1[5], mx[5];
#pragma unroll
            for (int mt = 1; mt < 5; ++mt) {
                const float sp0 = Sp[mt][0], sp1 = Sp[mt][1], sp2 = Sp[mt][2], sp3 = Sp[mt][3], c0 = Sc[mt][0], c1 = Sc[mt][1], c2 = Sc[mt][2], c3 = Sc[mt][3];
                float p0 = par ? sp1 : sp0, p1 = par ? sp3 : sp2, q0 = par ? c1 : c0, q1 = par ? c3 : c2;
                asm volatile("" : "+v"(p0), "+v"(p1), "+v"(q0), "+v"(q1));
                if (mt == 1 && grp == 0) { p0 = -3.0e38f; p1 = -3.0e38f; }
                s0[mt] = cur0 ? q0 : p0; s1[mt] = cur1 ? q1 : p1;
                mx[mt] = vmax(s0[mt], s1[mt]);
            }
#pragma unroll
            for (int mt = 1; mt < 5; ++mt) { auto r = __builtin_amdgcn_permlane32_swap(__float_as_uint(mx[mt]), __float_as_uint(mx[mt]), false, false); mx[mt] = vmax(__uint_as_float(r[0]), __uint_as_float(r[1])); }
#pragma unroll
            for (int mt = 1; mt < 5; ++mt) { auto r = __builtin_amdgcn_permlane16_swap(__float_as_uint(mx[mt]), __float_as_uint(mx[mt]), false, false); mx[mt] = vmax(__uint_as_float(r[0]), __uint_as_float(r[1])); }
#pragma unroll
            for (int mt = 1; mt < 5; ++mt) {
                const float sc = r1[mt] * (0.25f * 1.44269504088896341f);
                const float e0 = __builtin_amdgcn_exp2f((s0[mt] - mx[mt]) * sc), e1 = __builtin_amdgcn_exp2f((s1[mt] - mx[mt]) * sc);
                *(LAS u32x2*)(lds + ex + (mt - 1) * 512) = (u32x2){pk2(e0, e0), pk2(e1, e1)};
            }
            STAMP(SBASE + 2);
            BAR();
            STAMP(SBASE + 1);
            if constexpr (LAYER == 0) f1ok = __builtin_amdgcn_readfirstlane(*(LAS u32*)(lds + L_FLG));
        } else {
            if constexpr (LAYER == 0) {
                auto s = __builtin_amdgcn_permlane16_swap(__float_as_uint(accM[0][0]), __float_as_uint(accM[0][0]), false, false);
                auto t = __builtin_amdgcn_permlane32_swap(s[0], s[0], false, false);
                csV = __uint_as_float(t[0]);
                if (wave == 7) { WAIT_VM(0); pin(flg); const u32 ok = flag_ok(flg); *(LAS u32*)(lds + L_FLG) = ok; }
            }
            const int par = li & 1;
            const bool cur0 = (4 * lq + par) <= li, cur1 = (4 * lq + par + 2) <= li;
            const u32 pm = par ? 0xffff0000u : 0x0000ffffu;
            u32 mC0 = cur0 ? pm : 0u, mP0 = cur0 ? 0u : pm, mC1 = cur1 ? pm : 0u, mP1 = cur1 ? 0u : pm;
            u32 oo = (LAYER ? WS_O2 : WS_O1) + (u32)(4 * grp * 16384 + (4 * mem + hd) * 512 + lane * 8);
            asm volatile("" : "+v"(mC0), "+v"(mP0), "+v"(mC1), "+v"(mP1), "+v"(oo));
            s16x4 Vb[5];
#pragma unroll
            for (int mt = 0; mt < 5; ++mt) { Vb[mt] = pack4((accM[mt] - m4[mt] * csV) * r4[mt]); pin(Vb[mt]); }
            STAMP(SBASE);
            BAR();
            STAMP(SBASE + 1);
            u32x2 pw[4];
#pragma unroll
            for (int j = 0; j < 4; ++j) pw[j] = *(LAS u32x2*)(lds + ex + j * 512);
            if constexpr (LAYER == 0) f1ok = *(LAS u32*)(lds + L_FLG);
            SB();
            f32x4 O[4], L[4];
#pragma unroll
            for (int j = 0; j < 4; ++j) {
                const s16x8 P = __builtin_bit_cast(s16x8, (u32x4){pw[j][0] & mC0, pw[j][1] & mC1, pw[j][0] & mP0, pw[j][1] & mP1});
                L[j] = mfma32(ones8, P, zero4);
                O[j] = mfma32(cat8(Vb[j + 1], Vb[j]), P, zero4);
            }
#pragma unroll
            for (int j = 0; j < 4; ++j) {
                const float inv = __builtin_amdgcn_rcpf(L[j][0]);
                const u32x2 ow = {nosent(pk2(O[j][0] * inv, O[j][1] * inv)), nosent(pk2(O[j][2] * inv, O[j][3] * inv))};
                __builtin_amdgcn_raw_buffer_store_b64(ow, r_ws, oo, j * 16384, 16);
            }
            f1ok = __builtin_amdgcn_readfirstlane(f1ok);
            CFENCE();
        }
        if constexpr (LAYER == 0) { if (f1ok) ald4s(uu, WS_UT + (u32)(b * 8192 + opq(tid) * 16)); }
        SB();
        return f1ok;
    };
    const u32 f1ok = attention(IC<0>{});
    {
        const u32x4 sv = {SENT, SENT, SENT, SENT};
        const int t2 = opq(tid);
        const u32 tb = (u32)((4 * grp + hd) * 16384 + mem * 2048 + R * 1024 + (t2 & 63) * 16);
        __builtin_amdgcn_raw_buffer_store_b128(sv, r_ws, tb, WS_O2, 16);
        __builtin_amdgcn_raw_buffer_store_b128(sv, r_ws, tb, WS_X2, 16);
        __builtin_amdgcn_raw_buffer_store_b128(sv, r_ws, (u32)(b * 8192 + t2 * 16), WS_Y2, 16);
        CFENCE();
        SB();
    }
    STAMP(2);

    const int TI = 4 * grp + hd;
    u32x2 of[16];
    auto load_o = [&](u32 ws_o) {
        const int lane = opq(tid) & 63;
#pragma unroll
        for (int i = 0; i < 16; ++i) of[i] = __builtin_amdgcn_raw_buffer_load_b64(r_ws, ws_o + (u32)(TI * 16384 + R * 8192 + lane * 8) + (i & 7) * 512, (i >> 3) * 4096, 16);
    };
    u32 xw[8], yw[8];
    u32 woa[4];
    s16x8 F[8][4];
    auto wo_addr = [&]() {
        const int l2 = opq(tid) & 63, lq = l2 >> 4, tq = (l2 & 15) >> 2, tp = l2 & 3;
#pragma unroll
        for (int n = 0; n < 4; ++n) woa[n] = L_WO + (u32)(R * 32768 + (4 * lq + tq) * 128 + ((n ^ (((lq & 1) << 1) | (tq >> 1))) << 5) + tp * 8);
    };
    auto load_F = [&](int ks) {
#pragma unroll
        for (int n = 0; n < 4; ++n) F[ks][n] = cat8(trr(woa[n] + ks * 4096), trr(woa[n] + ks * 4096 + 2048));
    };

    {
        const int t2 = opq(tid), lane = t2 & 63, li = lane & 15, lq = lane >> 4;
        constexpr u32 OFFS = 0xEDA85410u;
        {
            WAIT_VM(R ? 7 : 3);
            pin(hd0); pin(hd1);
            const int j = t2 & 255;
            u32 cb = L_HC + R * 34816 + (j + 2) * 16;
            asm volatile("" : "+v"(cb));
            const u32 d[8] = {hd0[0], hd0[1], hd0[2], hd0[3], hd1[0], hd1[1], hd1[2], hd1[3]};
            u32 e[7];
#pragma unroll
            for (int i = 0; i < 7; ++i) e[i] = __builtin_amdgcn_alignbit(d[i + 1], d[i], 16);
            sfor<0, 8>([&](auto pc) {
                constexpr int p = decltype(pc)::value;
                constexpr u32 po = (272 * p + ((OFFS >> (4 * p)) & 15)) * 16;
                constexpr int h = p >> 1;
                const u32x4 w = (p & 1) ? (u32x4){e[h], e[h + 1], e[h + 2], e[h + 3]} : (u32x4){d[h], d[h + 1], d[h + 2], d[h + 3]};
                *(LAS u32x4*)(lds + cb + po) = w;
            });
            if (hd == 0) {
                const u32 h0 = __builtin_amdgcn_readfirstlane(d[0]), h1 = __builtin_amdgcn_readfirstlane(d[1]);
                const u32 h2 = __builtin_amdgcn_readfirstlane(d[2]), h3 = __builtin_amdgcn_readfirstlane(d[3]);
                if (lane < 16) {
                    const int p = lane & 7;
                    const u32 po = (272 * p + ((OFFS >> (4 * p)) & 15)) * 16;
                    const unsigned __int128 hv = ((unsigned __int128)(((u64)h3 << 32) | h2) << 64) | (((u64)h1 << 32) | h0);
                    const unsigned __int128 sv = (p == 0 || lane < 8) ? (unsigned __int128)0 : (hv << (16 * (8 - p)));
                    const u32x4 w = {(u32)sv, (u32)(sv >> 32), (u32)(sv >> 64), (u32)(sv >> 96)};
                    *(LAS u32x4*)(lds + L_HC + R * 34816 + po + (lane >> 3) * 16) = w;
                }
            }
            if (hd == 1 && lane < 30) *(LAS u32x4*)(lds + L_UR + R * 4576 + 4096 + lane * 16) = (u32x4){0, 0, 0, 0};
            STAMP(3);
            if (!f1ok) {
                const u32 fo = WS_F1 + (u32)(((b >> 5) * 32 + 31 - hd * 8 - (lane & 7)) * 128);
                flg = (u32x2){0u, 0u};
                spin_flags(flg, fo);
                ald4s(uu, WS_UT + (u32)(b * 8192 + t2 * 16));
            }
            asm volatile("s_waitcnt vmcnt(0)" : "+v"(uu) :: "memory");
            *(LAS u32x4*)(lds + L_UR + R * 4576 + j * 16) = uu;
        }
        BAR();
        if (R == 0 && tid == 0) __builtin_amdgcn_raw_buffer_store_b64((u32x2){tl, th}, r_ws, WS_F2 + (grp * 8 + mem) * 128, 0, 16);
        STAMP(4);
        const int ch = hd >> 1, e0 = (hd & 1) * 4;
        const int p = (li + 1) & 7;
        u32 aA = L_HC + ch * 34816 + (272 * p + ((OFFS >> (4 * p)) & 15) + ((li + 1) >> 3) + lq + 4 * e0) * 16;
        u32 aB = L_UR + ch * 4576 + (254 - 224 - 2 * li + lq + 4 * e0) * 16;
        asm volatile("" : "+v"(aA), "+v"(aB));
        f32x4 y[4];
        const u32 f2own = WS_F2 + (u32)((grp * 8 + 4 * R + (lane & 3)) * 128);
        u32 ooff = WS_O1 + (u32)(TI * 16384 + R * 8192 + lane * 8);
        bool early = false;
        {
            constexpr ConvSched S(R);
            s16x8 A[4][8], B[4][8];
            auto loads_at = [&](auto pc) {
                constexpr int p = decltype(pc)::value;
                sfor<0, 64>([&](auto ic) {
                    constexpr int i = decltype(ic)::value, e = i >> 4, f = i & 7;
                    if constexpr (S.slot[i] == p) {
                        if constexpr (i & 8) B[e][f] = ldsA(aB + 64 * e + 512 * (7 - f)); else A[e][f] = ldsA(aA + 64 * (8 * f + e));
                    }
                });
            };
            loads_at(IC<-1>{});
            SB();
            sfor<0, 72>([&](auto pc) {
                constexpr int p = decltype(pc)::value, e = p / 18, f = S.f[p % 18], i = S.i[p % 18], g = S.n[i] - f;
                y[i] = mfma32(A[e][f], B[e][g], (e == 0 && f == 0) ? zero4 : y[i]);
                loads_at(pc);
                if constexpr (p < 24 && (p % 3) == 1) issue_wo(IC<(p / 3)>{});
                if constexpr (p == 36) ald2s(flg, f2own);
                if constexpr (p == 54) { WAIT_VM(0); pin(flg); early = flag_ok(flg); }
                if constexpr (p >= 55 && p < 71) {
                    if (early) ald2o<((p - 55) & 7) * 512>(of[p - 55], ooff + ((p - 55) >> 3) * 4096, d_ws);
                }
                SB();
            });
        }
        STAMP(14);
        RBAR();
        {
            constexpr int N0[4] = {0, 3, 4, 7};
            u32 ra = L_HC + hd * 8192 + lane * 16;
            asm volatile("" : "+v"(ra));
#pragma unroll
            for (int i = 0; i < 4; ++i) *(LAS f32x4*)(lds + ra + (N0[i] ^ R) * 1024) = y[i];
        }
        SB();
        if constexpr (R == 0) {
#pragma unroll
            for (int n = 0; n < 4; ++n) { const u32x2 w = __builtin_amdgcn_raw_buffer_load_b64(r_x, (u32)((TI * 16 + li) * 1024 + mem * 128 + lq * 8) + n * 32, 0, 0); xw[2 * n] = w[0]; xw[2 * n + 1] = w[1]; }
        }
        u32 f2oth = 0; u32x2 flg2;
        if constexpr (R == 1) {
            if (wave == 7) {
                const int pb = 32 * mem + (lane & 31);
                f2oth = WS_F2 + (u32)((lane < 32 ? ((pb & 7) * 4 + (pb >> 6)) * 8 + ((pb >> 3) & 7) : max(grp * 8 - 8, 0) + (lane & 15)) * 128);
                CFENCE();
                ald2s(flg2, f2oth);
            }
        }
        wo_addr();
        SB();
        STAMP(37);
        BAR();
        STAMP(38);
        {
            const float D0 = bflo(dw), D1 = bfhi(dw);
            u32 ra = L_HC + wave * 1024 + lane * 16;
            const int tb = 256 * wave + 16 * li + 4 * lq;
            u32 ru = L_UR + (2044 - tb) * 2;
            asm volatile("" : "+v"(ra), "+v"(ru));
            const f32x4 y0 = *(LAS f32x4*)(lds + ra), y1 = *(LAS f32x4*)(lds + ra + 8192), y2 = *(LAS f32x4*)(lds + ra + 16384), y3 = *(LAS f32x4*)(lds + ra + 24576);
            const u32x2 ua = *(LAS u32x2*)(lds + ru), ub = *(LAS u32x2*)(lds + ru + 4576);
            load_F(0); load_F(1);
            SB();
            const f32x4 ya = y0 + y1, yb = y2 + y3;
            const u32x4 w = {nosent(pk2(ya[0] + bfhi(ua[1]) * D0, yb[0] + bfhi(ub[1]) * D1)), nosent(pk2(ya[1] + bflo(ua[1]) * D0, yb[1] + bflo(ub[1]) * D1)),
                             nosent(pk2(ya[2] + bfhi(ua[0]) * D0, yb[2] + bfhi(ub[0]) * D1)), nosent(pk2(ya[3] + bflo(ua[0]) * D0, yb[3] + bflo(ub[0]) * D1))};
            __builtin_amdgcn_raw_buffer_store_b128(w, r_ws, WS_Y2 + (u32)(b * 8192 + tb * 4), 0, 16);
        }
        SB();
        STAMP(5);
        if (!early) {
            spin_flags(flg, f2own);
            sfor<0, 16>([&](auto ic) { constexpr int i = decltype(ic)::value;
                ald2o<(i & 7) * 512>(of[i], ooff + (i >> 3) * 4096, d_ws); });
            WAIT_VM(0);
        }
        WAIT_VM(1);
        sfor<0, 16>([&](auto ic) { pin(of[decltype(ic)::value]); });
        STAMP(40);

        f32x4 acc[4];
#pragma unroll
        for (int ks = 0; ks < 8; ++ks) {
            if (ks + 2 < 8) load_F(ks + 2);
            SB();
#pragma unroll
            for (int n = 0; n < 4; ++n) acc[n] = mfma32(F[ks][n], __builtin_bit_cast(s16x8, (u32x4){of[2 * ks][0], of[2 * ks][1], of[2 * ks + 1][0], of[2 * ks + 1][1]}), ks ? acc[n] : zero4);
            SB();
        }
        STAMP(41);
        accM[0] = acc[0]; accM[1] = acc[1]; accM[2] = acc[2]; accM[3] = acc[3];
        if constexpr (R == 1) { if (wave == 7) { asm volatile("" : "+v"(flg2)); spin_flags(flg2, f2oth); } }
    }

    auto load_y = [&]() {
        const int l2 = opq(tid) & 63;
#pragma unroll
        for (int i = 0; i < 8; ++i)
            yw[i] = __builtin_amdgcn_raw_buffer_load_b32(r_ws, WS_Y2 + (u32)((32 * mem + 2 * (l2 >> 4)) * 8192 + (TI * 16 + (l2 & 15)) * 4), (8 * (i >> 1) + (i & 1)) * 8192, 16);
    };
    auto pub_stat = [&](u32 ws_st, float s, float q) {
        const int lane = opq(tid) & 63;
        s = xsum(s); q = xsum(q);
        const u32x4 sv = {__float_as_uint(s), tl, __float_as_uint(q), th};
        __builtin_amdgcn_raw_buffer_store_b128(sv, r_ws, lane < 16 ? ws_st + (u32)((TI * 8 + mem) * 256 + lane * 16) : 0xFFFFFF00u, 0, 16);
    };
    auto stat_off = [&](u32 ws_st, int ti) { const int l2 = opq(tid) & 63; return ws_st + (u32)(ti * 2048 + (l2 >> 4) * 512 + (l2 & 15) * 16); };
    auto fin_stat = [&](u32x4& a, u32x4& c, u32 off, float& mean, float& rstd) {
        _Pragma("unroll 1") for (int spin = 0; spin < SPIN_CAP; ++spin) {
            if (__all(((a[1] ^ tl) | (a[3] ^ th) | (c[1] ^ tl) | (c[3] ^ th)) == 0)) break;
            __builtin_amdgcn_s_sleep(1);
            asm volatile("buffer_load_dwordx4 %0, %2, %3, 0 offen sc1\n\tbuffer_load_dwordx4 %1, %2, %3, 0 offen offset:256 sc1\n\ts_waitcnt vmcnt(0)"
                         : "=&v"(a), "=&v"(c) : "v"(off), "s"(d_ws) : "memory");
        }
        const float ts = xsum(__uint_as_float(a[0]) + __uint_as_float(c[0])), tq = xsum(__uint_as_float(a[2]) + __uint_as_float(c[2]));
        mean = ts * (1.f / 512.f);
        rstd = __builtin_amdgcn_rsqf(fmaxf(tq * (1.f / 512.f) - mean * mean, 0.f) + 1e-5f);
    };
    auto wo_finish = [&](auto lc) {
        constexpr int LAYER = decltype(lc)::value;
        const int lane = opq(tid) & 63, li = lane & 15, lq = lane >> 4;
        u32 wx = L_WX + hd * 4096 + lane * 16;
        asm volatile("" : "+v"(wx));
        if constexpr (R == 1) {
#pragma unroll
            for (int n = 0; n < 4; ++n) *(LAS f32x4*)(lds + wx + n * 1024) = accM[n];
        }
        BAR();
        STAMP(LAYER ? 25 : 42);
        if constexpr (R == 0) {
            f32x4 t[4];
#pragma unroll
            for (int n = 0; n < 4; ++n) t[n] = *(LAS f32x4*)(lds + wx + n * 1024);
            if constexpr (!LAYER) load_y();
            const u32 so = stat_off(LAYER ? WS_ST3 : WS_ST1, TI);
            SB();
            float s = 0.f, q = 0.f, s2 = 0.f, q2 = 0.f;
#pragma unroll
            for (int n = 0; n < 4; ++n) {
                const f32x4 v = accM[n] + t[n];
                const u32 w0 = pk2(bflo(xw[2 * n]) + v[0], bfhi(xw[2 * n]) + v[1]), w1 = pk2(bflo(xw[2 * n + 1]) + v[2], bfhi(xw[2 * n + 1]) + v[3]);
                xw[2 * n] = w0; xw[2 * n + 1] = w1;
                s = dot2(w0, 0x3F803F80u, s); q = dot2(w0, w0, q); s2 = dot2(w1, 0x3F803F80u, s2); q2 = dot2(w1, w1, q2);
            }
            pub_stat(LAYER ? WS_ST3 : WS_ST1, s + s2, q + q2);
            STAMP(LAYER ? 46 : 43);
            WAIT_VM(0);
            STAMP(LAYER ? 26 : 44);
            u32x4 a = {0, 0, 0, 0}, c = {0, 0, 0, 0};
            float mean, rstd;
            fin_stat(a, c, so, mean, rstd);
            STAMP(LAYER ? 11 : 6);
            if constexpr (!LAYER) {
                _Pragma("unroll 1") for (int spin = 0; spin < SPIN_CAP; ++spin) {
                    if (__all(max(max(max(yw[0], yw[1]), max(yw[2], yw[3])), max(max(yw[4], yw[5]), max(yw[6], yw[7]))) != SENT)) break;
                    __builtin_amdgcn_s_sleep(1);
                    CFENCE();
                    load_y();
                }
            }
            STAMP(47);
            u32 w[8];
            const float nm = -mean * rstd;
#pragma unroll
            for (int i = 0; i < 8; ++i) {
                const float a = bflo(xw[i]), c = bfhi(xw[i]);
                w[i] = pk2(a + (a * rstd + nm) * bflo(yw[i]), c + (c * rstd + nm) * bfhi(yw[i]));
                if (!LAYER) w[i] = nosent(w[i]);
            }
#pragma unroll
            for (int j = 0; j < 2; ++j) {
                auto r0 = __builtin_amdgcn_permlane16_swap(w[4 * j], w[4 * j + 2], false, false);
                auto r1 = __builtin_amdgcn_permlane16_swap(w[4 * j + 1], w[4 * j + 3], false, false);
                const u32x4 o = {r0[0], r1[0], r0[1], r1[1]};
                const int c = 4 * j + 2 * (lq & 1) + (lq >> 1);
                if constexpr (LAYER) __builtin_amdgcn_raw_buffer_store_b128(o, r_out, (u32)((TI * 16 + li) * 1024 + mem * 128 + c * 16), 0, OUT_AUX);
                else __builtin_amdgcn_raw_buffer_store_b128(o, r_ws, WS_X2 + (u32)((TI * 8 + mem) * 2048 + li * 128 + ((c ^ (li >> 1)) << 4)), 0, 16);
            }
            if constexpr (!LAYER) {
                float s = 0.f, q = 0.f, s2 = 0.f, q2 = 0.f;
#pragma unroll
                for (int i = 0; i < 8; i += 2) {
                    xw[i] = w[i]; xw[i + 1] = w[i + 1];
                    s = dot2(w[i], 0x3F803F80u, s); q = dot2(w[i], w[i], q); s2 = dot2(w[i + 1], 0x3F803F80u, s2); q2 = dot2(w[i + 1], w[i + 1], q2);
                    asm volatile("" : "+v"(xw[i]), "+v"(xw[i + 1]), "+v"(yw[i]), "+v"(yw[i + 1]));
                }
                pub_stat(WS_ST2, s + s2, q + q2);
                STAMP(48);
                WAIT_VM(0);
            }
        }
    };
    wo_finish(IC<0>{});
    RBAR();
    STAMP(7);

    {
        const int l3 = opq(tid) & 63;
        const u32 g0 = (u32)((4 * grp - 1 + (wave >> 1)) * 16384 + (wave & 1) * 1024 + l3 * 16);
        const u32 g2 = (u32)((4 * grp + 3) * 16384 + wave * 256 + l3 * 16);
        auto x2_piece = [&](auto ic) {
            constexpr int i = decltype(ic)::value, s = i >> 1;
            const u32 kp = (u32)((s + rot) & 7);
            if constexpr (!(i & 1)) dma(IC<4>{}, IC<L_CX + s * 10240>{}, d_x2, wb1k, g0, kp * 2048);
            else dma(IC<5>{}, IC<L_CX + s * 10240 + 8192>{}, d_x2, wb256, g2, kp * 2048);
        };
        {
            const u32 po = (l3 & 8) ? (u32)((4 * grp + 3) * 16384 + wave * 256 + 252 + (l3 & 7) * 2048) : (u32)((4 * grp - 1 + (wave >> 1)) * 16384 + (wave & 1) * 1024 + 1020 + (l3 & 7) * 2048);
            _Pragma("unroll 1") for (int spin = 0; spin < SPIN_CAP; ++spin) {
                u32 pv;
                asm volatile("buffer_load_dword %0, %1, %2, 0 offen sc1\n\ts_waitcnt vmcnt(0)" : "=&v"(pv) : "v"(po), "s"(d_x2) : "memory");
                if (__all(pv != SENT)) break;
                __builtin_amdgcn_s_sleep(1);
            }
        }
        STAMP(39);
        u32 va = L_CX + wave * 1024 + l3 * 16, vq = L_CX + 8192 + wave * 256 + (l3 & 15) * 16;
        asm volatile("" : "+v"(va), "+v"(vq));
        u32x4 vr[4];
        auto ver_read = [&](auto cc) {
            constexpr int c = decltype(cc)::value;
            u32 a = va + c * 20480, q = vq + c * 20480;
            asm volatile("" : "+v"(a), "+v"(q) :: "memory");
            vr[0] = *(LAS u32x4*)(lds + a); vr[1] = *(LAS u32x4*)(lds + a + 10240); vr[2] = *(LAS u32x4*)(lds + q); vr[3] = *(LAS u32x4*)(lds + q + 10240);
        };
        auto ver_check = [&](auto cc) {
            constexpr int c = decltype(cc)::value;
            _Pragma("unroll 1") for (int spin = 0; spin < SPIN_CAP; ++spin) {
                const u32 m0 = max(max(max(vr[0][0], vr[0][1]), max(vr[0][2], vr[0][3])), max(max(vr[1][0], vr[1][1]), max(vr[1][2], vr[1][3])));
                const u32 m1 = max(max(max(vr[2][0], vr[2][1]), max(vr[2][2], vr[2][3])), max(max(vr[3][0], vr[3][1]), max(vr[3][2], vr[3][3])));
                if (__all(max(m0, m1) != SENT)) break;
                __builtin_amdgcn_s_sleep(1);
                sfor<4 * c, 4 * c + 4>([&](auto ic) { x2_piece(ic); });
                WAIT_VM(0);
                ver_read(cc);
            }
        };
        sfor<0, 8>([&](auto ic) { x2_piece(ic); });
        const int sti = max(4 * grp - 1 + min(wave, 4), 0);
        const u32 so = stat_off(WS_ST2, sti);
        const u32 xb0 = (u32)((l3 & 15) * 128 + (((l3 >> 4) ^ ((l3 & 15) >> 1)) << 4));
        WAIT_VM(4);
        STAMP(8);
        ver_read(IC<0>{});
        ver_check(IC<0>{});
        RBAR();
        STAMP(13);
        u32 xa;
        u32x4 sa, sc;
        auto pf_addr = [&](int st) { xa = L_CX + (st >> 1) * 10240 + (xb0 ^ ((st & 1) * 64)); asm volatile("" : "+v"(xa)); };
        pf_addr(0);
#pragma unroll
        for (int mt = 0; mt < 5; ++mt) Ab[0][mt] = ldsA(xa + mt * 2048);
        ald4s(sa, so); ald4s(sc, so + 256);
        sfor<0, 16>([&](auto stc) {
            constexpr int st = decltype(stc)::value;
            if constexpr (st == 2) { WAIT_VM(10); ver_read(IC<1>{}); }
            if constexpr (st == 3) { ver_check(IC<1>{}); RBAR(); STAMP(49); }
            if constexpr (st == 5) { WAIT_VM(4); ver_read(IC<2>{}); }
            if constexpr (st == 6) {
                ver_check(IC<2>{}); WAIT_VM(0); ver_read(IC<3>{});
                if (R == 0 || wave == 4) {
                    asm volatile("" : "+v"(sa), "+v"(sc));
                    float mean = 0.f, rstd = 1.f;
                    if (4 * grp - 1 + wave >= 0) fin_stat(sa, sc, so, mean, rstd);
                    if (l3 < 16) { *(LAS float*)(lds + L_TAB + wave * 64 + l3 * 4) = mean; *(LAS float*)(lds + L_TAB + 320 + wave * 64 + l3 * 4) = rstd; }
                }
            }
            if constexpr (st == 7) { ver_check(IC<3>{}); RBAR(); STAMP(50); CSTAMP(60); }
            if (st < 15) pf_addr(st + 1);
            if constexpr (st == 14) load_tab(true);
            SB();
            qkv_step(IC<1>{}, st, Ab[st & 1], [&](auto mc) {
                constexpr int mt = decltype(mc)::value;
                if (st < 15) Ab[(st + 1) & 1][mt] = ldsA(xa + mt * 2048);
                if constexpr (st < 2 && mt < 4) x2_piece(IC<8 + 4 * st + mt>{});
                SB();
            });
        });
    }
    STAMP(R ? 9 : 52);
    CSTAMP(61);
    attention(IC<1>{});
    STAMP(27);

    {
        wo_addr();
#pragma unroll
        for (int ks = 0; ks < 8; ++ks) load_F(ks);
        SB();
        WAIT_VM(0);
        RBAR();
        STAMP(10);
        load_o(WS_O2);
        SB();
        _Pragma("unroll 1") for (int spin = 0; spin < 64; ++spin) {
            u32 m0 = 0, m1 = 0;
#pragma unroll
            for (int i = 0; i < 16; i += 2) { m0 = max(max(m0, of[i][0]), of[i][1]); m1 = max(max(m1, of[i + 1][0]), of[i + 1][1]); }
            if (__all(max(m0, m1) != SENT)) break;
            _Pragma("unroll 1") for (int sp2 = 0; sp2 < SPIN_CAP; ++sp2) {
                CFENCE();
                __builtin_amdgcn_s_sleep(1);
                const u32 pv = __builtin_amdgcn_raw_buffer_load_b32(r_ws, WS_O2 + (u32)(TI * 16384 + R * 8192 + (opq(tid) & 15) * 512 + 508), 0, 16);
                if (__all(pv != SENT)) break;
            }
            CFENCE();
            load_o(WS_O2);
        }
        STAMP(24);
#pragma unroll
        for (int ks = 0; ks < 8; ++ks)
#pragma unroll
            for (int n = 0; n < 4; ++n) accM[n] = mfma32(F[ks][n], __builtin_bit_cast(s16x8, (u32x4){of[2 * ks][0], of[2 * ks][1], of[2 * ks + 1][0], of[2 * ks + 1][1]}), ks ? accM[n] : zero4);
        SB();
        STAMP(45);
    }
    wo_finish(IC<1>{});
    if constexpr (R == 1) {
        if (b == 0 && wave == 7) {
            const int l3 = opq(tid) & 63;
#pragma unroll
            for (int i = 0; i < 4; ++i) { flg = (u32x2){0u, 0u}; spin_flags(flg, WS_F1 + (u32)((i * 64 + l3) * 128)); }
            if (l3 == 0) __builtin_amdgcn_raw_buffer_store_b64(__builtin_bit_cast(u32x2, epoch + 1), r_ws, WS_EPOCH, 0, 16);
        }
    }
}

__global__ void __launch_bounds__(512, 2)
fused_kernel(const u16* __restrict__ gx, const u16* __restrict__ gwqkv, const u16* __restrict__ gwo,
             const u16* __restrict__ gbk, const u16* __restrict__ gbv, const u16* __restrict__ gsk,
             const u16* __restrict__ ghk, const u16* __restrict__ gdv, u16* __restrict__ gout, char* __restrict__ ws) {
    __shared__ __attribute__((aligned(16))) char lds_[L_TOTAL];
    const Args a = {gx, gwqkv, gwo, gbk, gbv, gsk, ghk, gdv, gout, ws};
    const int wave = __builtin_amdgcn_readfirstlane(threadIdx.x >> 6);
    if (wave < 4) body<0>(a, (LAS char*)lds_, wave); else body<1>(a, (LAS char*)lds_, wave);
}

extern "C" void kernel_launch(void* const* d_in, const int* in_sizes, int n_in,
                              void* d_out, int out_size, void* d_ws, size_t ws_size,
                              hipStream_t stream) {
    fused_kernel<<<256, 512, 0, stream>>>((const u16*)d_in[0], (const u16*)d_in[1], (const u16*)d_in[2],
                                          (const u16*)d_in[4], (const u16*)d_in[5], (const u16*)d_in[6],
                                          (const u16*)d_in[7], (const u16*)d_in[8], (u16*)d_out, (char*)d_ws);
}
```

```cpp
#define DIAG 16
#include <hip/hip_runtime.h>
#include <stdint.h>

typedef unsigned short u16;
typedef unsigned u32;
typedef unsigned long long u64;
typedef short s16x4 __attribute__((ext_vector_type(4)));
typedef short s16x8 __attribute__((ext_vector_type(8)));
typedef __bf16 bf16x2 __attribute__((ext_vector_type(2)));
typedef float f32x2 __attribute__((ext_vector_type(2)));
typedef float f32x4 __attribute__((ext_vector_type(4)));
typedef u32 u32x2 __attribute__((ext_vector_type(2)));
typedef u32 u32x4 __attribute__((ext_vector_type(4)));
#define LAS __attribute__((address_space(3)))
#define DEV __device__ __forceinline__
typedef __amdgpu_buffer_rsrc_t rsrc_t;

constexpr u32 WS_EPOCH = 0;
constexpr u32 WS_F1 = 32768;
constexpr u32 WS_F2 = 65536;
constexpr u32 WS_ST0 = 98304;
constexpr u32 WS_ST1 = 131072;
constexpr u32 WS_ST2 = WS_ST1 + 262144;
constexpr u32 WS_ST3 = WS_ST2 + 262144;
constexpr u32 WS_UT = 1u << 20;
constexpr u32 WS_O1 = WS_UT + (2u << 20);
constexpr u32 WS_O2 = WS_O1 + (2u << 20);
constexpr u32 WS_Y2 = WS_O2 + (2u << 20);
constexpr u32 WS_X2 = WS_Y2 + (2u << 20);
constexpr u32 WS_END = WS_X2 + (2u << 20);

constexpr u32 L_SLOT = 34816;
constexpr u32 L_KV = 4 * L_SLOT;
constexpr u32 L_K = L_KV, L_V = L_KV + 9216;
constexpr u32 L_SK = L_KV + 17408;
constexpr u32 L_WO = 0;
constexpr u32 L_CX = 65536;
constexpr u32 L_HC = L_CX;
constexpr u32 L_UR = L_CX + 69632;
constexpr u32 L_WX = L_CX + 40960;
constexpr u32 L_EX = 147456;
constexpr u32 L_TAB = L_EX + 12288;
constexpr u32 L_CS = L_TAB + 1024;
constexpr u32 L_FLG = L_TAB + 2048;
constexpr u32 L_DUMMY = L_FLG + 1024;
constexpr u32 L_TOTAL = 160 * 1024;
static_assert(L_DUMMY + 1024 <= L_TOTAL, "LDS map");

#define WO_POS 0
#define OUT_AUX 0
constexpr int SPIN_CAP = 1 << 14;
constexpr u32 SENT = 0xFFFFFFFFu;

#define WAIT_VM(n) asm volatile("s_waitcnt vmcnt(%0)" ::"i"(n) : "memory")
#define BAR() do { asm volatile("s_waitcnt lgkmcnt(0)" ::: "memory"); __builtin_amdgcn_s_barrier(); asm volatile("" ::: "memory"); } while (0)
#define RBAR() do { asm volatile("" ::: "memory"); __builtin_amdgcn_s_barrier(); asm volatile("" ::: "memory"); } while (0)
#define CFENCE() asm volatile("" ::: "memory")
#define SB() __builtin_amdgcn_sched_barrier(0)
template <int N> struct IC { static constexpr int value = N; };
template <int I, int N, class F> DEV void sfor(F&& f) { if constexpr (I < N) { f(IC<I>{}); sfor<I + 1, N>(f); } }

DEV f32x4 mfma32(s16x8 a, s16x8 b, f32x4 c) { return __builtin_amdgcn_mfma_f32_16x16x32_bf16(a, b, c, 0, 0, 0); }
DEV f32x4 mfma16(s16x4 a, s16x4 b, f32x4 c) { return __builtin_amdgcn_mfma_f32_16x16x16bf16_1k(a, b, c, 0, 0, 0); }
DEV float vmax(float a, float b) { float r; asm("v_max_f32 %0, %1, %2" : "=v"(r) : "v"(a), "v"(b)); return r; }
DEV u32 pk2(float lo, float hi) { f32x2 f = {lo, hi}; bf16x2 b = __builtin_convertvector(f, bf16x2); return __builtin_bit_cast(u32, b); }
DEV float bflo(u32 w) { return __uint_as_float(w << 16); }
DEV float bfhi(u32 w) { return __uint_as_float(w & 0xffff0000u); }
DEV float dot2(u32 a, u32 b, float c) { return __builtin_amdgcn_fdot2_f32_bf16(__builtin_bit_cast(bf16x2, a), __builtin_bit_cast(bf16x2, b), c, false); }
DEV u32 nosent(u32 w) { return min(w, 0xFFFFFFFEu); }
DEV float xsum(float v) {
    auto r = __builtin_amdgcn_permlane32_swap(__float_as_uint(v), __float_as_uint(v), false, false);
    v = __uint_as_float(r[0]) + __uint_as_float(r[1]);
    auto s = __builtin_amdgcn_permlane16_swap(__float_as_uint(v), __float_as_uint(v), false, false);
    return __uint_as_float(s[0]) + __uint_as_float(s[1]);
}
DEV float xmax(float v) {
    auto r = __builtin_amdgcn_permlane32_swap(__float_as_uint(v), __float_as_uint(v), false, false);
    v = vmax(__uint_as_float(r[0]), __uint_as_float(r[1]));
    auto s = __builtin_amdgcn_permlane16_swap(__float_as_uint(v), __float_as_uint(v), false, false);
    return vmax(__uint_as_float(s[0]), __uint_as_float(s[1]));
}
DEV float rowsum16(float v) {
    v += __builtin_bit_cast(float, __builtin_amdgcn_update_dpp(0, __builtin_bit_cast(int, v), 0xB1, 0xf, 0xf, true));
    v += __builtin_bit_cast(float, __builtin_amdgcn_update_dpp(0, __builtin_bit_cast(int, v), 0x4E, 0xf, 0xf, true));
    v += __builtin_bit_cast(float, __builtin_amdgcn_update_dpp(0, __builtin_bit_cast(int, v), 0x141, 0xf, 0xf, true));
    v += __builtin_bit_cast(float, __builtin_amdgcn_update_dpp(0, __builtin_bit_cast(int, v), 0x140, 0xf, 0xf, true));
    return v;
}
DEV float bcast_row0(float v) { return __builtin_bit_cast(float, __builtin_amdgcn_update_dpp(0, __builtin_bit_cast(int, v), 0x150, 0xf, 0xf, true)); }
template <int OFF> DEV void ald2o(u32x2& d, u32 voff, u32x4 rs) { asm volatile("buffer_load_dwordx2 %0, %1, %2, 0 offen offset:%3 sc1" : "=v"(d) : "v"(voff), "s"(rs), "i"(OFF) : "memory"); }
template <class T> DEV void pin(T& v) { asm volatile("" : "+v"(v)); }
DEV int opq(int v) { asm volatile("" : "+v"(v)); return v; }
DEV s16x8 cat8(s16x4 a, s16x4 b) { return __builtin_shufflevector(a, b, 0, 1, 2, 3, 4, 5, 6, 7); }
DEV s16x4 pack4(f32x4 v) { u32x2 w = {pk2(v[0], v[1]), pk2(v[2], v[3])}; return __builtin_bit_cast(s16x4, w); }
DEV u64 mix64(u64 z) { z += 0x9E3779B97F4A7C15ull; z = (z ^ (z >> 30)) * 0xBF58476D1CE4E5B9ull; z = (z ^ (z >> 27)) * 0x94D049BB133111EBull; return z ^ (z >> 31); }

struct ConvSched {
    int n[4], f[18], i[18], slot[64];
    constexpr ConvSched(int R) : n{}, f{}, i{}, slot{} {
        constexpr int LEAD = 9;
        const int N0[4] = {0, 3, 4, 7};
        for (int k = 0; k < 4; ++k) n[k] = N0[k] ^ R;
        int c = 0;
        for (int ff = 0; ff < 8; ++ff) for (int k = 3; k >= 0; --k) if (ff <= n[k]) { f[c] = ff; i[c] = k; ++c; }
        for (int k = 0; k < 64; ++k) slot[k] = 1000;
        for (int p = 71; p >= 0; --p) {
            const int e = p / 18, ff = f[p % 18], g = n[i[p % 18]] - ff, s = p - LEAD < -1 ? -1 : p - LEAD;
            slot[e * 16 + ff] = s; slot[e * 16 + 8 + g] = s;
        }
    }
};

#define STAMP(i) do { if constexpr ((i) == 0 || (i) == DIAG) { u64 t_; asm volatile("s_memrealtime %0\n\ts_waitcnt lgkmcnt(0)" : "=s"(t_)::"memory"); if ((i) == 0) dg0 = (u32)t_; else dg1 = (u32)t_; } } while (0)
#define CSTAMP(i) do { } while (0)

struct Args { const u16 *gx, *gwqkv, *gwo, *gbk, *gbv, *gsk, *ghk, *gdv; u16* gout; char* ws; };

template <int R>
DEV void body(const Args& A_, LAS char* const lds, const int wave) {
    const u16* gx = A_.gx; char* ws = A_.ws;
    u32 dg0 = 0, dg1 = 0;
    STAMP(0);
    const int b = blockIdx.x, tid = threadIdx.x, lane = tid & 63;
    const int hd = wave & 3;
    const int grp = (b & 7) * 4 + (b >> 6), mem = (b >> 3) & 7;
    const int T0 = grp * 64;
    const int rot = (mem + 2 * (grp & 3)) & 7;

    u64 epoch;
    asm volatile("s_load_dwordx2 %0, %1, 0x0" : "=s"(epoch) : "s"(ws));
    const u32 dw = *(const u32*)(A_.gdv + 2 * b);
    const rsrc_t r_x = __builtin_amdgcn_make_buffer_rsrc((void*)gx, 0, 2048 * 512 * 2, 0x00020000);
    const rsrc_t r_ws = __builtin_amdgcn_make_buffer_rsrc((void*)ws, 0, WS_END, 0x00020000);
    const rsrc_t r_out = __builtin_amdgcn_make_buffer_rsrc((void*)A_.gout, 0, 2048 * 512 * 2, 0x00020000);
    const u32 ldsbase = (u32)(size_t)lds;
    auto mkdesc = [](const void* p, u32 bytes) { const u64 a = (u64)p; return (u32x4){(u32)a, (u32)(a >> 32) & 0xffffu, bytes, 0x00020000u}; };
    const u32x4 d_x = mkdesc(gx, 2048 * 512 * 2), d_w = mkdesc(A_.gwqkv, 512 * 1536 * 2), d_wo = mkdesc(A_.gwo, 512 * 512 * 2);
    const u32x4 d_k = mkdesc(A_.gbk, 2048 * 512 * 2), d_v = mkdesc(A_.gbv, 2048 * 512 * 2), d_sk = mkdesc(A_.gsk, 512 * 4 * 2);
    const u32x4 d_h = mkdesc(A_.ghk, 512 * 2048 * 2), d_x2 = mkdesc(ws + WS_X2, 2u << 20), d_ws = mkdesc(ws, WS_END);
    const u32 wb1k = ldsbase + wave * 1024, wb256 = ldsbase + wave * 256;
#define DMA_ASM(pre, mods, post) asm volatile("s_add_u32 m0, %0, %4\n\t" pre "buffer_load_dwordx4 %1, %2, %3 offen " mods "lds" post \
                     :: "s"(sbase), "v"(voff), "s"(rs), "s"(soff), "i"(IMM) : "memory", "scc")
    const u32 xmlo = wave == 0 ? ~0xFFu : ~0u;
    auto dma = [&](auto kind, auto imm, u32x4 rs, u32 sbase, u32 voff, u32 soff) {
        constexpr int K = decltype(kind)::value; constexpr int IMM = decltype(imm)::value;
        if constexpr (K == 0) DMA_ASM("s_nop 0\n\t", "", "");
        else if constexpr (K == 1) DMA_ASM("s_mov_b64 exec, 0xffff\n\t", "", "\n\ts_mov_b64 exec, -1");
        else if constexpr (K == 2) {
            const u32 ml = xmlo;
            asm volatile("s_add_u32 m0, %0, %4\n\ts_mov_b32 exec_lo, %5\n\tbuffer_load_dwordx4 %1, %2, %3 offen lds\n\ts_mov_b32 exec_lo, -1"
                         :: "s"(sbase), "v"(voff), "s"(rs), "s"(soff), "i"(IMM), "s"(ml) : "memory", "scc");
        }
        else if constexpr (K == 4) DMA_ASM("s_nop 0\n\t", "sc1 ", "");
        else DMA_ASM("s_mov_b64 exec, 0xffff\n\t", "sc1 ", "\n\ts_mov_b64 exec, -1");
    };
    auto ald4 = [&](u32x4& d, u32x4 rs, u32 voff) { asm volatile("buffer_load_dwordx4 %0, %1, %2, 0 offen" : "=v"(d) : "v"(voff), "s"(rs) : "memory"); };
    auto ald4s = [&](u32x4& d, u32 voff) { asm volatile("buffer_load_dwordx4 %0, %1, %2, 0 offen sc1" : "=v"(d) : "v"(voff), "s"(d_ws) : "memory"); };
    auto ald2s = [&](u32x2& d, u32 voff) { asm volatile("buffer_load_dwordx2 %0, %1, %2, 0 offen sc1" : "=v"(d) : "v"(voff), "s"(d_ws) : "memory"); };

    const int pr = lane >> 3, pc = lane & 7;
    const int xr0 = 8 * wave + pr, xr2 = 64 + 2 * wave + pr;
    const u32 xv0 = (u32)((T0 - 16 + xr0) * 1024 + ((pc ^ ((xr0 >> 1) & 7)) << 4));
    const u32 xv2 = (u32)((T0 - 16 + xr2) * 1024 + ((pc ^ ((xr2 >> 1) & 7)) << 4));
    const int wfw = ((xr0 >> 1) & 1) | (((xr0 >> 3) & 1) << 1);
    const u32 wv0 = (u32)(xr0 * 3072 + mem * 128 + ((((pc >> 1) ^ wfw) * 2 + (pc & 1)) << 4));
    auto issue_piece = [&](auto sc, auto ic) {
        constexpr int s = decltype(sc)::value, i = decltype(ic)::value;
        const u32 kp = (u32)((s + rot) & 7);
        constexpr int sb = (s & 3) * L_SLOT;
        if constexpr (i == 0) dma(IC<R ? 0 : 2>{}, IC<sb>{}, d_x, wb1k, xv0, kp * 128);
        else if constexpr (i == 1) dma(IC<1>{}, IC<sb + 8192>{}, d_x, wb256, xv2, kp * 128);
        else dma(IC<0>{}, IC<sb + 10240 + (i - 2) * 8192>{}, d_w, wb1k, wv0, kp * (64 * 3072) + (i - 2) * 1024);
    };
    auto issue_stage = [&](auto sc) { issue_piece(sc, IC<0>{}); issue_piece(sc, IC<1>{}); issue_piece(sc, IC<2>{}); issue_piece(sc, IC<3>{}); issue_piece(sc, IC<4>{}); };
    auto issue_wo = [&](auto ic) {
        constexpr int i = decltype(ic)::value;
        const int l2 = opq(tid) & 63, pr = l2 >> 3, pc = l2 & 7;
        const u32 wov = (u32)((8 * wave + pr) * 1024 + mem * 128 + ((((pc >> 1) ^ ((pr >> 1) & 3)) * 2 + (pc & 1)) << 4));
        const u32 j = (u32)((i + 2 * grp) & 7);
        dma(IC<0>{}, IC<L_WO>{}, d_wo, wb1k + j * 8192, wov, j * 65536);
    };
    auto issue_kv = [&](int i) {
        const int l2 = opq(tid) & 63, pr = l2 >> 3, pc = l2 & 7;
        const u32 cb = (u32)(mem * 128);
        if (i == 0) dma(IC<0>{}, IC<L_K>{}, d_k, wb1k, (u32)((T0 - 8 + 8 * wave + pr) * 1024) + cb + ((pc ^ wave) << 4), 0);
        else if (i == 1) dma(IC<0>{}, IC<L_V>{}, d_v, wb1k, (u32)((T0 + 8 * wave + pr) * 1024) + cb + ((pc ^ wave) << 4), 0);
        else if (R == 0) dma(IC<0>{}, IC<L_K + 8192>{}, d_k, ldsbase, (u32)((T0 - 8 + 64 + pr) * 1024) + cb + (pc << 4), 0);
        else dma(IC<0>{}, IC<L_SK>{}, d_sk, ldsbase, (u32)(mem * 512 + l2 * 16), 0);
    };

    const int srow = T0 + 8 * mem + wave;
    u32x4 xrow;
    ald4(xrow, d_x, (u32)(srow * 1024 + lane * 16));
    sfor<2, 5>([&](auto ic) { issue_piece(IC<0>{}, ic); });
    issue_kv(0); issue_kv(1); issue_kv(2);
    asm volatile("s_waitcnt lgkmcnt(0)" : "+s"(epoch));
    const u64 tag = mix64(epoch);
    const u32 tl = (u32)tag, th = (u32)(tag >> 32);
    if (R == 1 && wave == 7 && lane < 32) *(LAS u32x4*)(lds + (lane >> 3) * L_SLOT + (lane & 7) * 16) = (u32x4){0x3F803F80u, 0x3F803F80u, 0x3F803F80u, 0x3F803F80u};
    {
        WAIT_VM(6);
        pin(xrow);
        float s = 0.f, q = 0.f;
#pragma unroll
        for (int i = 0; i < 4; ++i) { s = dot2(xrow[i], 0x3F803F80u, s); q = dot2(xrow[i], xrow[i], q); }
        s = rowsum16(s); q = rowsum16(q);
        s = xsum(s); q = xsum(q);
        const float m = s * (1.f / 512.f);
        const float r = __builtin_amdgcn_rsqf(fmaxf(q * (1.f / 512.f) - m * m, 0.f) + 1e-5f);
        const u32x4 sv = {__float_as_uint(m), tl, __float_as_uint(r), th};
        __builtin_amdgcn_raw_buffer_store_b128(sv, r_ws, lane == 0 ? WS_ST0 + (u32)(srow * 16) : 0xFFFFFF00u, 0, 16);
        CFENCE();
    }
    issue_piece(IC<0>{}, IC<0>{}); issue_piece(IC<0>{}, IC<1>{});
    sfor<2, 5>([&](auto ic) { issue_piece(IC<1>{}, ic); });
    issue_piece(IC<1>{}, IC<0>{}); issue_piece(IC<1>{}, IC<1>{});

    auto trr = [&](u32 off) { return __builtin_amdgcn_ds_read_tr16_b64_v4i16((LAS s16x4*)(lds + off)); };
    auto ldsA = [&](u32 off) { return *(LAS s16x8*)(lds + off); };
    auto flag_ok = [&](const u32x2& f) { return __all(f[0] == tl && f[1] == th); };
    auto spin_flags = [&](u32x2& f, u32 off) {
        _Pragma("unroll 1") for (int spin = 0; spin < SPIN_CAP; ++spin) {
            if (flag_ok(f)) break;
            __builtin_amdgcn_s_sleep(2);
            asm volatile("buffer_load_dwordx2 %0, %1, %2, 0 offen sc1\n\ts_waitcnt vmcnt(0)" : "=&v"(f) : "v"(off), "s"(d_ws) : "memory");
        }
    };

    s16x8 Wq[16], Wm[16];
    f32x4 accM[5], accQ[5];
    const f32x4 zero4 = {0.f, 0.f, 0.f, 0.f};
    const s16x8 ones8 = {0x3F80, 0x3F80, 0x3F80, 0x3F80, 0x3F80, 0x3F80, 0x3F80, 0x3F80};
    s16x8 Ab[3][5];
    auto qkv_step = [&](auto lc, int st, const s16x8 (&A)[5], auto fill) {
        sfor<0, 5>([&](auto mc) {
            constexpr int mt = decltype(mc)::value;
            const f32x4 cm = st ? accM[mt] : zero4;
            accM[mt] = R ? mfma32(A[mt], Wm[st], cm) : mfma32(Wm[st], A[mt], cm);
            if constexpr (R == 0 && (mt > 0 || decltype(lc)::value == 0)) accQ[mt] = mfma32(Wq[st], A[mt], st ? accQ[mt] : zero4);
            fill(mc);
        });
    };

    float m1[5], r1[5]; f32x4 m4[5], r4[5], csK, csQ;
    auto load_tab = [&](bool cs) {
        const int l2 = opq(tid) & 63;
        u32 ta = L_TAB + (R ? (l2 >> 4) * 16 : (l2 & 15) * 4);
        pin(ta);
        if (R == 0 && cs) { csK = *(LAS f32x4*)(lds + L_CS + hd * 128 + (l2 >> 4) * 16); csQ = *(LAS f32x4*)(lds + L_CS + hd * 128 + 64 + (l2 >> 4) * 16); }
#pragma unroll
        for (int mt = 0; mt < 5; ++mt) {
            if (R == 0) { m1[mt] = *(LAS float*)(lds + ta + mt * 64); r1[mt] = *(LAS float*)(lds + ta + 320 + mt * 64); }
            else { m4[mt] = *(LAS f32x4*)(lds + ta + mt * 64); r4[mt] = *(LAS f32x4*)(lds + ta + 320 + mt * 64); }
        }
    };

    u32x4 hd0, hd1;
    u32x2 flg;
    {
        const int li = lane & 15, lq = lane >> 4, tq = li >> 2, tp = lane & 3;
        const u32 xa0 = (u32)(li * 128 + ((lq ^ (li >> 1)) << 4));
        const u32 wa = (u32)((8 * lq + tq) * 128 + ((hd ^ (((tq >> 1) & 1) | ((lq & 1) << 1))) << 5) + tp * 8);
        u32 xa, wb;
        u32x4 s0v;
        auto pf_addr = [&](int st) {
            const u32 sb = (u32)((st >> 1) & 3) * L_SLOT;
            xa = sb + (xa0 ^ ((st & 1) * 64)); wb = sb + 10240 + (st & 1) * 4096 + wa;
            asm volatile("" : "+v"(xa), "+v"(wb));
        };
        auto pf_part = [&](int st, int mt) {
            if (mt == 0) Wm[st] = cat8(trr(wb + (1 + R) * 8192), trr(wb + (1 + R) * 8192 + 512));
            Ab[st & 1][mt] = ldsA(xa + mt * 2048);
            if (R == 0 && mt == 1) Wq[st] = cat8(trr(wb), trr(wb + 512));
        };
        STAMP(15);
        WAIT_VM(5);
        BAR();
        STAMP(16);
        pf_addr(0);
#pragma unroll
        for (int mt = 0; mt < 5; ++mt) pf_part(0, mt);
        sfor<0, 16>([&](auto stc) {
            constexpr int st = decltype(stc)::value;
            if constexpr ((st & 1) && st < 15) {
                constexpr int s1 = (st >> 1) + 1;
                constexpr int WN[8] = {0, 6, 6, 5, 5, 5, 6, 4};
                WAIT_VM(WN[s1]);
                if constexpr (s1 == 7) {
                    asm volatile("" : "+v"(s0v));
                    if (R == 0 || wave == 4) {
                        const int l2 = opq(tid) & 63;
                        if (T0 - 16 + wave * 16 >= 0) {
                            _Pragma("unroll 1") for (int spin = 0; spin < SPIN_CAP; ++spin) {
                                if (__all(s0v[1] == tl && s0v[3] == th)) break;
                                __builtin_amdgcn_s_sleep(2);
                                asm volatile("buffer_load_dwordx4 %0, %1, %2, 0 offen sc1\n\ts_waitcnt vmcnt(0)" : "=&v"(s0v) : "v"(WS_ST0 + (u32)((T0 - 16 + wave * 16 + (l2 & 15)) * 16)), "s"(d_ws) : "memory");
                            }
                        } else { s0v[0] = 0u; s0v[2] = 0x3F800000u; }
                        if (l2 < 16) { *(LAS u32*)(lds + L_TAB + wave * 64 + l2 * 4) = s0v[0]; *(LAS u32*)(lds + L_TAB + 320 + wave * 64 + l2 * 4) = s0v[2]; }
                        asm volatile("s_waitcnt lgkmcnt(0)" ::: "memory");
                    }
                }
                RBAR();
                STAMP(16 + s1);
                if constexpr (s1 == 3) {
                    if (R == 0 && tid == 0) __builtin_amdgcn_raw_buffer_store_b64((u32x2){tl, th}, r_ws, WS_F1 + (mem * 32 + grp) * 128, 0, 16);
                    CFENCE();
                }
                if constexpr (s1 == 5) {
                    const int row = T0 - 16 + wave * 16 + (opq(tid) & 15);
                    ald4s(s0v, WS_ST0 + (u32)(max(row, 0) * 16));
                }
            }
            if (st < 15) pf_addr(st + 1);
            if constexpr (st == 14) load_tab(false);
            SB();
            qkv_step(IC<0>{}, st, Ab[st & 1], [&](auto mc) {
                constexpr int mt = decltype(mc)::value;
                if (st < 15) pf_part(st + 1, mt);
                if constexpr (st == 0) issue_piece(IC<2>{}, mc);
                if constexpr (st >= 1 && st <= 10) {
                    constexpr int sn = ((st - 1) >> 1) + 3;
                    if constexpr ((st - 1) & 1) { if constexpr (mt == 1) issue_piece(IC<sn>{}, IC<3>{}); if constexpr (mt == 3) issue_piece(IC<sn>{}, IC<4>{}); }
                    else if constexpr (!(mt & 1)) issue_piece(IC<sn>{}, IC<(mt >> 1)>{});
                }
                if constexpr (st == 11 && mt == 0) { const u32 ho = (u32)(b * 8192 + opq(tid) * 16); ald4(hd0, d_h, ho); ald4(hd1, d_h, ho + 16); }
                if constexpr (st == 11 && (mt == 2 || mt == 4)) {
                    const int l2 = opq(tid) & 63;
                    dma(IC<0>{}, IC<L_DUMMY>{}, d_wo, ldsbase, (u32)((l2 >> 3) * 1024 + (l2 & 7) * 16), (u32)(((grp & 3) * 128 + wave * 16 + (mt - 2) * 4) * 1024 + mem * 128));
                }
                if constexpr (R == 1 && st == 14 && mt == 4) { if (wave == 7) ald2s(flg, WS_F1 + (u32)(((b >> 5) * 32 + (opq(tid) & 31)) * 128)); }
                SB();
            });
            SB();
            if constexpr (st == 0) {
                const int l2 = opq(tid) & 63, tg = l2 & 7, chl = l2 >> 3;
                const u32x2 skw = *(LAS u32x2*)(lds + L_SK + (8 * wave + chl) * 8);
                u32 ka = L_K + tg * 1024 + (((wave ^ tg) & 7) << 4) + chl * 2;
                u32 kb = L_K + tg * 1024 + (((wave ^ (tg + 1)) & 7) << 4) + chl * 2;
                asm volatile("" : "+v"(ka), "+v"(kb));
                u16 kh[11], vh[8];
#pragma unroll
                for (int i = 0; i < 11; ++i) kh[i] = *(LAS u16*)(lds + ((i + 5 < 8) ? ka : kb) + (i + 5) * 128);
#pragma unroll
                for (int i = 0; i < 8; ++i) vh[i] = *(LAS u16*)(lds + ka + (L_V - L_K) + i * 128);
                SB();
                const float a0 = bflo(skw[0]), a1 = bfhi(skw[0]), a2 = bflo(skw[1]), a3 = bfhi(skw[1]);
                float ua[8];
#pragma unroll
                for (int i = 0; i < 8; ++i) ua[i] = (bflo(kh[i]) * a3 + bflo(kh[i + 1]) * a2 + bflo(kh[i + 2]) * a1 + bflo(kh[i + 3]) * a0) * bflo(vh[i]);
                const u32x4 oa = {pk2(ua[7], ua[6]), pk2(ua[5], ua[4]), pk2(ua[3], ua[2]), pk2(ua[1], ua[0])};
                __builtin_amdgcn_raw_buffer_store_b128(oa, r_ws, WS_UT + (u32)((mem * 64 + 8 * wave + chl) * 4096 + (2040 - T0 - 8 * tg) * 2), 0, 16);
                CFENCE();
                SB();
            }
        });
    }
    STAMP(R ? 1 : 53);

    u32x4 uu;
    float csV;
    auto attention = [&](auto lc) {
        constexpr int LAYER = decltype(lc)::value;
        constexpr int SBASE = LAYER ? 32 : 28;
        const int lane = opq(tid) & 63, li = lane & 15, lq = lane >> 4;
        u32 ex = L_EX + hd * 2048 + lane * 8;
        pin(ex);
        u32 f1ok = 1;
        if constexpr (R == 0) {
            if constexpr (LAYER == 0) {
#pragma unroll
                for (int i = 0; i < 4; ++i) { csK[i] = bcast_row0(accM[0][i]); csQ[i] = bcast_row0(accQ[0][i]); }
                if (li == 0) { *(LAS f32x4*)(lds + L_CS + hd * 128 + lq * 16) = csK; *(LAS f32x4*)(lds + L_CS + hd * 128 + 64 + lq * 16) = csQ; }
            }
            const int par = li & 1;
            const bool cur0 = (4 * lq + par) <= li, cur1 = (4 * lq + par + 2) <= li;
            s16x4 Kb[5], Qb[5];
#pragma unroll
            for (int mt = 0; mt < 5; ++mt) {
                Kb[mt] = pack4((accM[mt] - m1[mt] * csK) * r1[mt]);
                if (mt) Qb[mt] = pack4(accQ[mt] - m1[mt] * csQ);
            }
            f32x4 Sc[5], Sp[5];
#pragma unroll
            for (int mt = 1; mt < 5; ++mt) { Sc[mt] = mfma16(Kb[mt], Qb[mt], zero4); Sp[mt] = mfma16(Kb[mt - 1], Qb[mt], zero4); }
            float s0[5], s1[5], mx[5];
#pragma unroll
            for (int mt = 1; mt < 5; ++mt) {
                const float sp0 = Sp[mt][0], sp1 = Sp[mt][1], sp2 = Sp[mt][2], sp3 = Sp[mt][3], c0 = Sc[mt][0], c1 = Sc[mt][1], c2 = Sc[mt][2], c3 = Sc[mt][3];
                float p0 = par ? sp1 : sp0, p1 = par ? sp3 : sp2, q0 = par ? c1 : c0, q1 = par ? c3 : c2;
                asm volatile("" : "+v"(p0), "+v"(p1), "+v"(q0), "+v"(q1));
                if (mt == 1 && grp == 0) { p0 = -3.0e38f; p1 = -3.0e38f; }
                s0[mt] = cur0 ? q0 : p0; s1[mt] = cur1 ? q1 : p1;
                mx[mt] = vmax(s0[mt], s1[mt]);
            }
#pragma unroll
            for (int mt = 1; mt < 5; ++mt) { auto r = __builtin_amdgcn_permlane32_swap(__float_as_uint(mx[mt]), __float_as_uint(mx[mt]), false, false); mx[mt] = vmax(__uint_as_float(r[0]), __uint_as_float(r[1])); }
#pragma unroll
            for (int mt = 1; mt < 5; ++mt) { auto r = __builtin_amdgcn_permlane16_swap(__float_as_uint(mx[mt]), __float_as_uint(mx[mt]), false, false); mx[mt] = vmax(__uint_as_float(r[0]), __uint_as_float(r[1])); }
#pragma unroll
            for (int mt = 1; mt < 5; ++mt) {
                const float sc = r1[mt] * (0.25f * 1.44269504088896341f);
                const float e0 = __builtin_amdgcn_exp2f((s0[mt] - mx[mt]) * sc), e1 = __builtin_amdgcn_exp2f((s1[mt] - mx[mt]) * sc);
                *(LAS u32x2*)(lds + ex + (mt - 1) * 512) = (u32x2){pk2(e0, e0), pk2(e1, e1)};
            }
            STAMP(SBASE + 2);
            BAR();
            STAMP(SBASE + 1);
            if constexpr (LAYER == 0) f1ok = __builtin_amdgcn_readfirstlane(*(LAS u32*)(lds + L_FLG));
        } else {
            if constexpr (LAYER == 0) {
                auto s = __builtin_amdgcn_permlane16_swap(__float_as_uint(accM[0][0]), __float_as_uint(accM[0][0]), false, false);
                auto t = __builtin_amdgcn_permlane32_swap(s[0], s[0], false, false);
                csV = __uint_as_float(t[0]);
                if (wave == 7) { WAIT_VM(0); pin(flg); const u32 ok = flag_ok(flg); *(LAS u32*)(lds + L_FLG) = ok; }
            }
            const int par = li & 1;
            const bool cur0 = (4 * lq + par) <= li, cur1 = (4 * lq + par + 2) <= li;
            const u32 pm = par ? 0xffff0000u : 0x0000ffffu;
            u32 mC0 = cur0 ? pm : 0u, mP0 = cur0 ? 0u : pm, mC1 = cur1 ? pm : 0u, mP1 = cur1 ? 0u : pm;
            u32 oo = (LAYER ? WS_O2 : WS_O1) + (u32)(4 * grp * 16384 + (4 * mem + hd) * 512 + lane * 8);
            asm volatile("" : "+v"(mC0), "+v"(mP0), "+v"(mC1), "+v"(mP1), "+v"(oo));
            s16x4 Vb[5];
#pragma unroll
            for (int mt = 0; mt < 5; ++mt) { Vb[mt] = pack4((accM[mt] - m4[mt] * csV) * r4[mt]); pin(Vb[mt]); }
            STAMP(SBASE);
            BAR();
            STAMP(SBASE + 1);
            u32x2 pw[4];
#pragma unroll
            for (int j = 0; j < 4; ++j) pw[j] = *(LAS u32x2*)(lds + ex + j * 512);
            if constexpr (LAYER == 0) f1ok = *(LAS u32*)(lds + L_FLG);
            SB();
            f32x4 O[4], L[4];
#pragma unroll
            for (int j = 0; j < 4; ++j) {
                const s16x8 P = __builtin_bit_cast(s16x8, (u32x4){pw[j][0] & mC0, pw[j][1] & mC1, pw[j][0] & mP0, pw[j][1] & mP1});
                L[j] = mfma32(ones8, P, zero4);
                O[j] = mfma32(cat8(Vb[j + 1], Vb[j]), P, zero4);
            }
#pragma unroll
            for (int j = 0; j < 4; ++j) {
                const float inv = __builtin_amdgcn_rcpf(L[j][0]);
                const u32x2 ow = {nosent(pk2(O[j][0] * inv, O[j][1] * inv)), nosent(pk2(O[j][2] * inv, O[j][3] * inv))};
                __builtin_amdgcn_raw_buffer_store_b64(ow, r_ws, oo, j * 16384, 16);
            }
            f1ok = __builtin_amdgcn_readfirstlane(f1ok);
            CFENCE();
        }
        if constexpr (LAYER == 0) { if (f1ok) ald4s(uu, WS_UT + (u32)(b * 8192 + opq(tid) * 16)); }
        SB();
        return f1ok;
    };
    const u32 f1ok = attention(IC<0>{});
    {
        const u32x4 sv = {SENT, SENT, SENT, SENT};
        const int t2 = opq(tid);
        const u32 tb = (u32)((4 * grp + hd) * 16384 + mem * 2048 + R * 1024 + (t2 & 63) * 16);
        __builtin_amdgcn_raw_buffer_store_b128(sv, r_ws, tb, WS_O2, 16);
        __builtin_amdgcn_raw_buffer_store_b128(sv, r_ws, tb, WS_X2, 16);
        __builtin_amdgcn_raw_buffer_store_b128(sv, r_ws, (u32)(b * 8192 + t2 * 16), WS_Y2, 16);
        CFENCE();
        SB();
    }
    STAMP(2);

    const int TI = 4 * grp + hd;
    u32x2 of[16];
    auto load_o = [&](u32 ws_o) {
        const int lane = opq(tid) & 63;
#pragma unroll
        for (int i = 0; i < 16; ++i) of[i] = __builtin_amdgcn_raw_buffer_load_b64(r_ws, ws_o + (u32)(TI * 16384 + R * 8192 + lane * 8) + (i & 7) * 512, (i >> 3) * 4096, 16);
    };
    u32 xw[8], yw[8];
    u32 woa[4];
    s16x8 F[8][4];
    auto wo_addr = [&]() {
        const int l2 = opq(tid) & 63, lq = l2 >> 4, tq = (l2 & 15) >> 2, tp = l2 & 3;
#pragma unroll
        for (int n = 0; n < 4; ++n) woa[n] = L_WO + (u32)(R * 32768 + (4 * lq + tq) * 128 + ((n ^ (((lq & 1) << 1) | (tq >> 1))) << 5) + tp * 8);
    };
    auto load_F = [&](int ks) {
#pragma unroll
        for (int n = 0; n < 4; ++n) F[ks][n] = cat8(trr(woa[n] + ks * 4096), trr(woa[n] + ks * 4096 + 2048));
    };

    {
        const int t2 = opq(tid), lane = t2 & 63, li = lane & 15, lq = lane >> 4;
        constexpr u32 OFFS = 0xEDA85410u;
        {
            WAIT_VM(R ? 7 : 3);
            pin(hd0); pin(hd1);
            const int j = t2 & 255;
            u32 cb = L_HC + R * 34816 + (j + 2) * 16;
            asm volatile("" : "+v"(cb));
            const u32 d[8] = {hd0[0], hd0[1], hd0[2], hd0[3], hd1[0], hd1[1], hd1[2], hd1[3]};
            u32 e[7];
#pragma unroll
            for (int i = 0; i < 7; ++i) e[i] = __builtin_amdgcn_alignbit(d[i + 1], d[i], 16);
            sfor<0, 8>([&](auto pc) {
                constexpr int p = decltype(pc)::value;
                constexpr u32 po = (272 * p + ((OFFS >> (4 * p)) & 15)) * 16;
                constexpr int h = p >> 1;
                const u32x4 w = (p & 1) ? (u32x4){e[h], e[h + 1], e[h + 2], e[h + 3]} : (u32x4){d[h], d[h + 1], d[h + 2], d[h + 3]};
                *(LAS u32x4*)(lds + cb + po) = w;
            });
            if (hd == 0) {
                const u32 h0 = __builtin_amdgcn_readfirstlane(d[0]), h1 = __builtin_amdgcn_readfirstlane(d[1]);
                const u32 h2 = __builtin_amdgcn_readfirstlane(d[2]), h3 = __builtin_amdgcn_readfirstlane(d[3]);
                if (lane < 16) {
                    const int p = lane & 7;
                    const u32 po = (272 * p + ((OFFS >> (4 * p)) & 15)) * 16;
                    const unsigned __int128 hv = ((unsigned __int128)(((u64)h3 << 32) | h2) << 64) | (((u64)h1 << 32) | h0);
                    const unsigned __int128 sv = (p == 0 || lane < 8) ? (unsigned __int128)0 : (hv << (16 * (8 - p)));
                    const u32x4 w = {(u32)sv, (u32)(sv >> 32), (u32)(sv >> 64), (u32)(sv >> 96)};
                    *(LAS u32x4*)(lds + L_HC + R * 34816 + po + (lane >> 3) * 16) = w;
                }
            }
            if (hd == 1 && lane < 30) *(LAS u32x4*)(lds + L_UR + R * 4576 + 4096 + lane * 16) = (u32x4){0, 0, 0, 0};
            STAMP(3);
            if (!f1ok) {
                const u32 fo = WS_F1 + (u32)(((b >> 5) * 32 + 31 - hd * 8 - (lane & 7)) * 128);
                flg = (u32x2){0u, 0u};
                spin_flags(flg, fo);
                ald4s(uu, WS_UT + (u32)(b * 8192 + t2 * 16));
            }
            asm volatile("s_waitcnt vmcnt(0)" : "+v"(uu) :: "memory");
            *(LAS u32x4*)(lds + L_UR + R * 4576 + j * 16) = uu;
        }
        BAR();
        if (R == 0 && tid == 0) __builtin_amdgcn_raw_buffer_store_b64((u32x2){tl, th}, r_ws, WS_F2 + (grp * 8 + mem) * 128, 0, 16);
        STAMP(4);
        const int ch = hd >> 1, e0 = (hd & 1) * 4;
        const int p = (li + 1) & 7;
        u32 aA = L_HC + ch * 34816 + (272 * p + ((OFFS >> (4 * p)) & 15) + ((li + 1) >> 3) + lq + 4 * e0) * 16;
        u32 aB = L_UR + ch * 4576 + (254 - 224 - 2 * li + lq + 4 * e0) * 16;
        asm volatile("" : "+v"(aA), "+v"(aB));
        f32x4 y[4];
        const u32 f2own = WS_F2 + (u32)((grp * 8 + 4 * R + (lane & 3)) * 128);
        u32 ooff = WS_O1 + (u32)(TI * 16384 + R * 8192 + lane * 8);
        bool early = false;
        {
            constexpr ConvSched S(R);
            s16x8 A[4][8], B[4][8];
            auto loads_at = [&](auto pc) {
                constexpr int p = decltype(pc)::value;
                sfor<0, 64>([&](auto ic) {
                    constexpr int i = decltype(ic)::value, e = i >> 4, f = i & 7;
                    if constexpr (S.slot[i] == p) {
                        if constexpr (i & 8) B[e][f] = ldsA(aB + 64 * e + 512 * (7 - f)); else A[e][f] = ldsA(aA + 64 * (8 * f + e));
                    }
                });
            };
            loads_at(IC<-1>{});
            SB();
            sfor<0, 72>([&](auto pc) {
                constexpr int p = decltype(pc)::value, e = p / 18, f = S.f[p % 18], i = S.i[p % 18], g = S.n[i] - f;
                y[i] = mfma32(A[e][f], B[e][g], (e == 0 && f == 0) ? zero4 : y[i]);
                loads_at(pc);
                if constexpr (p < 24 && (p % 3) == 1) issue_wo(IC<(p / 3)>{});
                if constexpr (p == 36) ald2s(flg, f2own);
                if constexpr (p == 54) { WAIT_VM(0); pin(flg); early = flag_ok(flg); }
                if constexpr (p >= 55 && p < 71) {
                    if (early) ald2o<((p - 55) & 7) * 512>(of[p - 55], ooff + ((p - 55) >> 3) * 4096, d_ws);
                }
                SB();
            });
        }
        STAMP(14);
        RBAR();
        {
            constexpr int N0[4] = {0, 3, 4, 7};
            u32 ra = L_HC + hd * 8192 + lane * 16;
            asm volatile("" : "+v"(ra));
#pragma unroll
            for (int i = 0; i < 4; ++i) *(LAS f32x4*)(lds + ra + (N0[i] ^ R) * 1024) = y[i];
        }
        SB();
        if constexpr (R == 0) {
#pragma unroll
            for (int n = 0; n < 4; ++n) { const u32x2 w = __builtin_amdgcn_raw_buffer_load_b64(r_x, (u32)((TI * 16 + li) * 1024 + mem * 128 + lq * 8) + n * 32, 0, 0); xw[2 * n] = w[0]; xw[2 * n + 1] = w[1]; }
        }
        u32 f2oth = 0; u32x2 flg2;
        if constexpr (R == 1) {
            if (wave == 7) {
                const int pb = 32 * mem + (lane & 31);
                f2oth = WS_F2 + (u32)((lane < 32 ? ((pb & 7) * 4 + (pb >> 6)) * 8 + ((pb >> 3) & 7) : max(grp * 8 - 8, 0) + (lane & 15)) * 128);
                CFENCE();
                ald2s(flg2, f2oth);
            }
        }
        wo_addr();
        SB();
        STAMP(37);
        BAR();
        STAMP(38);
        {
            const float D0 = bflo(dw), D1 = bfhi(dw);
            u32 ra = L_HC + wave * 1024 + lane * 16;
            const int tb = 256 * wave + 16 * li + 4 * lq;
            u32 ru = L_UR + (2044 - tb) * 2;
            asm volatile("" : "+v"(ra), "+v"(ru));
            const f32x4 y0 = *(LAS f32x4*)(lds + ra), y1 = *(LAS f32x4*)(lds + ra + 8192), y2 = *(LAS f32x4*)(lds + ra + 16384), y3 = *(LAS f32x4*)(lds + ra + 24576);
            const u32x2 ua = *(LAS u32x2*)(lds + ru), ub = *(LAS u32x2*)(lds + ru + 4576);
            load_F(0); load_F(1);
            SB();
            const f32x4 ya = y0 + y1, yb = y2 + y3;
            const u32x4 w = {nosent(pk2(ya[0] + bfhi(ua[1]) * D0, yb[0] + bfhi(ub[1]) * D1)), nosent(pk2(ya[1] + bflo(ua[1]) * D0, yb[1] + bflo(ub[1]) * D1)),
                             nosent(pk2(ya[2] + bfhi(ua[0]) * D0, yb[2] + bfhi(ub[0]) * D1)), nosent(pk2(ya[3] + bflo(ua[0]) * D0, yb[3] + bflo(ub[0]) * D1))};
            __builtin_amdgcn_raw_buffer_store_b128(w, r_ws, WS_Y2 + (u32)(b * 8192 + tb * 4), 0, 16);
        }
        SB();
        STAMP(5);
        if (!early) {
            spin_flags(flg, f2own);
            sfor<0, 16>([&](auto ic) { constexpr int i = decltype(ic)::value;
                ald2o<(i & 7) * 512>(of[i], ooff + (i >> 3) * 4096, d_ws); });
            WAIT_VM(0);
        }
        WAIT_VM(1);
        sfor<0, 16>([&](auto ic) { pin(of[decltype(ic)::value]); });
        STAMP(40);

        f32x4 acc[4];
#pragma unroll
        for (int ks = 0; ks < 8; ++ks) {
            if (ks + 2 < 8) load_F(ks + 2);
            SB();
#pragma unroll
            for (int n = 0; n < 4; ++n) acc[n] = mfma32(F[ks][n], __builtin_bit_cast(s16x8, (u32x4){of[2 * ks][0], of[2 * ks][1], of[2 * ks + 1][0], of[2 * ks + 1][1]}), ks ? acc[n] : zero4);
            SB();
        }
        STAMP(41);
        accM[0] = acc[0]; accM[1] = acc[1]; accM[2] = acc[2]; accM[3] = acc[3];
        if constexpr (R == 1) { if (wave == 7) { asm volatile("" : "+v"(flg2)); spin_flags(flg2, f2oth); } }
    }

    auto load_y = [&]() {
        const int l2 = opq(tid) & 63;
#pragma unroll
        for (int i = 0; i < 8; ++i)
            yw[i] = __builtin_amdgcn_raw_buffer_load_b32(r_ws, WS_Y2 + (u32)((32 * mem + 2 * (l2 >> 4)) * 8192 + (TI * 16 + (l2 & 15)) * 4), (8 * (i >> 1) + (i & 1)) * 8192, 16);
    };
    auto pub_stat = [&](u32 ws_st, float s, float q) {
        const int lane = opq(tid) & 63;
        s = xsum(s); q = xsum(q);
        const u32x4 sv = {__float_as_uint(s), tl, __float_as_uint(q), th};
        __builtin_amdgcn_raw_buffer_store_b128(sv, r_ws, lane < 16 ? ws_st + (u32)((TI * 8 + mem) * 256 + lane * 16) : 0xFFFFFF00u, 0, 16);
    };
    auto stat_off = [&](u32 ws_st, int ti) { const int l2 = opq(tid) & 63; return ws_st + (u32)(ti * 2048 + (l2 >> 4) * 512 + (l2 & 15) * 16); };
    auto fin_stat = [&](u32x4& a, u32x4& c, u32 off, float& mean, float& rstd) {
        _Pragma("unroll 1") for (int spin = 0; spin < SPIN_CAP; ++spin) {
            if (__all(((a[1] ^ tl) | (a[3] ^ th) | (c[1] ^ tl) | (c[3] ^ th)) == 0)) break;
            __builtin_amdgcn_s_sleep(1);
            asm volatile("buffer_load_dwordx4 %0, %2, %3, 0 offen sc1\n\tbuffer_load_dwordx4 %1, %2, %3, 0 offen offset:256 sc1\n\ts_waitcnt vmcnt(0)"
                         : "=&v"(a), "=&v"(c) : "v"(off), "s"(d_ws) : "memory");
        }
        const float ts = xsum(__uint_as_float(a[0]) + __uint_as_float(c[0])), tq = xsum(__uint_as_float(a[2]) + __uint_as_float(c[2]));
        mean = ts * (1.f / 512.f);
        rstd = __builtin_amdgcn_rsqf(fmaxf(tq * (1.f / 512.f) - mean * mean, 0.f) + 1e-5f);
    };
    auto wo_finish = [&](auto lc) {
        constexpr int LAYER = decltype(lc)::value;
        const int lane = opq(tid) & 63, li = lane & 15, lq = lane >> 4;
        u32 wx = L_WX + hd * 4096 + lane * 16;
        asm volatile("" : "+v"(wx));
        if constexpr (R == 1) {
#pragma unroll
            for (int n = 0; n < 4; ++n) *(LAS f32x4*)(lds + wx + n * 1024) = accM[n];
        }
        BAR();
        STAMP(LAYER ? 25 : 42);
        if constexpr (R == 0) {
            f32x4 t[4];
#pragma unroll
            for (int n = 0; n < 4; ++n) t[n] = *(LAS f32x4*)(lds + wx + n * 1024);
            if constexpr (!LAYER) load_y();
            const u32 so = stat_off(LAYER ? WS_ST3 : WS_ST1, TI);
            SB();
            float s = 0.f, q = 0.f, s2 = 0.f, q2 = 0.f;
#pragma unroll
            for (int n = 0; n < 4; ++n) {
                const f32x4 v = accM[n] + t[n];
                const u32 w0 = pk2(bflo(xw[2 * n]) + v[0], bfhi(xw[2 * n]) + v[1]), w1 = pk2(bflo(xw[2 * n + 1]) + v[2], bfhi(xw[2 * n + 1]) + v[3]);
                xw[2 * n] = w0; xw[2 * n + 1] = w1;
                s = dot2(w0, 0x3F803F80u, s); q = dot2(w0, w0, q); s2 = dot2(w1, 0x3F803F80u, s2); q2 = dot2(w1, w1, q2);
            }
            pub_stat(LAYER ? WS_ST3 : WS_ST1, s + s2, q + q2);
            STAMP(LAYER ? 46 : 43);
            WAIT_VM(0);
            STAMP(LAYER ? 26 : 44);
            u32x4 a = {0, 0, 0, 0}, c = {0, 0, 0, 0};
            float mean, rstd;
            fin_stat(a, c, so, mean, rstd);
            STAMP(LAYER ? 11 : 6);
            if constexpr (!LAYER) {
                _Pragma("unroll 1") for (int spin = 0; spin < SPIN_CAP; ++spin) {
                    if (__all(max(max(max(yw[0], yw[1]), max(yw[2], yw[3])), max(max(yw[4], yw[5]), max(yw[6], yw[7]))) != SENT)) break;
                    __builtin_amdgcn_s_sleep(1);
                    CFENCE();
                    load_y();
                }
            }
            STAMP(47);
            u32 w[8];
            const float nm = -mean * rstd;
#pragma unroll
            for (int i = 0; i < 8; ++i) {
                const float a = bflo(xw[i]), c = bfhi(xw[i]);
                w[i] = pk2(a + (a * rstd + nm) * bflo(yw[i]), c + (c * rstd + nm) * bfhi(yw[i]));
                if (!LAYER) w[i] = nosent(w[i]);
            }
#pragma unroll
            for (int j = 0; j < 2; ++j) {
                auto r0 = __builtin_amdgcn_permlane16_swap(w[4 * j], w[4 * j + 2], false, false);
                auto r1 = __builtin_amdgcn_permlane16_swap(w[4 * j + 1], w[4 * j + 3], false, false);
                const u32x4 o = {r0[0], r1[0], r0[1], r1[1]};
                const int c = 4 * j + 2 * (lq & 1) + (lq >> 1);
                if constexpr (LAYER) __builtin_amdgcn_raw_buffer_store_b128(o, r_out, (u32)((TI * 16 + li) * 1024 + mem * 128 + c * 16), 0, OUT_AUX);
                else __builtin_amdgcn_raw_buffer_store_b128(o, r_ws, WS_X2 + (u32)((TI * 8 + mem) * 2048 + li * 128 + ((c ^ (li >> 1)) << 4)), 0, 16);
            }
            if constexpr (!LAYER) {
                float s = 0.f, q = 0.f, s2 = 0.f, q2 = 0.f;
#pragma unroll
                for (int i = 0; i < 8; i += 2) {
                    xw[i] = w[i]; xw[i + 1] = w[i + 1];
                    s = dot2(w[i], 0x3F803F80u, s); q = dot2(w[i], w[i], q); s2 = dot2(w[i + 1], 0x3F803F80u, s2); q2 = dot2(w[i + 1], w[i + 1], q2);
                    asm volatile("" : "+v"(xw[i]), "+v"(xw[i + 1]), "+v"(yw[i]), "+v"(yw[i + 1]));
                }
                pub_stat(WS_ST2, s + s2, q + q2);
                STAMP(48);
                WAIT_VM(0);
            }
        }
    };
    wo_finish(IC<0>{});
    RBAR();
    STAMP(7);

    {
        const int l3 = opq(tid) & 63;
        const u32 g0 = (u32)((4 * grp - 1 + (wave >> 1)) * 16384 + (wave & 1) * 1024 + l3 * 16);
        const u32 g2 = (u32)((4 * grp + 3) * 16384 + wave * 256 + l3 * 16);
        auto x2_piece = [&](auto ic) {
            constexpr int i = decltype(ic)::value, s = i >> 1;
            const u32 kp = (u32)((s + rot) & 7);
            if constexpr (!(i & 1)) dma(IC<4>{}, IC<L_CX + s * 10240>{}, d_x2, wb1k, g0, kp * 2048);
            else dma(IC<5>{}, IC<L_CX + s * 10240 + 8192>{}, d_x2, wb256, g2, kp * 2048);
        };
        {
            const u32 po = (l3 & 8) ? (u32)((4 * grp + 3) * 16384 + wave * 256 + 252 + (l3 & 7) * 2048) : (u32)((4 * grp - 1 + (wave >> 1)) * 16384 + (wave & 1) * 1024 + 1020 + (l3 & 7) * 2048);
            _Pragma("unroll 1") for (int spin = 0; spin < SPIN_CAP; ++spin) {
                u32 pv;
                asm volatile("buffer_load_dword %0, %1, %2, 0 offen sc1\n\ts_waitcnt vmcnt(0)" : "=&v"(pv) : "v"(po), "s"(d_x2) : "memory");
                if (__all(pv != SENT)) break;
                __builtin_amdgcn_s_sleep(1);
            }
        }
        STAMP(39);
        u32 va = L_CX + wave * 1024 + l3 * 16, vq = L_CX + 8192 + wave * 256 + (l3 & 15) * 16;
        asm volatile("" : "+v"(va), "+v"(vq));
        u32x4 vr[4];
        auto ver_read = [&](auto cc) {
            constexpr int c = decltype(cc)::value;
            u32 a = va + c * 20480, q = vq + c * 20480;
            asm volatile("" : "+v"(a), "+v"(q) :: "memory");
            vr[0] = *(LAS u32x4*)(lds + a); vr[1] = *(LAS u32x4*)(lds + a + 10240); vr[2] = *(LAS u32x4*)(lds + q); vr[3] = *(LAS u32x4*)(lds + q + 10240);
        };
        auto ver_check = [&](auto cc) {
            constexpr int c = decltype(cc)::value;
            _Pragma("unroll 1") for (int spin = 0; spin < SPIN_CAP; ++spin) {
                const u32 m0 = max(max(max(vr[0][0], vr[0][1]), max(vr[0][2], vr[0][3])), max(max(vr[1][0], vr[1][1]), max(vr[1][2], vr[1][3])));
                const u32 m1 = max(max(max(vr[2][0], vr[2][1]), max(vr[2][2], vr[2][3])), max(max(vr[3][0], vr[3][1]), max(vr[3][2], vr[3][3])));
                if (__all(max(m0, m1) != SENT)) break;
                __builtin_amdgcn_s_sleep(1);
                sfor<4 * c, 4 * c + 4>([&](auto ic) { x2_piece(ic); });
                WAIT_VM(0);
                ver_read(cc);
            }
        };
        sfor<0, 8>([&](auto ic) { x2_piece(ic); });
        const int sti = max(4 * grp - 1 + min(wave, 4), 0);
        const u32 so = stat_off(WS_ST2, sti);
        const u32 xb0 = (u32)((l3 & 15) * 128 + (((l3 >> 4) ^ ((l3 & 15) >> 1)) << 4));
        WAIT_VM(4);
        STAMP(8);
        ver_read(IC<0>{});
        ver_check(IC<0>{});
        RBAR();
        STAMP(13);
        u32 xa;
        u32x4 sa, sc;
        auto pf_addr = [&](int st) { xa = L_CX + (st >> 1) * 10240 + (xb0 ^ ((st & 1) * 64)); asm volatile("" : "+v"(xa)); };
        pf_addr(0);
#pragma unroll
        for (int mt = 0; mt < 5; ++mt) Ab[0][mt] = ldsA(xa + mt * 2048);
        ald4s(sa, so); ald4s(sc, so + 256);
        sfor<0, 16>([&](auto stc) {
            constexpr int st = decltype(stc)::value;
            if constexpr (st == 2) { WAIT_VM(10); ver_read(IC<1>{}); }
            if constexpr (st == 3) { ver_check(IC<1>{}); RBAR(); STAMP(49); }
            if constexpr (st == 5) { WAIT_VM(4); ver_read(IC<2>{}); }
            if constexpr (st == 6) {
                ver_check(IC<2>{}); WAIT_VM(0); ver_read(IC<3>{});
                if (R == 0 || wave == 4) {
                    asm volatile("" : "+v"(sa), "+v"(sc));
                    float mean = 0.f, rstd = 1.f;
                    if (4 * grp - 1 + wave >= 0) fin_stat(sa, sc, so, mean, rstd);
                    if (l3 < 16) { *(LAS float*)(lds + L_TAB + wave * 64 + l3 * 4) = mean; *(LAS float*)(lds + L_TAB + 320 + wave * 64 + l3 * 4) = rstd; }
                }
            }
            if constexpr (st == 7) { ver_check(IC<3>{}); RBAR(); STAMP(50); CSTAMP(60); }
            if (st < 15) pf_addr(st + 1);
            if constexpr (st == 14) load_tab(true);
            SB();
            qkv_step(IC<1>{}, st, Ab[st & 1], [&](auto mc) {
                constexpr int mt = decltype(mc)::value;
                if (st < 15) Ab[(st + 1) & 1][mt] = ldsA(xa + mt * 2048);
                if constexpr (st < 2 && mt < 4) x2_piece(IC<8 + 4 * st + mt>{});
                SB();
            });
        });
    }
    STAMP(R ? 9 : 52);
    CSTAMP(61);
    attention(IC<1>{});
    STAMP(27);

    {
        wo_addr();
#pragma unroll
        for (int ks = 0; ks < 8; ++ks) load_F(ks);
        SB();
        WAIT_VM(0);
        RBAR();
        STAMP(10);
        load_o(WS_O2);
        SB();
        _Pragma("unroll 1") for (int spin = 0; spin < 64; ++spin) {
            u32 m0 = 0, m1 = 0;
#pragma unroll
            for (int i = 0; i < 16; i += 2) { m0 = max(max(m0, of[i][0]), of[i][1]); m1 = max(max(m1, of[i + 1][0]), of[i + 1][1]); }
            if (__all(max(m0, m1) != SENT)) break;
            _Pragma("unroll 1") for (int sp2 = 0; sp2 < SPIN_CAP; ++sp2) {
                CFENCE();
                __builtin_amdgcn_s_sleep(1);
                const u32 pv = __builtin_amdgcn_raw_buffer_load_b32(r_ws, WS_O2 + (u32)(TI * 16384 + R * 8192 + (opq(tid) & 15) * 512 + 508), 0, 16);
                if (__all(pv != SENT)) break;
            }
            CFENCE();
            load_o(WS_O2);
        }
        STAMP(24);
#pragma unroll
        for (int ks = 0; ks < 8; ++ks)
#pragma unroll
            for (int n = 0; n < 4; ++n) accM[n] = mfma32(F[ks][n], __builtin_bit_cast(s16x8, (u32x4){of[2 * ks][0], of[2 * ks][1], of[2 * ks + 1][0], of[2 * ks + 1][1]}), ks ? accM[n] : zero4);
        SB();
        STAMP(45);
    }
    wo_finish(IC<1>{});
    if constexpr (R == 1) {
        if (b == 0 && wave == 7) {
            const int l3 = opq(tid) & 63;
#pragma unroll
            for (int i = 0; i < 4; ++i) { flg = (u32x2){0u, 0u}; spin_flags(flg, WS_F1 + (u32)((i * 64 + l3) * 128)); }
            if (l3 == 0) __builtin_amdgcn_raw_buffer_store_b64(__builtin_bit_cast(u32x2, epoch + 1), r_ws, WS_EPOCH, 0, 16);
        }
    }
    if constexpr (R == 0) {
        WAIT_VM(0);
        STAMP(12);
        const u32 d = dg1 - dg0;
        u64 t0_; asm volatile("s_memrealtime %0\n\ts_waitcnt lgkmcnt(0)" : "=s"(t0_)::"memory");
        _Pragma("unroll 1") for (int i = 0; i < 100000; ++i) { u64 t_; asm volatile("s_memrealtime %0\n\ts_waitcnt lgkmcnt(0)" : "=s"(t_)::"memory"); if ((u32)t_ - (u32)t0_ >= d) break; }
    }
}

__global__ void __launch_bounds__(512, 2)
fused_kernel(const u16* __restrict__ gx, const u16* __restrict__ gwqkv, const u16* __restrict__ gwo,
             const u16* __restrict__ gbk, const u16* __restrict__ gbv, const u16* __restrict__ gsk,
             const u16* __restrict__ ghk, const u16* __restrict__ gdv, u16* __restrict__ gout, char* __restrict__ ws) {
    __shared__ __attribute__((aligned(16))) char lds_[L_TOTAL];
    const Args a = {gx, gwqkv, gwo, gbk, gbv, gsk, ghk, gdv, gout, ws};
    const int wave = __builtin_amdgcn_readfirstlane(threadIdx.x >> 6);
    if (wave < 4) body<0>(a, (LAS char*)lds_, wave); else body<1>(a, (LAS char*)lds_, wave);
}

extern "C" void kernel_launch(void* const* d_in, const int* in_sizes, int n_in,
                              void* d_out, int out_size, void* d_ws, size_t ws_size,
                              hipStream_t stream) {
    fused_kernel<<<256, 512, 0, stream>>>((const u16*)d_in[0], (const u16*)d_in[1], (const u16*)d_in[2],
                                          (const u16*)d_in[4], (const u16*)d_in[5], (const u16*)d_in[6],
                                          (const u16*)d_in[7], (const u16*)d_in[8], (u16*)d_out, (char*)d_ws);
}
```

```cpp
#include <hip/hip_runtime.h>
#include <stdint.h>

typedef unsigned short u16;
typedef unsigned u32;
typedef unsigned long long u64;
typedef short s16x4 __attribute__((ext_vector_type(4)));
typedef short s16x8 __attribute__((ext_vector_type(8)));
typedef __bf16 bf16x2 __attribute__((ext_vector_type(2)));
typedef float f32x2 __attribute__((ext_vector_type(2)));
typedef float f32x4 __attribute__((ext_vector_type(4)));
typedef u32 u32x2 __attribute__((ext_vector_type(2)));
typedef u32 u32x4 __attribute__((ext_vector_type(4)));
#define LAS __attribute__((address_space(3)))
#define DEV __device__ __forceinline__
typedef __amdgpu_buffer_rsrc_t rsrc_t;

constexpr u32 WS_EPOCH = 0;
constexpr u32 WS_F1 = 32768;
constexpr u32 WS_F2 = 65536;
constexpr u32 WS_ST0 = 98304;
constexpr u32 WS_ST1 = 131072;
constexpr u32 WS_ST2 = WS_ST1 + 262144;
constexpr u32 WS_ST3 = WS_ST2 + 262144;
constexpr u32 WS_UT = 1u << 20;
constexpr u32 WS_O1 = WS_UT + (2u << 20);
constexpr u32 WS_O2 = WS_O1 + (2u << 20);
constexpr u32 WS_Y2 = WS_O2 + (2u << 20);
constexpr u32 WS_X2 = WS_Y2 + (2u << 20);
constexpr u32 WS_END = WS_X2 + (2u << 20);

constexpr u32 L_SLOT = 34816;
constexpr u32 L_KV = 4 * L_SLOT;
constexpr u32 L_K = L_KV, L_V = L_KV + 9216;
constexpr u32 L_SK = L_KV + 17408;
constexpr u32 L_WO = 0;
constexpr u32 L_CX = 65536;
constexpr u32 L_HC = L_CX;
constexpr u32 L_UR = L_CX + 69632;
constexpr u32 L_WX = L_CX + 40960;
constexpr u32 L_EX = 147456;
constexpr u32 L_TAB = L_EX + 12288;
constexpr u32 L_CS = L_TAB + 1024;
constexpr u32 L_FLG = L_TAB + 2048;
constexpr u32 L_DUMMY = L_FLG + 1024;
constexpr u32 L_TOTAL = 160 * 1024;
static_assert(L_DUMMY + 1024 <= L_TOTAL, "LDS map");

#define WO_POS 0
#define OUT_AUX 0
constexpr int SPIN_CAP = 1 << 14;
constexpr u32 SENT = 0xFFFFFFFFu;

#define WAIT_VM(n) asm volatile("s_waitcnt vmcnt(%0)" ::"i"(n) : "memory")
#define BAR() do { asm volatile("s_waitcnt lgkmcnt(0)" ::: "memory"); __builtin_amdgcn_s_barrier(); asm volatile("" ::: "memory"); } while (0)
#define RBAR() do { asm volatile("" ::: "memory"); __builtin_amdgcn_s_barrier(); asm volatile("" ::: "memory"); } while (0)
#define CFENCE() asm volatile("" ::: "memory")
#define SB() __builtin_amdgcn_sched_barrier(0)
template <int N> struct IC { static constexpr int value = N; };
template <int I, int N, class F> DEV void sfor(F&& f) { if constexpr (I < N) { f(IC<I>{}); sfor<I + 1, N>(f); } }

DEV f32x4 mfma32(s16x8 a, s16x8 b, f32x4 c) { return __builtin_amdgcn_mfma_f32_16x16x32_bf16(a, b, c, 0, 0, 0); }
DEV f32x4 mfma16(s16x4 a, s16x4 b, f32x4 c) { return __builtin_amdgcn_mfma_f32_16x16x16bf16_1k(a, b, c, 0, 0, 0); }
DEV float vmax(float a, float b) { float r; asm("v_max_f32 %0, %1, %2" : "=v"(r) : "v"(a), "v"(b)); return r; }
DEV u32 pk2(float lo, float hi) { f32x2 f = {lo, hi}; bf16x2 b = __builtin_convertvector(f, bf16x2); return __builtin_bit_cast(u32, b); }
DEV float bflo(u32 w) { return __uint_as_float(w << 16); }
DEV float bfhi(u32 w) { return __uint_as_float(w & 0xffff0000u); }
DEV float dot2(u32 a, u32 b, float c) { return __builtin_amdgcn_fdot2_f32_bf16(__builtin_bit_cast(bf16x2, a), __builtin_bit_cast(bf16x2, b), c, false); }
DEV u32 nosent(u32 w) { return min(w, 0xFFFFFFFEu); }
DEV float xsum(float v) {
    auto r = __builtin_amdgcn_permlane32_swap(__float_as_uint(v), __float_as_uint(v), false, false);
    v = __uint_as_float(r[0]) + __uint_as_float(r[1]);
    auto s = __builtin_amdgcn_permlane16_swap(__float_as_uint(v), __float_as_uint(v), false, false);
    return __uint_as_float(s[0]) + __uint_as_float(s[1]);
}
DEV float xmax(float v) {
    auto r = __builtin_amdgcn_permlane32_swap(__float_as_uint(v), __float_as_uint(v), false, false);
    v = vmax(__uint_as_float(r[0]), __uint_as_float(r[1]));
    auto s = __builtin_amdgcn_permlane16_swap(__float_as_uint(v), __float_as_uint(v), false, false);
    return vmax(__uint_as_float(s[0]), __uint_as_float(s[1]));
}
DEV float rowsum16(float v) {
    v += __builtin_bit_cast(float, __builtin_amdgcn_update_dpp(0, __builtin_bit_cast(int, v), 0xB1, 0xf, 0xf, true));
    v += __builtin_bit_cast(float, __builtin_amdgcn_update_dpp(0, __builtin_bit_cast(int, v), 0x4E, 0xf, 0xf, true));
    v += __builtin_bit_cast(float, __builtin_amdgcn_update_dpp(0, __builtin_bit_cast(int, v), 0x141, 0xf, 0xf, true));
    v += __builtin_bit_cast(float, __builtin_amdgcn_update_dpp(0, __builtin_bit_cast(int, v), 0x140, 0xf, 0xf, true));
    return v;
}
DEV float bcast_row0(float v) { return __builtin_bit_cast(float, __builtin_amdgcn_update_dpp(0, __builtin_bit_cast(int, v), 0x150, 0xf, 0xf, true)); }
template <int OFF> DEV void ald2o(u32x2& d, u32 voff, u32x4 rs) { asm volatile("buffer_load_dwordx2 %0, %1, %2, 0 offen offset:%3 sc1" : "=v"(d) : "v"(voff), "s"(rs), "i"(OFF) : "memory"); }
template <class T> DEV void pin(T& v) { asm volatile("" : "+v"(v)); }
DEV int opq(int v) { asm volatile("" : "+v"(v)); return v; }
DEV s16x8 cat8(s16x4 a, s16x4 b) { return __builtin_shufflevector(a, b, 0, 1, 2, 3, 4, 5, 6, 7); }
DEV s16x4 pack4(f32x4 v) { u32x2 w = {pk2(v[0], v[1]), pk2(v[2], v[3])}; return __builtin_bit_cast(s16x4, w); }
DEV u64 mix64(u64 z) { z += 0x9E3779B97F4A7C15ull; z = (z ^ (z >> 30)) * 0xBF58476D1CE4E5B9ull; z = (z ^ (z >> 27)) * 0x94D049BB133111EBull; return z ^ (z >> 31); }

struct ConvSched {
    int n[4], f[18], i[18], slot[64];
    constexpr ConvSched(int R) : n{}, f{}, i{}, slot{} {
        constexpr int LEAD = 9;
        const int N0[4] = {0, 3, 4, 7};
        for (int k = 0; k < 4; ++k) n[k] = N0[k] ^ R;
        int c = 0;
        for (int ff = 0; ff < 8; ++ff) for (int k = 3; k >= 0; --k) if (ff <= n[k]) { f[c] = ff; i[c] = k; ++c; }
        for (int k = 0; k < 64; ++k) slot[k] = 1000;
        for (int p = 71; p >= 0; --p) {
            const int e = p / 18, ff = f[p % 18], g = n[i[p % 18]] - ff, s = p - LEAD < -1 ? -1 : p - LEAD;
            slot[e * 16 + ff] = s; slot[e * 16 + 8 + g] = s;
        }
    }
};

#define STAMP(i) do { } while (0)
#define CSTAMP(i) do { } while (0)

struct Args { const u16 *gx, *gwqkv, *gwo, *gbk, *gbv, *gsk, *ghk, *gdv; u16* gout; char* ws; };

template <int R>
DEV void body(const Args& A_, LAS char* const lds, const int wave) {
    const u16* gx = A_.gx; char* ws = A_.ws;
    STAMP(0);
    const int b = blockIdx.x, tid = threadIdx.x, lane = tid & 63;
    const int hd = wave & 3;
    const int grp = (b & 7) * 4 + (b >> 6), mem = (b >> 3) & 7;
    const int T0 = grp * 64;
    const int rot = (mem + 2 * (grp & 3)) & 7;

    u64 epoch;
    asm volatile("s_load_dwordx2 %0, %1, 0x0" : "=s"(epoch) : "s"(ws));
    const u32 dw = *(const u32*)(A_.gdv + 2 * b);
    const rsrc_t r_x = __builtin_amdgcn_make_buffer_rsrc((void*)gx, 0, 2048 * 512 * 2, 0x00020000);
    const rsrc_t r_ws = __builtin_amdgcn_make_buffer_rsrc((void*)ws, 0, WS_END, 0x00020000);
    const rsrc_t r_out = __builtin_amdgcn_make_buffer_rsrc((void*)A_.gout, 0, 2048 * 512 * 2, 0x00020000);
    const u32 ldsbase = (u32)(size_t)lds;
    auto mkdesc = [](const void* p, u32 bytes) { const u64 a = (u64)p; return (u32x4){(u32)a, (u32)(a >> 32) & 0xffffu, bytes, 0x00020000u}; };
    const u32x4 d_x = mkdesc(gx, 2048 * 512 * 2), d_w = mkdesc(A_.gwqkv, 512 * 1536 * 2), d_wo = mkdesc(A_.gwo, 512 * 512 * 2);
    const u32x4 d_k = mkdesc(A_.gbk, 2048 * 512 * 2), d_v = mkdesc(A_.gbv, 2048 * 512 * 2), d_sk = mkdesc(A_.gsk, 512 * 4 * 2);
    const u32x4 d_h = mkdesc(A_.ghk, 512 * 2048 * 2), d_x2 = mkdesc(ws + WS_X2, 2u << 20), d_ws = mkdesc(ws, WS_END);
    const u32 wb1k = ldsbase + wave * 1024, wb256 = ldsbase + wave * 256;
#define DMA_ASM(pre, mods, post) asm volatile("s_add_u32 m0, %0, %4\n\t" pre "buffer_load_dwordx4 %1, %2, %3 offen " mods "lds" post \
                     :: "s"(sbase), "v"(voff), "s"(rs), "s"(soff), "i"(IMM) : "memory", "scc")
    const u32 xmlo = wave == 0 ? ~0xFFu : ~0u;
    auto dma = [&](auto kind, auto imm, u32x4 rs, u32 sbase, u32 voff, u32 soff) {
        constexpr int K = decltype(kind)::value; constexpr int IMM = decltype(imm)::value;
        if constexpr (K == 0) DMA_ASM("s_nop 0\n\t", "", "");
        else if constexpr (K == 1) DMA_ASM("s_mov_b64 exec, 0xffff\n\t", "", "\n\ts_mov_b64 exec, -1");
        else if constexpr (K == 2) {
            const u32 ml = xmlo;
            asm volatile("s_add_u32 m0, %0, %4\n\ts_mov_b32 exec_lo, %5\n\tbuffer_load_dwordx4 %1, %2, %3 offen lds\n\ts_mov_b32 exec_lo, -1"
                         :: "s"(sbase), "v"(voff), "s"(rs), "s"(soff), "i"(IMM), "s"(ml) : "memory", "scc");
        }
        else if constexpr (K == 4) DMA_ASM("s_nop 0\n\t", "sc1 ", "");
        else DMA_ASM("s_mov_b64 exec, 0xffff\n\t", "sc1 ", "\n\ts_mov_b64 exec, -1");
    };
    auto ald4 = [&](u32x4& d, u32x4 rs, u32 voff) { asm volatile("buffer_load_dwordx4 %0, %1, %2, 0 offen" : "=v"(d) : "v"(voff), "s"(rs) : "memory"); };
    auto ald4s = [&](u32x4& d, u32 voff) { asm volatile("buffer_load_dwordx4 %0, %1, %2, 0 offen sc1" : "=v"(d) : "v"(voff), "s"(d_ws) : "memory"); };
    auto ald2s = [&](u32x2& d, u32 voff) { asm volatile("buffer_load_dwordx2 %0, %1, %2, 0 offen sc1" : "=v"(d) : "v"(voff), "s"(d_ws) : "memory"); };

    const int pr = lane >> 3, pc = lane & 7;
    const int xr0 = 8 * wave + pr, xr2 = 64 + 2 * wave + pr;
    const u32 xv0 = (u32)((T0 - 16 + xr0) * 1024 + ((pc ^ ((xr0 >> 1) & 7)) << 4));
    const u32 xv2 = (u32)((T0 - 16 + xr2) * 1024 + ((pc ^ ((xr2 >> 1) & 7)) << 4));
    const int wfw = ((xr0 >> 1) & 1) | (((xr0 >> 3) & 1) << 1);
    const u32 wv0 = (u32)(xr0 * 3072 + mem * 128 + ((((pc >> 1) ^ wfw) * 2 + (pc & 1)) << 4));
    auto issue_piece = [&](auto sc, auto ic) {
        constexpr int s = decltype(sc)::value, i = decltype(ic)::value;
        const u32 kp = (u32)((s + rot) & 7);
        constexpr int sb = (s & 3) * L_SLOT;
        if constexpr (i == 0) dma(IC<R ? 0 : 2>{}, IC<sb>{}, d_x, wb1k, xv0, kp * 128);
        else if constexpr (i == 1) dma(IC<1>{}, IC<sb + 8192>{}, d_x, wb256, xv2, kp * 128);
        else dma(IC<0>{}, IC<sb + 10240 + (i - 2) * 8192>{}, d_w, wb1k, wv0, kp * (64 * 3072) + (i - 2) * 1024);
    };
    auto issue_stage = [&](auto sc) { issue_piece(sc, IC<0>{}); issue_piece(sc, IC<1>{}); issue_piece(sc, IC<2>{}); issue_piece(sc, IC<3>{}); issue_piece(sc, IC<4>{}); };
    auto issue_wo = [&](auto ic) {
        constexpr int i = decltype(ic)::value;
        const int l2 = opq(tid) & 63, pr = l2 >> 3, pc = l2 & 7;
        const u32 wov = (u32)((8 * wave + pr) * 1024 + mem * 128 + ((((pc >> 1) ^ ((pr >> 1) & 3)) * 2 + (pc & 1)) << 4));
        const u32 j = (u32)((i + 2 * grp) & 7);
        dma(IC<0>{}, IC<L_WO>{}, d_wo, wb1k + j * 8192, wov, j * 65536);
    };
    auto issue_kv = [&](int i) {
        const int l2 = opq(tid) & 63, pr = l2 >> 3, pc = l2 & 7;
        const u32 cb = (u32)(mem * 128);
        if (i == 0) dma(IC<0>{}, IC<L_K>{}, d_k, wb1k, (u32)((T0 - 8 + 8 * wave + pr) * 1024) + cb + ((pc ^ wave) << 4), 0);
        else if (i == 1) dma(IC<0>{}, IC<L_V>{}, d_v, wb1k, (u32)((T0 + 8 * wave + pr) * 1024) + cb + ((pc ^ wave) << 4), 0);
        else if (R == 0) dma(IC<0>{}, IC<L_K + 8192>{}, d_k, ldsbase, (u32)((T0 - 8 + 64 + pr) * 1024) + cb + (pc << 4), 0);
        else dma(IC<0>{}, IC<L_SK>{}, d_sk, ldsbase, (u32)(mem * 512 + l2 * 16), 0);
    };

    const int srow = T0 + 8 * mem + wave;
    u32x4 xrow;
    ald4(xrow, d_x, (u32)(srow * 1024 + lane * 16));
    sfor<2, 5>([&](auto ic) { issue_piece(IC<0>{}, ic); });
    issue_kv(0); issue_kv(1); issue_kv(2);
    asm volatile("s_waitcnt lgkmcnt(0)" : "+s"(epoch));
    const u64 tag = mix64(epoch);
    const u32 tl = (u32)tag, th = (u32)(tag >> 32);
    if (R == 1 && wave == 7 && lane < 32) *(LAS u32x4*)(lds + (lane >> 3) * L_SLOT + (lane & 7) * 16) = (u32x4){0x3F803F80u, 0x3F803F80u, 0x3F803F80u, 0x3F803F80u};
    {
        WAIT_VM(6);
        pin(xrow);
        float s = 0.f, q = 0.f;
#pragma unroll
        for (int i = 0; i < 4; ++i) { s = dot2(xrow[i], 0x3F803F80u, s); q = dot2(xrow[i], xrow[i], q); }
        s = rowsum16(s); q = rowsum16(q);
        s = xsum(s); q = xsum(q);
        const float m = s * (1.f / 512.f);
        const float r = __builtin_amdgcn_rsqf(fmaxf(q * (1.f / 512.f) - m * m, 0.f) + 1e-5f);
        const u32x4 sv = {__float_as_uint(m), tl, __float_as_uint(r), th};
        __builtin_amdgcn_raw_buffer_store_b128(sv, r_ws, lane == 0 ? WS_ST0 + (u32)(srow * 16) : 0xFFFFFF00u, 0, 16);
        CFENCE();
    }
    issue_piece(IC<0>{}, IC<0>{}); issue_piece(IC<0>{}, IC<1>{});
    sfor<2, 5>([&](auto ic) { issue_piece(IC<1>{}, ic); });
    issue_piece(IC<1>{}, IC<0>{}); issue_piece(IC<1>{}, IC<1>{});

    auto trr = [&](u32 off) { return __builtin_amdgcn_ds_read_tr16_b64_v4i16((LAS s16x4*)(lds + off)); };
    auto ldsA = [&](u32 off) { return *(LAS s16x8*)(lds + off); };
    auto flag_ok = [&](const u32x2& f) { return __all(f[0] == tl && f[1] == th); };
    auto spin_flags = [&](u32x2& f, u32 off) {
        _Pragma("unroll 1") for (int spin = 0; spin < SPIN_CAP; ++spin) {
            if (flag_ok(f)) break;
            __builtin_amdgcn_s_sleep(2);
            asm volatile("buffer_load_dwordx2 %0, %1, %2, 0 offen sc1\n\ts_waitcnt vmcnt(0)" : "=&v"(f) : "v"(off), "s"(d_ws) : "memory");
        }
    };

    s16x8 Wq[16], Wm[16];
    f32x4 accM[5], accQ[5];
    const f32x4 zero4 = {0.f, 0.f, 0.f, 0.f};
    const s16x8 ones8 = {0x3F80, 0x3F80, 0x3F80, 0x3F80, 0x3F80, 0x3F80, 0x3F80, 0x3F80};
    s16x8 Ab[3][5];
    auto qkv_step = [&](auto lc, int st, const s16x8 (&A)[5], auto fill) {
        sfor<0, 5>([&](auto mc) {
            constexpr int mt = decltype(mc)::value;
            const f32x4 cm = st ? accM[mt] : zero4;
            accM[mt] = R ? mfma32(A[mt], Wm[st], cm) : mfma32(Wm[st], A[mt], cm);
            if constexpr (R == 0 && (mt > 0 || decltype(lc)::value == 0)) accQ[mt] = mfma32(Wq[st], A[mt], st ? accQ[mt] : zero4);
            fill(mc);
        });
    };

    float m1[5], r1[5]; f32x4 m4[5], r4[5], csK, csQ;
    auto load_tab = [&](bool cs) {
        const int l2 = opq(tid) & 63;
        u32 ta = L_TAB + (R ? (l2 >> 4) * 16 : (l2 & 15) * 4);
        pin(ta);
        if (R == 0 && cs) { csK = *(LAS f32x4*)(lds + L_CS + hd * 128 + (l2 >> 4) * 16); csQ = *(LAS f32x4*)(lds + L_CS + hd * 128 + 64 + (l2 >> 4) * 16); }
#pragma unroll
        for (int mt = 0; mt < 5; ++mt) {
            if (R == 0) { m1[mt] = *(LAS float*)(lds + ta + mt * 64); r1[mt] = *(LAS float*)(lds + ta + 320 + mt * 64); }
            else { m4[mt] = *(LAS f32x4*)(lds + ta + mt * 64); r4[mt] = *(LAS f32x4*)(lds + ta + 320 + mt * 64); }
        }
    };

    u32x4 hd0, hd1;
    u32x2 flg;
    {
        const int li = lane & 15, lq = lane >> 4, tq = li >> 2, tp = lane & 3;
        const u32 xa0 = (u32)(li * 128 + ((lq ^ (li >> 1)) << 4));
        const u32 wa = (u32)((8 * lq + tq) * 128 + ((hd ^ (((tq >> 1) & 1) | ((lq & 1) << 1))) << 5) + tp * 8);
        u32 xa, wb;
        u32x4 s0v;
        auto pf_addr = [&](int st) {
            const u32 sb = (u32)((st >> 1) & 3) * L_SLOT;
            xa = sb + (xa0 ^ ((st & 1) * 64)); wb = sb + 10240 + (st & 1) * 4096 + wa;
            asm volatile("" : "+v"(xa), "+v"(wb));
        };
        auto pf_part = [&](int st, int mt) {
            if (mt == 0) Wm[st] = cat8(trr(wb + (1 + R) * 8192), trr(wb + (1 + R) * 8192 + 512));
            Ab[st & 1][mt] = ldsA(xa + mt * 2048);
            if (R == 0 && mt == 1) Wq[st] = cat8(trr(wb), trr(wb + 512));
        };
        STAMP(15);
        WAIT_VM(5);
        BAR();
        STAMP(16);
        pf_addr(0);
#pragma unroll
        for (int mt = 0; mt < 5; ++mt) pf_part(0, mt);
        sfor<0, 16>([&](auto stc) {
            constexpr int st = decltype(stc)::value;
            if constexpr ((st & 1) && st < 15) {
                constexpr int s1 = (st >> 1) + 1;
                constexpr int WN[8] = {0, 6, 6, 5, 5, 5, 6, 4};
                WAIT_VM(WN[s1]);
                if constexpr (s1 == 7) {
                    asm volatile("" : "+v"(s0v));
                    if (R == 0 || wave == 4) {
                        const int l2 = opq(tid) & 63;
                        if (T0 - 16 + wave * 16 >= 0) {
                            _Pragma("unroll 1") for (int spin = 0; spin < SPIN_CAP; ++spin) {
                                if (__all(s0v[1] == tl && s0v[3] == th)) break;
                                __builtin_amdgcn_s_sleep(2);
                                asm volatile("buffer_load_dwordx4 %0, %1, %2, 0 offen sc1\n\ts_waitcnt vmcnt(0)" : "=&v"(s0v) : "v"(WS_ST0 + (u32)((T0 - 16 + wave * 16 + (l2 & 15)) * 16)), "s"(d_ws) : "memory");
                            }
                        } else { s0v[0] = 0u; s0v[2] = 0x3F800000u; }
                        if (l2 < 16) { *(LAS u32*)(lds + L_TAB + wave * 64 + l2 * 4) = s0v[0]; *(LAS u32*)(lds + L_TAB + 320 + wave * 64 + l2 * 4) = s0v[2]; }
                        asm volatile("s_waitcnt lgkmcnt(0)" ::: "memory");
                    }
                }
                RBAR();
                STAMP(16 + s1);
                if constexpr (s1 == 3) {
                    if (R == 0 && tid == 0) __builtin_amdgcn_raw_buffer_store_b64((u32x2){tl, th}, r_ws, WS_F1 + (mem * 32 + grp) * 128, 0, 16);
                    CFENCE();
                }
                if constexpr (s1 == 5) {
                    const int row = T0 - 16 + wave * 16 + (opq(tid) & 15);
                    ald4s(s0v, WS_ST0 + (u32)(max(row, 0) * 16));
                }
            }
            if (st < 15) pf_addr(st + 1);
            if constexpr (st == 14) load_tab(false);
            SB();
            qkv_step(IC<0>{}, st, Ab[st & 1], [&](auto mc) {
                constexpr int mt = decltype(mc)::value;
                if (st < 15) pf_part(st + 1, mt);
                if constexpr (st == 0) issue_piece(IC<2>{}, mc);
                if constexpr (st >= 1 && st <= 10) {
                    constexpr int sn = ((st - 1) >> 1) + 3;
                    if constexpr ((st - 1) & 1) { if constexpr (mt == 1) issue_piece(IC<sn>{}, IC<3>{}); if constexpr (mt == 3) issue_piece(IC<sn>{}, IC<4>{}); }
                    else if constexpr (!(mt & 1)) issue_piece(IC<sn>{}, IC<(mt >> 1)>{});
                }
                if constexpr (st == 11 && mt == 0) { const u32 ho = (u32)(b * 8192 + opq(tid) * 16); ald4(hd0, d_h, ho); ald4(hd1, d_h, ho + 16); }
                if constexpr (st == 11 && (mt == 2 || mt == 4)) {
                    const int l2 = opq(tid) & 63;
                    dma(IC<0>{}, IC<L_DUMMY>{}, d_wo, ldsbase, (u32)((l2 >> 3) * 1024 + (l2 & 7) * 16), (u32)(((grp & 3) * 128 + wave * 16 + (mt - 2) * 4) * 1024 + mem * 128));
                }
                if constexpr (R == 1 && st == 14 && mt == 4) { if (wave == 7) ald2s(flg, WS_F1 + (u32)(((b >> 5) * 32 + (opq(tid) & 31)) * 128)); }
                SB();
            });
            SB();
            if constexpr (st == 0) {
                const int l2 = opq(tid) & 63, tg = l2 & 7, chl = l2 >> 3;
                const u32x2 skw = *(LAS u32x2*)(lds + L_SK + (8 * wave + chl) * 8);
                u32 ka = L_K + tg * 1024 + (((wave ^ tg) & 7) << 4) + chl * 2;
                u32 kb = L_K + tg * 1024 + (((wave ^ (tg + 1)) & 7) << 4) + chl * 2;
                asm volatile("" : "+v"(ka), "+v"(kb));
                u16 kh[11], vh[8];
#pragma unroll
                for (int i = 0; i < 11; ++i) kh[i] = *(LAS u16*)(lds + ((i + 5 < 8) ? ka : kb) + (i + 5) * 128);
#pragma unroll
                for (int i = 0; i < 8; ++i) vh[i] = *(LAS u16*)(lds + ka + (L_V - L_K) + i * 128);
                SB();
                const float a0 = bflo(skw[0]), a1 = bfhi(skw[0]), a2 = bflo(skw[1]), a3 = bfhi(skw[1]);
                float ua[8];
#pragma unroll
                for (int i = 0; i < 8; ++i) ua[i] = (bflo(kh[i]) * a3 + bflo(kh[i + 1]) * a2 + bflo(kh[i + 2]) * a1 + bflo(kh[i + 3]) * a0) * bflo(vh[i]);
                const u32x4 oa = {pk2(ua[7], ua[6]), pk2(ua[5], ua[4]), pk2(ua[3], ua[2]), pk2(ua[1], ua[0])};
                __builtin_amdgcn_raw_buffer_store_b128(oa, r_ws, WS_UT + (u32)((mem * 64 + 8 * wave + chl) * 4096 + (2040 - T0 - 8 * tg) * 2), 0, 16);
                CFENCE();
                SB();
            }
        });
    }
    STAMP(R ? 1 : 53);

    u32x4 uu;
    float csV;
    auto attention = [&](auto lc) {
        constexpr int LAYER = decltype(lc)::value;
        constexpr int SBASE = LAYER ? 32 : 28;
        const int lane = opq(tid) & 63, li = lane & 15, lq = lane >> 4;
        u32 ex = L_EX + hd * 2048 + lane * 8;
        pin(ex);
        u32 f1ok = 1;
        if constexpr (R == 0) {
            if constexpr (LAYER == 0) {
#pragma unroll
                for (int i = 0; i < 4; ++i) { csK[i] = bcast_row0(accM[0][i]); csQ[i] = bcast_row0(accQ[0][i]); }
                if (li == 0) { *(LAS f32x4*)(lds + L_CS + hd * 128 + lq * 16) = csK; *(LAS f32x4*)(lds + L_CS + hd * 128 + 64 + lq * 16) = csQ; }
            }
            const int par = li & 1;
            const bool cur0 = (4 * lq + par) <= li, cur1 = (4 * lq + par + 2) <= li;
            s16x4 Kb[5], Qb[5];
#pragma unroll
            for (int mt = 0; mt < 5; ++mt) {
                Kb[mt] = pack4((accM[mt] - m1[mt] * csK) * r1[mt]);
                if (mt) Qb[mt] = pack4(accQ[mt] - m1[mt] * csQ);
            }
            f32x4 Sc[5], Sp[5];
#pragma unroll
            for (int mt = 1; mt < 5; ++mt) { Sc[mt] = mfma16(Kb[mt], Qb[mt], zero4); Sp[mt] = mfma16(Kb[mt - 1], Qb[mt], zero4); }
            float s0[5], s1[5], mx[5];
#pragma unroll
            for (int mt = 1; mt < 5; ++mt) {
                const float sp0 = Sp[mt][0], sp1 = Sp[mt][1], sp2 = Sp[mt][2], sp3 = Sp[mt][3], c0 = Sc[mt][0], c1 = Sc[mt][1], c2 = Sc[mt][2], c3 = Sc[mt][3];
                float p0 = par ? sp1 : sp0, p1 = par ? sp3 : sp2, q0 = par ? c1 : c0, q1 = par ? c3 : c2;
                asm volatile("" : "+v"(p0), "+v"(p1), "+v"(q0), "+v"(q1));
                if (mt == 1 && grp == 0) { p0 = -3.0e38f; p1 = -3.0e38f; }
                s0[mt] = cur0 ? q0 : p0; s1[mt] = cur1 ? q1 : p1;
                mx[mt] = vmax(s0[mt], s1[mt]);
            }
#pragma unroll
            for (int mt = 1; mt < 5; ++mt) { auto r = __builtin_amdgcn_permlane32_swap(__float_as_uint(mx[mt]), __float_as_uint(mx[mt]), false, false); mx[mt] = vmax(__uint_as_float(r[0]), __uint_as_float(r[1])); }
#pragma unroll
            for (int mt = 1; mt < 5; ++mt) { auto r = __builtin_amdgcn_permlane16_swap(__float_as_uint(mx[mt]), __float_as_uint(mx[mt]), false, false); mx[mt] = vmax(__uint_as_float(r[0]), __uint_as_float(r[1])); }
#pragma unroll
            for (int mt = 1; mt < 5; ++mt) {
                const float sc = r1[mt] * (0.25f * 1.44269504088896341f);
                const float e0 = __builtin_amdgcn_exp2f((s0[mt] - mx[mt]) * sc), e1 = __builtin_amdgcn_exp2f((s1[mt] - mx[mt]) * sc);
                *(LAS u32x2*)(lds + ex + (mt - 1) * 512) = (u32x2){pk2(e0, e0), pk2(e1, e1)};
            }
            STAMP(SBASE + 2);
            BAR();
            STAMP(SBASE + 1);
            if constexpr (LAYER == 0) f1ok = __builtin_amdgcn_readfirstlane(*(LAS u32*)(lds + L_FLG));
        } else {
            if constexpr (LAYER == 0) {
                auto s = __builtin_amdgcn_permlane16_swap(__float_as_uint(accM[0][0]), __float_as_uint(accM[0][0]), false, false);
                auto t = __builtin_amdgcn_permlane32_swap(s[0], s[0], false, false);
                csV = __uint_as_float(t[0]);
                if (wave == 7) { WAIT_VM(0); pin(flg); const u32 ok = flag_ok(flg); *(LAS u32*)(lds + L_FLG) = ok; }
            }
            const int par = li & 1;
            const bool cur0 = (4 * lq + par) <= li, cur1 = (4 * lq + par + 2) <= li;
            const u32 pm = par ? 0xffff0000u : 0x0000ffffu;
            u32 mC0 = cur0 ? pm : 0u, mP0 = cur0 ? 0u : pm, mC1 = cur1 ? pm : 0u, mP1 = cur1 ? 0u : pm;
            u32 oo = (LAYER ? WS_O2 : WS_O1) + (u32)(4 * grp * 16384 + (4 * mem + hd) * 512 + lane * 8);
            asm volatile("" : "+v"(mC0), "+v"(mP0), "+v"(mC1), "+v"(mP1), "+v"(oo));
            s16x4 Vb[5];
#pragma unroll
            for (int mt = 0; mt < 5; ++mt) { Vb[mt] = pack4((accM[mt] - m4[mt] * csV) * r4[mt]); pin(Vb[mt]); }
            STAMP(SBASE);
            BAR();
            STAMP(SBASE + 1);
            u32x2 pw[4];
#pragma unroll
            for (int j = 0; j < 4; ++j) pw[j] = *(LAS u32x2*)(lds + ex + j * 512);
            if constexpr (LAYER == 0) f1ok = *(LAS u32*)(lds + L_FLG);
            SB();
            f32x4 O[4], L[4];
#pragma unroll
            for (int j = 0; j < 4; ++j) {
                const s16x8 P = __builtin_bit_cast(s16x8, (u32x4){pw[j][0] & mC0, pw[j][1] & mC1, pw[j][0] & mP0, pw[j][1] & mP1});
                L[j] = mfma32(ones8, P, zero4);
                O[j] = mfma32(cat8(Vb[j + 1], Vb[j]), P, zero4);
            }
#pragma unroll
            for (int j = 0; j < 4; ++j) {
                const float inv = __builtin_amdgcn_rcpf(L[j][0]);
                const u32x2 ow = {nosent(pk2(O[j][0] * inv, O[j][1] * inv)), nosent(pk2(O[j][2] * inv, O[j][3] * inv))};
                __builtin_amdgcn_raw_buffer_store_b64(ow, r_ws, oo, j * 16384, 16);
            }
            f1ok = __builtin_amdgcn_readfirstlane(f1ok);
            CFENCE();
        }
        if constexpr (LAYER == 0) { if (f1ok) ald4s(uu, WS_UT + (u32)(b * 8192 + opq(tid) * 16)); }
        SB();
        return f1ok;
    };
    const u32 f1ok = attention(IC<0>{});
    {
        const u32x4 sv = {SENT, SENT, SENT, SENT};
        const int t2 = opq(tid);
        const u32 tb = (u32)((4 * grp + hd) * 16384 + mem * 2048 + R * 1024 + (t2 & 63) * 16);
        __builtin_amdgcn_raw_buffer_store_b128(sv, r_ws, tb, WS_O2, 16);
        __builtin_amdgcn_raw_buffer_store_b128(sv, r_ws, tb, WS_X2, 16);
        __builtin_amdgcn_raw_buffer_store_b128(sv, r_ws, (u32)(b * 8192 + t2 * 16), WS_Y2, 16);
        CFENCE();
        SB();
    }
    STAMP(2);

    const int TI = 4 * grp + hd;
    u32x2 of[16];
    auto load_o = [&](u32 ws_o) {
        const int lane = opq(tid) & 63;
#pragma unroll
        for (int i = 0; i < 16; ++i) of[i] = __builtin_amdgcn_raw_buffer_load_b64(r_ws, ws_o + (u32)(TI * 16384 + R * 8192 + lane * 8) + (i & 7) * 512, (i >> 3) * 4096, 16);
    };
    u32 xw[8], yw[8];
    u32 woa[4];
    s16x8 F[8][4];
    auto wo_addr = [&]() {
        const int l2 = opq(tid) & 63, lq = l2 >> 4, tq = (l2 & 15) >> 2, tp = l2 & 3;
#pragma unroll
        for (int n = 0; n < 4; ++n) woa[n] = L_WO + (u32)(R * 32768 + (4 * lq + tq) * 128 + ((n ^ (((lq & 1) << 1) | (tq >> 1))) << 5) + tp * 8);
    };
    auto load_F = [&](int ks) {
#pragma unroll
        for (int n = 0; n < 4; ++n) F[ks][n] = cat8(trr(woa[n] + ks * 4096), trr(woa[n] + ks * 4096 + 2048));
    };

    {
        const int t2 = opq(tid), lane = t2 & 63, li = lane & 15, lq = lane >> 4;
        constexpr u32 OFFS = 0xEDA85410u;
        {
            WAIT_VM(R ? 7 : 3);
            pin(hd0); pin(hd1);
            const int j = t2 & 255;
            u32 cb = L_HC + R * 34816 + (j + 2) * 16;
            asm volatile("" : "+v"(cb));
            const u32 d[8] = {hd0[0], hd0[1], hd0[2], hd0[3], hd1[0], hd1[1], hd1[2], hd1[3]};
            u32 e[7];
#pragma unroll
            for (int i = 0; i < 7; ++i) e[i] = __builtin_amdgcn_alignbit(d[i + 1], d[i], 16);
            sfor<0, 8>([&](auto pc) {
                constexpr int p = decltype(pc)::value;
                constexpr u32 po = (272 * p + ((OFFS >> (4 * p)) & 15)) * 16;
                constexpr int h = p >> 1;
                const u32x4 w = (p & 1) ? (u32x4){e[h], e[h + 1], e[h + 2], e[h + 3]} : (u32x4){d[h], d[h + 1], d[h + 2], d[h + 3]};
                *(LAS u32x4*)(lds + cb + po) = w;
            });
            if (hd == 0) {
                const u32 h0 = __builtin_amdgcn_readfirstlane(d[0]), h1 = __builtin_amdgcn_readfirstlane(d[1]);
                const u32 h2 = __builtin_amdgcn_readfirstlane(d[2]), h3 = __builtin_amdgcn_readfirstlane(d[3]);
                if (lane < 16) {
                    const int p = lane & 7;
                    const u32 po = (272 * p + ((OFFS >> (4 * p)) & 15)) * 16;
                    const unsigned __int128 hv = ((unsigned __int128)(((u64)h3 << 32) | h2) << 64) | (((u64)h1 << 32) | h0);
                    const unsigned __int128 sv = (p == 0 || lane < 8) ? (unsigned __int128)0 : (hv << (16 * (8 - p)));
                    const u32x4 w = {(u32)sv, (u32)(sv >> 32), (u32)(sv >> 64), (u32)(sv >> 96)};
                    *(LAS u32x4*)(lds + L_HC + R * 34816 + po + (lane >> 3) * 16) = w;
                }
            }
            if (hd == 1 && lane < 30) *(LAS u32x4*)(lds + L_UR + R * 4576 + 4096 + lane * 16) = (u32x4){0, 0, 0, 0};
            STAMP(3);
            if (!f1ok) {
                const u32 fo = WS_F1 + (u32)(((b >> 5) * 32 + 31 - hd * 8 - (lane & 7)) * 128);
                flg = (u32x2){0u, 0u};
                spin_flags(flg, fo);
                ald4s(uu, WS_UT + (u32)(b * 8192 + t2 * 16));
            }
            asm volatile("s_waitcnt vmcnt(0)" : "+v"(uu) :: "memory");
            *(LAS u32x4*)(lds + L_UR + R * 4576 + j * 16) = uu;
        }
        BAR();
        if (R == 0 && tid == 0) __builtin_amdgcn_raw_buffer_store_b64((u32x2){tl, th}, r_ws, WS_F2 + (grp * 8 + mem) * 128, 0, 16);
        STAMP(4);
        const int ch = hd >> 1, e0 = (hd & 1) * 4;
        const int p = (li + 1) & 7;
        u32 aA = L_HC + ch * 34816 + (272 * p + ((OFFS >> (4 * p)) & 15) + ((li + 1) >> 3) + lq + 4 * e0) * 16;
        u32 aB = L_UR + ch * 4576 + (254 - 224 - 2 * li + lq + 4 * e0) * 16;
        asm volatile("" : "+v"(aA), "+v"(aB));
        f32x4 y[4];
        const u32 f2own = WS_F2 + (u32)((grp * 8 + 4 * R + (lane & 3)) * 128);
        u32 ooff = WS_O1 + (u32)(TI * 16384 + R * 8192 + lane * 8);
        bool early = false;
        {
            constexpr ConvSched S(R);
            s16x8 A[4][8], B[4][8];
            auto loads_at = [&](auto pc) {
                constexpr int p = decltype(pc)::value;
                sfor<0, 64>([&](auto ic) {
                    constexpr int i = decltype(ic)::value, e = i >> 4, f = i & 7;
                    if constexpr (S.slot[i] == p) {
                        if constexpr (i & 8) B[e][f] = ldsA(aB + 64 * e + 512 * (7 - f)); else A[e][f] = ldsA(aA + 64 * (8 * f + e));
                    }
                });
            };
            loads_at(IC<-1>{});
            SB();
            sfor<0, 72>([&](auto pc) {
                constexpr int p = decltype(pc)::value, e = p / 18, f = S.f[p % 18], i = S.i[p % 18], g = S.n[i] - f;
                y[i] = mfma32(A[e][f], B[e][g], (e == 0 && f == 0) ? zero4 : y[i]);
                loads_at(pc);
                if constexpr (p < 24 && (p % 3) == 1) issue_wo(IC<(p / 3)>{});
                if constexpr (p == 36) ald2s(flg, f2own);
                if constexpr (p == 54) { WAIT_VM(0); pin(flg); early = flag_ok(flg); }
                if constexpr (p >= 55 && p < 71) {
                    if (early) ald2o<((p - 55) & 7) * 512>(of[p - 55], ooff + ((p - 55) >> 3) * 4096, d_ws);
                }
                SB();
            });
        }
        STAMP(14);
        RBAR();
        {
            constexpr int N0[4] = {0, 3, 4, 7};
            u32 ra = L_HC + hd * 8192 + lane * 16;
            asm volatile("" : "+v"(ra));
#pragma unroll
            for (int i = 0; i < 4; ++i) *(LAS f32x4*)(lds + ra + (N0[i] ^ R) * 1024) = y[i];
        }
        SB();
        if constexpr (R == 0) {
#pragma unroll
            for (int n = 0; n < 4; ++n) { const u32x2 w = __builtin_amdgcn_raw_buffer_load_b64(r_x, (u32)((TI * 16 + li) * 1024 + mem * 128 + lq * 8) + n * 32, 0, 0); xw[2 * n] = w[0]; xw[2 * n + 1] = w[1]; }
        }
        u32 f2oth = 0; u32x2 flg2;
        if constexpr (R == 1) {
            if (wave == 7) {
                const int pb = 32 * mem + (lane & 31);
                f2oth = WS_F2 + (u32)((lane < 32 ? ((pb & 7) * 4 + (pb >> 6)) * 8 + ((pb >> 3) & 7) : max(grp * 8 - 8, 0) + (lane & 15)) * 128);
                CFENCE();
                ald2s(flg2, f2oth);
            }
        }
        wo_addr();
        SB();
        STAMP(37);
        BAR();
        STAMP(38);
        {
            const float D0 = bflo(dw), D1 = bfhi(dw);
            u32 ra = L_HC + wave * 1024 + lane * 16;
            const int tb = 256 * wave + 16 * li + 4 * lq;
            u32 ru = L_UR + (2044 - tb) * 2;
            asm volatile("" : "+v"(ra), "+v"(ru));
            const f32x4 y0 = *(LAS f32x4*)(lds + ra), y1 = *(LAS f32x4*)(lds + ra + 8192), y2 = *(LAS f32x4*)(lds + ra + 16384), y3 = *(LAS f32x4*)(lds + ra + 24576);
            const u32x2 ua = *(LAS u32x2*)(lds + ru), ub = *(LAS u32x2*)(lds + ru + 4576);
            load_F(0); load_F(1);
            SB();
            const f32x4 ya = y0 + y1, yb = y2 + y3;
            const u32x4 w = {nosent(pk2(ya[0] + bfhi(ua[1]) * D0, yb[0] + bfhi(ub[1]) * D1)), nosent(pk2(ya[1] + bflo(ua[1]) * D0, yb[1] + bflo(ub[1]) * D1)),
                             nosent(pk2(ya[2] + bfhi(ua[0]) * D0, yb[2] + bfhi(ub[0]) * D1)), nosent(pk2(ya[3] + bflo(ua[0]) * D0, yb[3] + bflo(ub[0]) * D1))};
            __builtin_amdgcn_raw_buffer_store_b128(w, r_ws, WS_Y2 + (u32)(b * 8192 + tb * 4), 0, 16);
        }
        SB();
        STAMP(5);
        if (!early) {
            spin_flags(flg, f2own);
            sfor<0, 16>([&](auto ic) { constexpr int i = decltype(ic)::value;
                ald2o<(i & 7) * 512>(of[i], ooff + (i >> 3) * 4096, d_ws); });
            WAIT_VM(0);
        }
        WAIT_VM(1);
        sfor<0, 16>([&](auto ic) { pin(of[decltype(ic)::value]); });
        STAMP(40);

        f32x4 acc[4];
#pragma unroll
        for (int ks = 0; ks < 8; ++ks) {
            if (ks + 2 < 8) load_F(ks + 2);
            SB();
#pragma unroll
            for (int n = 0; n < 4; ++n) acc[n] = mfma32(F[ks][n], __builtin_bit_cast(s16x8, (u32x4){of[2 * ks][0], of[2 * ks][1], of[2 * ks + 1][0], of[2 * ks + 1][1]}), ks ? acc[n] : zero4);
            SB();
        }
        STAMP(41);
        accM[0] = acc[0]; accM[1] = acc[1]; accM[2] = acc[2]; accM[3] = acc[3];
        if constexpr (R == 1) { if (wave == 7) { asm volatile("" : "+v"(flg2)); spin_flags(flg2, f2oth); } }
    }

    auto load_y = [&]() {
        const int l2 = opq(tid) & 63;
#pragma unroll
        for (int i = 0; i < 8; ++i)
            yw[i] = __builtin_amdgcn_raw_buffer_load_b32(r_ws, WS_Y2 + (u32)((32 * mem + 2 * (l2 >> 4)) * 8192 + (TI * 16 + (l2 & 15)) * 4), (8 * (i >> 1) + (i & 1)) * 8192, 16);
    };
    auto pub_stat = [&](u32 ws_st, float s, float q) {
        const int lane = opq(tid) & 63;
        s = xsum(s); q = xsum(q);
        const u32x4 sv = {__float_as_uint(s), tl, __float_as_uint(q), th};
        __builtin_amdgcn_raw_buffer_store_b128(sv, r_ws, lane < 16 ? ws_st + (u32)((TI * 8 + mem) * 256 + lane * 16) : 0xFFFFFF00u, 0, 16);
    };
    auto stat_off = [&](u32 ws_st, int ti) { const int l2 = opq(tid) & 63; return ws_st + (u32)(ti * 2048 + (l2 >> 4) * 512 + (l2 & 15) * 16); };
    auto fin_stat = [&](u32x4& a, u32x4& c, u32 off, float& mean, float& rstd) {
        _Pragma("unroll 1") for (int spin = 0; spin < SPIN_CAP; ++spin) {
            if (__all(((a[1] ^ tl) | (a[3] ^ th) | (c[1] ^ tl) | (c[3] ^ th)) == 0)) break;
            __builtin_amdgcn_s_sleep(1);
            asm volatile("buffer_load_dwordx4 %0, %2, %3, 0 offen sc1\n\tbuffer_load_dwordx4 %1, %2, %3, 0 offen offset:256 sc1\n\ts_waitcnt vmcnt(0)"
                         : "=&v"(a), "=&v"(c) : "v"(off), "s"(d_ws) : "memory");
        }
        const float ts = xsum(__uint_as_float(a[0]) + __uint_as_float(c[0])), tq = xsum(__uint_as_float(a[2]) + __uint_as_float(c[2]));
        mean = ts * (1.f / 512.f);
        rstd = __builtin_amdgcn_rsqf(fmaxf(tq * (1.f / 512.f) - mean * mean, 0.f) + 1e-5f);
    };
    auto wo_finish = [&](auto lc) {
        constexpr int LAYER = decltype(lc)::value;
        const int lane = opq(tid) & 63, li = lane & 15, lq = lane >> 4;
        u32 wx = L_WX + hd * 4096 + lane * 16;
        asm volatile("" : "+v"(wx));
        if constexpr (R == 1) {
#pragma unroll
            for (int n = 0; n < 4; ++n) *(LAS f32x4*)(lds + wx + n * 1024) = accM[n];
        }
        BAR();
        STAMP(LAYER ? 25 : 42);
        if constexpr (R == 0) {
            f32x4 t[4];
#pragma unroll
            for (int n = 0; n < 4; ++n) t[n] = *(LAS f32x4*)(lds + wx + n * 1024);
            if constexpr (!LAYER) load_y();
            const u32 so = stat_off(LAYER ? WS_ST3 : WS_ST1, TI);
            SB();
            float s = 0.f, q = 0.f, s2 = 0.f, q2 = 0.f;
#pragma unroll
            for (int n = 0; n < 4; ++n) {
                const f32x4 v = accM[n] + t[n];
                const u32 w0 = pk2(bflo(xw[2 * n]) + v[0], bfhi(xw[2 * n]) + v[1]), w1 = pk2(bflo(xw[2 * n + 1]) + v[2], bfhi(xw[2 * n + 1]) + v[3]);
                xw[2 * n] = w0; xw[2 * n + 1] = w1;
                s = dot2(w0, 0x3F803F80u, s); q = dot2(w0, w0, q); s2 = dot2(w1, 0x3F803F80u, s2); q2 = dot2(w1, w1, q2);
            }
            pub_stat(LAYER ? WS_ST3 : WS_ST1, s + s2, q + q2);
            STAMP(LAYER ? 46 : 43);
            WAIT_VM(0);
            STAMP(LAYER ? 26 : 44);
            u32x4 a = {0, 0, 0, 0}, c = {0, 0, 0, 0};
            float mean, rstd;
            fin_stat(a, c, so, mean, rstd);
            STAMP(LAYER ? 11 : 6);
            if constexpr (!LAYER) {
                _Pragma("unroll 1") for (int spin = 0; spin < SPIN_CAP; ++spin) {
                    if (__all(max(max(max(yw[0], yw[1]), max(yw[2], yw[3])), max(max(yw[4], yw[5]), max(yw[6], yw[7]))) != SENT)) break;
                    __builtin_amdgcn_s_sleep(1);
                    CFENCE();
                    load_y();
                }
            }
            STAMP(47);
            u32 w[8];
            const float nm = -mean * rstd;
#pragma unroll
            for (int i = 0; i < 8; ++i) {
                const float a = bflo(xw[i]), c = bfhi(xw[i]);
                w[i] = pk2(a + (a * rstd + nm) * bflo(yw[i]), c + (c * rstd + nm) * bfhi(yw[i]));
                if (!LAYER) w[i] = nosent(w[i]);
            }
#pragma unroll
            for (int j = 0; j < 2; ++j) {
                auto r0 = __builtin_amdgcn_permlane16_swap(w[4 * j], w[4 * j + 2], false, false);
                auto r1 = __builtin_amdgcn_permlane16_swap(w[4 * j + 1], w[4 * j + 3], false, false);
                const u32x4 o = {r0[0], r1[0], r0[1], r1[1]};
                const int c = 4 * j + 2 * (lq & 1) + (lq >> 1);
                if constexpr (LAYER) __builtin_amdgcn_raw_buffer_store_b128(o, r_out, (u32)((TI * 16 + li) * 1024 + mem * 128 + c * 16), 0, OUT_AUX);
                else __builtin_amdgcn_raw_buffer_store_b128(o, r_ws, WS_X2 + (u32)((TI * 8 + mem) * 2048 + li * 128 + ((c ^ (li >> 1)) << 4)), 0, 16);
            }
            if constexpr (!LAYER) {
                float s = 0.f, q = 0.f, s2 = 0.f, q2 = 0.f;
#pragma unroll
                for (int i = 0; i < 8; i += 2) {
                    xw[i] = w[i]; xw[i + 1] = w[i + 1];
                    s = dot2(w[i], 0x3F803F80u, s); q = dot2(w[i], w[i], q); s2 = dot2(w[i + 1], 0x3F803F80u, s2); q2 = dot2(w[i + 1], w[i + 1], q2);
                    asm volatile("" : "+v"(xw[i]), "+v"(xw[i + 1]), "+v"(yw[i]), "+v"(yw[i + 1]));
                }
                pub_stat(WS_ST2, s + s2, q + q2);
                STAMP(48);
                WAIT_VM(0);
            }
        }
    };
    wo_finish(IC<0>{});
    RBAR();
    STAMP(7);

    {
        const int l3 = opq(tid) & 63;
        const u32 g0 = (u32)((4 * grp - 1 + (wave >> 1)) * 16384 + (wave & 1) * 1024 + l3 * 16);
        const u32 g2 = (u32)((4 * grp + 3) * 16384 + wave * 256 + l3 * 16);
        auto x2_piece = [&](auto ic) {
            constexpr int i = decltype(ic)::value, s = i >> 1;
            const u32 kp = (u32)((s + rot) & 7);
            if constexpr (!(i & 1)) dma(IC<4>{}, IC<L_CX + s * 10240>{}, d_x2, wb1k, g0, kp * 2048);
            else dma(IC<5>{}, IC<L_CX + s * 10240 + 8192>{}, d_x2, wb256, g2, kp * 2048);
        };
        {
            const u32 po = (l3 & 8) ? (u32)((4 * grp + 3) * 16384 + wave * 256 + 252 + (l3 & 7) * 2048) : (u32)((4 * grp - 1 + (wave >> 1)) * 16384 + (wave & 1) * 1024 + 1020 + (l3 & 7) * 2048);
            _Pragma("unroll 1") for (int spin = 0; spin < SPIN_CAP; ++spin) {
                u32 pv;
                asm volatile("buffer_load_dword %0, %1, %2, 0 offen sc1\n\ts_waitcnt vmcnt(0)" : "=&v"(pv) : "v"(po), "s"(d_x2) : "memory");
                if (__all(pv != SENT)) break;
                __builtin_amdgcn_s_sleep(1);
            }
        }
        STAMP(39);
        u32 va = L_CX + wave * 1024 + l3 * 16, vq = L_CX + 8192 + wave * 256 + (l3 & 15) * 16;
        asm volatile("" : "+v"(va), "+v"(vq));
        u32x4 vr[4];
        auto ver_read = [&](auto cc) {
            constexpr int c = decltype(cc)::value;
            u32 a = va + c * 20480, q = vq + c * 20480;
            asm volatile("" : "+v"(a), "+v"(q) :: "memory");
            vr[0] = *(LAS u32x4*)(lds + a); vr[1] = *(LAS u32x4*)(lds + a + 10240); vr[2] = *(LAS u32x4*)(lds + q); vr[3] = *(LAS u32x4*)(lds + q + 10240);
        };
        auto ver_check = [&](auto cc) {
            constexpr int c = decltype(cc)::value;
            _Pragma("unroll 1") for (int spin = 0; spin < SPIN_CAP; ++spin) {
                const u32 m0 = max(max(max(vr[0][0], vr[0][1]), max(vr[0][2], vr[0][3])), max(max(vr[1][0], vr[1][1]), max(vr[1][2], vr[1][3])));
                const u32 m1 = max(max(max(vr[2][0], vr[2][1]), max(vr[2][2], vr[2][3])), max(max(vr[3][0], vr[3][1]), max(vr[3][2], vr[3][3])));
                if (__all(max(m0, m1) != SENT)) break;
                __builtin_amdgcn_s_sleep(1);
                sfor<4 * c, 4 * c + 4>([&](auto ic) { x2_piece(ic); });
                WAIT_VM(0);
                ver_read(cc);
            }
        };
        sfor<0, 8>([&](auto ic) { x2_piece(ic); });
        const int sti = max(4 * grp - 1 + min(wave, 4), 0);
        const u32 so = stat_off(WS_ST2, sti);
        const u32 xb0 = (u32)((l3 & 15) * 128 + (((l3 >> 4) ^ ((l3 & 15) >> 1)) << 4));
        WAIT_VM(4);
        STAMP(8);
        ver_read(IC<0>{});
        ver_check(IC<0>{});
        RBAR();
        STAMP(13);
        u32 xa;
        u32x4 sa, sc;
        auto pf_addr = [&](int st) { xa = L_CX + (st >> 1) * 10240 + (xb0 ^ ((st & 1) * 64)); asm volatile("" : "+v"(xa)); };
        pf_addr(0);
#pragma unroll
        for (int mt = 0; mt < 5; ++mt) Ab[0][mt] = ldsA(xa + mt * 2048);
        ald4s(sa, so); ald4s(sc, so + 256);
        sfor<0, 16>([&](auto stc) {
            constexpr int st = decltype(stc)::value;
            if constexpr (st == 2) { WAIT_VM(10); ver_read(IC<1>{}); }
            if constexpr (st == 3) { ver_check(IC<1>{}); RBAR(); STAMP(49); }
            if constexpr (st == 5) { WAIT_VM(4); ver_read(IC<2>{}); }
            if constexpr (st == 6) {
                ver_check(IC<2>{}); WAIT_VM(0); ver_read(IC<3>{});
                if (R == 0 || wave == 4) {
                    asm volatile("" : "+v"(sa), "+v"(sc));
                    float mean = 0.f, rstd = 1.f;
                    if (4 * grp - 1 + wave >= 0) fin_stat(sa, sc, so, mean, rstd);
                    if (l3 < 16) { *(LAS float*)(lds + L_TAB + wave * 64 + l3 * 4) = mean; *(LAS float*)(lds + L_TAB + 320 + wave * 64 + l3 * 4) = rstd; }
                }
            }
            if constexpr (st == 7) { ver_check(IC<3>{}); RBAR(); STAMP(50); CSTAMP(60); }
            if (st < 15) pf_addr(st + 1);
            if constexpr (st == 14) load_tab(true);
            SB();
            qkv_step(IC<1>{}, st, Ab[st & 1], [&](auto mc) {
                constexpr int mt = decltype(mc)::value;
                if (st < 15) Ab[(st + 1) & 1][mt] = ldsA(xa + mt * 2048);
                if constexpr (st < 2 && mt < 4) x2_piece(IC<8 + 4 * st + mt>{});
                SB();
            });
        });
    }
    STAMP(R ? 9 : 52);
    CSTAMP(61);
    attention(IC<1>{});
    STAMP(27);

    {
        wo_addr();
#pragma unroll
        for (int ks = 0; ks < 8; ++ks) load_F(ks);
        SB();
        WAIT_VM(0);
        RBAR();
        STAMP(10);
        load_o(WS_O2);
        SB();
        _Pragma("unroll 1") for (int spin = 0; spin < 64; ++spin) {
            u32 m0 = 0, m1 = 0;
#pragma unroll
            for (int i = 0; i < 16; i += 2) { m0 = max(max(m0, of[i][0]), of[i][1]); m1 = max(max(m1, of[i + 1][0]), of[i + 1][1]); }
            if (__all(max(m0, m1) != SENT)) break;
            _Pragma("unroll 1") for (int sp2 = 0; sp2 < SPIN_CAP; ++sp2) {
                CFENCE();
                __builtin_amdgcn_s_sleep(1);
                const u32 pv = __builtin_amdgcn_raw_buffer_load_b32(r_ws, WS_O2 + (u32)(TI * 16384 + R * 8192 + (opq(tid) & 15) * 512 + 508), 0, 16);
                if (__all(pv != SENT)) break;
            }
            CFENCE();
            load_o(WS_O2);
        }
        STAMP(24);
#pragma unroll
        for (int ks = 0; ks < 8; ++ks)
#pragma unroll
            for (int n = 0; n < 4; ++n) accM[n] = mfma32(F[ks][n], __builtin_bit_cast(s16x8, (u32x4){of[2 * ks][0], of[2 * ks][1], of[2 * ks + 1][0], of[2 * ks + 1][1]}), ks ? accM[n] : zero4);
        SB();
        STAMP(45);
    }
    wo_finish(IC<1>{});
    if constexpr (R == 1) {
        if (b == 0 && wave == 7) {
            const int l3 = opq(tid) & 63;
#pragma unroll
            for (int i = 0; i < 4; ++i) { flg = (u32x2){0u, 0u}; spin_flags(flg, WS_F1 + (u32)((i * 64 + l3) * 128)); }
            if (l3 == 0) __builtin_amdgcn_raw_buffer_store_b64(__builtin_bit_cast(u32x2, epoch + 1), r_ws, WS_EPOCH, 0, 16);
        }
    }
}

__global__ void __launch_bounds__(512, 2)
fused_kernel(const u16* __restrict__ gx, const u16* __restrict__ gwqkv, const u16* __restrict__ gwo,
             const u16* __restrict__ gbk, const u16* __restrict__ gbv, const u16* __restrict__ gsk,
             const u16* __restrict__ ghk, const u16* __restrict__ gdv, u16* __restrict__ gout, char* __restrict__ ws) {
    __shared__ __attribute__((aligned(16))) char lds_[L_TOTAL];
    const Args a = {gx, gwqkv, gwo, gbk, gbv, gsk, ghk, gdv, gout, ws};
    const int wave = __builtin_amdgcn_readfirstlane(threadIdx.x >> 6);
    if (wave < 4) body<0>(a, (LAS char*)lds_, wave); else body<1>(a, (LAS char*)lds_, wave);
}

extern "C" void kernel_launch(void* const* d_in, const int* in_sizes, int n_in,
                              void* d_out, int out_size, void* d_ws, size_t ws_size,
                              hipStream_t stream) {
    fused_kernel<<<256, 512, 0, stream>>>((const u16*)d_in[0], (const u16*)d_in[1], (const u16*)d_in[2],
                                          (const u16*)d_in[4], (const u16*)d_in[5], (const u16*)d_in[6],
                                          (const u16*)d_in[7], (const u16*)d_in[8], (u16*)d_out, (char*)d_ws);
}
```

```cpp
#define NO_PROBE
#include <hip/hip_runtime.h>
#include <stdint.h>

typedef unsigned short u16;
typedef unsigned u32;
typedef unsigned long long u64;
typedef short s16x4 __attribute__((ext_vector_type(4)));
typedef short s16x8 __attribute__((ext_vector_type(8)));
typedef __bf16 bf16x2 __attribute__((ext_vector_type(2)));
typedef float f32x2 __attribute__((ext_vector_type(2)));
typedef float f32x4 __attribute__((ext_vector_type(4)));
typedef u32 u32x2 __attribute__((ext_vector_type(2)));
typedef u32 u32x4 __attribute__((ext_vector_type(4)));
#define LAS __attribute__((address_space(3)))
#define DEV __device__ __forceinline__
typedef __amdgpu_buffer_rsrc_t rsrc_t;

constexpr u32 WS_EPOCH = 0;
constexpr u32 WS_F1 = 32768;
constexpr u32 WS_F2 = 65536;
constexpr u32 WS_ST0 = 98304;
constexpr u32 WS_ST1 = 131072;
constexpr u32 WS_ST2 = WS_ST1 + 262144;
constexpr u32 WS_ST3 = WS_ST2 + 262144;
constexpr u32 WS_UT = 1u << 20;
constexpr u32 WS_O1 = WS_UT + (2u << 20);
constexpr u32 WS_O2 = WS_O1 + (2u << 20);
constexpr u32 WS_Y2 = WS_O2 + (2u << 20);
constexpr u32 WS_X2 = WS_Y2 + (2u << 20);
constexpr u32 WS_END = WS_X2 + (2u << 20);

constexpr u32 L_SLOT = 34816;
constexpr u32 L_KV = 4 * L_SLOT;
constexpr u32 L_K = L_KV, L_V = L_KV + 9216;
constexpr u32 L_SK = L_KV + 17408;
constexpr u32 L_WO = 0;
constexpr u32 L_CX = 65536;
constexpr u32 L_HC = L_CX;
constexpr u32 L_UR = L_CX + 69632;
constexpr u32 L_WX = L_CX + 40960;
constexpr u32 L_EX = 147456;
constexpr u32 L_TAB = L_EX + 12288;
constexpr u32 L_CS = L_TAB + 1024;
constexpr u32 L_FLG = L_TAB + 2048;
constexpr u32 L_DUMMY = L_FLG + 1024;
constexpr u32 L_TOTAL = 160 * 1024;
static_assert(L_DUMMY + 1024 <= L_TOTAL, "LDS map");

#define WO_POS 0
#define OUT_AUX 0
constexpr int SPIN_CAP = 1 << 14;
constexpr u32 SENT = 0xFFFFFFFFu;

#define WAIT_VM(n) asm volatile("s_waitcnt vmcnt(%0)" ::"i"(n) : "memory")
#define BAR() do { asm volatile("s_waitcnt lgkmcnt(0)" ::: "memory"); __builtin_amdgcn_s_barrier(); asm volatile("" ::: "memory"); } while (0)
#define RBAR() do { asm volatile("" ::: "memory"); __builtin_amdgcn_s_barrier(); asm volatile("" ::: "memory"); } while (0)
#define CFENCE() asm volatile("" ::: "memory")
#define SB() __builtin_amdgcn_sched_barrier(0)
template <int N> struct IC { static constexpr int value = N; };
template <int I, int N, class F> DEV void sfor(F&& f) { if constexpr (I < N) { f(IC<I>{}); sfor<I + 1, N>(f); } }

DEV f32x4 mfma32(s16x8 a, s16x8 b, f32x4 c) { return __builtin_amdgcn_mfma_f32_16x16x32_bf16(a, b, c, 0, 0, 0); }
DEV f32x4 mfma16(s16x4 a, s16x4 b, f32x4 c) { return __builtin_amdgcn_mfma_f32_16x16x16bf16_1k(a, b, c, 0, 0, 0); }
DEV float vmax(float a, float b) { float r; asm("v_max_f32 %0, %1, %2" : "=v"(r) : "v"(a), "v"(b)); return r; }
DEV u32 pk2(float lo, float hi) { f32x2 f = {lo, hi}; bf16x2 b = __builtin_convertvector(f, bf16x2); return __builtin_bit_cast(u32, b); }
DEV float bflo(u32 w) { return __uint_as_float(w << 16); }
DEV float bfhi(u32 w) { return __uint_as_float(w & 0xffff0000u); }
DEV float dot2(u32 a, u32 b, float c) { return __builtin_amdgcn_fdot2_f32_bf16(__builtin_bit_cast(bf16x2, a), __builtin_bit_cast(bf16x2, b), c, false); }
DEV u32 nosent(u32 w) { return min(w, 0xFFFFFFFEu); }
DEV float xsum(float v) {
    auto r = __builtin_amdgcn_permlane32_swap(__float_as_uint(v), __float_as_uint(v), false, false);
    v = __uint_as_float(r[0]) + __uint_as_float(r[1]);
    auto s = __builtin_amdgcn_permlane16_swap(__float_as_uint(v), __float_as_uint(v), false, false);
    return __uint_as_float(s[0]) + __uint_as_float(s[1]);
}
DEV float xmax(float v) {
    auto r = __builtin_amdgcn_permlane32_swap(__float_as_uint(v), __float_as_uint(v), false, false);
    v = vmax(__uint_as_float(r[0]), __uint_as_float(r[1]));
    auto s = __builtin_amdgcn_permlane16_swap(__float_as_uint(v), __float_as_uint(v), false, false);
    return vmax(__uint_as_float(s[0]), __uint_as_float(s[1]));
}
DEV float rowsum16(float v) {
    v += __builtin_bit_cast(float, __builtin_amdgcn_update_dpp(0, __builtin_bit_cast(int, v), 0xB1, 0xf, 0xf, true));
    v += __builtin_bit_cast(float, __builtin_amdgcn_update_dpp(0, __builtin_bit_cast(int, v), 0x4E, 0xf, 0xf, true));
    v += __builtin_bit_cast(float, __builtin_amdgcn_update_dpp(0, __builtin_bit_cast(int, v), 0x141, 0xf, 0xf, true));
    v += __builtin_bit_cast(float, __builtin_amdgcn_update_dpp(0, __builtin_bit_cast(int, v), 0x140, 0xf, 0xf, true));
    return v;
}
DEV float bcast_row0(float v) { return __builtin_bit_cast(float, __builtin_amdgcn_update_dpp(0, __builtin_bit_cast(int, v), 0x150, 0xf, 0xf, true)); }
template <int OFF> DEV void ald4o(u32x4& d, u32 voff, u32x4 rs) { asm volatile("buffer_load_dwordx4 %0, %1, %2, 0 offen offset:%3 sc1" : "=v"(d) : "v"(voff), "s"(rs), "i"(OFF) : "memory"); }
template <class T> DEV void pin(T& v) { asm volatile("" : "+v"(v)); }
DEV int opq(int v) { asm volatile("" : "+v"(v)); return v; }
DEV s16x8 cat8(s16x4 a, s16x4 b) { return __builtin_shufflevector(a, b, 0, 1, 2, 3, 4, 5, 6, 7); }
DEV s16x4 pack4(f32x4 v) { u32x2 w = {pk2(v[0], v[1]), pk2(v[2], v[3])}; return __builtin_bit_cast(s16x4, w); }
DEV u64 mix64(u64 z) { z += 0x9E3779B97F4A7C15ull; z = (z ^ (z >> 30)) * 0xBF58476D1CE4E5B9ull; z = (z ^ (z >> 27)) * 0x94D049BB133111EBull; return z ^ (z >> 31); }

struct ConvSched {
    int n[4], f[18], i[18], slot[64];
    constexpr ConvSched(int R) : n{}, f{}, i{}, slot{} {
        constexpr int LEAD = 9;
        const int N0[4] = {0, 3, 4, 7};
        for (int k = 0; k < 4; ++k) n[k] = N0[k] ^ R;
        int c = 0;
        for (int ff = 0; ff < 8; ++ff) for (int k = 3; k >= 0; --k) if (ff <= n[k]) { f[c] = ff; i[c] = k; ++c; }
        for (int k = 0; k < 64; ++k) slot[k] = 1000;
        for (int p = 71; p >= 0; --p) {
            const int e = p / 18, ff = f[p % 18], g = n[i[p % 18]] - ff, s = p - LEAD < -1 ? -1 : p - LEAD;
            slot[e * 16 + ff] = s; slot[e * 16 + 8 + g] = s;
        }
    }
};

#define STAMP(i) do { } while (0)
#define CSTAMP(i) do { } while (0)

struct Args { const u16 *gx, *gwqkv, *gwo, *gbk, *gbv, *gsk, *ghk, *gdv; u16* gout; char* ws; };

template <int R>
DEV void body(const Args& A_, LAS char* const lds, const int wave) {
    const u16* gx = A_.gx; char* ws = A_.ws;
    STAMP(0);
    const int b = blockIdx.x, tid = threadIdx.x, lane = tid & 63;
    const int hd = wave & 3;
    const int grp = (b & 7) * 4 + (b >> 6), mem = (b >> 3) & 7;
    const int T0 = grp * 64;
    const int rot = (mem + 2 * (grp & 3)) & 7;

    u64 epoch;
    asm volatile("s_load_dwordx2 %0, %1, 0x0" : "=s"(epoch) : "s"(ws));
    const u32 dw = *(const u32*)(A_.gdv + 2 * b);
    const rsrc_t r_x = __builtin_amdgcn_make_buffer_rsrc((void*)gx, 0, 2048 * 512 * 2, 0x00020000);
    const rsrc_t r_ws = __builtin_amdgcn_make_buffer_rsrc((void*)ws, 0, WS_END, 0x00020000);
    const rsrc_t r_out = __builtin_amdgcn_make_buffer_rsrc((void*)A_.gout, 0, 2048 * 512 * 2, 0x00020000);
    const u32 ldsbase = (u32)(size_t)lds;
    auto mkdesc = [](const void* p, u32 bytes) { const u64 a = (u64)p; return (u32x4){(u32)a, (u32)(a >> 32) & 0xffffu, bytes, 0x00020000u}; };
    const u32x4 d_x = mkdesc(gx, 2048 * 512 * 2), d_w = mkdesc(A_.gwqkv, 512 * 1536 * 2), d_wo = mkdesc(A_.gwo, 512 * 512 * 2);
    const u32x4 d_k = mkdesc(A_.gbk, 2048 * 512 * 2), d_v = mkdesc(A_.gbv, 2048 * 512 * 2), d_sk = mkdesc(A_.gsk, 512 * 4 * 2);
    const u32x4 d_h = mkdesc(A_.ghk, 512 * 2048 * 2), d_x2 = mkdesc(ws + WS_X2, 2u << 20), d_ws = mkdesc(ws, WS_END);
    const u32 wb1k = ldsbase + wave * 1024, hb1k = ldsbase + hd * 1024, hb512 = ldsbase + hd * 512;
#define DMA_ASM(pre, mods, post) asm volatile("s_add_u32 m0, %0, %4\n\t" pre "buffer_load_dwordx4 %1, %2, %3 offen " mods "lds" post \
                     :: "s"(sbase), "v"(voff), "s"(rs), "s"(soff), "i"(IMM) : "memory", "scc")
    const u32 xmlo = hd == 0 ? ~0xFFu : ~0u;
    auto dma = [&](auto kind, auto imm, u32x4 rs, u32 sbase, u32 voff, u32 soff) {
        constexpr int K = decltype(kind)::value; constexpr int IMM = decltype(imm)::value;
        if constexpr (K == 0) DMA_ASM("s_nop 0\n\t", "", "");
        else if constexpr (K == 1) DMA_ASM("s_mov_b32 exec_hi, 0\n\t", "", "\n\ts_mov_b32 exec_hi, -1");
        else if constexpr (K == 2) {
            const u32 ml = xmlo;
            asm volatile("s_add_u32 m0, %0, %4\n\ts_mov_b32 exec_lo, %5\n\tbuffer_load_dwordx4 %1, %2, %3 offen lds\n\ts_mov_b32 exec_lo, -1"
                         :: "s"(sbase), "v"(voff), "s"(rs), "s"(soff), "i"(IMM), "s"(ml) : "memory", "scc");
        }
        else if constexpr (K == 4) DMA_ASM("s_nop 0\n\t", "sc1 ", "");
        else DMA_ASM("s_mov_b32 exec_hi, 0\n\t", "sc1 ", "\n\ts_mov_b32 exec_hi, -1");
    };
    auto ald4 = [&](u32x4& d, u32x4 rs, u32 voff) { asm volatile("buffer_load_dwordx4 %0, %1, %2, 0 offen" : "=v"(d) : "v"(voff), "s"(rs) : "memory"); };
    auto ald4s = [&](u32x4& d, u32 voff) { asm volatile("buffer_load_dwordx4 %0, %1, %2, 0 offen sc1" : "=v"(d) : "v"(voff), "s"(d_ws) : "memory"); };
    auto ald2s = [&](u32x2& d, u32 voff) { asm volatile("buffer_load_dwordx2 %0, %1, %2, 0 offen sc1" : "=v"(d) : "v"(voff), "s"(d_ws) : "memory"); };

    const int pr = lane >> 3, pc = lane & 7;
    const int xr0 = 8 * hd + pr, xr2 = 64 + 4 * hd + pr;
    const u32 xv0 = (u32)((T0 - 16 + xr0) * 1024 + ((pc ^ ((xr0 >> 1) & 7)) << 4));
    const u32 xv2 = (u32)((T0 - 16 + xr2) * 1024 + ((pc ^ ((xr2 >> 1) & 7)) << 4));
    const int wfw = ((xr0 >> 1) & 1) | (((xr0 >> 3) & 1) << 1);
    const u32 wv0 = (u32)(xr0 * 3072 + mem * 128 + ((((pc >> 1) ^ wfw) * 2 + (pc & 1)) << 4));
    auto issue_piece = [&](auto sc, auto ic) {
        constexpr int s = decltype(sc)::value, i = decltype(ic)::value;
        const u32 kp = (u32)((s + rot) & 7);
        constexpr int sb = (s & 3) * L_SLOT;
        if constexpr (i == 0) dma(IC<2>{}, IC<sb>{}, d_x, hb1k, xv0, kp * 128);
        else if constexpr (i == 1) dma(IC<0>{}, IC<sb + 4096>{}, d_x, hb1k, xv0 + 32768, kp * 128);
        else if constexpr (i == 2) dma(IC<1>{}, IC<sb + 8192>{}, d_x, hb512, xv2, kp * 128);
        else dma(IC<0>{}, IC<sb + 10240 + (i - 3) * 4096>{}, d_w, hb1k, wv0, kp * (64 * 3072) + ((i - 3) >> 1) * 1024 + ((i - 3) & 1) * (32 * 3072));
    };
    auto issue_wo = [&](auto ic) {
        constexpr int i = decltype(ic)::value;
        const int l2 = opq(tid) & 63, pr = l2 >> 3, pc = l2 & 7;
        const u32 wov = (u32)((8 * hd + pr) * 1024 + mem * 128 + ((((pc >> 1) ^ (((pr >> 1) & 1) | ((hd & 1) << 1))) * 2 + (pc & 1)) << 4));
        const u32 j = (u32)((i + 2 * grp) & 7);
        dma(IC<0>{}, IC<L_WO + (i & 8) * 4096>{}, d_wo, hb1k + j * 4096, wov, ((i & 8) + j) * 32768);
    };
    auto issue_kv = [&](int i) {
        const int l2 = opq(tid) & 63, pr = l2 >> 3, pc = l2 & 7, p = hd + 4 * (i & 1);
        const u32 cb = (u32)(mem * 128);
        if (i < 2) dma(IC<0>{}, IC<L_K>{}, d_k, hb1k + (i & 1) * 4096, (u32)((T0 - 8 + 8 * p + pr) * 1024) + cb + ((pc ^ p) << 4), 0);
        else if (i < 4) dma(IC<0>{}, IC<L_V>{}, d_v, hb1k + (i & 1) * 4096, (u32)((T0 + 8 * p + pr) * 1024) + cb + ((pc ^ p) << 4), 0);
        else if (hd == 0) dma(IC<0>{}, IC<L_K + 8192>{}, d_k, ldsbase, (u32)((T0 - 8 + 64 + pr) * 1024) + cb + (pc << 4), 0);
        else dma(IC<0>{}, IC<L_SK>{}, d_sk, ldsbase, (u32)(mem * 512 + l2 * 16), 0);
    };

    const int srow = T0 + 8 * mem + 2 * hd;
    u32x4 xrow[2];
    sfor<0, 9>([&](auto ic) { issue_piece(IC<1 - R>{}, ic); });
    asm volatile("s_waitcnt lgkmcnt(0)" : "+s"(epoch));
    const u64 tag = mix64(epoch);
    const u32 tl = (u32)tag, th = (u32)(tag >> 32);
    if (R == 1 && wave == 7 && lane < 32) *(LAS u32x4*)(lds + (lane >> 3) * L_SLOT + (lane & 7) * 16) = (u32x4){0x3F803F80u, 0x3F803F80u, 0x3F803F80u, 0x3F803F80u};

    auto trr = [&](u32 off) { return __builtin_amdgcn_ds_read_tr16_b64_v4i16((LAS s16x4*)(lds + off)); };
    auto ldsA = [&](u32 off) { return *(LAS s16x8*)(lds + off); };
    auto flag_ok = [&](const u32x2& f) { return __all(f[0] == tl && f[1] == th); };
    auto spin_flags = [&](u32x2& f, u32 off) {
        _Pragma("unroll 1") for (int spin = 0; spin < SPIN_CAP; ++spin) {
            if (flag_ok(f)) break;
            __builtin_amdgcn_s_sleep(2);
            asm volatile("buffer_load_dwordx2 %0, %1, %2, 0 offen sc1\n\ts_waitcnt vmcnt(0)" : "=&v"(f) : "v"(off), "s"(d_ws) : "memory");
        }
    };

    s16x8 Wq[16], Wm[16];
    f32x4 accM[5], accQ[5];
    const f32x4 zero4 = {0.f, 0.f, 0.f, 0.f};
    const s16x8 ones8 = {0x3F80, 0x3F80, 0x3F80, 0x3F80, 0x3F80, 0x3F80, 0x3F80, 0x3F80};
    s16x8 Ab[3][5];
    auto qkv_step = [&](auto lc, int st, const s16x8 (&A)[5], auto fill) {
        sfor<0, 5>([&](auto mc) {
            constexpr int mt = decltype(mc)::value;
            const f32x4 cm = st ? accM[mt] : zero4;
            accM[mt] = R ? mfma32(A[mt], Wm[st], cm) : mfma32(Wm[st], A[mt], cm);
            if constexpr (R == 0 && (mt > 0 || decltype(lc)::value == 0)) accQ[mt] = mfma32(Wq[st], A[mt], st ? accQ[mt] : zero4);
            fill(mc);
        });
    };

    float m1[5], r1[5]; f32x4 m4[5], r4[5], csK, csQ;
    auto load_tab = [&](bool cs) {
        const int l2 = opq(tid) & 63;
        u32 ta = L_TAB + (R ? (l2 >> 4) * 16 : (l2 & 15) * 4);
        pin(ta);
        if (R == 0 && cs) { csK = *(LAS f32x4*)(lds + L_CS + hd * 128 + (l2 >> 4) * 16); csQ = *(LAS f32x4*)(lds + L_CS + hd * 128 + 64 + (l2 >> 4) * 16); }
#pragma unroll
        for (int mt = 0; mt < 5; ++mt) {
            if (R == 0) { m1[mt] = *(LAS float*)(lds + ta + mt * 64); r1[mt] = *(LAS float*)(lds + ta + 320 + mt * 64); }
            else { m4[mt] = *(LAS f32x4*)(lds + ta + mt * 64); r4[mt] = *(LAS f32x4*)(lds + ta + 320 + mt * 64); }
        }
    };

    u32x4 hr[4];
    u32x2 flg;
    {
        const int li = lane & 15, lq = lane >> 4, tq = li >> 2, tp = lane & 3;
        const u32 xa0 = (u32)(li * 128 + ((lq ^ (li >> 1)) << 4));
        const u32 wa = (u32)((8 * lq + tq) * 128 + ((hd ^ (((tq >> 1) & 1) | ((lq & 1) << 1))) << 5) + tp * 8);
        u32 xa, wb, kw[11], vw[8];
        u32x4 s0v, skw;
        auto pf_addr = [&](int st) {
            const u32 sb = (u32)((st >> 1) & 3) * L_SLOT;
            xa = sb + (xa0 ^ ((st & 1) * 64)); wb = sb + 10240 + (st & 1) * 4096 + wa;
            asm volatile("" : "+v"(xa), "+v"(wb));
        };
        auto pf_part = [&](int st, int mt) {
            if (mt == 0) Wm[st] = cat8(trr(wb + (1 + R) * 8192), trr(wb + (1 + R) * 8192 + 512));
            Ab[st & 1][mt] = ldsA(xa + mt * 2048);
            if (R == 0 && mt == 1) Wq[st] = cat8(trr(wb), trr(wb + 512));
        };
        STAMP(15);
        if constexpr (R == 1) WAIT_VM(0);
        BAR();
        STAMP(16);
        pf_addr(0);
#pragma unroll
        for (int mt = 0; mt < 5; ++mt) pf_part(0, mt);
        sfor<0, 16>([&](auto stc) {
            constexpr int st = decltype(stc)::value;
            if constexpr ((st & 1) && st < 15) {
                constexpr int s1 = (st >> 1) + 1;
                if constexpr (R == 0) {
                    if constexpr (s1 == 1) WAIT_VM(7);
                    if constexpr (s1 == 2) WAIT_VM(1);
                    if constexpr (s1 == 5) WAIT_VM(0);
                } else {
                    constexpr int WN[8] = {0, 63, 9, 9, 9, 9, 10, 8};
                    if constexpr (s1 > 1) WAIT_VM(WN[s1]);
                    if constexpr (s1 == 7) {
                        pin(s0v);
                        const int l2 = opq(tid) & 63, tr = hd * 20 + min(l2, 19), row = T0 - 16 + tr;
                        _Pragma("unroll 1") for (int spin = 0; spin < SPIN_CAP; ++spin) {
                            if (__builtin_expect(__all(row < 0 || (s0v[1] == tl && s0v[3] == th)), 1)) break;
                            __builtin_amdgcn_s_sleep(2);
                            ald4s(s0v, WS_ST0 + (u32)(max(row, 0) * 16));
                            WAIT_VM(0);
                            pin(s0v);
                        }
                        *(LAS u32*)(lds + L_TAB + tr * 4) = row < 0 ? 0u : s0v[0];
                        *(LAS u32*)(lds + L_TAB + 320 + tr * 4) = row < 0 ? 0x3F800000u : s0v[2];
                        asm volatile("s_waitcnt lgkmcnt(0)" ::: "memory");
                    }
                }
                RBAR();
                STAMP(16 + s1);
                if constexpr (s1 == 5 && R == 0) { if (tid == 0) __builtin_amdgcn_raw_buffer_store_b64((u32x2){tl, th}, r_ws, WS_F1 + (mem * 32 + grp) * 128, 0, 16); CFENCE(); }
                if constexpr (s1 == 5 && R == 1) { const int row = T0 - 16 + hd * 20 + min(opq(tid) & 63, 19); ald4s(s0v, WS_ST0 + (u32)(max(row, 0) * 16)); }
            }
            if (st < 15) pf_addr(st + 1);
            if constexpr (st == 14) load_tab(false);
            SB();
            qkv_step(IC<0>{}, st, Ab[st & 1], [&](auto mc) {
                constexpr int mt = decltype(mc)::value;
                if (st < 15) pf_part(st + 1, mt);
                if constexpr (R == 1) {
                    if constexpr (st == 0) { issue_piece(IC<2>{}, IC<2 * mt>{}); if constexpr (mt < 4) issue_piece(IC<2>{}, IC<2 * mt + 1>{}); }
                    if constexpr (st >= 1 && st <= 10 && ((st - 1) & 1) * 5 + mt < 9) issue_piece(IC<((st - 1) >> 1) + 3>{}, IC<((st - 1) & 1) * 5 + mt>{});
                    if constexpr (st == 13 && mt == 0) { if (wave == 7) ald2s(flg, WS_F1 + (u32)(((b >> 5) * 32 + (opq(tid) & 31)) * 128)); }
                    if constexpr (st >= 11 && st <= 14 && mt < 4) issue_wo(IC<(st - 11) * 4 + mt>{});
                } else {
                    if constexpr (st == 0) {
                        if constexpr (mt == 0) { const u32 xo = (u32)(srow * 1024 + (opq(tid) & 63) * 16); ald4(xrow[0], d_x, xo); ald4(xrow[1], d_x, xo + 1024); }
                        issue_kv(mt);
                    }
                    if constexpr (st == 11 && mt < 2) { const u32 ho = (u32)(b * 8192 + mt * 4096 + opq(tid) * 16); ald4(hr[2 * mt], d_h, ho); ald4(hr[2 * mt + 1], d_h, ho + 16); }
                }
                SB();
            });
            if constexpr (R == 0 && st == 2) {
                WAIT_VM(5);
                pin(xrow[0]); pin(xrow[1]);
                float s[2] = {0.f, 0.f}, q[2] = {0.f, 0.f};
#pragma unroll
                for (int i = 0; i < 4; ++i)
#pragma unroll
                    for (int k = 0; k < 2; ++k) { s[k] = dot2(xrow[k][i], 0x3F803F80u, s[k]); q[k] = dot2(xrow[k][i], xrow[k][i], q[k]); }
                auto ss = __builtin_amdgcn_permlane32_swap(__float_as_uint(s[0]), __float_as_uint(s[1]), false, false);
                auto qq = __builtin_amdgcn_permlane32_swap(__float_as_uint(q[0]), __float_as_uint(q[1]), false, false);
                float sv1 = rowsum16(__uint_as_float(ss[0]) + __uint_as_float(ss[1])), qv1 = rowsum16(__uint_as_float(qq[0]) + __uint_as_float(qq[1]));
                auto s2 = __builtin_amdgcn_permlane16_swap(__float_as_uint(sv1), __float_as_uint(sv1), false, false);
                auto q2 = __builtin_amdgcn_permlane16_swap(__float_as_uint(qv1), __float_as_uint(qv1), false, false);
                const float m = (__uint_as_float(s2[0]) + __uint_as_float(s2[1])) * (1.f / 512.f);
                const float r = __builtin_amdgcn_rsqf(fmaxf((__uint_as_float(q2[0]) + __uint_as_float(q2[1])) * (1.f / 512.f) - m * m, 0.f) + 1e-5f);
                const u32x4 sv = {__float_as_uint(m), tl, __float_as_uint(r), th};
                const int l2 = opq(tid) & 63;
                __builtin_amdgcn_raw_buffer_store_b128(sv, r_ws, (l2 & 31) == 0 ? WS_ST0 + (u32)((srow + (l2 >> 5)) * 16) : 0xFFFFFF00u, 0, 16);
                CFENCE();
                SB();
            }
            if constexpr (R == 0 && (st == 4 || st == 6)) {
                const int l2 = opq(tid) & 63, tg = l2 & 7, cp = 8 * hd + (l2 >> 3);
                if constexpr (st == 4) {
                    skw = *(LAS u32x4*)(lds + L_SK + cp * 16);
                    u32 ka = L_K + tg * 1024 + ((((cp >> 2) ^ tg) & 7) << 4) + (cp & 3) * 4;
                    u32 kb = L_K + tg * 1024 + ((((cp >> 2) ^ (tg + 1)) & 7) << 4) + (cp & 3) * 4;
                    pin(ka); pin(kb);
#pragma unroll
                    for (int i = 0; i < 11; ++i) kw[i] = *(LAS u32*)(lds + ((i + 5 < 8) ? ka : kb) + (i + 5) * 128);
#pragma unroll
                    for (int i = 0; i < 8; ++i) vw[i] = *(LAS u32*)(lds + ka + (L_V - L_K) + i * 128);
                    SB();
                }
                constexpr bool HI = st == 6;
                auto ex = [&](u32 w) { return HI ? bfhi(w) : bflo(w); };
                const float a0 = bflo(skw[2 * HI]), a1 = bfhi(skw[2 * HI]), a2 = bflo(skw[2 * HI + 1]), a3 = bfhi(skw[2 * HI + 1]);
                float ua[8];
#pragma unroll
                for (int i = 0; i < 8; ++i) ua[i] = (ex(kw[i]) * a3 + ex(kw[i + 1]) * a2 + ex(kw[i + 2]) * a1 + ex(kw[i + 3]) * a0) * ex(vw[i]);
                const u32x4 oa = {pk2(ua[7], ua[6]), pk2(ua[5], ua[4]), pk2(ua[3], ua[2]), pk2(ua[1], ua[0])};
                __builtin_amdgcn_raw_buffer_store_b128(oa, r_ws, WS_UT + (u32)((mem * 64 + 2 * cp) * 4096 + (2040 - T0 - 8 * tg) * 2), HI * 4096, 16);
                CFENCE();
                SB();
            }
        });
    }
    STAMP(R ? 1 : 53);

    u32x4 uu;
    float csV;
    auto attention = [&](auto lc) {
        constexpr int LAYER = decltype(lc)::value;
        constexpr int SBASE = LAYER ? 32 : 28;
        const int lane = opq(tid) & 63, li = lane & 15, lq = lane >> 4;
        u32 ex = L_EX + hd * 2048 + lane * 8;
        pin(ex);
        u32 f1ok = 1;
        if constexpr (R == 0) {
            if constexpr (LAYER == 0) {
#pragma unroll
                for (int i = 0; i < 4; ++i) { csK[i] = bcast_row0(accM[0][i]); csQ[i] = bcast_row0(accQ[0][i]); }
                if (li == 0) { *(LAS f32x4*)(lds + L_CS + hd * 128 + lq * 16) = csK; *(LAS f32x4*)(lds + L_CS + hd * 128 + 64 + lq * 16) = csQ; }
            }
            const int par = li & 1;
            const bool cur0 = (4 * lq + par) <= li, cur1 = (4 * lq + par + 2) <= li;
            s16x4 Kb[5], Qb[5];
#pragma unroll
            for (int mt = 0; mt < 5; ++mt) {
                Kb[mt] = pack4((accM[mt] - m1[mt] * csK) * r1[mt]);
                if (mt) Qb[mt] = pack4(accQ[mt] - m1[mt] * csQ);
            }
            f32x4 Sc[5], Sp[5];
#pragma unroll
            for (int mt = 1; mt < 5; ++mt) { Sc[mt] = mfma16(Kb[mt], Qb[mt], zero4); Sp[mt] = mfma16(Kb[mt - 1], Qb[mt], zero4); }
            float s0[5], s1[5], mx[5];
#pragma unroll
            for (int mt = 1; mt < 5; ++mt) {
                const float sp0 = Sp[mt][0], sp1 = Sp[mt][1], sp2 = Sp[mt][2], sp3 = Sp[mt][3], c0 = Sc[mt][0], c1 = Sc[mt][1], c2 = Sc[mt][2], c3 = Sc[mt][3];
                float p0 = par ? sp1 : sp0, p1 = par ? sp3 : sp2, q0 = par ? c1 : c0, q1 = par ? c3 : c2;
                asm volatile("" : "+v"(p0), "+v"(p1), "+v"(q0), "+v"(q1));
                if (mt == 1 && grp == 0) { p0 = -3.0e38f; p1 = -3.0e38f; }
                s0[mt] = cur0 ? q0 : p0; s1[mt] = cur1 ? q1 : p1;
                mx[mt] = vmax(s0[mt], s1[mt]);
            }
#pragma unroll
            for (int mt = 1; mt < 5; ++mt) { auto r = __builtin_amdgcn_permlane32_swap(__float_as_uint(mx[mt]), __float_as_uint(mx[mt]), false, false); mx[mt] = vmax(__uint_as_float(r[0]), __uint_as_float(r[1])); }
#pragma unroll
            for (int mt = 1; mt < 5; ++mt) { auto r = __builtin_amdgcn_permlane16_swap(__float_as_uint(mx[mt]), __float_as_uint(mx[mt]), false, false); mx[mt] = vmax(__uint_as_float(r[0]), __uint_as_float(r[1])); }
#pragma unroll
            for (int mt = 1; mt < 5; ++mt) {
                const float sc = r1[mt] * (0.25f * 1.44269504088896341f);
                const float e0 = __builtin_amdgcn_exp2f((s0[mt] - mx[mt]) * sc), e1 = __builtin_amdgcn_exp2f((s1[mt] - mx[mt]) * sc);
                *(LAS u32x2*)(lds + ex + (mt - 1) * 512) = (u32x2){pk2(e0, e0), pk2(e1, e1)};
            }
            STAMP(SBASE + 2);
            BAR();
            STAMP(SBASE + 1);
            if constexpr (LAYER == 0) f1ok = __builtin_amdgcn_readfirstlane(*(LAS u32*)(lds + L_FLG));
        } else {
            if constexpr (LAYER == 0) {
                auto s = __builtin_amdgcn_permlane16_swap(__float_as_uint(accM[0][0]), __float_as_uint(accM[0][0]), false, false);
                auto t = __builtin_amdgcn_permlane32_swap(s[0], s[0], false, false);
                csV = __uint_as_float(t[0]);
                if (wave == 7) { WAIT_VM(8); pin(flg); const u32 ok = flag_ok(flg); *(LAS u32*)(lds + L_FLG) = ok; }
            }
            const int par = li & 1;
            const bool cur0 = (4 * lq + par) <= li, cur1 = (4 * lq + par + 2) <= li;
            const u32 pm = par ? 0xffff0000u : 0x0000ffffu;
            u32 mC0 = cur0 ? pm : 0u, mP0 = cur0 ? 0u : pm, mC1 = cur1 ? pm : 0u, mP1 = cur1 ? 0u : pm;
            u32 oo = (LAYER ? WS_O2 : WS_O1) + (u32)((4 * grp + (lq & 1)) * 16384 + (4 * mem + hd) * 512 + (lq >> 1) * 256 + li * 16);
            asm volatile("" : "+v"(mC0), "+v"(mP0), "+v"(mC1), "+v"(mP1), "+v"(oo));
            s16x4 Vb[5];
#pragma unroll
            for (int mt = 0; mt < 5; ++mt) { Vb[mt] = pack4((accM[mt] - m4[mt] * csV) * r4[mt]); pin(Vb[mt]); }
            STAMP(SBASE);
            BAR();
            STAMP(SBASE + 1);
            u32x2 pw[4];
#pragma unroll
            for (int j = 0; j < 4; ++j) pw[j] = *(LAS u32x2*)(lds + ex + j * 512);
            if constexpr (LAYER == 0) f1ok = *(LAS u32*)(lds + L_FLG);
            SB();
            f32x4 O[4], L[4];
#pragma unroll
            for (int j = 0; j < 4; ++j) {
                const s16x8 P = __builtin_bit_cast(s16x8, (u32x4){pw[j][0] & mC0, pw[j][1] & mC1, pw[j][0] & mP0, pw[j][1] & mP1});
                L[j] = mfma32(ones8, P, zero4);
                O[j] = mfma32(cat8(Vb[j + 1], Vb[j]), P, zero4);
            }
            u32 ow[4][2];
#pragma unroll
            for (int j = 0; j < 4; ++j) {
                const float inv = __builtin_amdgcn_rcpf(L[j][0]);
                ow[j][0] = nosent(pk2(O[j][0] * inv, O[j][1] * inv)); ow[j][1] = nosent(pk2(O[j][2] * inv, O[j][3] * inv));
            }
#pragma unroll
            for (int j = 0; j < 4; j += 2) {
                auto r0 = __builtin_amdgcn_permlane16_swap(ow[j][0], ow[j + 1][0], false, false);
                auto r1 = __builtin_amdgcn_permlane16_swap(ow[j][1], ow[j + 1][1], false, false);
                __builtin_amdgcn_raw_buffer_store_b128((u32x4){r0[0], r1[0], r0[1], r1[1]}, r_ws, oo, j * 16384, 16);
            }
            f1ok = __builtin_amdgcn_readfirstlane(f1ok);
            CFENCE();
        }
        if constexpr (LAYER == 0) { if (f1ok) ald4s(uu, WS_UT + (u32)(b * 8192 + opq(tid) * 16)); }
        SB();
        return f1ok;
    };
    const u32 f1ok = attention(IC<0>{});
    {
        const u32x4 sv = {SENT, SENT, SENT, SENT};
        const int t2 = opq(tid);
        const u32 tb = (u32)((4 * grp + hd) * 16384 + mem * 2048 + R * 1024 + (t2 & 63) * 16);
        __builtin_amdgcn_raw_buffer_store_b128(sv, r_ws, tb, WS_O2, 16);
        __builtin_amdgcn_raw_buffer_store_b128(sv, r_ws, tb, WS_X2, 16);
        __builtin_amdgcn_raw_buffer_store_b128(sv, r_ws, (u32)(b * 8192 + t2 * 16), WS_Y2, 16);
        CFENCE();
        SB();
    }
    STAMP(2);

    const int TI = 4 * grp + hd;
    u32x4 of[8];
    auto load_o = [&](u32 ws_o) {
        const int lane = opq(tid) & 63;
#pragma unroll
        for (int i = 0; i < 8; ++i) of[i] = __builtin_amdgcn_raw_buffer_load_b128(r_ws, ws_o + (u32)(TI * 16384 + R * 8192 + lane * 16) + (i & 3) * 1024, (i >> 2) * 4096, 16);
    };
    u32 xw[8], yw[8];
    u32 woa[4];
    auto resid = [&](int n) { return R ? zero4 : (f32x4){bflo(xw[2 * n]), bfhi(xw[2 * n]), bflo(xw[2 * n + 1]), bfhi(xw[2 * n + 1])}; };
    s16x8 F[8][4];
    auto wo_addr = [&]() {
        const int l2 = opq(tid) & 63, lq = l2 >> 4, tq = (l2 & 15) >> 2, tp = l2 & 3;
#pragma unroll
        for (int n = 0; n < 4; ++n) woa[n] = L_WO + (u32)(R * 32768 + (8 * lq + tq) * 128 + ((n ^ (((lq & 1) << 1) | (tq >> 1))) << 5) + tp * 8);
    };
    auto load_F = [&](int ks) {
#pragma unroll
        for (int n = 0; n < 4; ++n) F[ks][n] = cat8(trr(woa[n] + ks * 4096), trr(woa[n] + ks * 4096 + 512));
    };

    {
        const int t2 = opq(tid), lane = t2 & 63, li = lane & 15, lq = lane >> 4;
        constexpr u32 OFFS = 0xEDA85410u;
        {
            const int j = t2 & 255;
            if constexpr (R == 0) {
                if (f1ok) WAIT_VM(4); else WAIT_VM(3);
                u32 cb = L_HC + (j + 2) * 16;
                pin(cb);
#pragma unroll
                for (int ch = 0; ch < 2; ++ch) {
                    pin(hr[2 * ch]); pin(hr[2 * ch + 1]);
                    const u32 d[8] = {hr[2 * ch][0], hr[2 * ch][1], hr[2 * ch][2], hr[2 * ch][3], hr[2 * ch + 1][0], hr[2 * ch + 1][1], hr[2 * ch + 1][2], hr[2 * ch + 1][3]};
                    u32 e[7];
#pragma unroll
                    for (int i = 0; i < 7; ++i) e[i] = __builtin_amdgcn_alignbit(d[i + 1], d[i], 16);
                    if (ch) { cb += 34816; pin(cb); }
#pragma unroll
                    for (int p = 0; p < 8; ++p) {
                        const u32 po = (272 * p + ((OFFS >> (4 * p)) & 15)) * 16;
                        const int h = p >> 1;
                        const u32x4 w = (p & 1) ? (u32x4){e[h], e[h + 1], e[h + 2], e[h + 3]} : (u32x4){d[h], d[h + 1], d[h + 2], d[h + 3]};
                        *(LAS u32x4*)(lds + cb + po) = w;
                    }
                }
                if (hd == 0) {
                    u32 hh[4];
#pragma unroll
                    for (int i = 0; i < 4; ++i) { const u32 a = __builtin_amdgcn_readfirstlane(hr[0][i]), c = __builtin_amdgcn_readfirstlane(hr[2][i]); hh[i] = (lane & 16) ? c : a; }
                    if (lane < 32) {
                        const int p = lane & 7;
                        const u32 po = (272 * p + ((OFFS >> (4 * p)) & 15)) * 16;
                        const unsigned __int128 hv = ((unsigned __int128)(((u64)hh[3] << 32) | hh[2]) << 64) | (((u64)hh[1] << 32) | hh[0]);
                        const unsigned __int128 sv = (p == 0 || !(lane & 8)) ? (unsigned __int128)0 : (hv << (16 * (8 - p)));
                        const u32x4 w = {(u32)sv, (u32)(sv >> 32), (u32)(sv >> 64), (u32)(sv >> 96)};
                        *(LAS u32x4*)(lds + L_HC + (lane >> 4) * 34816 + po + ((lane >> 3) & 1) * 16) = w;
                    }
                }
            }
            if (hd == 1 && lane < 30) *(LAS u32x4*)(lds + L_UR + R * 4576 + 4096 + lane * 16) = (u32x4){0, 0, 0, 0};
            STAMP(3);
            if (!f1ok) {
                const u32 fo = WS_F1 + (u32)(((b >> 5) * 32 + 31 - hd * 8 - (lane & 7)) * 128);
                flg = (u32x2){0u, 0u};
                spin_flags(flg, fo);
                ald4s(uu, WS_UT + (u32)(b * 8192 + t2 * 16));
            }
            WAIT_VM(0);
            pin(uu);
            *(LAS u32x4*)(lds + L_UR + R * 4576 + j * 16) = uu;
        }
        BAR();
        if (R == 0 && tid == 0) __builtin_amdgcn_raw_buffer_store_b64((u32x2){tl, th}, r_ws, WS_F2 + (grp * 8 + mem) * 128, 0, 16);
        STAMP(4);
        const int ch = hd >> 1, e0 = (hd & 1) * 4;
        const int p = (li + 1) & 7;
        u32 aA = L_HC + ch * 34816 + (272 * p + ((OFFS >> (4 * p)) & 15) + ((li + 1) >> 3) + lq + 4 * e0) * 16;
        u32 aB = L_UR + ch * 4576 + (254 - 224 - 2 * li + lq + 4 * e0) * 16;
        asm volatile("" : "+v"(aA), "+v"(aB));
        f32x4 y[4];
        const u32 f2own = WS_F2 + (u32)((grp * 8 + 4 * R + (lane & 3)) * 128);
        u32 ooff = WS_O1 + (u32)(TI * 16384 + R * 8192 + lane * 16);
        bool early = false;
        {
            constexpr ConvSched S(R);
            s16x8 A[4][8], B[4][8];
            auto loads_at = [&](auto pc) {
                constexpr int p = decltype(pc)::value;
                sfor<0, 64>([&](auto ic) {
                    constexpr int i = decltype(ic)::value, e = i >> 4, f = i & 7;
                    if constexpr (S.slot[i] == p) {
                        if constexpr (i & 8) B[e][f] = ldsA(aB + 64 * e + 512 * (7 - f)); else A[e][f] = ldsA(aA + 64 * (8 * f + e));
                    }
                });
            };
            loads_at(IC<-1>{});
            SB();
            sfor<0, 72>([&](auto pc) {
                constexpr int p = decltype(pc)::value, e = p / 18, f = S.f[p % 18], i = S.i[p % 18], g = S.n[i] - f;
                y[i] = mfma32(A[e][f], B[e][g], (e == 0 && f == 0) ? zero4 : y[i]);
                loads_at(pc);
                if constexpr (p == 42) ald2s(flg, f2own);
                if constexpr (p == 54) { WAIT_VM(0); pin(flg); early = flag_ok(flg); }
                if constexpr (p >= 55 && p < 71 && (p & 1)) {
                    constexpr int i = (p - 55) >> 1;
                    if (early) ald4o<(i & 3) * 1024>(of[i], ooff + (i >> 2) * 4096, d_ws);
                }
                SB();
            });
        }
        STAMP(14);
        RBAR();
        {
            constexpr int N0[4] = {0, 3, 4, 7};
            u32 ra = L_HC + hd * 8192 + lane * 16;
            asm volatile("" : "+v"(ra));
#pragma unroll
            for (int i = 0; i < 4; ++i) *(LAS f32x4*)(lds + ra + (N0[i] ^ R) * 1024) = y[i];
        }
        SB();
        if constexpr (R == 0) {
#pragma unroll
            for (int n = 0; n < 4; ++n) { const u32x2 w = __builtin_amdgcn_raw_buffer_load_b64(r_x, (u32)((TI * 16 + li) * 1024 + mem * 128 + lq * 8) + n * 32, 0, 0); xw[2 * n] = w[0]; xw[2 * n + 1] = w[1]; }
        }
        u32 f2oth = 0; u32x2 flg2;
        if constexpr (R == 1) {
            if (wave == 7) {
                const int pb = 32 * mem + (lane & 31);
                f2oth = WS_F2 + (u32)((lane < 32 ? ((pb & 7) * 4 + (pb >> 6)) * 8 + ((pb >> 3) & 7) : max(grp * 8 - 8, 0) + (lane & 15)) * 128);
                CFENCE();
                ald2s(flg2, f2oth);
            }
        }
        wo_addr();
        SB();
        STAMP(37);
        BAR();
        STAMP(38);
        {
            const float D0 = bflo(dw), D1 = bfhi(dw);
            u32 ra = L_HC + wave * 1024 + lane * 16;
            const int tb = 256 * wave + 16 * li + 4 * lq;
            u32 ru = L_UR + (2044 - tb) * 2;
            asm volatile("" : "+v"(ra), "+v"(ru));
            const f32x4 y0 = *(LAS f32x4*)(lds + ra), y1 = *(LAS f32x4*)(lds + ra + 8192), y2 = *(LAS f32x4*)(lds + ra + 16384), y3 = *(LAS f32x4*)(lds + ra + 24576);
            const u32x2 ua = *(LAS u32x2*)(lds + ru), ub = *(LAS u32x2*)(lds + ru + 4576);
            load_F(0); load_F(1);
            SB();
            const f32x4 ya = y0 + y1, yb = y2 + y3;
            const u32x4 w = {nosent(pk2(ya[0] + bfhi(ua[1]) * D0, yb[0] + bfhi(ub[1]) * D1)), nosent(pk2(ya[1] + bflo(ua[1]) * D0, yb[1] + bflo(ub[1]) * D1)),
                             nosent(pk2(ya[2] + bfhi(ua[0]) * D0, yb[2] + bfhi(ub[0]) * D1)), nosent(pk2(ya[3] + bflo(ua[0]) * D0, yb[3] + bflo(ub[0]) * D1))};
            __builtin_amdgcn_raw_buffer_store_b128(w, r_ws, WS_Y2 + (u32)(b * 8192 + tb * 4), 0, 16);
        }
        SB();
        STAMP(5);
        if (!early) {
            spin_flags(flg, f2own);
            sfor<0, 8>([&](auto ic) { constexpr int i = decltype(ic)::value;
                ald4o<(i & 3) * 1024>(of[i], ooff + (i >> 2) * 4096, d_ws); });
            WAIT_VM(0);
        }
        WAIT_VM(1);
        sfor<0, 8>([&](auto ic) { pin(of[decltype(ic)::value]); });
        STAMP(40);

        f32x4 acc[4];
#pragma unroll
        for (int ks = 0; ks < 8; ++ks) {
            if (ks + 2 < 8) load_F(ks + 2);
            SB();
#pragma unroll
            for (int n = 0; n < 4; ++n) acc[n] = mfma32(F[ks][n], __builtin_bit_cast(s16x8, of[ks]), ks ? acc[n] : resid(n));
            SB();
        }
        STAMP(41);
        accM[0] = acc[0]; accM[1] = acc[1]; accM[2] = acc[2]; accM[3] = acc[3];
        if constexpr (R == 1) { if (wave == 7) { asm volatile("" : "+v"(flg2)); spin_flags(flg2, f2oth); } }
    }

    auto load_y = [&]() {
        const int l2 = opq(tid) & 63;
#pragma unroll
        for (int i = 0; i < 8; ++i)
            yw[i] = __builtin_amdgcn_raw_buffer_load_b32(r_ws, WS_Y2 + (u32)((32 * mem + 2 * (l2 >> 4)) * 8192 + (TI * 16 + (l2 & 15)) * 4), (8 * (i >> 1) + (i & 1)) * 8192, 16);
    };
    auto pub_off = [&](u32 ws_st) {
        const int lane = opq(tid) & 63;
        return (lane & 16) ? 0xFFFFFF00u : ws_st + (u32)((TI * 8 + mem) * 256 + (lane & 15) * 16 + (lane >> 5) * 8);
    };
    auto pub_stat = [&](u32 off, u32 tg, float s, float q) {
        auto r = __builtin_amdgcn_permlane32_swap(__float_as_uint(s), __float_as_uint(q), false, false);
        const float v = __uint_as_float(r[0]) + __uint_as_float(r[1]);
        auto t = __builtin_amdgcn_permlane16_swap(__float_as_uint(v), __float_as_uint(v), false, false);
        __builtin_amdgcn_raw_buffer_store_b64((u32x2){__float_as_uint(__uint_as_float(t[0]) + __uint_as_float(t[1])), tg}, r_ws, off, 0, 16);
    };
    auto stat_off = [&](u32 ws_st, int ti) { const int l2 = opq(tid) & 63; return ws_st + (u32)(ti * 2048 + (l2 >> 4) * 512 + (l2 & 15) * 16); };
    auto fin_stat = [&](u32x4& a, u32x4& c, u32 off, float& mean, float& rstd) {
        _Pragma("unroll 1") for (int spin = 0; spin < SPIN_CAP; ++spin) {
            if (__builtin_expect(__all(((a[1] ^ tl) | (a[3] ^ th) | (c[1] ^ tl) | (c[3] ^ th)) == 0), 1)) break;
            __builtin_amdgcn_s_sleep(1);
            asm volatile("buffer_load_dwordx4 %0, %2, %3, 0 offen sc1\n\tbuffer_load_dwordx4 %1, %2, %3, 0 offen offset:256 sc1\n\ts_waitcnt vmcnt(0)"
                         : "=&v"(a), "=&v"(c) : "v"(off), "s"(d_ws) : "memory");
        }
        auto r = __builtin_amdgcn_permlane32_swap(__float_as_uint(__uint_as_float(a[0]) + __uint_as_float(c[0])), __float_as_uint(__uint_as_float(a[2]) + __uint_as_float(c[2])), false, false);
        const float v = __uint_as_float(r[0]) + __uint_as_float(r[1]);
        auto t = __builtin_amdgcn_permlane16_swap(__float_as_uint(v), __float_as_uint(v), false, false);
        const u32 v2 = __float_as_uint(__uint_as_float(t[0]) + __uint_as_float(t[1]));
        auto u = __builtin_amdgcn_permlane32_swap(v2, v2, false, false);
        const float ts = __uint_as_float(u[0]), tq = __uint_as_float(u[1]);
        mean = ts * (1.f / 512.f);
        rstd = __builtin_amdgcn_rsqf(fmaxf(tq * (1.f / 512.f) - mean * mean, 0.f) + 1e-5f);
    };
    auto regroup = [&](u32 (&w)[8]) {
#pragma unroll
        for (int j = 0; j < 2; ++j) {
            auto r0 = __builtin_amdgcn_permlane16_swap(w[4 * j], w[4 * j + 2], false, false);
            auto r1 = __builtin_amdgcn_permlane16_swap(w[4 * j + 1], w[4 * j + 3], false, false);
            w[4 * j] = r0[0]; w[4 * j + 1] = r1[0]; w[4 * j + 2] = r0[1]; w[4 * j + 3] = r1[1];
        }
    };
    auto wo_finish = [&](auto lc) {
        constexpr int LAYER = decltype(lc)::value;
        const int lane = opq(tid) & 63, li = lane & 15, lq = lane >> 4;
        u32 wx = L_WX + hd * 4096 + lane * 16;
        pin(wx);
        u32 po = 0, so = 0, tg = 0, oa = 0;
        if constexpr (R == 1) {
#pragma unroll
            for (int n = 0; n < 4; ++n) *(LAS f32x4*)(lds + wx + n * 1024) = accM[n];
        } else {
            po = pub_off(LAYER ? WS_ST3 : WS_ST1); so = stat_off(LAYER ? WS_ST3 : WS_ST1, TI); tg = lane < 32 ? tl : th;
            const int c = 2 * (lq & 1) + (lq >> 1);
            oa = LAYER ? (u32)((TI * 16 + li) * 1024 + mem * 128 + c * 16) : WS_X2 + (u32)((TI * 8 + mem) * 2048 + li * 128 + ((c ^ (li >> 1)) << 4));
            pin(po); pin(so); pin(tg); pin(oa);
        }
        BAR();
        STAMP(LAYER ? 25 : 42);
        if constexpr (R == 0) {
            f32x4 t[4];
#pragma unroll
            for (int n = 0; n < 4; ++n) t[n] = *(LAS f32x4*)(lds + wx + n * 1024);
            SB();
            float s = 0.f, q = 0.f, s2 = 0.f, q2 = 0.f;
#pragma unroll
            for (int n = 0; n < 4; ++n) {
                const f32x4 v = accM[n] + t[n];
                const u32 w0 = pk2(v[0], v[1]), w1 = pk2(v[2], v[3]);
                xw[2 * n] = w0; xw[2 * n + 1] = w1;
                s = dot2(w0, 0x3F803F80u, s); q = dot2(w0, w0, q); s2 = dot2(w1, 0x3F803F80u, s2); q2 = dot2(w1, w1, q2);
            }
            pub_stat(po, tg, s + s2, q + q2);
            CFENCE();
            if constexpr (!LAYER) load_y();
            regroup(xw);
            SB();
            STAMP(LAYER ? 46 : 43);
            WAIT_VM(0);
            STAMP(LAYER ? 26 : 44);
            if constexpr (!LAYER) {
#pragma unroll
                for (int i = 0; i < 8; ++i) pin(yw[i]);
            }
            u32x4 a, c;
            ald4s(a, so); ald4s(c, so + 256);
            if constexpr (!LAYER) {
                _Pragma("unroll 1") for (int spin = 0; spin < SPIN_CAP; ++spin) {
                    if (__builtin_expect(__all(max(max(max(yw[0], yw[1]), max(yw[2], yw[3])), max(max(yw[4], yw[5]), max(yw[6], yw[7]))) != SENT), 1)) break;
                    __builtin_amdgcn_s_sleep(1);
                    CFENCE();
                    load_y();
                }
                regroup(yw);
            }
            f32x2 xa[8], ya[8], xy[8];
#pragma unroll
            for (int i = 0; i < 8; ++i) {
                xa[i] = (f32x2){bflo(xw[i]), bfhi(xw[i])}; ya[i] = (f32x2){bflo(yw[i]), bfhi(yw[i])}; xy[i] = xa[i] * ya[i];
                pin(xa[i]); pin(ya[i]); pin(xy[i]);
            }
            WAIT_VM(0);
            pin(a); pin(c);
            float mean, rstd;
            fin_stat(a, c, so, mean, rstd);
            STAMP(LAYER ? 11 : 6);
            const float nm = -mean * rstd;
#pragma unroll
            for (int i = 0; i < 8; ++i) {
                const f32x2 o = xy[i] * rstd + (ya[i] * nm + xa[i]);
                xw[i] = pk2(o[0], o[1]);
                if (!LAYER) xw[i] = nosent(xw[i]);
            }
#pragma unroll
            for (int j = 0; j < 2; ++j) {
                const u32x4 o = {xw[4 * j], xw[4 * j + 1], xw[4 * j + 2], xw[4 * j + 3]};
                if constexpr (LAYER) __builtin_amdgcn_raw_buffer_store_b128(o, r_out, oa + j * 64, 0, OUT_AUX);
                else __builtin_amdgcn_raw_buffer_store_b128(o, r_ws, oa ^ (j * 64), 0, 16);
            }
            if constexpr (!LAYER) {
                float s = 0.f, q = 0.f, s2 = 0.f, q2 = 0.f;
#pragma unroll
                for (int i = 0; i < 8; i += 2) { s = dot2(xw[i], 0x3F803F80u, s); q = dot2(xw[i], xw[i], q); s2 = dot2(xw[i + 1], 0x3F803F80u, s2); q2 = dot2(xw[i + 1], xw[i + 1], q2); }
                pub_stat(po + (WS_ST2 - WS_ST1) * !(lane & 16), tg, s + s2, q + q2);
                CFENCE();
                regroup(xw);
#pragma unroll
                for (int i = 0; i < 8; ++i) { pin(xw[i]); pin(yw[i]); }
                STAMP(48);
                WAIT_VM(0);
            }
        }
    };
    wo_finish(IC<0>{});
    RBAR();
    STAMP(7);

    {
        const int l3 = opq(tid) & 63;
        const u32 g0 = (u32)((4 * grp - 1 + (hd >> 1)) * 16384 + (hd & 1) * 1024 + l3 * 16);
        const u32 g2 = (u32)((4 * grp + 3) * 16384 + hd * 512 + l3 * 16);
        auto x2_piece = [&](auto ic) {
            constexpr int i = decltype(ic)::value, s = i / 3, p = i % 3;
            const u32 kp = (u32)((s + rot) & 7);
            if constexpr (p == 0) dma(IC<4>{}, IC<L_CX + s * 10240>{}, d_x2, hb1k, g0, kp * 2048);
            else if constexpr (p == 1) dma(IC<4>{}, IC<L_CX + s * 10240 + 4096>{}, d_x2, hb1k, g0 + 32768, kp * 2048);
            else dma(IC<5>{}, IC<L_CX + s * 10240 + 8192>{}, d_x2, hb512, g2, kp * 2048);
        };
        u32 va = L_CX + hd * 1024 + l3 * 16, vq = L_CX + 8192 + hd * 512 + (l3 & 31) * 16 + (l3 >> 5) * 10240;
        pin(va); pin(vq);
        u32x4 vr[5];
        auto ver_read = [&](auto cc) {
            constexpr int c = decltype(cc)::value;
            u32 a = va + c * 20480, q = vq + c * 20480;
            pin(a); pin(q); CFENCE();
            vr[0] = *(LAS u32x4*)(lds + a); vr[1] = *(LAS u32x4*)(lds + a + 4096); vr[2] = *(LAS u32x4*)(lds + a + 10240); vr[3] = *(LAS u32x4*)(lds + a + 14336);
            vr[4] = *(LAS u32x4*)(lds + q);
        };
        auto ver_check = [&](auto cc) {
            constexpr int c = decltype(cc)::value;
            _Pragma("unroll 1") for (int spin = 0; spin < SPIN_CAP; ++spin) {
                u32 m0 = 0, m1 = 0;
#pragma unroll
                for (int i = 0; i < 5; ++i) { m0 = max(max(m0, vr[i][0]), vr[i][1]); m1 = max(max(m1, vr[i][2]), vr[i][3]); }
                if (__builtin_expect(__all(max(m0, m1) != SENT), 1)) break;
                __builtin_amdgcn_s_sleep(1);
                sfor<6 * c, 6 * c + 6>([&](auto ic) { x2_piece(ic); });
                WAIT_VM(0);
                ver_read(cc);
            }
        };
        const u32 xb0 = (u32)((l3 & 15) * 128 + (((l3 >> 4) ^ ((l3 & 15) >> 1)) << 4));
        if constexpr (R == 1) {
            STAMP(39);
            sfor<0, 12>([&](auto ic) { x2_piece(ic); });
            WAIT_VM(6);
            STAMP(8);
            ver_read(IC<0>{});
            ver_check(IC<0>{});
        } else {
            u32x4 sa, sc, ha, hc;
            const u32 so = stat_off(WS_ST2, 4 * grp + hd);
            const bool halo = hd == 0 && grp > 0;
            ald4s(sa, so); ald4s(sc, so + 256);
            if (halo) { ald4s(ha, so - 2048); ald4s(hc, so - 2048 + 256); }
            WAIT_VM(0);
            pin(sa); pin(sc);
            float mean, rstd;
            fin_stat(sa, sc, so, mean, rstd);
            if (l3 < 16) { *(LAS float*)(lds + L_TAB + 64 + hd * 64 + l3 * 4) = mean; *(LAS float*)(lds + L_TAB + 320 + 64 + hd * 64 + l3 * 4) = rstd; }
            if (halo) {
                pin(ha); pin(hc);
                fin_stat(ha, hc, so - 2048, mean, rstd);
                if (l3 < 16) { *(LAS float*)(lds + L_TAB + l3 * 4) = mean; *(LAS float*)(lds + L_TAB + 320 + l3 * 4) = rstd; }
            }
        }
        BAR();
        STAMP(13);
        u32 xa;
        auto pf_addr = [&](int st) { xa = L_CX + (st >> 1) * 10240 + (xb0 ^ ((st & 1) * 64)); pin(xa); };
        pf_addr(0);
#pragma unroll
        for (int mt = 0; mt < 5; ++mt) Ab[0][mt] = ldsA(xa + mt * 2048);
        sfor<0, 16>([&](auto stc) {
            constexpr int st = decltype(stc)::value;
            if constexpr (R == 1) {
                if constexpr (st == 2) { WAIT_VM(10); ver_read(IC<1>{}); }
                if constexpr (st == 3) ver_check(IC<1>{});
                if constexpr (st == 5) { WAIT_VM(6); ver_read(IC<2>{}); }
                if constexpr (st == 6) { ver_check(IC<2>{}); WAIT_VM(0); ver_read(IC<3>{}); }
                if constexpr (st == 7) ver_check(IC<3>{});
            }
            if constexpr (st == 3) { RBAR(); STAMP(49); }
            if constexpr (st == 7) { RBAR(); STAMP(50); CSTAMP(60); }
            if (st < 15) pf_addr(st + 1);
            if constexpr (st == 14) load_tab(true);
            SB();
            qkv_step(IC<1>{}, st, Ab[st & 1], [&](auto mc) {
                constexpr int mt = decltype(mc)::value;
                if (st < 15) Ab[(st + 1) & 1][mt] = ldsA(xa + mt * 2048);
                if constexpr (R == 1 && 12 + 5 * st + mt < 24) x2_piece(IC<12 + 5 * st + mt>{});
                SB();
            });
        });
    }
    STAMP(R ? 9 : 52);
    CSTAMP(61);
    attention(IC<1>{});
    STAMP(27);

    {
        wo_addr();
#pragma unroll
        for (int ks = 0; ks < 8; ++ks) load_F(ks);
        SB();
        WAIT_VM(0);
        RBAR();
        STAMP(10);
        load_o(WS_O2);
        SB();
        _Pragma("unroll 1") for (int spin = 0; spin < 64; ++spin) {
            u32 m0 = 0, m1 = 0;
#pragma unroll
            for (int ks = 0; ks < 8; ++ks) {
#pragma unroll
                for (int n = 0; n < 4; ++n) accM[n] = mfma32(F[ks][n], __builtin_bit_cast(s16x8, of[ks]), ks ? accM[n] : resid(n));
                m0 = max(max(m0, of[ks][0]), of[ks][1]); m1 = max(max(m1, of[ks][2]), of[ks][3]);
                SB();
            }
            if (__builtin_expect(__all(max(m0, m1) != SENT), 1)) break;
            _Pragma("unroll 1") for (int sp2 = 0; sp2 < SPIN_CAP; ++sp2) {
                CFENCE();
                __builtin_amdgcn_s_sleep(1);
                const u32 pv = __builtin_amdgcn_raw_buffer_load_b32(r_ws, WS_O2 + (u32)(TI * 16384 + R * 8192 + (opq(tid) & 15) * 512 + 508), 0, 16);
                if (__all(pv != SENT)) break;
            }
            CFENCE();
            load_o(WS_O2);
        }
        SB();
        STAMP(45);
    }
    wo_finish(IC<1>{});
    if constexpr (R == 1) {
        if (b == 0 && wave == 7) {
            const int l3 = opq(tid) & 63;
#pragma unroll
            for (int i = 0; i < 4; ++i) { flg = (u32x2){0u, 0u}; spin_flags(flg, WS_F1 + (u32)((i * 64 + l3) * 128)); }
            if (l3 == 0) __builtin_amdgcn_raw_buffer_store_b64(__builtin_bit_cast(u32x2, epoch + 1), r_ws, WS_EPOCH, 0, 16);
        }
    }
}

__global__ void __launch_bounds__(512, 2)
fused_kernel(const u16* __restrict__ gx, const u16* __restrict__ gwqkv, const u16* __restrict__ gwo,
             const u16* __restrict__ gbk, const u16* __restrict__ gbv, const u16* __restrict__ gsk,
             const u16* __restrict__ ghk, const u16* __restrict__ gdv, u16* __restrict__ gout, char* __restrict__ ws) {
    __shared__ __attribute__((aligned(16))) char lds_[L_TOTAL];
    const Args a = {gx, gwqkv, gwo, gbk, gbv, gsk, ghk, gdv, gout, ws};
    const int wave = __builtin_amdgcn_readfirstlane(threadIdx.x >> 6);
    if (wave < 4) body<0>(a, (LAS char*)lds_, wave); else body<1>(a, (LAS char*)lds_, wave);
}

extern "C" void kernel_launch(void* const* d_in, const int* in_sizes, int n_in,
                              void* d_out, int out_size, void* d_ws, size_t ws_size,
                              hipStream_t stream) {
    fused_kernel<<<256, 512, 0, stream>>>((const u16*)d_in[0], (const u16*)d_in[1], (const u16*)d_in[2],
                                          (const u16*)d_in[4], (const u16*)d_in[5], (const u16*)d_in[6],
                                          (const u16*)d_in[7], (const u16*)d_in[8], (u16*)d_out, (char*)d_ws);
}
```

```cpp
#define CODE_LINES 312
#define ROT 2
#define PRO 2
#define NO_PROBE
#include <hip/hip_runtime.h>
#include <stdint.h>

typedef unsigned short u16;
typedef unsigned u32;
typedef unsigned long long u64;
typedef short s16x4 __attribute__((ext_vector_type(4)));
typedef short s16x8 __attribute__((ext_vector_type(8)));
typedef __bf16 bf16x2 __attribute__((ext_vector_type(2)));
typedef float f32x2 __attribute__((ext_vector_type(2)));
typedef float f32x4 __attribute__((ext_vector_type(4)));
typedef u32 u32x2 __attribute__((ext_vector_type(2)));
typedef u32 u32x4 __attribute__((ext_vector_type(4)));
#define LAS __attribute__((address_space(3)))
#define DEV __device__ __forceinline__
typedef __amdgpu_buffer_rsrc_t rsrc_t;

constexpr u32 WS_EPOCH = 0;
constexpr u32 WS_F1 = 32768;
constexpr u32 WS_F2 = 65536;
constexpr u32 WS_ST0 = 98304;
constexpr u32 WS_ST1 = 131072;
constexpr u32 WS_ST2 = WS_ST1 + 262144;
constexpr u32 WS_ST3 = WS_ST2 + 262144;
constexpr u32 WS_UT = 1u << 20;
constexpr u32 WS_O1 = WS_UT + (2u << 20);
constexpr u32 WS_O2 = WS_O1 + (2u << 20);
constexpr u32 WS_Y2 = WS_O2 + (2u << 20);
constexpr u32 WS_X2 = WS_Y2 + (2u << 20);
constexpr u32 WS_END = WS_X2 + (2u << 20);

constexpr u32 L_SLOT = 34816;
constexpr u32 L_KV = 4 * L_SLOT;
constexpr u32 L_K = L_KV, L_V = L_KV + 9216;
constexpr u32 L_SK = L_KV + 17408;
constexpr u32 L_WO = 0;
constexpr u32 L_CX = 65536;
constexpr u32 L_HC = L_CX;
constexpr u32 L_UR = L_CX + 69632;
constexpr u32 L_WX = L_CX + 40960;
constexpr u32 L_EX = 147456;
constexpr u32 L_TAB = L_EX + 12288;
constexpr u32 L_CS = L_TAB + 1024;
constexpr u32 L_FLG = L_TAB + 2048;
constexpr u32 L_DUMMY = L_FLG + 1024;
constexpr u32 L_TOTAL = 160 * 1024;
static_assert(L_DUMMY + 1024 <= L_TOTAL, "LDS map");

#define WO_POS 0
#define OUT_AUX 0
constexpr int SPIN_CAP = 1 << 14;
constexpr u32 SENT = 0xFFFFFFFFu;

#define WAIT_VM(n) asm volatile("s_waitcnt vmcnt(%0)" ::"i"(n) : "memory")
#define BAR() do { asm volatile("s_waitcnt lgkmcnt(0)" ::: "memory"); __builtin_amdgcn_s_barrier(); asm volatile("" ::: "memory"); } while (0)
#define RBAR() do { asm volatile("" ::: "memory"); __builtin_amdgcn_s_barrier(); asm volatile("" ::: "memory"); } while (0)
#define CFENCE() asm volatile("" ::: "memory")
#define SB() __builtin_amdgcn_sched_barrier(0)
template <int N> struct IC { static constexpr int value = N; };
template <int I, int N, class F> DEV void sfor(F&& f) { if constexpr (I < N) { f(IC<I>{}); sfor<I + 1, N>(f); } }

DEV f32x4 mfma32(s16x8 a, s16x8 b, f32x4 c) { return __builtin_amdgcn_mfma_f32_16x16x32_bf16(a, b, c, 0, 0, 0); }
DEV f32x4 mfma16(s16x4 a, s16x4 b, f32x4 c) { return __builtin_amdgcn_mfma_f32_16x16x16bf16_1k(a, b, c, 0, 0, 0); }
DEV float vmax(float a, float b) { float r; asm("v_max_f32 %0, %1, %2" : "=v"(r) : "v"(a), "v"(b)); return r; }
DEV u32 pk2(float lo, float hi) { f32x2 f = {lo, hi}; bf16x2 b = __builtin_convertvector(f, bf16x2); return __builtin_bit_cast(u32, b); }
DEV float bflo(u32 w) { return __uint_as_float(w << 16); }
DEV float bfhi(u32 w) { return __uint_as_float(w & 0xffff0000u); }
DEV float dot2(u32 a, u32 b, float c) { return __builtin_amdgcn_fdot2_f32_bf16(__builtin_bit_cast(bf16x2, a), __builtin_bit_cast(bf16x2, b), c, false); }
DEV u32 nosent(u32 w) { return min(w, 0xFFFFFFFEu); }
DEV float xsum(float v) {
    auto r = __builtin_amdgcn_permlane32_swap(__float_as_uint(v), __float_as_uint(v), false, false);
    v = __uint_as_float(r[0]) + __uint_as_float(r[1]);
    auto s = __builtin_amdgcn_permlane16_swap(__float_as_uint(v), __float_as_uint(v), false, false);
    return __uint_as_float(s[0]) + __uint_as_float(s[1]);
}
DEV float xmax(float v) {
    auto r = __builtin_amdgcn_permlane32_swap(__float_as_uint(v), __float_as_uint(v), false, false);
    v = vmax(__uint_as_float(r[0]), __uint_as_float(r[1]));
    auto s = __builtin_amdgcn_permlane16_swap(__float_as_uint(v), __float_as_uint(v), false, false);
    return vmax(__uint_as_float(s[0]), __uint_as_float(s[1]));
}
DEV float rowsum16(float v) {
    v += __builtin_bit_cast(float, __builtin_amdgcn_update_dpp(0, __builtin_bit_cast(int, v), 0xB1, 0xf, 0xf, true));
    v += __builtin_bit_cast(float, __builtin_amdgcn_update_dpp(0, __builtin_bit_cast(int, v), 0x4E, 0xf, 0xf, true));
    v += __builtin_bit_cast(float, __builtin_amdgcn_update_dpp(0, __builtin_bit_cast(int, v), 0x141, 0xf, 0xf, true));
    v += __builtin_bit_cast(float, __builtin_amdgcn_update_dpp(0, __builtin_bit_cast(int, v), 0x140, 0xf, 0xf, true));
    return v;
}
DEV float bcast_row0(float v) { return __builtin_bit_cast(float, __builtin_amdgcn_update_dpp(0, __builtin_bit_cast(int, v), 0x150, 0xf, 0xf, true)); }
template <int OFF> DEV void ald4o(u32x4& d, u32 voff, u32x4 rs) { asm volatile("buffer_load_dwordx4 %0, %1, %2, 0 offen offset:%3 sc1" : "=v"(d) : "v"(voff), "s"(rs), "i"(OFF) : "memory"); }
template <class T> DEV void pin(T& v) { asm volatile("" : "+v"(v)); }
DEV int opq(int v) { asm volatile("" : "+v"(v)); return v; }
DEV s16x8 cat8(s16x4 a, s16x4 b) { return __builtin_shufflevector(a, b, 0, 1, 2, 3, 4, 5, 6, 7); }
DEV s16x4 pack4(f32x4 v) { u32x2 w = {pk2(v[0], v[1]), pk2(v[2], v[3])}; return __builtin_bit_cast(s16x4, w); }
DEV u64 mix64(u64 z) { z += 0x9E3779B97F4A7C15ull; z = (z ^ (z >> 30)) * 0xBF58476D1CE4E5B9ull; z = (z ^ (z >> 27)) * 0x94D049BB133111EBull; return z ^ (z >> 31); }

struct ConvSched {
    int n[4], f[18], i[18], slot[64];
    constexpr ConvSched(int R) : n{}, f{}, i{}, slot{} {
        constexpr int LEAD = 9;
        const int N0[4] = {0, 3, 4, 7};
        for (int k = 0; k < 4; ++k) n[k] = N0[k] ^ R;
        int c = 0;
        for (int ff = 0; ff < 8; ++ff) for (int k = 3; k >= 0; --k) if (ff <= n[k]) { f[c] = ff; i[c] = k; ++c; }
        for (int k = 0; k < 64; ++k) slot[k] = 1000;
        for (int p = 71; p >= 0; --p) {
            const int e = p / 18, ff = f[p % 18], g = n[i[p % 18]] - ff, s = p - LEAD < -1 ? -1 : p - LEAD;
            slot[e * 16 + ff] = s; slot[e * 16 + 8 + g] = s;
        }
    }
};

#define STAMP(i) do { } while (0)
#define CSTAMP(i) do { } while (0)

struct Args { const u16 *gx, *gwqkv, *gwo, *gbk, *gbv, *gsk, *ghk, *gdv; u16* gout; char* ws; };

template <int R>
DEV void body(const Args& A_, LAS char* const lds, const int wave, const u64 pc0) {
    const u16* gx = A_.gx; char* ws = A_.ws;
    STAMP(0);
    const int b = blockIdx.x, tid = threadIdx.x, lane = tid & 63;
    const int hd = wave & 3;
    const int grp = (b & 7) * 4 + (b >> 6), mem = (b >> 3) & 7;
    const int T0 = grp * 64;
    const int rot = (mem + ROT * (grp & 3)) & 7;

    u64 epoch;
    asm volatile("s_load_dwordx2 %0, %1, 0x0" : "=s"(epoch) : "s"(ws));
    u32 dw;
    asm volatile("s_load_dword %0, %1, 0x0" : "=s"(dw) : "s"(A_.gdv + 2 * b));
    const rsrc_t r_x = __builtin_amdgcn_make_buffer_rsrc((void*)gx, 0, 2048 * 512 * 2, 0x00020000);
    const rsrc_t r_ws = __builtin_amdgcn_make_buffer_rsrc((void*)ws, 0, WS_END, 0x00020000);
    const rsrc_t r_out = __builtin_amdgcn_make_buffer_rsrc((void*)A_.gout, 0, 2048 * 512 * 2, 0x00020000);
    const u32 ldsbase = (u32)(size_t)lds;
    auto mkdesc = [](const void* p, u32 bytes) { const u64 a = (u64)p; return (u32x4){(u32)a, (u32)(a >> 32) & 0xffffu, bytes, 0x00020000u}; };
    const u32x4 d_x = mkdesc(gx, 2048 * 512 * 2), d_w = mkdesc(A_.gwqkv, 512 * 1536 * 2), d_wo = mkdesc(A_.gwo, 512 * 512 * 2);
    const u32x4 d_k = mkdesc(A_.gbk, 2048 * 512 * 2), d_v = mkdesc(A_.gbv, 2048 * 512 * 2), d_sk = mkdesc(A_.gsk, 512 * 4 * 2);
    const u32x4 d_h = mkdesc(A_.ghk, 512 * 2048 * 2), d_x2 = mkdesc(ws + WS_X2, 2u << 20), d_ws = mkdesc(ws, WS_END);
    const u32 wb1k = ldsbase + wave * 1024, hb1k = ldsbase + hd * 1024, hb512 = ldsbase + hd * 512;
#define DMA_ASM(pre, mods, post) asm volatile("s_add_u32 m0, %0, %4\n\t" pre "buffer_load_dwordx4 %1, %2, %3 offen " mods "lds" post \
                     :: "s"(sbase), "v"(voff), "s"(rs), "s"(soff), "i"(IMM) : "memory", "scc")
    const u32 xmlo = hd == 0 ? ~0xFFu : ~0u;
    auto dma = [&](auto kind, auto imm, u32x4 rs, u32 sbase, u32 voff, u32 soff) {
        constexpr int K = decltype(kind)::value; constexpr int IMM = decltype(imm)::value;
        if constexpr (K == 0) DMA_ASM("s_nop 0\n\t", "", "");
        else if constexpr (K == 1) DMA_ASM("s_mov_b32 exec_hi, 0\n\t", "", "\n\ts_mov_b32 exec_hi, -1");
        else if constexpr (K == 2) {
            const u32 ml = xmlo;
            asm volatile("s_add_u32 m0, %0, %4\n\ts_mov_b32 exec_lo, %5\n\tbuffer_load_dwordx4 %1, %2, %3 offen lds\n\ts_mov_b32 exec_lo, -1"
                         :: "s"(sbase), "v"(voff), "s"(rs), "s"(soff), "i"(IMM), "s"(ml) : "memory", "scc");
        }
        else if constexpr (K == 4) DMA_ASM("s_nop 0\n\t", "sc1 ", "");
        else DMA_ASM("s_mov_b32 exec_hi, 0\n\t", "sc1 ", "\n\ts_mov_b32 exec_hi, -1");
    };
    auto ald4 = [&](u32x4& d, u32x4 rs, u32 voff) { asm volatile("buffer_load_dwordx4 %0, %1, %2, 0 offen" : "=v"(d) : "v"(voff), "s"(rs) : "memory"); };
    auto ald4s = [&](u32x4& d, u32 voff) { asm volatile("buffer_load_dwordx4 %0, %1, %2, 0 offen sc1" : "=v"(d) : "v"(voff), "s"(d_ws) : "memory"); };
    auto ald2s = [&](u32x2& d, u32 voff) { asm volatile("buffer_load_dwordx2 %0, %1, %2, 0 offen sc1" : "=v"(d) : "v"(voff), "s"(d_ws) : "memory"); };

    const int pr = lane >> 3, pc = lane & 7;
    const int xr0 = 8 * hd + pr, xr2 = 64 + 4 * hd + pr;
    const u32 xv0 = (u32)((T0 - 16 + xr0) * 1024 + ((pc ^ ((xr0 >> 1) & 7)) << 4));
    const u32 xv2 = (u32)((T0 - 16 + xr2) * 1024 + ((pc ^ ((xr2 >> 1) & 7)) << 4));
    const int wfw = ((xr0 >> 1) & 1) | (((xr0 >> 3) & 1) << 1);
    const u32 wv0 = (u32)(xr0 * 3072 + mem * 128 + ((((pc >> 1) ^ wfw) * 2 + (pc & 1)) << 4));
    auto issue_piece = [&](auto sc, auto ic) {
        constexpr int s = decltype(sc)::value, i = decltype(ic)::value;
        const u32 kp = (u32)((s + rot) & 7);
        constexpr int sb = (s & 3) * L_SLOT;
        if constexpr (i == 0) dma(IC<2>{}, IC<sb>{}, d_x, hb1k, xv0, kp * 128);
        else if constexpr (i == 1) dma(IC<0>{}, IC<sb + 4096>{}, d_x, hb1k, xv0 + 32768, kp * 128);
        else if constexpr (i == 2) dma(IC<1>{}, IC<sb + 8192>{}, d_x, hb512, xv2, kp * 128);
        else dma(IC<0>{}, IC<sb + 10240 + (i - 3) * 4096>{}, d_w, hb1k, wv0, kp * (64 * 3072) + ((i - 3) >> 1) * 1024 + ((i - 3) & 1) * (32 * 3072));
    };
    auto issue_wo = [&](auto ic) {
        constexpr int i = decltype(ic)::value;
        const int l2 = opq(tid) & 63, pr = l2 >> 3, pc = l2 & 7;
        const u32 wov = (u32)((8 * hd + pr) * 1024 + mem * 128 + ((((pc >> 1) ^ (((pr >> 1) & 1) | ((hd & 1) << 1))) * 2 + (pc & 1)) << 4));
        const u32 j = (u32)((i + 2 * grp) & 7);
        dma(IC<0>{}, IC<L_WO + (i & 8) * 4096>{}, d_wo, hb1k + j * 4096, wov, ((i & 8) + j) * 32768);
    };
    auto issue_kv = [&](int i) {
        const int l2 = opq(tid) & 63, pr = l2 >> 3, pc = l2 & 7, p = hd + 4 * (i & 1);
        const u32 cb = (u32)(mem * 128);
        if (i < 2) dma(IC<0>{}, IC<L_K>{}, d_k, hb1k + (i & 1) * 4096, (u32)((T0 - 8 + 8 * p + pr) * 1024) + cb + ((pc ^ p) << 4), 0);
        else if (i < 4) dma(IC<0>{}, IC<L_V>{}, d_v, hb1k + (i & 1) * 4096, (u32)((T0 + 8 * p + pr) * 1024) + cb + ((pc ^ p) << 4), 0);
        else if (hd == 0) dma(IC<0>{}, IC<L_K + 8192>{}, d_k, ldsbase, (u32)((T0 - 8 + 64 + pr) * 1024) + cb + (pc << 4), 0);
        else dma(IC<0>{}, IC<L_SK>{}, d_sk, ldsbase, (u32)(mem * 512 + l2 * 16), 0);
    };

    const int srow = T0 + 8 * mem + 2 * hd;
    u32x4 xrow[2];
    sfor<0, 9>([&](auto ic) { if constexpr ((decltype(ic)::value & 1) != R) issue_piece(IC<0>{}, ic); });
    sfor<0, 9>([&](auto ic) { if constexpr ((decltype(ic)::value & 1) != R) issue_piece(IC<1>{}, ic); });
    {
        const u32 ln = (u32)((b >> 3) * 8 + wave);
        const u64 pa = (pc0 & ~127ull) + ln * 128, pb = pa + (ln + 256 < CODE_LINES ? 32768 : 0);
        u32 j0, j1;
        asm volatile("s_load_dword %0, %2, 0x0\n\ts_load_dword %1, %3, 0x0\n\ts_waitcnt lgkmcnt(0)" : "=&s"(j0), "=&s"(j1) : "s"(pa), "s"(pb));
    }
    asm volatile("s_waitcnt lgkmcnt(0)" : "+s"(epoch), "+s"(dw));
    const u64 tag = mix64(epoch);
    const u32 tl = (u32)tag, th = (u32)(tag >> 32);
    if (R == 1 && wave == 7 && lane < 32) *(LAS u32x4*)(lds + (lane >> 3) * L_SLOT + (lane & 7) * 16) = (u32x4){0x3F803F80u, 0x3F803F80u, 0x3F803F80u, 0x3F803F80u};

    auto trr = [&](u32 off) { return __builtin_amdgcn_ds_read_tr16_b64_v4i16((LAS s16x4*)(lds + off)); };
    auto ldsA = [&](u32 off) { return *(LAS s16x8*)(lds + off); };
    auto flag_ok = [&](const u32x2& f) { return __all(f[0] == tl && f[1] == th); };
    auto spin_flags = [&](u32x2& f, u32 off) {
        _Pragma("unroll 1") for (int spin = 0; spin < SPIN_CAP; ++spin) {
            if (__builtin_expect(flag_ok(f), 1)) break;
            __builtin_amdgcn_s_sleep(2);
            asm volatile("buffer_load_dwordx2 %0, %1, %2, 0 offen sc1\n\ts_waitcnt vmcnt(0)" : "=&v"(f) : "v"(off), "s"(d_ws) : "memory");
        }
    };

    s16x8 Wq[16], Wm[16];
    f32x4 accM[5], accQ[5];
    const f32x4 zero4 = {0.f, 0.f, 0.f, 0.f};
    const s16x8 ones8 = {0x3F80, 0x3F80, 0x3F80, 0x3F80, 0x3F80, 0x3F80, 0x3F80, 0x3F80};
    s16x8 Ab[3][5];
    auto qkv_step = [&](auto lc, int st, const s16x8 (&A)[5], auto fill) {
        sfor<0, 5>([&](auto mc) {
            constexpr int mt = decltype(mc)::value;
            const f32x4 cm = st ? accM[mt] : zero4;
            accM[mt] = R ? mfma32(A[mt], Wm[st], cm) : mfma32(Wm[st], A[mt], cm);
            if constexpr (R == 0 && (mt > 0 || decltype(lc)::value == 0)) accQ[mt] = mfma32(Wq[st], A[mt], st ? accQ[mt] : zero4);
            fill(mc);
        });
    };

    float m1[5], r1[5]; f32x4 m4[5], r4[5], csK, csQ;
    auto load_tab = [&](bool cs) {
        const int l2 = opq(tid) & 63;
        u32 ta = L_TAB + (R ? (l2 >> 4) * 16 : (l2 & 15) * 4);
        pin(ta);
        if (R == 0 && cs) { csK = *(LAS f32x4*)(lds + L_CS + hd * 128 + (l2 >> 4) * 16); csQ = *(LAS f32x4*)(lds + L_CS + hd * 128 + 64 + (l2 >> 4) * 16); }
#pragma unroll
        for (int mt = 0; mt < 5; ++mt) {
            if (R == 0) { m1[mt] = *(LAS float*)(lds + ta + mt * 64); r1[mt] = *(LAS float*)(lds + ta + 320 + mt * 64); }
            else { m4[mt] = *(LAS f32x4*)(lds + ta + mt * 64); r4[mt] = *(LAS f32x4*)(lds + ta + 320 + mt * 64); }
        }
    };

    u32x4 hr[4];
    u32x2 flg;
    {
        const int li = lane & 15, lq = lane >> 4, tq = li >> 2, tp = lane & 3;
        const u32 xa0 = (u32)(li * 128 + ((lq ^ (li >> 1)) << 4));
        const u32 wa = (u32)((8 * lq + tq) * 128 + ((hd ^ (((tq >> 1) & 1) | ((lq & 1) << 1))) << 5) + tp * 8);
        u32 xa, wb, kw[11], vw[8];
        u32x4 s0v, skw;
        auto pf_addr = [&](int st) {
            const u32 sb = (u32)((st >> 1) & 3) * L_SLOT;
            xa = sb + (xa0 ^ ((st & 1) * 64)); wb = sb + 10240 + (st & 1) * 4096 + wa;
            asm volatile("" : "+v"(xa), "+v"(wb));
        };
        auto pf_part = [&](int st, int mt) {
            if (mt == 0) Wm[st] = cat8(trr(wb + (1 + R) * 8192), trr(wb + (1 + R) * 8192 + 512));
            Ab[st & 1][mt] = ldsA(xa + mt * 2048);
            if (R == 0 && mt == 1) Wq[st] = cat8(trr(wb), trr(wb + 512));
        };
        STAMP(15);
        WAIT_VM(R ? 5 : 4);
        BAR();
        STAMP(16);
        pf_addr(0);
#pragma unroll
        for (int mt = 0; mt < 5; ++mt) pf_part(0, mt);
        sfor<0, 16>([&](auto stc) {
            constexpr int st = decltype(stc)::value;
            if constexpr ((st & 1) && st < 15) {
                constexpr int s1 = (st >> 1) + 1;
                if constexpr (R == 0) {
                    if constexpr (s1 == 1) WAIT_VM(7);
                    if constexpr (s1 == 2) WAIT_VM(1);
                    if constexpr (s1 == 5) WAIT_VM(0);
                } else {
                    constexpr int WN[8] = {0, 9, 9, 9, 9, 9, 10, 10};
                    WAIT_VM(WN[s1]);
                    if constexpr (s1 == 7) {
                        pin(s0v);
                        const int l2 = opq(tid) & 63, tr = hd * 20 + min(l2, 19), row = T0 - 16 + tr;
                        _Pragma("unroll 1") for (int spin = 0; spin < SPIN_CAP; ++spin) {
                            if (__builtin_expect(__all(row < 0 || (s0v[1] == tl && s0v[3] == th)), 1)) break;
                            __builtin_amdgcn_s_sleep(2);
                            ald4s(s0v, WS_ST0 + (u32)(max(row, 0) * 16));
                            WAIT_VM(0);
                            pin(s0v);
                        }
                        *(LAS u32*)(lds + L_TAB + tr * 4) = row < 0 ? 0u : s0v[0];
                        *(LAS u32*)(lds + L_TAB + 320 + tr * 4) = row < 0 ? 0x3F800000u : s0v[2];
                        asm volatile("s_waitcnt lgkmcnt(0)" ::: "memory");
                    }
                }
                RBAR();
                STAMP(16 + s1);
                if constexpr (s1 == 5 && R == 0) { if (tid == 0) __builtin_amdgcn_raw_buffer_store_b64((u32x2){tl, th}, r_ws, WS_F1 + (mem * 32 + grp) * 128, 0, 16); CFENCE(); }
                if constexpr (s1 == 5 && R == 1) { const int row = T0 - 16 + hd * 20 + min(opq(tid) & 63, 19); ald4s(s0v, WS_ST0 + (u32)(max(row, 0) * 16)); }
            }
            if (st < 15) pf_addr(st + 1);
            if constexpr (st == 14) load_tab(false);
            SB();
            qkv_step(IC<0>{}, st, Ab[st & 1], [&](auto mc) {
                constexpr int mt = decltype(mc)::value;
                if (st < 15) pf_part(st + 1, mt);
                if constexpr (R == 1) {
                    if constexpr (st == 0) { issue_piece(IC<2>{}, IC<2 * mt>{}); if constexpr (mt < 4) issue_piece(IC<2>{}, IC<2 * mt + 1>{}); }
                    if constexpr (st >= 1 && st <= 10 && ((st - 1) & 1) * 5 + mt < 9) issue_piece(IC<((st - 1) >> 1) + 3>{}, IC<((st - 1) & 1) * 5 + mt>{});
                    if constexpr (st == 11 && mt == 0) { const u32 ho = (u32)(b * 8192 + opq(tid) * 16); ald4(hr[2], d_h, ho); ald4(hr[3], d_h, ho + 16); }
                    if constexpr (st == 13 && mt == 0) { if (wave == 7) { const int l2 = opq(tid) & 63; ald2s(flg, WS_F1 + (u32)((l2 < 32 ? (b >> 5) * 32 + l2 : (l2 & 7) * 32 + grp) * 128)); } }
                    if constexpr (st >= 11 && st <= 14 && mt < 4) issue_wo(IC<(st - 11) * 4 + mt>{});
                } else {
                    if constexpr (st == 0) {
                        if constexpr (mt == 0) { const u32 xo = (u32)(srow * 1024 + (opq(tid) & 63) * 16); ald4(xrow[0], d_x, xo); ald4(xrow[1], d_x, xo + 1024); }
                        issue_kv(mt);
                    }
                    if constexpr (st == 11 && mt < 2) { const u32 ho = (u32)(b * 8192 + mt * 4096 + opq(tid) * 16); ald4(hr[2 * mt], d_h, ho); ald4(hr[2 * mt + 1], d_h, ho + 16); }
                }
                SB();
            });
            if constexpr (R == 0 && st == 2) {
                WAIT_VM(5);
                pin(xrow[0]); pin(xrow[1]);
                float s[2] = {0.f, 0.f}, q[2] = {0.f, 0.f};
#pragma unroll
                for (int i = 0; i < 4; ++i)
#pragma unroll
                    for (int k = 0; k < 2; ++k) { s[k] = dot2(xrow[k][i], 0x3F803F80u, s[k]); q[k] = dot2(xrow[k][i], xrow[k][i], q[k]); }
                auto ss = __builtin_amdgcn_permlane32_swap(__float_as_uint(s[0]), __float_as_uint(s[1]), false, false);
                auto qq = __builtin_amdgcn_permlane32_swap(__float_as_uint(q[0]), __float_as_uint(q[1]), false, false);
                float sv1 = rowsum16(__uint_as_float(ss[0]) + __uint_as_float(ss[1])), qv1 = rowsum16(__uint_as_float(qq[0]) + __uint_as_float(qq[1]));
                auto s2 = __builtin_amdgcn_permlane16_swap(__float_as_uint(sv1), __float_as_uint(sv1), false, false);
                auto q2 = __builtin_amdgcn_permlane16_swap(__float_as_uint(qv1), __float_as_uint(qv1), false, false);
                const float m = (__uint_as_float(s2[0]) + __uint_as_float(s2[1])) * (1.f / 512.f);
                const float r = __builtin_amdgcn_rsqf(fmaxf((__uint_as_float(q2[0]) + __uint_as_float(q2[1])) * (1.f / 512.f) - m * m, 0.f) + 1e-5f);
                const u32x4 sv = {__float_as_uint(m), tl, __float_as_uint(r), th};
                const int l2 = opq(tid) & 63;
                __builtin_amdgcn_raw_buffer_store_b128(sv, r_ws, (l2 & 31) == 0 ? WS_ST0 + (u32)((srow + (l2 >> 5)) * 16) : 0xFFFFFF00u, 0, 16);
                CFENCE();
                SB();
            }
            if constexpr (R == 0 && (st == 4 || st == 6)) {
                const int l2 = opq(tid) & 63, tg = l2 & 7, cp = 8 * hd + (l2 >> 3);
                if constexpr (st == 4) {
                    skw = *(LAS u32x4*)(lds + L_SK + cp * 16);
                    u32 ka = L_K + tg * 1024 + ((((cp >> 2) ^ tg) & 7) << 4) + (cp & 3) * 4;
                    u32 kb = L_K + tg * 1024 + ((((cp >> 2) ^ (tg + 1)) & 7) << 4) + (cp & 3) * 4;
                    pin(ka); pin(kb);
#pragma unroll
                    for (int i = 0; i < 11; ++i) kw[i] = *(LAS u32*)(lds + ((i + 5 < 8) ? ka : kb) + (i + 5) * 128);
#pragma unroll
                    for (int i = 0; i < 8; ++i) vw[i] = *(LAS u32*)(lds + ka + (L_V - L_K) + i * 128);
                    SB();
                }
                constexpr bool HI = st == 6;
                auto ex = [&](u32 w) { return HI ? bfhi(w) : bflo(w); };
                const float a0 = bflo(skw[2 * HI]), a1 = bfhi(skw[2 * HI]), a2 = bflo(skw[2 * HI + 1]), a3 = bfhi(skw[2 * HI + 1]);
                float ua[8];
#pragma unroll
                for (int i = 0; i < 8; ++i) ua[i] = (ex(kw[i]) * a3 + ex(kw[i + 1]) * a2 + ex(kw[i + 2]) * a1 + ex(kw[i + 3]) * a0) * ex(vw[i]);
                const u32x4 oa = {pk2(ua[7], ua[6]), pk2(ua[5], ua[4]), pk2(ua[3], ua[2]), pk2(ua[1], ua[0])};
                __builtin_amdgcn_raw_buffer_store_b128(oa, r_ws, WS_UT + (u32)((mem * 64 + 2 * cp) * 4096 + (2040 - T0 - 8 * tg) * 2), HI * 4096, 16);
                __builtin_amdgcn_raw_buffer_store_b128((u32x4){SENT, SENT, SENT, SENT}, r_ws, (u32)((4 * grp + hd) * 16384 + mem * 2048 + HI * 1024 + l2 * 16), WS_O1, 16);
                CFENCE();
                SB();
            }
        });
    }
    STAMP(R ? 1 : 53);

    u32x4 uu;
    float csV;
    auto attention = [&](auto lc) {
        constexpr int LAYER = decltype(lc)::value;
        constexpr int SBASE = LAYER ? 32 : 28;
        const int lane = opq(tid) & 63, li = lane & 15, lq = lane >> 4;
        u32 ex = L_EX + hd * 2048 + lane * 8;
        pin(ex);
        u32 f1ok = 1;
        if constexpr (R == 0) {
            if constexpr (LAYER == 0) {
#pragma unroll
                for (int i = 0; i < 4; ++i) { csK[i] = bcast_row0(accM[0][i]); csQ[i] = bcast_row0(accQ[0][i]); }
                if (li == 0) { *(LAS f32x4*)(lds + L_CS + hd * 128 + lq * 16) = csK; *(LAS f32x4*)(lds + L_CS + hd * 128 + 64 + lq * 16) = csQ; }
            }
            const int par = li & 1;
            const bool cur0 = (4 * lq + par) <= li, cur1 = (4 * lq + par + 2) <= li;
            s16x4 Kb[5], Qb[5];
#pragma unroll
            for (int mt = 0; mt < 5; ++mt) {
                Kb[mt] = pack4((accM[mt] - m1[mt] * csK) * r1[mt]);
                if (mt) Qb[mt] = pack4(accQ[mt] - m1[mt] * csQ);
            }
            f32x4 Sc[5], Sp[5];
#pragma unroll
            for (int mt = 1; mt < 5; ++mt) { Sc[mt] = mfma16(Kb[mt], Qb[mt], zero4); Sp[mt] = mfma16(Kb[mt - 1], Qb[mt], zero4); }
            float s0[5], s1[5], mx[5];
#pragma unroll
            for (int mt = 1; mt < 5; ++mt) {
                const float sp0 = Sp[mt][0], sp1 = Sp[mt][1], sp2 = Sp[mt][2], sp3 = Sp[mt][3], c0 = Sc[mt][0], c1 = Sc[mt][1], c2 = Sc[mt][2], c3 = Sc[mt][3];
                float p0 = par ? sp1 : sp0, p1 = par ? sp3 : sp2, q0 = par ? c1 : c0, q1 = par ? c3 : c2;
                asm volatile("" : "+v"(p0), "+v"(p1), "+v"(q0), "+v"(q1));
                if (mt == 1 && grp == 0) { p0 = -3.0e38f; p1 = -3.0e38f; }
                s0[mt] = cur0 ? q0 : p0; s1[mt] = cur1 ? q1 : p1;
                mx[mt] = vmax(s0[mt], s1[mt]);
            }
#pragma unroll
            for (int mt = 1; mt < 5; ++mt) { auto r = __builtin_amdgcn_permlane32_swap(__float_as_uint(mx[mt]), __float_as_uint(mx[mt]), false, false); mx[mt] = vmax(__uint_as_float(r[0]), __uint_as_float(r[1])); }
#pragma unroll
            for (int mt = 1; mt < 5; ++mt) { auto r = __builtin_amdgcn_permlane16_swap(__float_as_uint(mx[mt]), __float_as_uint(mx[mt]), false, false); mx[mt] = vmax(__uint_as_float(r[0]), __uint_as_float(r[1])); }
#pragma unroll
            for (int mt = 1; mt < 5; ++mt) {
                const float sc = r1[mt] * (0.25f * 1.44269504088896341f);
                const float e0 = __builtin_amdgcn_exp2f((s0[mt] - mx[mt]) * sc), e1 = __builtin_amdgcn_exp2f((s1[mt] - mx[mt]) * sc);
                *(LAS u32x2*)(lds + ex + (mt - 1) * 512) = (u32x2){pk2(e0, e0), pk2(e1, e1)};
            }
            STAMP(SBASE + 2);
            BAR();
            STAMP(SBASE + 1);
            if constexpr (LAYER == 0) f1ok = __builtin_amdgcn_readfirstlane(*(LAS u32*)(lds + L_FLG));
        } else {
            if constexpr (LAYER == 0) {
                auto s = __builtin_amdgcn_permlane16_swap(__float_as_uint(accM[0][0]), __float_as_uint(accM[0][0]), false, false);
                auto t = __builtin_amdgcn_permlane32_swap(s[0], s[0], false, false);
                csV = __uint_as_float(t[0]);
                if (wave == 7) { WAIT_VM(8); pin(flg); const u32 ok = flag_ok(flg); *(LAS u32*)(lds + L_FLG) = ok; }
            }
            const int par = li & 1;
            const bool cur0 = (4 * lq + par) <= li, cur1 = (4 * lq + par + 2) <= li;
            const u32 pm = par ? 0xffff0000u : 0x0000ffffu;
            u32 mC0 = cur0 ? pm : 0u, mP0 = cur0 ? 0u : pm, mC1 = cur1 ? pm : 0u, mP1 = cur1 ? 0u : pm;
            u32 oo = (LAYER ? WS_O2 : WS_O1) + (u32)((4 * grp + (lq & 1)) * 16384 + (4 * mem + hd) * 512 + (lq >> 1) * 256 + li * 16);
            asm volatile("" : "+v"(mC0), "+v"(mP0), "+v"(mC1), "+v"(mP1), "+v"(oo));
            s16x4 Vb[5];
#pragma unroll
            for (int mt = 0; mt < 5; ++mt) { Vb[mt] = pack4((accM[mt] - m4[mt] * csV) * r4[mt]); pin(Vb[mt]); }
            STAMP(SBASE);
            BAR();
            STAMP(SBASE + 1);
            u32x2 pw[4];
#pragma unroll
            for (int j = 0; j < 4; ++j) pw[j] = *(LAS u32x2*)(lds + ex + j * 512);
            if constexpr (LAYER == 0) {
                f1ok = __builtin_amdgcn_readfirstlane(*(LAS u32*)(lds + L_FLG));
                if (f1ok) ald4s(uu, WS_UT + (u32)(b * 8192 + opq(tid) * 16));
            }
            SB();
            f32x4 O[4], L[4];
#pragma unroll
            for (int j = 0; j < 4; ++j) {
                const s16x8 P = __builtin_bit_cast(s16x8, (u32x4){pw[j][0] & mC0, pw[j][1] & mC1, pw[j][0] & mP0, pw[j][1] & mP1});
                L[j] = mfma32(ones8, P, zero4);
                O[j] = mfma32(cat8(Vb[j + 1], Vb[j]), P, zero4);
            }
            u32 ow[4][2];
#pragma unroll
            for (int j = 0; j < 4; ++j) {
                const float inv = __builtin_amdgcn_rcpf(L[j][0]);
                ow[j][0] = nosent(pk2(O[j][0] * inv, O[j][1] * inv)); ow[j][1] = nosent(pk2(O[j][2] * inv, O[j][3] * inv));
            }
#pragma unroll
            for (int j = 0; j < 4; j += 2) {
                auto r0 = __builtin_amdgcn_permlane16_swap(ow[j][0], ow[j + 1][0], false, false);
                auto r1 = __builtin_amdgcn_permlane16_swap(ow[j][1], ow[j + 1][1], false, false);
                __builtin_amdgcn_raw_buffer_store_b128((u32x4){r0[0], r1[0], r0[1], r1[1]}, r_ws, oo, j * 16384, 16);
            }
            CFENCE();
        }
        if constexpr (LAYER == 0 && R == 0) { if (f1ok) ald4s(uu, WS_UT + (u32)(b * 8192 + opq(tid) * 16)); }
        SB();
        return f1ok;
    };
    const u32 f1ok = attention(IC<0>{});
    {
        const u32x4 sv = {SENT, SENT, SENT, SENT};
        const int t2 = opq(tid);
        const u32 tb = (u32)((4 * grp + hd) * 16384 + mem * 2048 + R * 1024 + (t2 & 63) * 16);
        __builtin_amdgcn_raw_buffer_store_b128(sv, r_ws, tb, WS_O2, 16);
        __builtin_amdgcn_raw_buffer_store_b128(sv, r_ws, tb, WS_X2, 16);
        __builtin_amdgcn_raw_buffer_store_b128(sv, r_ws, (u32)(b * 8192 + t2 * 16), WS_Y2, 16);
        CFENCE();
        SB();
    }
    STAMP(2);

    const int TI = 4 * grp + hd;
    u32x4 of[8];
    auto load_o = [&](u32 ws_o) {
        const int lane = opq(tid) & 63;
#pragma unroll
        for (int i = 0; i < 8; ++i) of[i] = __builtin_amdgcn_raw_buffer_load_b128(r_ws, ws_o + (u32)(TI * 16384 + R * 8192 + lane * 16) + (i & 3) * 1024, (i >> 2) * 4096, 16);
    };
    u32 xw[8], yw[8];
    u32 woa[4];
    auto resid = [&](int n) { return R ? zero4 : (f32x4){bflo(xw[2 * n]), bfhi(xw[2 * n]), bflo(xw[2 * n + 1]), bfhi(xw[2 * n + 1])}; };
    s16x8 F[8][4];
    auto wo_addr = [&]() {
        const int l2 = opq(tid) & 63, lq = l2 >> 4, tq = (l2 & 15) >> 2, tp = l2 & 3;
#pragma unroll
        for (int n = 0; n < 4; ++n) woa[n] = L_WO + (u32)(R * 32768 + (8 * lq + tq) * 128 + ((n ^ (((lq & 1) << 1) | (tq >> 1))) << 5) + tp * 8);
    };
    auto load_F = [&](int ks) {
#pragma unroll
        for (int n = 0; n < 4; ++n) F[ks][n] = cat8(trr(woa[n] + ks * 4096), trr(woa[n] + ks * 4096 + 512));
    };

    {
        const int t2 = opq(tid), lane = t2 & 63, li = lane & 15, lq = lane >> 4;
        constexpr u32 OFFS = 0xEDA85410u;
        {
            const int j = t2 & 255;
            {
                if constexpr (R == 0) { if (f1ok) WAIT_VM(4); else WAIT_VM(3); }
                else WAIT_VM(5);
                u32 cb = L_HC + R * 34816 + (j + 2) * 16;
                pin(cb);
#pragma unroll
                for (int ch = R; ch < 2; ++ch) {
                    pin(hr[2 * ch]); pin(hr[2 * ch + 1]);
                    const u32 d[8] = {hr[2 * ch][0], hr[2 * ch][1], hr[2 * ch][2], hr[2 * ch][3], hr[2 * ch + 1][0], hr[2 * ch + 1][1], hr[2 * ch + 1][2], hr[2 * ch + 1][3]};
                    u32 e[7];
#pragma unroll
                    for (int i = 0; i < 7; ++i) e[i] = __builtin_amdgcn_alignbit(d[i + 1], d[i], 16);
                    if (ch > R) { cb += 34816; pin(cb); }
#pragma unroll
                    for (int p = (R ? 4 : 0); p < ((ch && !R) ? 4 : 8); ++p) {
                        const u32 po = (272 * p + ((OFFS >> (4 * p)) & 15)) * 16;
                        const int h = p >> 1;
                        const u32x4 w = (p & 1) ? (u32x4){e[h], e[h + 1], e[h + 2], e[h + 3]} : (u32x4){d[h], d[h + 1], d[h + 2], d[h + 3]};
                        *(LAS u32x4*)(lds + cb + po) = w;
                    }
                }
            }
            if constexpr (R == 0) {
                if (hd == 0) {
                    u32 hh[4];
#pragma unroll
                    for (int i = 0; i < 4; ++i) { const u32 a = __builtin_amdgcn_readfirstlane(hr[0][i]), c = __builtin_amdgcn_readfirstlane(hr[2][i]); hh[i] = (lane & 16) ? c : a; }
                    if (lane < 32) {
                        const int p = lane & 7;
                        const u32 po = (272 * p + ((OFFS >> (4 * p)) & 15)) * 16;
                        const unsigned __int128 hv = ((unsigned __int128)(((u64)hh[3] << 32) | hh[2]) << 64) | (((u64)hh[1] << 32) | hh[0]);
                        const unsigned __int128 sv = (p == 0 || !(lane & 8)) ? (unsigned __int128)0 : (hv << (16 * (8 - p)));
                        const u32x4 w = {(u32)sv, (u32)(sv >> 32), (u32)(sv >> 64), (u32)(sv >> 96)};
                        *(LAS u32x4*)(lds + L_HC + (lane >> 4) * 34816 + po + ((lane >> 3) & 1) * 16) = w;
                    }
                }
            }
            if (hd == 1 && lane < 30) *(LAS u32x4*)(lds + L_UR + R * 4576 + 4096 + lane * 16) = (u32x4){0, 0, 0, 0};
            STAMP(3);
            if (!f1ok) {
                const u32 fo = WS_F1 + (u32)(((lane & 8) ? (lane & 7) * 32 + grp : (b >> 5) * 32 + 31 - hd * 8 - (lane & 7)) * 128);
                flg = (u32x2){0u, 0u};
                spin_flags(flg, fo);
                ald4s(uu, WS_UT + (u32)(b * 8192 + t2 * 16));
            }
            if (f1ok) WAIT_VM(R ? 5 : 3); else WAIT_VM(0);
            pin(uu);
            *(LAS u32x4*)(lds + L_UR + R * 4576 + j * 16) = uu;
        }
        BAR();
        STAMP(4);
        const int ch = hd >> 1, e0 = (hd & 1) * 4;
        const int p = (li + 1) & 7;
        u32 aA = L_HC + ch * 34816 + (272 * p + ((OFFS >> (4 * p)) & 15) + ((li + 1) >> 3) + lq + 4 * e0) * 16;
        u32 aB = L_UR + ch * 4576 + (254 - 224 - 2 * li + lq + 4 * e0) * 16;
        asm volatile("" : "+v"(aA), "+v"(aB));
        f32x4 y[4];
        u32 ooff = WS_O1 + (u32)(TI * 16384 + R * 8192 + lane * 16);
        {
            constexpr ConvSched S(R);
            s16x8 A[4][8], B[4][8];
            auto loads_at = [&](auto pc) {
                constexpr int p = decltype(pc)::value;
                sfor<0, 64>([&](auto ic) {
                    constexpr int i = decltype(ic)::value, e = i >> 4, f = i & 7;
                    if constexpr (S.slot[i] == p) {
                        if constexpr (i & 8) B[e][f] = ldsA(aB + 64 * e + 512 * (7 - f)); else A[e][f] = ldsA(aA + 64 * (8 * f + e));
                    }
                });
            };
            loads_at(IC<-1>{});
            SB();
            sfor<0, 72>([&](auto pc) {
                constexpr int p = decltype(pc)::value, e = p / 18, f = S.f[p % 18], i = S.i[p % 18], g = S.n[i] - f;
                y[i] = mfma32(A[e][f], B[e][g], (e == 0 && f == 0) ? zero4 : y[i]);
                loads_at(pc);
                if constexpr (p == 54) WAIT_VM(0);
                if constexpr (p >= 55 && p < 71 && (p & 1)) {
                    constexpr int i = (p - 55) >> 1;
                    ald4o<(i & 3) * 1024>(of[i], ooff + (i >> 2) * 4096, d_ws);
                }
                SB();
            });
        }
        STAMP(14);
        RBAR();
        if (R == 0 && tid == 0) __builtin_amdgcn_raw_buffer_store_b64((u32x2){tl, th}, r_ws, WS_F2 + (grp * 8 + mem) * 128, 0, 16);
        {
            constexpr int N0[4] = {0, 3, 4, 7};
            u32 ra = L_HC + hd * 8192 + lane * 16;
            asm volatile("" : "+v"(ra));
#pragma unroll
            for (int i = 0; i < 4; ++i) *(LAS f32x4*)(lds + ra + (N0[i] ^ R) * 1024) = y[i];
        }
        SB();
        if constexpr (R == 0) {
#pragma unroll
            for (int n = 0; n < 4; ++n) { const u32x2 w = __builtin_amdgcn_raw_buffer_load_b64(r_x, (u32)((TI * 16 + li) * 1024 + mem * 128 + lq * 8) + n * 32, 0, 0); xw[2 * n] = w[0]; xw[2 * n + 1] = w[1]; }
        }
        wo_addr();
        SB();
        STAMP(37);
        BAR();
        STAMP(38);
        {
            const float D0 = bflo(dw), D1 = bfhi(dw);
            u32 ra = L_HC + wave * 1024 + lane * 16;
            const int tb = 256 * wave + 16 * li + 4 * lq;
            u32 ru = L_UR + (2044 - tb) * 2;
            asm volatile("" : "+v"(ra), "+v"(ru));
            const f32x4 y0 = *(LAS f32x4*)(lds + ra), y1 = *(LAS f32x4*)(lds + ra + 8192), y2 = *(LAS f32x4*)(lds + ra + 16384), y3 = *(LAS f32x4*)(lds + ra + 24576);
            const u32x2 ua = *(LAS u32x2*)(lds + ru), ub = *(LAS u32x2*)(lds + ru + 4576);
            load_F(0); load_F(1);
            SB();
            const f32x4 ya = y0 + y1, yb = y2 + y3;
            const u32x4 w = {nosent(pk2(ya[0] + bfhi(ua[1]) * D0, yb[0] + bfhi(ub[1]) * D1)), nosent(pk2(ya[1] + bflo(ua[1]) * D0, yb[1] + bflo(ub[1]) * D1)),
                             nosent(pk2(ya[2] + bfhi(ua[0]) * D0, yb[2] + bfhi(ub[0]) * D1)), nosent(pk2(ya[3] + bflo(ua[0]) * D0, yb[3] + bflo(ub[0]) * D1))};
            __builtin_amdgcn_raw_buffer_store_b128(w, r_ws, WS_Y2 + (u32)(b * 8192 + tb * 4), 0, 16);
        }
        SB();
        STAMP(5);
        WAIT_VM(1);
        STAMP(40);

        u32 f2oth = 0;
        _Pragma("unroll 1") for (int attempt = 0; attempt < 64; ++attempt) {
            sfor<0, 8>([&](auto ic) { pin(of[decltype(ic)::value]); });
            u32 m0 = 0, m1 = 0;
#pragma unroll
            for (int ks = 0; ks < 8; ++ks) {
                load_F((ks + 2) & 7);
                if (R == 1 && ks == 4 && wave == 7) {
                    const int pb = 32 * mem + (lane & 31);
                    f2oth = WS_F2 + (u32)((lane < 32 ? ((pb & 7) * 4 + (pb >> 6)) * 8 + ((pb >> 3) & 7) : max(grp * 8 - 8, 0) + (lane & 15)) * 128);
                    ald2s(flg, f2oth);
                }
                SB();
#pragma unroll
                for (int n = 0; n < 4; ++n) accM[n] = mfma32(F[ks][n], __builtin_bit_cast(s16x8, of[ks]), ks ? accM[n] : resid(n));
                m0 = max(max(m0, of[ks][0]), of[ks][1]); m1 = max(max(m1, of[ks][2]), of[ks][3]);
                SB();
            }
            if (__builtin_expect(__all(max(m0, m1) != SENT), 1)) break;
            __builtin_amdgcn_s_sleep(4);
            WAIT_VM(0);
            sfor<0, 8>([&](auto ic) { constexpr int i = decltype(ic)::value; ald4o<(i & 3) * 1024>(of[i], ooff + (i >> 2) * 4096, d_ws); });
            WAIT_VM(0);
        }
        STAMP(41);
        if constexpr (R == 1) { if (wave == 7) { WAIT_VM(0); pin(flg); spin_flags(flg, f2oth); } }
    }

    auto load_y = [&]() {
        const int l2 = opq(tid) & 63;
#pragma unroll
        for (int i = 0; i < 8; ++i)
            yw[i] = __builtin_amdgcn_raw_buffer_load_b32(r_ws, WS_Y2 + (u32)((32 * mem + 2 * (l2 >> 4)) * 8192 + (TI * 16 + (l2 & 15)) * 4), (8 * (i >> 1) + (i & 1)) * 8192, 16);
    };
    auto pub_off = [&](u32 ws_st) {
        const int lane = opq(tid) & 63;
        return (lane & 16) ? 0xFFFFFF00u : ws_st + (u32)((TI * 8 + mem) * 256 + (lane & 15) * 16 + (lane >> 5) * 8);
    };
    auto pub_stat = [&](u32 off, u32 tg, float s, float q) {
        auto r = __builtin_amdgcn_permlane32_swap(__float_as_uint(s), __float_as_uint(q), false, false);
        const float v = __uint_as_float(r[0]) + __uint_as_float(r[1]);
        auto t = __builtin_amdgcn_permlane16_swap(__float_as_uint(v), __float_as_uint(v), false, false);
        __builtin_amdgcn_raw_buffer_store_b64((u32x2){__float_as_uint(__uint_as_float(t[0]) + __uint_as_float(t[1])), tg}, r_ws, off, 0, 16);
    };
    auto stat_off = [&](u32 ws_st, int ti) { const int l2 = opq(tid) & 63; return ws_st + (u32)(ti * 2048 + (l2 >> 4) * 512 + (l2 & 15) * 16); };
    auto fin_stat = [&](u32x4& a, u32x4& c, u32 off, float& mean, float& rstd) {
        _Pragma("unroll 1") for (int spin = 0; spin < SPIN_CAP; ++spin) {
            if (__builtin_expect(__all(((a[1] ^ tl) | (a[3] ^ th) | (c[1] ^ tl) | (c[3] ^ th)) == 0), 1)) break;
            __builtin_amdgcn_s_sleep(1);
            asm volatile("buffer_load_dwordx4 %0, %2, %3, 0 offen sc1\n\tbuffer_load_dwordx4 %1, %2, %3, 0 offen offset:256 sc1\n\ts_waitcnt vmcnt(0)"
                         : "=&v"(a), "=&v"(c) : "v"(off), "s"(d_ws) : "memory");
        }
        auto r = __builtin_amdgcn_permlane32_swap(__float_as_uint(__uint_as_float(a[0]) + __uint_as_float(c[0])), __float_as_uint(__uint_as_float(a[2]) + __uint_as_float(c[2])), false, false);
        const float v = __uint_as_float(r[0]) + __uint_as_float(r[1]);
        auto t = __builtin_amdgcn_permlane16_swap(__float_as_uint(v), __float_as_uint(v), false, false);
        const u32 v2 = __float_as_uint(__uint_as_float(t[0]) + __uint_as_float(t[1]));
        auto u = __builtin_amdgcn_permlane32_swap(v2, v2, false, false);
        const float ts = __uint_as_float(u[0]), tq = __uint_as_float(u[1]);
        mean = ts * (1.f / 512.f);
        rstd = __builtin_amdgcn_rsqf(fmaxf(tq * (1.f / 512.f) - mean * mean, 0.f) + 1e-5f);
    };
    auto regroup = [&](u32 (&w)[8]) {
#pragma unroll
        for (int j = 0; j < 2; ++j) {
            auto r0 = __builtin_amdgcn_permlane16_swap(w[4 * j], w[4 * j + 2], false, false);
            auto r1 = __builtin_amdgcn_permlane16_swap(w[4 * j + 1], w[4 * j + 3], false, false);
            w[4 * j] = r0[0]; w[4 * j + 1] = r1[0]; w[4 * j + 2] = r0[1]; w[4 * j + 3] = r1[1];
        }
    };
    auto wo_finish = [&](auto lc) {
        constexpr int LAYER = decltype(lc)::value;
        const int lane = opq(tid) & 63, li = lane & 15, lq = lane >> 4;
        u32 wx = L_WX + hd * 4096 + lane * 16;
        pin(wx);
        u32 po = 0, so = 0, tg = 0, oa = 0;
        if constexpr (R == 1) {
#pragma unroll
            for (int n = 0; n < 4; ++n) *(LAS f32x4*)(lds + wx + n * 1024) = accM[n];
        } else {
            po = pub_off(LAYER ? WS_ST3 : WS_ST1); so = stat_off(LAYER ? WS_ST3 : WS_ST1, TI); tg = lane < 32 ? tl : th;
            const int c = 2 * (lq & 1) + (lq >> 1);
            oa = LAYER ? (u32)((TI * 16 + li) * 1024 + mem * 128 + c * 16) : WS_X2 + (u32)((TI * 8 + mem) * 2048 + li * 128 + ((c ^ (li >> 1)) << 4));
            pin(po); pin(so); pin(tg); pin(oa);
        }
        BAR();
        STAMP(LAYER ? 25 : 42);
        if constexpr (R == 0) {
            f32x4 t[4];
#pragma unroll
            for (int n = 0; n < 4; ++n) t[n] = *(LAS f32x4*)(lds + wx + n * 1024);
            SB();
            float s = 0.f, q = 0.f, s2 = 0.f, q2 = 0.f;
#pragma unroll
            for (int n = 0; n < 4; ++n) {
                const f32x4 v = accM[n] + t[n];
                const u32 w0 = pk2(v[0], v[1]), w1 = pk2(v[2], v[3]);
                xw[2 * n] = w0; xw[2 * n + 1] = w1;
                s = dot2(w0, 0x3F803F80u, s); q = dot2(w0, w0, q); s2 = dot2(w1, 0x3F803F80u, s2); q2 = dot2(w1, w1, q2);
            }
            pub_stat(po, tg, s + s2, q + q2);
            CFENCE();
            if constexpr (!LAYER) load_y();
            regroup(xw);
            SB();
            STAMP(LAYER ? 46 : 43);
            WAIT_VM(0);
            STAMP(LAYER ? 26 : 44);
            if constexpr (!LAYER) {
#pragma unroll
                for (int i = 0; i < 8; ++i) pin(yw[i]);
            }
            u32x4 a, c;
            ald4s(a, so); ald4s(c, so + 256);
            if constexpr (!LAYER) {
                _Pragma("unroll 1") for (int spin = 0; spin < SPIN_CAP; ++spin) {
                    if (__builtin_expect(__all(max(max(max(yw[0], yw[1]), max(yw[2], yw[3])), max(max(yw[4], yw[5]), max(yw[6], yw[7]))) != SENT), 1)) break;
                    __builtin_amdgcn_s_sleep(1);
                    CFENCE();
                    load_y();
                }
                regroup(yw);
            }
            f32x2 xa[8], ya[8], xy[8];
#pragma unroll
            for (int i = 0; i < 8; ++i) {
                xa[i] = (f32x2){bflo(xw[i]), bfhi(xw[i])}; ya[i] = (f32x2){bflo(yw[i]), bfhi(yw[i])}; xy[i] = xa[i] * ya[i];
                pin(xa[i]); pin(ya[i]); pin(xy[i]);
            }
            WAIT_VM(0);
            pin(a); pin(c);
            float mean, rstd;
            fin_stat(a, c, so, mean, rstd);
            STAMP(LAYER ? 11 : 6);
            const float nm = -mean * rstd;
#pragma unroll
            for (int i = 0; i < 8; ++i) {
                const f32x2 o = xy[i] * rstd + (ya[i] * nm + xa[i]);
                xw[i] = pk2(o[0], o[1]);
                if (!LAYER) xw[i] = nosent(xw[i]);
            }
#pragma unroll
            for (int j = 0; j < 2; ++j) {
                const u32x4 o = {xw[4 * j], xw[4 * j + 1], xw[4 * j + 2], xw[4 * j + 3]};
                if constexpr (LAYER) __builtin_amdgcn_raw_buffer_store_b128(o, r_out, oa + j * 64, 0, OUT_AUX);
                else __builtin_amdgcn_raw_buffer_store_b128(o, r_ws, oa ^ (j * 64), 0, 16);
            }
            if constexpr (!LAYER) {
                float s = 0.f, q = 0.f, s2 = 0.f, q2 = 0.f;
#pragma unroll
                for (int i = 0; i < 8; i += 2) { s = dot2(xw[i], 0x3F803F80u, s); q = dot2(xw[i], xw[i], q); s2 = dot2(xw[i + 1], 0x3F803F80u, s2); q2 = dot2(xw[i + 1], xw[i + 1], q2); }
                pub_stat(po + (WS_ST2 - WS_ST1) * !(lane & 16), tg, s + s2, q + q2);
                CFENCE();
                regroup(xw);
#pragma unroll
                for (int i = 0; i < 8; ++i) { pin(xw[i]); pin(yw[i]); }
                STAMP(48);
                WAIT_VM(0);
            }
        }
    };
    wo_finish(IC<0>{});
    RBAR();
    STAMP(7);

    {
        const int l3 = opq(tid) & 63;
        const u32 g0 = (u32)((4 * grp - 1 + (hd >> 1)) * 16384 + (hd & 1) * 1024 + l3 * 16);
        const u32 g2 = (u32)((4 * grp + 3) * 16384 + hd * 512 + l3 * 16);
        auto x2_piece = [&](auto ic) {
            constexpr int i = decltype(ic)::value, s = i / 3, p = i % 3;
            const u32 kp = (u32)((s + rot) & 7);
            if constexpr (p == 0) dma(IC<4>{}, IC<L_CX + s * 10240>{}, d_x2, hb1k, g0, kp * 2048);
            else if constexpr (p == 1) dma(IC<4>{}, IC<L_CX + s * 10240 + 4096>{}, d_x2, hb1k, g0 + 32768, kp * 2048);
            else dma(IC<5>{}, IC<L_CX + s * 10240 + 8192>{}, d_x2, hb512, g2, kp * 2048);
        };
        u32 va = L_CX + hd * 1024 + l3 * 16, vq = L_CX + 8192 + hd * 512 + (l3 & 31) * 16 + (l3 >> 5) * 10240;
        pin(va); pin(vq);
        u32x4 vr[5];
        auto ver_read = [&](auto cc) {
            constexpr int c = decltype(cc)::value;
            u32 a = va + c * 20480, q = vq + c * 20480;
            pin(a); pin(q); CFENCE();
            vr[0] = *(LAS u32x4*)(lds + a); vr[1] = *(LAS u32x4*)(lds + a + 4096); vr[2] = *(LAS u32x4*)(lds + a + 10240); vr[3] = *(LAS u32x4*)(lds + a + 14336);
            vr[4] = *(LAS u32x4*)(lds + q);
        };
        auto ver_check = [&](auto cc) {
            constexpr int c = decltype(cc)::value;
            _Pragma("unroll 1") for (int spin = 0; spin < SPIN_CAP; ++spin) {
                u32 m0 = 0, m1 = 0;
#pragma unroll
                for (int i = 0; i < 5; ++i) { m0 = max(max(m0, vr[i][0]), vr[i][1]); m1 = max(max(m1, vr[i][2]), vr[i][3]); }
                if (__builtin_expect(__all(max(m0, m1) != SENT), 1)) break;
                __builtin_amdgcn_s_sleep(1);
                sfor<6 * c, 6 * c + 6>([&](auto ic) { x2_piece(ic); });
                WAIT_VM(0);
                ver_read(cc);
            }
        };
        const u32 xb0 = (u32)((l3 & 15) * 128 + (((l3 >> 4) ^ ((l3 & 15) >> 1)) << 4));
        if constexpr (R == 1) {
            STAMP(39);
            sfor<0, 12>([&](auto ic) { x2_piece(ic); });
            WAIT_VM(6);
            STAMP(8);
            ver_read(IC<0>{});
            ver_check(IC<0>{});
        } else {
            u32x4 sa, sc, ha, hc;
            const u32 so = stat_off(WS_ST2, 4 * grp + hd);
            const bool halo = hd == 0 && grp > 0;
            ald4s(sa, so); ald4s(sc, so + 256);
            if (halo) { ald4s(ha, so - 2048); ald4s(hc, so - 2048 + 256); }
            WAIT_VM(0);
            pin(sa); pin(sc);
            float mean, rstd;
            fin_stat(sa, sc, so, mean, rstd);
            if (l3 < 16) { *(LAS float*)(lds + L_TAB + 64 + hd * 64 + l3 * 4) = mean; *(LAS float*)(lds + L_TAB + 320 + 64 + hd * 64 + l3 * 4) = rstd; }
            if (halo) {
                pin(ha); pin(hc);
                fin_stat(ha, hc, so - 2048, mean, rstd);
                if (l3 < 16) { *(LAS float*)(lds + L_TAB + l3 * 4) = mean; *(LAS float*)(lds + L_TAB + 320 + l3 * 4) = rstd; }
            }
        }
        BAR();
        STAMP(13);
        u32 xa;
        auto pf_addr = [&](int st) { xa = L_CX + (st >> 1) * 10240 + (xb0 ^ ((st & 1) * 64)); pin(xa); };
        pf_addr(0);
#pragma unroll
        for (int mt = 0; mt < 5; ++mt) Ab[0][mt] = ldsA(xa + mt * 2048);
        sfor<0, 16>([&](auto stc) {
            constexpr int st = decltype(stc)::value;
            if constexpr (R == 1) {
                if constexpr (st == 2) { WAIT_VM(10); ver_read(IC<1>{}); }
                if constexpr (st == 3) ver_check(IC<1>{});
                if constexpr (st == 5) { WAIT_VM(6); ver_read(IC<2>{}); }
                if constexpr (st == 6) { ver_check(IC<2>{}); WAIT_VM(0); ver_read(IC<3>{}); }
                if constexpr (st == 7) ver_check(IC<3>{});
            }
            if constexpr (st == 3) { RBAR(); STAMP(49); }
            if constexpr (st == 7) { RBAR(); STAMP(50); CSTAMP(60); }
            if (st < 15) pf_addr(st + 1);
            if constexpr (st == 14) load_tab(true);
            SB();
            qkv_step(IC<1>{}, st, Ab[st & 1], [&](auto mc) {
                constexpr int mt = decltype(mc)::value;
                if (st < 15) Ab[(st + 1) & 1][mt] = ldsA(xa + mt * 2048);
                if constexpr (R == 1 && 12 + 5 * st + mt < 24) x2_piece(IC<12 + 5 * st + mt>{});
                SB();
            });
        });
    }
    STAMP(R ? 9 : 52);
    CSTAMP(61);
    attention(IC<1>{});
    STAMP(27);

    {
        wo_addr();
#pragma unroll
        for (int ks = 0; ks < 8; ++ks) load_F(ks);
        SB();
        WAIT_VM(0);
        RBAR();
        STAMP(10);
        load_o(WS_O2);
        SB();
        _Pragma("unroll 1") for (int spin = 0; spin < 64; ++spin) {
            u32 m0 = 0, m1 = 0;
#pragma unroll
            for (int ks = 0; ks < 8; ++ks) {
#pragma unroll
                for (int n = 0; n < 4; ++n) accM[n] = mfma32(F[ks][n], __builtin_bit_cast(s16x8, of[ks]), ks ? accM[n] : resid(n));
                m0 = max(max(m0, of[ks][0]), of[ks][1]); m1 = max(max(m1, of[ks][2]), of[ks][3]);
                SB();
            }
            if (__builtin_expect(__all(max(m0, m1) != SENT), 1)) break;
            _Pragma("unroll 1") for (int sp2 = 0; sp2 < SPIN_CAP; ++sp2) {
                CFENCE();
                __builtin_amdgcn_s_sleep(1);
                const u32 pv = __builtin_amdgcn_raw_buffer_load_b32(r_ws, WS_O2 + (u32)(TI * 16384 + R * 8192 + (opq(tid) & 15) * 512 + 508), 0, 16);
                if (__all(pv != SENT)) break;
            }
            CFENCE();
            load_o(WS_O2);
        }
        SB();
        STAMP(45);
    }
    wo_finish(IC<1>{});
    if constexpr (R == 1) {
        if (b == 0 && wave == 7) {
            const int l3 = opq(tid) & 63;
#pragma unroll
            for (int i = 0; i < 4; ++i) { flg = (u32x2){0u, 0u}; spin_flags(flg, WS_F1 + (u32)((i * 64 + l3) * 128)); }
            if (l3 == 0) __builtin_amdgcn_raw_buffer_store_b64(__builtin_bit_cast(u32x2, epoch + 1), r_ws, WS_EPOCH, 0, 16);
        }
    }
}

__global__ void __launch_bounds__(512, 2)
fused_kernel(const u16* __restrict__ gx, const u16* __restrict__ gwqkv, const u16* __restrict__ gwo,
             const u16* __restrict__ gbk, const u16* __restrict__ gbv, const u16* __restrict__ gsk,
             const u16* __restrict__ ghk, const u16* __restrict__ gdv, u16* __restrict__ gout, char* __restrict__ ws) {
    __shared__ __attribute__((aligned(16))) char lds_[L_TOTAL];
    typedef u32 u32x16 __attribute__((ext_vector_type(16)));
    u32x16 ka; u32x4 kb;
    {
        auto kp = __builtin_amdgcn_kernarg_segment_ptr();
        asm volatile("s_load_dwordx16 %0, %2, 0x0\n\ts_load_dwordx4 %1, %2, 0x40\n\ts_waitcnt lgkmcnt(0)" : "=&s"(ka), "=&s"(kb) : "s"(kp) : "memory");
    }
    auto mkp = [](u32 lo, u32 hi) { return (char*)(((u64)hi << 32) | lo); };
    const Args a = {(const u16*)mkp(ka[0], ka[1]), (const u16*)mkp(ka[2], ka[3]), (const u16*)mkp(ka[4], ka[5]), (const u16*)mkp(ka[6], ka[7]), (const u16*)mkp(ka[8], ka[9]),
                    (const u16*)mkp(ka[10], ka[11]), (const u16*)mkp(ka[12], ka[13]), (const u16*)mkp(ka[14], ka[15]), (u16*)mkp(kb[0], kb[1]), mkp(kb[2], kb[3])};
    const int wave = __builtin_amdgcn_readfirstlane(threadIdx.x >> 6);
    u64 pc0;
    asm volatile("s_getpc_b64 %0" : "=s"(pc0));
    if (wave < 4) body<0>(a, (LAS char*)lds_, wave, pc0); else body<1>(a, (LAS char*)lds_, wave, pc0);
}

extern "C" void kernel_launch(void* const* d_in, const int* in_sizes, int n_in,
                              void* d_out, int out_size, void* d_ws, size_t ws_size,
                              hipStream_t stream) {
    fused_kernel<<<256, 512, 0, stream>>>((const u16*)d_in[0], (const u16*)d_in[1], (const u16*)d_in[2],
                                          (const u16*)d_in[4], (const u16*)d_in[5], (const u16*)d_in[6],
                                          (const u16*)d_in[7], (const u16*)d_in[8], (u16*)d_out, (char*)d_ws);
}
```

```cpp
#define DIAG 17
#define ROT 2
#define PRO 2
#define NO_PROBE
#include <hip/hip_runtime.h>
#include <stdint.h>

typedef unsigned short u16;
typedef unsigned u32;
typedef unsigned long long u64;
typedef short s16x4 __attribute__((ext_vector_type(4)));
typedef short s16x8 __attribute__((ext_vector_type(8)));
typedef __bf16 bf16x2 __attribute__((ext_vector_type(2)));
typedef float f32x2 __attribute__((ext_vector_type(2)));
typedef float f32x4 __attribute__((ext_vector_type(4)));
typedef u32 u32x2 __attribute__((ext_vector_type(2)));
typedef u32 u32x4 __attribute__((ext_vector_type(4)));
#define LAS __attribute__((address_space(3)))
#define DEV __device__ __forceinline__
typedef __amdgpu_buffer_rsrc_t rsrc_t;

constexpr u32 WS_EPOCH = 0;
constexpr u32 WS_F1 = 32768;
constexpr u32 WS_F2 = 65536;
constexpr u32 WS_ST0 = 98304;
constexpr u32 WS_ST1 = 131072;
constexpr u32 WS_ST2 = WS_ST1 + 262144;
constexpr u32 WS_ST3 = WS_ST2 + 262144;
constexpr u32 WS_UT = 1u << 20;
constexpr u32 WS_O1 = WS_UT + (2u << 20);
constexpr u32 WS_O2 = WS_O1 + (2u << 20);
constexpr u32 WS_Y2 = WS_O2 + (2u << 20);
constexpr u32 WS_X2 = WS_Y2 + (2u << 20);
constexpr u32 WS_END = WS_X2 + (2u << 20);

constexpr u32 L_SLOT = 34816;
constexpr u32 L_KV = 4 * L_SLOT;
constexpr u32 L_K = L_KV, L_V = L_KV + 9216;
constexpr u32 L_SK = L_KV + 17408;
constexpr u32 L_WO = 0;
constexpr u32 L_CX = 65536;
constexpr u32 L_HC = L_CX;
constexpr u32 L_UR = L_CX + 69632;
constexpr u32 L_WX = L_CX + 40960;
constexpr u32 L_EX = 147456;
constexpr u32 L_TAB = L_EX + 12288;
constexpr u32 L_CS = L_TAB + 1024;
constexpr u32 L_FLG = L_TAB + 2048;
constexpr u32 L_DUMMY = L_FLG + 1024;
constexpr u32 L_TOTAL = 160 * 1024;
static_assert(L_DUMMY + 1024 <= L_TOTAL, "LDS map");

#define WO_POS 0
#define OUT_AUX 0
constexpr int SPIN_CAP = 1 << 14;
constexpr u32 SENT = 0xFFFFFFFFu;

#define WAIT_VM(n) asm volatile("s_waitcnt vmcnt(%0)" ::"i"(n) : "memory")
#define BAR() do { asm volatile("s_waitcnt lgkmcnt(0)" ::: "memory"); __builtin_amdgcn_s_barrier(); asm volatile("" ::: "memory"); } while (0)
#define RBAR() do { asm volatile("" ::: "memory"); __builtin_amdgcn_s_barrier(); asm volatile("" ::: "memory"); } while (0)
#define CFENCE() asm volatile("" ::: "memory")
#define SB() __builtin_amdgcn_sched_barrier(0)
template <int N> struct IC { static constexpr int value = N; };
template <int I, int N, class F> DEV void sfor(F&& f) { if constexpr (I < N) { f(IC<I>{}); sfor<I + 1, N>(f); } }

DEV f32x4 mfma32(s16x8 a, s16x8 b, f32x4 c) { return __builtin_amdgcn_mfma_f32_16x16x32_bf16(a, b, c, 0, 0, 0); }
DEV f32x4 mfma16(s16x4 a, s16x4 b, f32x4 c) { return __builtin_amdgcn_mfma_f32_16x16x16bf16_1k(a, b, c, 0, 0, 0); }
DEV float vmax(float a, float b) { float r; asm("v_max_f32 %0, %1, %2" : "=v"(r) : "v"(a), "v"(b)); return r; }
DEV u32 pk2(float lo, float hi) { f32x2 f = {lo, hi}; bf16x2 b = __builtin_convertvector(f, bf16x2); return __builtin_bit_cast(u32, b); }
DEV float bflo(u32 w) { return __uint_as_float(w << 16); }
DEV float bfhi(u32 w) { return __uint_as_float(w & 0xffff0000u); }
DEV float dot2(u32 a, u32 b, float c) { return __builtin_amdgcn_fdot2_f32_bf16(__builtin_bit_cast(bf16x2, a), __builtin_bit_cast(bf16x2, b), c, false); }
DEV u32 nosent(u32 w) { return min(w, 0xFFFFFFFEu); }
DEV float xsum(float v) {
    auto r = __builtin_amdgcn_permlane32_swap(__float_as_uint(v), __float_as_uint(v), false, false);
    v = __uint_as_float(r[0]) + __uint_as_float(r[1]);
    auto s = __builtin_amdgcn_permlane16_swap(__float_as_uint(v), __float_as_uint(v), false, false);
    return __uint_as_float(s[0]) + __uint_as_float(s[1]);
}
DEV float xmax(float v) {
    auto r = __builtin_amdgcn_permlane32_swap(__float_as_uint(v), __float_as_uint(v), false, false);
    v = vmax(__uint_as_float(r[0]), __uint_as_float(r[1]));
    auto s = __builtin_amdgcn_permlane16_swap(__float_as_uint(v), __float_as_uint(v), false, false);
    return vmax(__uint_as_float(s[0]), __uint_as_float(s[1]));
}
DEV float rowsum16(float v) {
    v += __builtin_bit_cast(float, __builtin_amdgcn_update_dpp(0, __builtin_bit_cast(int, v), 0xB1, 0xf, 0xf, true));
    v += __builtin_bit_cast(float, __builtin_amdgcn_update_dpp(0, __builtin_bit_cast(int, v), 0x4E, 0xf, 0xf, true));
    v += __builtin_bit_cast(float, __builtin_amdgcn_update_dpp(0, __builtin_bit_cast(int, v), 0x141, 0xf, 0xf, true));
    v += __builtin_bit_cast(float, __builtin_amdgcn_update_dpp(0, __builtin_bit_cast(int, v), 0x140, 0xf, 0xf, true));
    return v;
}
DEV float bcast_row0(float v) { return __builtin_bit_cast(float, __builtin_amdgcn_update_dpp(0, __builtin_bit_cast(int, v), 0x150, 0xf, 0xf, true)); }
template <int OFF> DEV void ald4o(u32x4& d, u32 voff, u32x4 rs) { asm volatile("buffer_load_dwordx4 %0, %1, %2, 0 offen offset:%3 sc1" : "=v"(d) : "v"(voff), "s"(rs), "i"(OFF) : "memory"); }
template <class T> DEV void pin(T& v) { asm volatile("" : "+v"(v)); }
DEV int opq(int v) { asm volatile("" : "+v"(v)); return v; }
DEV s16x8 cat8(s16x4 a, s16x4 b) { return __builtin_shufflevector(a, b, 0, 1, 2, 3, 4, 5, 6, 7); }
DEV s16x4 pack4(f32x4 v) { u32x2 w = {pk2(v[0], v[1]), pk2(v[2], v[3])}; return __builtin_bit_cast(s16x4, w); }
DEV u64 mix64(u64 z) { z += 0x9E3779B97F4A7C15ull; z = (z ^ (z >> 30)) * 0xBF58476D1CE4E5B9ull; z = (z ^ (z >> 27)) * 0x94D049BB133111EBull; return z ^ (z >> 31); }

struct ConvSched {
    int n[4], f[18], i[18], slot[64];
    constexpr ConvSched(int R) : n{}, f{}, i{}, slot{} {
        constexpr int LEAD = 9;
        const int N0[4] = {0, 3, 4, 7};
        for (int k = 0; k < 4; ++k) n[k] = N0[k] ^ R;
        int c = 0;
        for (int ff = 0; ff < 8; ++ff) for (int k = 3; k >= 0; --k) if (ff <= n[k]) { f[c] = ff; i[c] = k; ++c; }
        for (int k = 0; k < 64; ++k) slot[k] = 1000;
        for (int p = 71; p >= 0; --p) {
            const int e = p / 18, ff = f[p % 18], g = n[i[p % 18]] - ff, s = p - LEAD < -1 ? -1 : p - LEAD;
            slot[e * 16 + ff] = s; slot[e * 16 + 8 + g] = s;
        }
    }
};

#define STAMP(i) do { if constexpr ((i) == 0 || (i) == DIAG) { u64 t_; asm volatile("s_memrealtime %0\n\ts_waitcnt lgkmcnt(0)" : "=s"(t_)::"memory"); if ((i) == 0) dg0 = (u32)t_; else dg1 = (u32)t_; } } while (0)
#define CSTAMP(i) do { } while (0)

struct Args { const u16 *gx, *gwqkv, *gwo, *gbk, *gbv, *gsk, *ghk, *gdv; u16* gout; char* ws; };

template <int R>
DEV void body(const Args& A_, LAS char* const lds, const int wave) {
    const u16* gx = A_.gx; char* ws = A_.ws;
    u32 dg0 = 0, dg1 = 0;
    STAMP(0);
    const int b = blockIdx.x, tid = threadIdx.x, lane = tid & 63;
    const int hd = wave & 3;
    const int grp = (b & 7) * 4 + (b >> 6), mem = (b >> 3) & 7;
    const int T0 = grp * 64;
    const int rot = (mem + ROT * (grp & 3)) & 7;

    u64 epoch;
    asm volatile("s_load_dwordx2 %0, %1, 0x0" : "=s"(epoch) : "s"(ws));
    u32 dw;
    asm volatile("s_load_dword %0, %1, 0x0" : "=s"(dw) : "s"(A_.gdv + 2 * b));
    const rsrc_t r_x = __builtin_amdgcn_make_buffer_rsrc((void*)gx, 0, 2048 * 512 * 2, 0x00020000);
    const rsrc_t r_ws = __builtin_amdgcn_make_buffer_rsrc((void*)ws, 0, WS_END, 0x00020000);
    const rsrc_t r_out = __builtin_amdgcn_make_buffer_rsrc((void*)A_.gout, 0, 2048 * 512 * 2, 0x00020000);
    const u32 ldsbase = (u32)(size_t)lds;
    auto mkdesc = [](const void* p, u32 bytes) { const u64 a = (u64)p; return (u32x4){(u32)a, (u32)(a >> 32) & 0xffffu, bytes, 0x00020000u}; };
    const u32x4 d_x = mkdesc(gx, 2048 * 512 * 2), d_w = mkdesc(A_.gwqkv, 512 * 1536 * 2), d_wo = mkdesc(A_.gwo, 512 * 512 * 2);
    const u32x4 d_k = mkdesc(A_.gbk, 2048 * 512 * 2), d_v = mkdesc(A_.gbv, 2048 * 512 * 2), d_sk = mkdesc(A_.gsk, 512 * 4 * 2);
    const u32x4 d_h = mkdesc(A_.ghk, 512 * 2048 * 2), d_x2 = mkdesc(ws + WS_X2, 2u << 20), d_ws = mkdesc(ws, WS_END);
    const u32 wb1k = ldsbase + wave * 1024, hb1k = ldsbase + hd * 1024, hb512 = ldsbase + hd * 512;
#define DMA_ASM(pre, mods, post) asm volatile("s_add_u32 m0, %0, %4\n\t" pre "buffer_load_dwordx4 %1, %2, %3 offen " mods "lds" post \
                     :: "s"(sbase), "v"(voff), "s"(rs), "s"(soff), "i"(IMM) : "memory", "scc")
    const u32 xmlo = hd == 0 ? ~0xFFu : ~0u;
    auto dma = [&](auto kind, auto imm, u32x4 rs, u32 sbase, u32 voff, u32 soff) {
        constexpr int K = decltype(kind)::value; constexpr int IMM = decltype(imm)::value;
        if constexpr (K == 0) DMA_ASM("s_nop 0\n\t", "", "");
        else if constexpr (K == 1) DMA_ASM("s_mov_b32 exec_hi, 0\n\t", "", "\n\ts_mov_b32 exec_hi, -1");
        else if constexpr (K == 2) {
            const u32 ml = xmlo;
            asm volatile("s_add_u32 m0, %0, %4\n\ts_mov_b32 exec_lo, %5\n\tbuffer_load_dwordx4 %1, %2, %3 offen lds\n\ts_mov_b32 exec_lo, -1"
                         :: "s"(sbase), "v"(voff), "s"(rs), "s"(soff), "i"(IMM), "s"(ml) : "memory", "scc");
        }
        else if constexpr (K == 4) DMA_ASM("s_nop 0\n\t", "sc1 ", "");
        else DMA_ASM("s_mov_b32 exec_hi, 0\n\t", "sc1 ", "\n\ts_mov_b32 exec_hi, -1");
    };
    auto ald4 = [&](u32x4& d, u32x4 rs, u32 voff) { asm volatile("buffer_load_dwordx4 %0, %1, %2, 0 offen" : "=v"(d) : "v"(voff), "s"(rs) : "memory"); };
    auto ald4s = [&](u32x4& d, u32 voff) { asm volatile("buffer_load_dwordx4 %0, %1, %2, 0 offen sc1" : "=v"(d) : "v"(voff), "s"(d_ws) : "memory"); };
    auto ald2s = [&](u32x2& d, u32 voff) { asm volatile("buffer_load_dwordx2 %0, %1, %2, 0 offen sc1" : "=v"(d) : "v"(voff), "s"(d_ws) : "memory"); };

    const int pr = lane >> 3, pc = lane & 7;
    const int xr0 = 8 * hd + pr, xr2 = 64 + 4 * hd + pr;
    const u32 xv0 = (u32)((T0 - 16 + xr0) * 1024 + ((pc ^ ((xr0 >> 1) & 7)) << 4));
    const u32 xv2 = (u32)((T0 - 16 + xr2) * 1024 + ((pc ^ ((xr2 >> 1) & 7)) << 4));
    const int wfw = ((xr0 >> 1) & 1) | (((xr0 >> 3) & 1) << 1);
    const u32 wv0 = (u32)(xr0 * 3072 + mem * 128 + ((((pc >> 1) ^ wfw) * 2 + (pc & 1)) << 4));
    auto issue_piece = [&](auto sc, auto ic) {
        constexpr int s = decltype(sc)::value, i = decltype(ic)::value;
        const u32 kp = (u32)((s + rot) & 7);
        constexpr int sb = (s & 3) * L_SLOT;
        if constexpr (i == 0) dma(IC<2>{}, IC<sb>{}, d_x, hb1k, xv0, kp * 128);
        else if constexpr (i == 1) dma(IC<0>{}, IC<sb + 4096>{}, d_x, hb1k, xv0 + 32768, kp * 128);
        else if constexpr (i == 2) dma(IC<1>{}, IC<sb + 8192>{}, d_x, hb512, xv2, kp * 128);
        else dma(IC<0>{}, IC<sb + 10240 + (i - 3) * 4096>{}, d_w, hb1k, wv0, kp * (64 * 3072) + ((i - 3) >> 1) * 1024 + ((i - 3) & 1) * (32 * 3072));
    };
    auto issue_wo = [&](auto ic) {
        constexpr int i = decltype(ic)::value;
        const int l2 = opq(tid) & 63, pr = l2 >> 3, pc = l2 & 7;
        const u32 wov = (u32)((8 * hd + pr) * 1024 + mem * 128 + ((((pc >> 1) ^ (((pr >> 1) & 1) | ((hd & 1) << 1))) * 2 + (pc & 1)) << 4));
        const u32 j = (u32)((i + 2 * grp) & 7);
        dma(IC<0>{}, IC<L_WO + (i & 8) * 4096>{}, d_wo, hb1k + j * 4096, wov, ((i & 8) + j) * 32768);
    };
    auto issue_kv = [&](int i) {
        const int l2 = opq(tid) & 63, pr = l2 >> 3, pc = l2 & 7, p = hd + 4 * (i & 1);
        const u32 cb = (u32)(mem * 128);
        if (i < 2) dma(IC<0>{}, IC<L_K>{}, d_k, hb1k + (i & 1) * 4096, (u32)((T0 - 8 + 8 * p + pr) * 1024) + cb + ((pc ^ p) << 4), 0);
        else if (i < 4) dma(IC<0>{}, IC<L_V>{}, d_v, hb1k + (i & 1) * 4096, (u32)((T0 + 8 * p + pr) * 1024) + cb + ((pc ^ p) << 4), 0);
        else if (hd == 0) dma(IC<0>{}, IC<L_K + 8192>{}, d_k, ldsbase, (u32)((T0 - 8 + 64 + pr) * 1024) + cb + (pc << 4), 0);
        else dma(IC<0>{}, IC<L_SK>{}, d_sk, ldsbase, (u32)(mem * 512 + l2 * 16), 0);
    };

    const int srow = T0 + 8 * mem + 2 * hd;
    u32x4 xrow[2];
    sfor<0, 9>([&](auto ic) { if constexpr ((decltype(ic)::value & 1) != R) issue_piece(IC<0>{}, ic); });
    sfor<0, 9>([&](auto ic) { if constexpr ((decltype(ic)::value & 1) != R) issue_piece(IC<1>{}, ic); });
    asm volatile("s_waitcnt lgkmcnt(0)" : "+s"(epoch), "+s"(dw));
    const u64 tag = mix64(epoch);
    const u32 tl = (u32)tag, th = (u32)(tag >> 32);
    if (R == 1 && wave == 7 && lane < 32) *(LAS u32x4*)(lds + (lane >> 3) * L_SLOT + (lane & 7) * 16) = (u32x4){0x3F803F80u, 0x3F803F80u, 0x3F803F80u, 0x3F803F80u};

    auto trr = [&](u32 off) { return __builtin_amdgcn_ds_read_tr16_b64_v4i16((LAS s16x4*)(lds + off)); };
    auto ldsA = [&](u32 off) { return *(LAS s16x8*)(lds + off); };
    auto flag_ok = [&](const u32x2& f) { return __all(f[0] == tl && f[1] == th); };
    auto spin_flags = [&](u32x2& f, u32 off) {
        _Pragma("unroll 1") for (int spin = 0; spin < SPIN_CAP; ++spin) {
            if (__builtin_expect(flag_ok(f), 1)) break;
            __builtin_amdgcn_s_sleep(2);
            asm volatile("buffer_load_dwordx2 %0, %1, %2, 0 offen sc1\n\ts_waitcnt vmcnt(0)" : "=&v"(f) : "v"(off), "s"(d_ws) : "memory");
        }
    };

    s16x8 Wq[16], Wm[16];
    f32x4 accM[5], accQ[5];
    const f32x4 zero4 = {0.f, 0.f, 0.f, 0.f};
    const s16x8 ones8 = {0x3F80, 0x3F80, 0x3F80, 0x3F80, 0x3F80, 0x3F80, 0x3F80, 0x3F80};
    s16x8 Ab[3][5];
    auto qkv_step = [&](auto lc, int st, const s16x8 (&A)[5], auto fill) {
        sfor<0, 5>([&](auto mc) {
            constexpr int mt = decltype(mc)::value;
            const f32x4 cm = st ? accM[mt] : zero4;
            accM[mt] = R ? mfma32(A[mt], Wm[st], cm) : mfma32(Wm[st], A[mt], cm);
            if constexpr (R == 0 && (mt > 0 || decltype(lc)::value == 0)) accQ[mt] = mfma32(Wq[st], A[mt], st ? accQ[mt] : zero4);
            fill(mc);
        });
    };

    float m1[5], r1[5]; f32x4 m4[5], r4[5], csK, csQ;
    auto load_tab = [&](bool cs) {
        const int l2 = opq(tid) & 63;
        u32 ta = L_TAB + (R ? (l2 >> 4) * 16 : (l2 & 15) * 4);
        pin(ta);
        if (R == 0 && cs) { csK = *(LAS f32x4*)(lds + L_CS + hd * 128 + (l2 >> 4) * 16); csQ = *(LAS f32x4*)(lds + L_CS + hd * 128 + 64 + (l2 >> 4) * 16); }
#pragma unroll
        for (int mt = 0; mt < 5; ++mt) {
            if (R == 0) { m1[mt] = *(LAS float*)(lds + ta + mt * 64); r1[mt] = *(LAS float*)(lds + ta + 320 + mt * 64); }
            else { m4[mt] = *(LAS f32x4*)(lds + ta + mt * 64); r4[mt] = *(LAS f32x4*)(lds + ta + 320 + mt * 64); }
        }
    };

    u32x4 hr[4];
    u32x2 flg;
    {
        const int li = lane & 15, lq = lane >> 4, tq = li >> 2, tp = lane & 3;
        const u32 xa0 = (u32)(li * 128 + ((lq ^ (li >> 1)) << 4));
        const u32 wa = (u32)((8 * lq + tq) * 128 + ((hd ^ (((tq >> 1) & 1) | ((lq & 1) << 1))) << 5) + tp * 8);
        u32 xa, wb, kw[11], vw[8];
        u32x4 s0v, skw;
        auto pf_addr = [&](int st) {
            const u32 sb = (u32)((st >> 1) & 3) * L_SLOT;
            xa = sb + (xa0 ^ ((st & 1) * 64)); wb = sb + 10240 + (st & 1) * 4096 + wa;
            asm volatile("" : "+v"(xa), "+v"(wb));
        };
        auto pf_part = [&](int st, int mt) {
            if (mt == 0) Wm[st] = cat8(trr(wb + (1 + R) * 8192), trr(wb + (1 + R) * 8192 + 512));
            Ab[st & 1][mt] = ldsA(xa + mt * 2048);
            if (R == 0 && mt == 1) Wq[st] = cat8(trr(wb), trr(wb + 512));
        };
        STAMP(15);
        WAIT_VM(R ? 5 : 4);
        BAR();
        STAMP(16);
        pf_addr(0);
#pragma unroll
        for (int mt = 0; mt < 5; ++mt) pf_part(0, mt);
        sfor<0, 16>([&](auto stc) {
            constexpr int st = decltype(stc)::value;
            if constexpr ((st & 1) && st < 15) {
                constexpr int s1 = (st >> 1) + 1;
                if constexpr (R == 0) {
                    if constexpr (s1 == 1) WAIT_VM(7);
                    if constexpr (s1 == 2) WAIT_VM(1);
                    if constexpr (s1 == 5) WAIT_VM(0);
                } else {
                    constexpr int WN[8] = {0, 9, 9, 9, 9, 9, 10, 10};
                    WAIT_VM(WN[s1]);
                    if constexpr (s1 == 7) {
                        pin(s0v);
                        const int l2 = opq(tid) & 63, tr = hd * 20 + min(l2, 19), row = T0 - 16 + tr;
                        _Pragma("unroll 1") for (int spin = 0; spin < SPIN_CAP; ++spin) {
                            if (__builtin_expect(__all(row < 0 || (s0v[1] == tl && s0v[3] == th)), 1)) break;
                            __builtin_amdgcn_s_sleep(2);
                            ald4s(s0v, WS_ST0 + (u32)(max(row, 0) * 16));
                            WAIT_VM(0);
                            pin(s0v);
                        }
                        *(LAS u32*)(lds + L_TAB + tr * 4) = row < 0 ? 0u : s0v[0];
                        *(LAS u32*)(lds + L_TAB + 320 + tr * 4) = row < 0 ? 0x3F800000u : s0v[2];
                        asm volatile("s_waitcnt lgkmcnt(0)" ::: "memory");
                    }
                }
                RBAR();
                STAMP(16 + s1);
                if constexpr (s1 == 5 && R == 0) { if (tid == 0) __builtin_amdgcn_raw_buffer_store_b64((u32x2){tl, th}, r_ws, WS_F1 + (mem * 32 + grp) * 128, 0, 16); CFENCE(); }
                if constexpr (s1 == 5 && R == 1) { const int row = T0 - 16 + hd * 20 + min(opq(tid) & 63, 19); ald4s(s0v, WS_ST0 + (u32)(max(row, 0) * 16)); }
            }
            if (st < 15) pf_addr(st + 1);
            if constexpr (st == 14) load_tab(false);
            SB();
            qkv_step(IC<0>{}, st, Ab[st & 1], [&](auto mc) {
                constexpr int mt = decltype(mc)::value;
                if (st < 15) pf_part(st + 1, mt);
                if constexpr (R == 1) {
                    if constexpr (st == 0) { issue_piece(IC<2>{}, IC<2 * mt>{}); if constexpr (mt < 4) issue_piece(IC<2>{}, IC<2 * mt + 1>{}); }
                    if constexpr (st >= 1 && st <= 10 && ((st - 1) & 1) * 5 + mt < 9) issue_piece(IC<((st - 1) >> 1) + 3>{}, IC<((st - 1) & 1) * 5 + mt>{});
                    if constexpr (st == 11 && mt == 0) { const u32 ho = (u32)(b * 8192 + opq(tid) * 16); ald4(hr[2], d_h, ho); ald4(hr[3], d_h, ho + 16); }
                    if constexpr (st == 13 && mt == 0) { if (wave == 7) { const int l2 = opq(tid) & 63; ald2s(flg, WS_F1 + (u32)((l2 < 32 ? (b >> 5) * 32 + l2 : (l2 & 7) * 32 + grp) * 128)); } }
                    if constexpr (st >= 11 && st <= 14 && mt < 4) issue_wo(IC<(st - 11) * 4 + mt>{});
                } else {
                    if constexpr (st == 0) {
                        if constexpr (mt == 0) { const u32 xo = (u32)(srow * 1024 + (opq(tid) & 63) * 16); ald4(xrow[0], d_x, xo); ald4(xrow[1], d_x, xo + 1024); }
                        issue_kv(mt);
                    }
                    if constexpr (st == 11 && mt < 2) { const u32 ho = (u32)(b * 8192 + mt * 4096 + opq(tid) * 16); ald4(hr[2 * mt], d_h, ho); ald4(hr[2 * mt + 1], d_h, ho + 16); }
                }
                SB();
            });
            if constexpr (R == 0 && st == 2) {
                WAIT_VM(5);
                pin(xrow[0]); pin(xrow[1]);
                float s[2] = {0.f, 0.f}, q[2] = {0.f, 0.f};
#pragma unroll
                for (int i = 0; i < 4; ++i)
#pragma unroll
                    for (int k = 0; k < 2; ++k) { s[k] = dot2(xrow[k][i], 0x3F803F80u, s[k]); q[k] = dot2(xrow[k][i], xrow[k][i], q[k]); }
                auto ss = __builtin_amdgcn_permlane32_swap(__float_as_uint(s[0]), __float_as_uint(s[1]), false, false);
                auto qq = __builtin_amdgcn_permlane32_swap(__float_as_uint(q[0]), __float_as_uint(q[1]), false, false);
                float sv1 = rowsum16(__uint_as_float(ss[0]) + __uint_as_float(ss[1])), qv1 = rowsum16(__uint_as_float(qq[0]) + __uint_as_float(qq[1]));
                auto s2 = __builtin_amdgcn_permlane16_swap(__float_as_uint(sv1), __float_as_uint(sv1), false, false);
                auto q2 = __builtin_amdgcn_permlane16_swap(__float_as_uint(qv1), __float_as_uint(qv1), false, false);
                const float m = (__uint_as_float(s2[0]) + __uint_as_float(s2[1])) * (1.f / 512.f);
                const float r = __builtin_amdgcn_rsqf(fmaxf((__uint_as_float(q2[0]) + __uint_as_float(q2[1])) * (1.f / 512.f) - m * m, 0.f) + 1e-5f);
                const u32x4 sv = {__float_as_uint(m), tl, __float_as_uint(r), th};
                const int l2 = opq(tid) & 63;
                __builtin_amdgcn_raw_buffer_store_b128(sv, r_ws, (l2 & 31) == 0 ? WS_ST0 + (u32)((srow + (l2 >> 5)) * 16) : 0xFFFFFF00u, 0, 16);
                CFENCE();
                SB();
            }
            if constexpr (R == 0 && (st == 4 || st == 6)) {
                const int l2 = opq(tid) & 63, tg = l2 & 7, cp = 8 * hd + (l2 >> 3);
                if constexpr (st == 4) {
                    skw = *(LAS u32x4*)(lds + L_SK + cp * 16);
                    u32 ka = L_K + tg * 1024 + ((((cp >> 2) ^ tg) & 7) << 4) + (cp & 3) * 4;
                    u32 kb = L_K + tg * 1024 + ((((cp >> 2) ^ (tg + 1)) & 7) << 4) + (cp & 3) * 4;
                    pin(ka); pin(kb);
#pragma unroll
                    for (int i = 0; i < 11; ++i) kw[i] = *(LAS u32*)(lds + ((i + 5 < 8) ? ka : kb) + (i + 5) * 128);
#pragma unroll
                    for (int i = 0; i < 8; ++i) vw[i] = *(LAS u32*)(lds + ka + (L_V - L_K) + i * 128);
                    SB();
                }
                constexpr bool HI = st == 6;
                auto ex = [&](u32 w) { return HI ? bfhi(w) : bflo(w); };
                const float a0 = bflo(skw[2 * HI]), a1 = bfhi(skw[2 * HI]), a2 = bflo(skw[2 * HI + 1]), a3 = bfhi(skw[2 * HI + 1]);
                float ua[8];
#pragma unroll
                for (int i = 0; i < 8; ++i) ua[i] = (ex(kw[i]) * a3 + ex(kw[i + 1]) * a2 + ex(kw[i + 2]) * a1 + ex(kw[i + 3]) * a0) * ex(vw[i]);
                const u32x4 oa = {pk2(ua[7], ua[6]), pk2(ua[5], ua[4]), pk2(ua[3], ua[2]), pk2(ua[1], ua[0])};
                __builtin_amdgcn_raw_buffer_store_b128(oa, r_ws, WS_UT + (u32)((mem * 64 + 2 * cp) * 4096 + (2040 - T0 - 8 * tg) * 2), HI * 4096, 16);
                __builtin_amdgcn_raw_buffer_store_b128((u32x4){SENT, SENT, SENT, SENT}, r_ws, (u32)((4 * grp + hd) * 16384 + mem * 2048 + HI * 1024 + l2 * 16), WS_O1, 16);
                CFENCE();
                SB();
            }
        });
    }
    STAMP(R ? 1 : 53);

    u32x4 uu;
    float csV;
    auto attention = [&](auto lc) {
        constexpr int LAYER = decltype(lc)::value;
        constexpr int SBASE = LAYER ? 32 : 28;
        const int lane = opq(tid) & 63, li = lane & 15, lq = lane >> 4;
        u32 ex = L_EX + hd * 2048 + lane * 8;
        pin(ex);
        u32 f1ok = 1;
        if constexpr (R == 0) {
            if constexpr (LAYER == 0) {
#pragma unroll
                for (int i = 0; i < 4; ++i) { csK[i] = bcast_row0(accM[0][i]); csQ[i] = bcast_row0(accQ[0][i]); }
                if (li == 0) { *(LAS f32x4*)(lds + L_CS + hd * 128 + lq * 16) = csK; *(LAS f32x4*)(lds + L_CS + hd * 128 + 64 + lq * 16) = csQ; }
            }
            const int par = li & 1;
            const bool cur0 = (4 * lq + par) <= li, cur1 = (4 * lq + par + 2) <= li;
            s16x4 Kb[5], Qb[5];
#pragma unroll
            for (int mt = 0; mt < 5; ++mt) {
                Kb[mt] = pack4((accM[mt] - m1[mt] * csK) * r1[mt]);
                if (mt) Qb[mt] = pack4(accQ[mt] - m1[mt] * csQ);
            }
            f32x4 Sc[5], Sp[5];
#pragma unroll
            for (int mt = 1; mt < 5; ++mt) { Sc[mt] = mfma16(Kb[mt], Qb[mt], zero4); Sp[mt] = mfma16(Kb[mt - 1], Qb[mt], zero4); }
            float s0[5], s1[5], mx[5];
#pragma unroll
            for (int mt = 1; mt < 5; ++mt) {
                const float sp0 = Sp[mt][0], sp1 = Sp[mt][1], sp2 = Sp[mt][2], sp3 = Sp[mt][3], c0 = Sc[mt][0], c1 = Sc[mt][1], c2 = Sc[mt][2], c3 = Sc[mt][3];
                float p0 = par ? sp1 : sp0, p1 = par ? sp3 : sp2, q0 = par ? c1 : c0, q1 = par ? c3 : c2;
                asm volatile("" : "+v"(p0), "+v"(p1), "+v"(q0), "+v"(q1));
                if (mt == 1 && grp == 0) { p0 = -3.0e38f; p1 = -3.0e38f; }
                s0[mt] = cur0 ? q0 : p0; s1[mt] = cur1 ? q1 : p1;
                mx[mt] = vmax(s0[mt], s1[mt]);
            }
#pragma unroll
            for (int mt = 1; mt < 5; ++mt) { auto r = __builtin_amdgcn_permlane32_swap(__float_as_uint(mx[mt]), __float_as_uint(mx[mt]), false, false); mx[mt] = vmax(__uint_as_float(r[0]), __uint_as_float(r[1])); }
#pragma unroll
            for (int mt = 1; mt < 5; ++mt) { auto r = __builtin_amdgcn_permlane16_swap(__float_as_uint(mx[mt]), __float_as_uint(mx[mt]), false, false); mx[mt] = vmax(__uint_as_float(r[0]), __uint_as_float(r[1])); }
#pragma unroll
            for (int mt = 1; mt < 5; ++mt) {
                const float sc = r1[mt] * (0.25f * 1.44269504088896341f);
                const float e0 = __builtin_amdgcn_exp2f((s0[mt] - mx[mt]) * sc), e1 = __builtin_amdgcn_exp2f((s1[mt] - mx[mt]) * sc);
                *(LAS u32x2*)(lds + ex + (mt - 1) * 512) = (u32x2){pk2(e0, e0), pk2(e1, e1)};
            }
            STAMP(SBASE + 2);
            BAR();
            STAMP(SBASE + 1);
            if constexpr (LAYER == 0) f1ok = __builtin_amdgcn_readfirstlane(*(LAS u32*)(lds + L_FLG));
        } else {
            if constexpr (LAYER == 0) {
                auto s = __builtin_amdgcn_permlane16_swap(__float_as_uint(accM[0][0]), __float_as_uint(accM[0][0]), false, false);
                auto t = __builtin_amdgcn_permlane32_swap(s[0], s[0], false, false);
                csV = __uint_as_float(t[0]);
                if (wave == 7) { WAIT_VM(8); pin(flg); const u32 ok = flag_ok(flg); *(LAS u32*)(lds + L_FLG) = ok; }
            }
            const int par = li & 1;
            const bool cur0 = (4 * lq + par) <= li, cur1 = (4 * lq + par + 2) <= li;
            const u32 pm = par ? 0xffff0000u : 0x0000ffffu;
            u32 mC0 = cur0 ? pm : 0u, mP0 = cur0 ? 0u : pm, mC1 = cur1 ? pm : 0u, mP1 = cur1 ? 0u : pm;
            u32 oo = (LAYER ? WS_O2 : WS_O1) + (u32)((4 * grp + (lq & 1)) * 16384 + (4 * mem + hd) * 512 + (lq >> 1) * 256 + li * 16);
            asm volatile("" : "+v"(mC0), "+v"(mP0), "+v"(mC1), "+v"(mP1), "+v"(oo));
            s16x4 Vb[5];
#pragma unroll
            for (int mt = 0; mt < 5; ++mt) { Vb[mt] = pack4((accM[mt] - m4[mt] * csV) * r4[mt]); pin(Vb[mt]); }
            STAMP(SBASE);
            BAR();
            STAMP(SBASE + 1);
            u32x2 pw[4];
#pragma unroll
            for (int j = 0; j < 4; ++j) pw[j] = *(LAS u32x2*)(lds + ex + j * 512);
            if constexpr (LAYER == 0) {
                f1ok = __builtin_amdgcn_readfirstlane(*(LAS u32*)(lds + L_FLG));
                if (f1ok) ald4s(uu, WS_UT + (u32)(b * 8192 + opq(tid) * 16));
            }
            SB();
            f32x4 O[4], L[4];
#pragma unroll
            for (int j = 0; j < 4; ++j) {
                const s16x8 P = __builtin_bit_cast(s16x8, (u32x4){pw[j][0] & mC0, pw[j][1] & mC1, pw[j][0] & mP0, pw[j][1] & mP1});
                L[j] = mfma32(ones8, P, zero4);
                O[j] = mfma32(cat8(Vb[j + 1], Vb[j]), P, zero4);
            }
            u32 ow[4][2];
#pragma unroll
            for (int j = 0; j < 4; ++j) {
                const float inv = __builtin_amdgcn_rcpf(L[j][0]);
                ow[j][0] = nosent(pk2(O[j][0] * inv, O[j][1] * inv)); ow[j][1] = nosent(pk2(O[j][2] * inv, O[j][3] * inv));
            }
#pragma unroll
            for (int j = 0; j < 4; j += 2) {
                auto r0 = __builtin_amdgcn_permlane16_swap(ow[j][0], ow[j + 1][0], false, false);
                auto r1 = __builtin_amdgcn_permlane16_swap(ow[j][1], ow[j + 1][1], false, false);
                __builtin_amdgcn_raw_buffer_store_b128((u32x4){r0[0], r1[0], r0[1], r1[1]}, r_ws, oo, j * 16384, 16);
            }
            CFENCE();
        }
        if constexpr (LAYER == 0 && R == 0) { if (f1ok) ald4s(uu, WS_UT + (u32)(b * 8192 + opq(tid) * 16)); }
        SB();
        return f1ok;
    };
    const u32 f1ok = attention(IC<0>{});
    {
        const u32x4 sv = {SENT, SENT, SENT, SENT};
        const int t2 = opq(tid);
        const u32 tb = (u32)((4 * grp + hd) * 16384 + mem * 2048 + R * 1024 + (t2 & 63) * 16);
        __builtin_amdgcn_raw_buffer_store_b128(sv, r_ws, tb, WS_O2, 16);
        __builtin_amdgcn_raw_buffer_store_b128(sv, r_ws, tb, WS_X2, 16);
        __builtin_amdgcn_raw_buffer_store_b128(sv, r_ws, (u32)(b * 8192 + t2 * 16), WS_Y2, 16);
        CFENCE();
        SB();
    }
    STAMP(2);

    const int TI = 4 * grp + hd;
    u32x4 of[8];
    auto load_o = [&](u32 ws_o) {
        const int lane = opq(tid) & 63;
#pragma unroll
        for (int i = 0; i < 8; ++i) of[i] = __builtin_amdgcn_raw_buffer_load_b128(r_ws, ws_o + (u32)(TI * 16384 + R * 8192 + lane * 16) + (i & 3) * 1024, (i >> 2) * 4096, 16);
    };
    u32 xw[8], yw[8];
    u32 woa[4];
    auto resid = [&](int n) { return R ? zero4 : (f32x4){bflo(xw[2 * n]), bfhi(xw[2 * n]), bflo(xw[2 * n + 1]), bfhi(xw[2 * n + 1])}; };
    s16x8 F[8][4];
    auto wo_addr = [&]() {
        const int l2 = opq(tid) & 63, lq = l2 >> 4, tq = (l2 & 15) >> 2, tp = l2 & 3;
#pragma unroll
        for (int n = 0; n < 4; ++n) woa[n] = L_WO + (u32)(R * 32768 + (8 * lq + tq) * 128 + ((n ^ (((lq & 1) << 1) | (tq >> 1))) << 5) + tp * 8);
    };
    auto load_F = [&](int ks) {
#pragma unroll
        for (int n = 0; n < 4; ++n) F[ks][n] = cat8(trr(woa[n] + ks * 4096), trr(woa[n] + ks * 4096 + 512));
    };

    {
        const int t2 = opq(tid), lane = t2 & 63, li = lane & 15, lq = lane >> 4;
        constexpr u32 OFFS = 0xEDA85410u;
        {
            const int j = t2 & 255;
            {
                if constexpr (R == 0) { if (f1ok) WAIT_VM(4); else WAIT_VM(3); }
                else WAIT_VM(5);
                u32 cb = L_HC + R * 34816 + (j + 2) * 16;
                pin(cb);
#pragma unroll
                for (int ch = R; ch < 2; ++ch) {
                    pin(hr[2 * ch]); pin(hr[2 * ch + 1]);
                    const u32 d[8] = {hr[2 * ch][0], hr[2 * ch][1], hr[2 * ch][2], hr[2 * ch][3], hr[2 * ch + 1][0], hr[2 * ch + 1][1], hr[2 * ch + 1][2], hr[2 * ch + 1][3]};
                    u32 e[7];
#pragma unroll
                    for (int i = 0; i < 7; ++i) e[i] = __builtin_amdgcn_alignbit(d[i + 1], d[i], 16);
                    if (ch > R) { cb += 34816; pin(cb); }
#pragma unroll
                    for (int p = (R ? 4 : 0); p < ((ch && !R) ? 4 : 8); ++p) {
                        const u32 po = (272 * p + ((OFFS >> (4 * p)) & 15)) * 16;
                        const int h = p >> 1;
                        const u32x4 w = (p & 1) ? (u32x4){e[h], e[h + 1], e[h + 2], e[h + 3]} : (u32x4){d[h], d[h + 1], d[h + 2], d[h + 3]};
                        *(LAS u32x4*)(lds + cb + po) = w;
                    }
                }
            }
            if constexpr (R == 0) {
                if (hd == 0) {
                    u32 hh[4];
#pragma unroll
                    for (int i = 0; i < 4; ++i) { const u32 a = __builtin_amdgcn_readfirstlane(hr[0][i]), c = __builtin_amdgcn_readfirstlane(hr[2][i]); hh[i] = (lane & 16) ? c : a; }
                    if (lane < 32) {
                        const int p = lane & 7;
                        const u32 po = (272 * p + ((OFFS >> (4 * p)) & 15)) * 16;
                        const unsigned __int128 hv = ((unsigned __int128)(((u64)hh[3] << 32) | hh[2]) << 64) | (((u64)hh[1] << 32) | hh[0]);
                        const unsigned __int128 sv = (p == 0 || !(lane & 8)) ? (unsigned __int128)0 : (hv << (16 * (8 - p)));
                        const u32x4 w = {(u32)sv, (u32)(sv >> 32), (u32)(sv >> 64), (u32)(sv >> 96)};
                        *(LAS u32x4*)(lds + L_HC + (lane >> 4) * 34816 + po + ((lane >> 3) & 1) * 16) = w;
                    }
                }
            }
            if (hd == 1 && lane < 30) *(LAS u32x4*)(lds + L_UR + R * 4576 + 4096 + lane * 16) = (u32x4){0, 0, 0, 0};
            STAMP(3);
            if (!f1ok) {
                const u32 fo = WS_F1 + (u32)(((lane & 8) ? (lane & 7) * 32 + grp : (b >> 5) * 32 + 31 - hd * 8 - (lane & 7)) * 128);
                flg = (u32x2){0u, 0u};
                spin_flags(flg, fo);
                ald4s(uu, WS_UT + (u32)(b * 8192 + t2 * 16));
            }
            if (f1ok) WAIT_VM(R ? 5 : 3); else WAIT_VM(0);
            pin(uu);
            *(LAS u32x4*)(lds + L_UR + R * 4576 + j * 16) = uu;
        }
        BAR();
        STAMP(4);
        const int ch = hd >> 1, e0 = (hd & 1) * 4;
        const int p = (li + 1) & 7;
        u32 aA = L_HC + ch * 34816 + (272 * p + ((OFFS >> (4 * p)) & 15) + ((li + 1) >> 3) + lq + 4 * e0) * 16;
        u32 aB = L_UR + ch * 4576 + (254 - 224 - 2 * li + lq + 4 * e0) * 16;
        asm volatile("" : "+v"(aA), "+v"(aB));
        f32x4 y[4];
        u32 ooff = WS_O1 + (u32)(TI * 16384 + R * 8192 + lane * 16);
        {
            constexpr ConvSched S(R);
            s16x8 A[4][8], B[4][8];
            auto loads_at = [&](auto pc) {
                constexpr int p = decltype(pc)::value;
                sfor<0, 64>([&](auto ic) {
                    constexpr int i = decltype(ic)::value, e = i >> 4, f = i & 7;
                    if constexpr (S.slot[i] == p) {
                        if constexpr (i & 8) B[e][f] = ldsA(aB + 64 * e + 512 * (7 - f)); else A[e][f] = ldsA(aA + 64 * (8 * f + e));
                    }
                });
            };
            loads_at(IC<-1>{});
            SB();
            sfor<0, 72>([&](auto pc) {
                constexpr int p = decltype(pc)::value, e = p / 18, f = S.f[p % 18], i = S.i[p % 18], g = S.n[i] - f;
                y[i] = mfma32(A[e][f], B[e][g], (e == 0 && f == 0) ? zero4 : y[i]);
                loads_at(pc);
                if constexpr (p == 54) WAIT_VM(0);
                if constexpr (p >= 55 && p < 71 && (p & 1)) {
                    constexpr int i = (p - 55) >> 1;
                    ald4o<(i & 3) * 1024>(of[i], ooff + (i >> 2) * 4096, d_ws);
                }
                SB();
            });
        }
        STAMP(14);
        RBAR();
        if (R == 0 && tid == 0) __builtin_amdgcn_raw_buffer_store_b64((u32x2){tl, th}, r_ws, WS_F2 + (grp * 8 + mem) * 128, 0, 16);
        {
            constexpr int N0[4] = {0, 3, 4, 7};
            u32 ra = L_HC + hd * 8192 + lane * 16;
            asm volatile("" : "+v"(ra));
#pragma unroll
            for (int i = 0; i < 4; ++i) *(LAS f32x4*)(lds + ra + (N0[i] ^ R) * 1024) = y[i];
        }
        SB();
        if constexpr (R == 0) {
#pragma unroll
            for (int n = 0; n < 4; ++n) { const u32x2 w = __builtin_amdgcn_raw_buffer_load_b64(r_x, (u32)((TI * 16 + li) * 1024 + mem * 128 + lq * 8) + n * 32, 0, 0); xw[2 * n] = w[0]; xw[2 * n + 1] = w[1]; }
        }
        wo_addr();
        SB();
        STAMP(37);
        BAR();
        STAMP(38);
        {
            const float D0 = bflo(dw), D1 = bfhi(dw);
            u32 ra = L_HC + wave * 1024 + lane * 16;
            const int tb = 256 * wave + 16 * li + 4 * lq;
            u32 ru = L_UR + (2044 - tb) * 2;
            asm volatile("" : "+v"(ra), "+v"(ru));
            const f32x4 y0 = *(LAS f32x4*)(lds + ra), y1 = *(LAS f32x4*)(lds + ra + 8192), y2 = *(LAS f32x4*)(lds + ra + 16384), y3 = *(LAS f32x4*)(lds + ra + 24576);
            const u32x2 ua = *(LAS u32x2*)(lds + ru), ub = *(LAS u32x2*)(lds + ru + 4576);
            load_F(0); load_F(1);
            SB();
            const f32x4 ya = y0 + y1, yb = y2 + y3;
            const u32x4 w = {nosent(pk2(ya[0] + bfhi(ua[1]) * D0, yb[0] + bfhi(ub[1]) * D1)), nosent(pk2(ya[1] + bflo(ua[1]) * D0, yb[1] + bflo(ub[1]) * D1)),
                             nosent(pk2(ya[2] + bfhi(ua[0]) * D0, yb[2] + bfhi(ub[0]) * D1)), nosent(pk2(ya[3] + bflo(ua[0]) * D0, yb[3] + bflo(ub[0]) * D1))};
            __builtin_amdgcn_raw_buffer_store_b128(w, r_ws, WS_Y2 + (u32)(b * 8192 + tb * 4), 0, 16);
        }
        SB();
        STAMP(5);
        WAIT_VM(1);
        STAMP(40);

        u32 f2oth = 0;
        _Pragma("unroll 1") for (int attempt = 0; attempt < 64; ++attempt) {
            sfor<0, 8>([&](auto ic) { pin(of[decltype(ic)::value]); });
            u32 m0 = 0, m1 = 0;
#pragma unroll
            for (int ks = 0; ks < 8; ++ks) {
                load_F((ks + 2) & 7);
                if (R == 1 && ks == 4 && wave == 7) {
                    const int pb = 32 * mem + (lane & 31);
                    f2oth = WS_F2 + (u32)((lane < 32 ? ((pb & 7) * 4 + (pb >> 6)) * 8 + ((pb >> 3) & 7) : max(grp * 8 - 8, 0) + (lane & 15)) * 128);
                    ald2s(flg, f2oth);
                }
                SB();
#pragma unroll
                for (int n = 0; n < 4; ++n) accM[n] = mfma32(F[ks][n], __builtin_bit_cast(s16x8, of[ks]), ks ? accM[n] : resid(n));
                m0 = max(max(m0, of[ks][0]), of[ks][1]); m1 = max(max(m1, of[ks][2]), of[ks][3]);
                SB();
            }
            if (__builtin_expect(__all(max(m0, m1) != SENT), 1)) break;
            __builtin_amdgcn_s_sleep(4);
            WAIT_VM(0);
            sfor<0, 8>([&](auto ic) { constexpr int i = decltype(ic)::value; ald4o<(i & 3) * 1024>(of[i], ooff + (i >> 2) * 4096, d_ws); });
            WAIT_VM(0);
        }
        STAMP(41);
        if constexpr (R == 1) { if (wave == 7) { WAIT_VM(0); pin(flg); spin_flags(flg, f2oth); } }
    }

    auto load_y = [&]() {
        const int l2 = opq(tid) & 63;
#pragma unroll
        for (int i = 0; i < 8; ++i)
            yw[i] = __builtin_amdgcn_raw_buffer_load_b32(r_ws, WS_Y2 + (u32)((32 * mem + 2 * (l2 >> 4)) * 8192 + (TI * 16 + (l2 & 15)) * 4), (8 * (i >> 1) + (i & 1)) * 8192, 16);
    };
    auto pub_off = [&](u32 ws_st) {
        const int lane = opq(tid) & 63;
        return (lane & 16) ? 0xFFFFFF00u : ws_st + (u32)((TI * 8 + mem) * 256 + (lane & 15) * 16 + (lane >> 5) * 8);
    };
    auto pub_stat = [&](u32 off, u32 tg, float s, float q) {
        auto r = __builtin_amdgcn_permlane32_swap(__float_as_uint(s), __float_as_uint(q), false, false);
        const float v = __uint_as_float(r[0]) + __uint_as_float(r[1]);
        auto t = __builtin_amdgcn_permlane16_swap(__float_as_uint(v), __float_as_uint(v), false, false);
        __builtin_amdgcn_raw_buffer_store_b64((u32x2){__float_as_uint(__uint_as_float(t[0]) + __uint_as_float(t[1])), tg}, r_ws, off, 0, 16);
    };
    auto stat_off = [&](u32 ws_st, int ti) { const int l2 = opq(tid) & 63; return ws_st + (u32)(ti * 2048 + (l2 >> 4) * 512 + (l2 & 15) * 16); };
    auto fin_stat = [&](u32x4& a, u32x4& c, u32 off, float& mean, float& rstd) {
        _Pragma("unroll 1") for (int spin = 0; spin < SPIN_CAP; ++spin) {
            if (__builtin_expect(__all(((a[1] ^ tl) | (a[3] ^ th) | (c[1] ^ tl) | (c[3] ^ th)) == 0), 1)) break;
            __builtin_amdgcn_s_sleep(1);
            asm volatile("buffer_load_dwordx4 %0, %2, %3, 0 offen sc1\n\tbuffer_load_dwordx4 %1, %2, %3, 0 offen offset:256 sc1\n\ts_waitcnt vmcnt(0)"
                         : "=&v"(a), "=&v"(c) : "v"(off), "s"(d_ws) : "memory");
        }
        auto r = __builtin_amdgcn_permlane32_swap(__float_as_uint(__uint_as_float(a[0]) + __uint_as_float(c[0])), __float_as_uint(__uint_as_float(a[2]) + __uint_as_float(c[2])), false, false);
        const float v = __uint_as_float(r[0]) + __uint_as_float(r[1]);
        auto t = __builtin_amdgcn_permlane16_swap(__float_as_uint(v), __float_as_uint(v), false, false);
        const u32 v2 = __float_as_uint(__uint_as_float(t[0]) + __uint_as_float(t[1]));
        auto u = __builtin_amdgcn_permlane32_swap(v2, v2, false, false);
        const float ts = __uint_as_float(u[0]), tq = __uint_as_float(u[1]);
        mean = ts * (1.f / 512.f);
        rstd = __builtin_amdgcn_rsqf(fmaxf(tq * (1.f / 512.f) - mean * mean, 0.f) + 1e-5f);
    };
    auto regroup = [&](u32 (&w)[8]) {
#pragma unroll
        for (int j = 0; j < 2; ++j) {
            auto r0 = __builtin_amdgcn_permlane16_swap(w[4 * j], w[4 * j + 2], false, false);
            auto r1 = __builtin_amdgcn_permlane16_swap(w[4 * j + 1], w[4 * j + 3], false, false);
            w[4 * j] = r0[0]; w[4 * j + 1] = r1[0]; w[4 * j + 2] = r0[1]; w[4 * j + 3] = r1[1];
        }
    };
    auto wo_finish = [&](auto lc) {
        constexpr int LAYER = decltype(lc)::value;
        const int lane = opq(tid) & 63, li = lane & 15, lq = lane >> 4;
        u32 wx = L_WX + hd * 4096 + lane * 16;
        pin(wx);
        u32 po = 0, so = 0, tg = 0, oa = 0;
        if constexpr (R == 1) {
#pragma unroll
            for (int n = 0; n < 4; ++n) *(LAS f32x4*)(lds + wx + n * 1024) = accM[n];
        } else {
            po = pub_off(LAYER ? WS_ST3 : WS_ST1); so = stat_off(LAYER ? WS_ST3 : WS_ST1, TI); tg = lane < 32 ? tl : th;
            const int c = 2 * (lq & 1) + (lq >> 1);
            oa = LAYER ? (u32)((TI * 16 + li) * 1024 + mem * 128 + c * 16) : WS_X2 + (u32)((TI * 8 + mem) * 2048 + li * 128 + ((c ^ (li >> 1)) << 4));
            pin(po); pin(so); pin(tg); pin(oa);
        }
        BAR();
        STAMP(LAYER ? 25 : 42);
        if constexpr (R == 0) {
            f32x4 t[4];
#pragma unroll
            for (int n = 0; n < 4; ++n) t[n] = *(LAS f32x4*)(lds + wx + n * 1024);
            SB();
            float s = 0.f, q = 0.f, s2 = 0.f, q2 = 0.f;
#pragma unroll
            for (int n = 0; n < 4; ++n) {
                const f32x4 v = accM[n] + t[n];
                const u32 w0 = pk2(v[0], v[1]), w1 = pk2(v[2], v[3]);
                xw[2 * n] = w0; xw[2 * n + 1] = w1;
                s = dot2(w0, 0x3F803F80u, s); q = dot2(w0, w0, q); s2 = dot2(w1, 0x3F803F80u, s2); q2 = dot2(w1, w1, q2);
            }
            pub_stat(po, tg, s + s2, q + q2);
            CFENCE();
            if constexpr (!LAYER) load_y();
            regroup(xw);
            SB();
            STAMP(LAYER ? 46 : 43);
            WAIT_VM(0);
            STAMP(LAYER ? 26 : 44);
            if constexpr (!LAYER) {
#pragma unroll
                for (int i = 0; i < 8; ++i) pin(yw[i]);
            }
            u32x4 a, c;
            ald4s(a, so); ald4s(c, so + 256);
            if constexpr (!LAYER) {
                _Pragma("unroll 1") for (int spin = 0; spin < SPIN_CAP; ++spin) {
                    if (__builtin_expect(__all(max(max(max(yw[0], yw[1]), max(yw[2], yw[3])), max(max(yw[4], yw[5]), max(yw[6], yw[7]))) != SENT), 1)) break;
                    __builtin_amdgcn_s_sleep(1);
                    CFENCE();
                    load_y();
                }
                regroup(yw);
            }
            f32x2 xa[8], ya[8], xy[8];
#pragma unroll
            for (int i = 0; i < 8; ++i) {
                xa[i] = (f32x2){bflo(xw[i]), bfhi(xw[i])}; ya[i] = (f32x2){bflo(yw[i]), bfhi(yw[i])}; xy[i] = xa[i] * ya[i];
                pin(xa[i]); pin(ya[i]); pin(xy[i]);
            }
            WAIT_VM(0);
            pin(a); pin(c);
            float mean, rstd;
            fin_stat(a, c, so, mean, rstd);
            STAMP(LAYER ? 11 : 6);
            const float nm = -mean * rstd;
#pragma unroll
            for (int i = 0; i < 8; ++i) {
                const f32x2 o = xy[i] * rstd + (ya[i] * nm + xa[i]);
                xw[i] = pk2(o[0], o[1]);
                if (!LAYER) xw[i] = nosent(xw[i]);
            }
#pragma unroll
            for (int j = 0; j < 2; ++j) {
                const u32x4 o = {xw[4 * j], xw[4 * j + 1], xw[4 * j + 2], xw[4 * j + 3]};
                if constexpr (LAYER) __builtin_amdgcn_raw_buffer_store_b128(o, r_out, oa + j * 64, 0, OUT_AUX);
                else __builtin_amdgcn_raw_buffer_store_b128(o, r_ws, oa ^ (j * 64), 0, 16);
            }
            if constexpr (!LAYER) {
                float s = 0.f, q = 0.f, s2 = 0.f, q2 = 0.f;
#pragma unroll
                for (int i = 0; i < 8; i += 2) { s = dot2(xw[i], 0x3F803F80u, s); q = dot2(xw[i], xw[i], q); s2 = dot2(xw[i + 1], 0x3F803F80u, s2); q2 = dot2(xw[i + 1], xw[i + 1], q2); }
                pub_stat(po + (WS_ST2 - WS_ST1) * !(lane & 16), tg, s + s2, q + q2);
                CFENCE();
                regroup(xw);
#pragma unroll
                for (int i = 0; i < 8; ++i) { pin(xw[i]); pin(yw[i]); }
                STAMP(48);
                WAIT_VM(0);
            }
        }
    };
    wo_finish(IC<0>{});
    RBAR();
    STAMP(7);

    {
        const int l3 = opq(tid) & 63;
        const u32 g0 = (u32)((4 * grp - 1 + (hd >> 1)) * 16384 + (hd & 1) * 1024 + l3 * 16);
        const u32 g2 = (u32)((4 * grp + 3) * 16384 + hd * 512 + l3 * 16);
        auto x2_piece = [&](auto ic) {
            constexpr int i = decltype(ic)::value, s = i / 3, p = i % 3;
            const u32 kp = (u32)((s + rot) & 7);
            if constexpr (p == 0) dma(IC<4>{}, IC<L_CX + s * 10240>{}, d_x2, hb1k, g0, kp * 2048);
            else if constexpr (p == 1) dma(IC<4>{}, IC<L_CX + s * 10240 + 4096>{}, d_x2, hb1k, g0 + 32768, kp * 2048);
            else dma(IC<5>{}, IC<L_CX + s * 10240 + 8192>{}, d_x2, hb512, g2, kp * 2048);
        };
        u32 va = L_CX + hd * 1024 + l3 * 16, vq = L_CX + 8192 + hd * 512 + (l3 & 31) * 16 + (l3 >> 5) * 10240;
        pin(va); pin(vq);
        u32x4 vr[5];
        auto ver_read = [&](auto cc) {
            constexpr int c = decltype(cc)::value;
            u32 a = va + c * 20480, q = vq + c * 20480;
            pin(a); pin(q); CFENCE();
            vr[0] = *(LAS u32x4*)(lds + a); vr[1] = *(LAS u32x4*)(lds + a + 4096); vr[2] = *(LAS u32x4*)(lds + a + 10240); vr[3] = *(LAS u32x4*)(lds + a + 14336);
            vr[4] = *(LAS u32x4*)(lds + q);
        };
        auto ver_check = [&](auto cc) {
            constexpr int c = decltype(cc)::value;
            _Pragma("unroll 1") for (int spin = 0; spin < SPIN_CAP; ++spin) {
                u32 m0 = 0, m1 = 0;
#pragma unroll
                for (int i = 0; i < 5; ++i) { m0 = max(max(m0, vr[i][0]), vr[i][1]); m1 = max(max(m1, vr[i][2]), vr[i][3]); }
                if (__builtin_expect(__all(max(m0, m1) != SENT), 1)) break;
                __builtin_amdgcn_s_sleep(1);
                sfor<6 * c, 6 * c + 6>([&](auto ic) { x2_piece(ic); });
                WAIT_VM(0);
                ver_read(cc);
            }
        };
        const u32 xb0 = (u32)((l3 & 15) * 128 + (((l3 >> 4) ^ ((l3 & 15) >> 1)) << 4));
        if constexpr (R == 1) {
            STAMP(39);
            sfor<0, 12>([&](auto ic) { x2_piece(ic); });
            WAIT_VM(6);
            STAMP(8);
            ver_read(IC<0>{});
            ver_check(IC<0>{});
        } else {
            u32x4 sa, sc, ha, hc;
            const u32 so = stat_off(WS_ST2, 4 * grp + hd);
            const bool halo = hd == 0 && grp > 0;
            ald4s(sa, so); ald4s(sc, so + 256);
            if (halo) { ald4s(ha, so - 2048); ald4s(hc, so - 2048 + 256); }
            WAIT_VM(0);
            pin(sa); pin(sc);
            float mean, rstd;
            fin_stat(sa, sc, so, mean, rstd);
            if (l3 < 16) { *(LAS float*)(lds + L_TAB + 64 + hd * 64 + l3 * 4) = mean; *(LAS float*)(lds + L_TAB + 320 + 64 + hd * 64 + l3 * 4) = rstd; }
            if (halo) {
                pin(ha); pin(hc);
                fin_stat(ha, hc, so - 2048, mean, rstd);
                if (l3 < 16) { *(LAS float*)(lds + L_TAB + l3 * 4) = mean; *(LAS float*)(lds + L_TAB + 320 + l3 * 4) = rstd; }
            }
        }
        BAR();
        STAMP(13);
        u32 xa;
        auto pf_addr = [&](int st) { xa = L_CX + (st >> 1) * 10240 + (xb0 ^ ((st & 1) * 64)); pin(xa); };
        pf_addr(0);
#pragma unroll
        for (int mt = 0; mt < 5; ++mt) Ab[0][mt] = ldsA(xa + mt * 2048);
        sfor<0, 16>([&](auto stc) {
            constexpr int st = decltype(stc)::value;
            if constexpr (R == 1) {
                if constexpr (st == 2) { WAIT_VM(10); ver_read(IC<1>{}); }
                if constexpr (st == 3) ver_check(IC<1>{});
                if constexpr (st == 5) { WAIT_VM(6); ver_read(IC<2>{}); }
                if constexpr (st == 6) { ver_check(IC<2>{}); WAIT_VM(0); ver_read(IC<3>{}); }
                if constexpr (st == 7) ver_check(IC<3>{});
            }
            if constexpr (st == 3) { RBAR(); STAMP(49); }
            if constexpr (st == 7) { RBAR(); STAMP(50); CSTAMP(60); }
            if (st < 15) pf_addr(st + 1);
            if constexpr (st == 14) load_tab(true);
            SB();
            qkv_step(IC<1>{}, st, Ab[st & 1], [&](auto mc) {
                constexpr int mt = decltype(mc)::value;
                if (st < 15) Ab[(st + 1) & 1][mt] = ldsA(xa + mt * 2048);
                if constexpr (R == 1 && 12 + 5 * st + mt < 24) x2_piece(IC<12 + 5 * st + mt>{});
                SB();
            });
        });
    }
    STAMP(R ? 9 : 52);
    CSTAMP(61);
    attention(IC<1>{});
    STAMP(27);

    {
        wo_addr();
#pragma unroll
        for (int ks = 0; ks < 8; ++ks) load_F(ks);
        SB();
        WAIT_VM(0);
        RBAR();
        STAMP(10);
        load_o(WS_O2);
        SB();
        _Pragma("unroll 1") for (int spin = 0; spin < 64; ++spin) {
            u32 m0 = 0, m1 = 0;
#pragma unroll
            for (int ks = 0; ks < 8; ++ks) {
#pragma unroll
                for (int n = 0; n < 4; ++n) accM[n] = mfma32(F[ks][n], __builtin_bit_cast(s16x8, of[ks]), ks ? accM[n] : resid(n));
                m0 = max(max(m0, of[ks][0]), of[ks][1]); m1 = max(max(m1, of[ks][2]), of[ks][3]);
                SB();
            }
            if (__builtin_expect(__all(max(m0, m1) != SENT), 1)) break;
            _Pragma("unroll 1") for (int sp2 = 0; sp2 < SPIN_CAP; ++sp2) {
                CFENCE();
                __builtin_amdgcn_s_sleep(1);
                const u32 pv = __builtin_amdgcn_raw_buffer_load_b32(r_ws, WS_O2 + (u32)(TI * 16384 + R * 8192 + (opq(tid) & 15) * 512 + 508), 0, 16);
                if (__all(pv != SENT)) break;
            }
            CFENCE();
            load_o(WS_O2);
        }
        SB();
        STAMP(45);
    }
    wo_finish(IC<1>{});
    if constexpr (R == 1) {
        if (b == 0 && wave == 7) {
            const int l3 = opq(tid) & 63;
#pragma unroll
            for (int i = 0; i < 4; ++i) { flg = (u32x2){0u, 0u}; spin_flags(flg, WS_F1 + (u32)((i * 64 + l3) * 128)); }
            if (l3 == 0) __builtin_amdgcn_raw_buffer_store_b64(__builtin_bit_cast(u32x2, epoch + 1), r_ws, WS_EPOCH, 0, 16);
        }
    }
    if constexpr (R == 0) {
        WAIT_VM(0);
        STAMP(12);
        const u32 d = dg1 - dg0;
        u64 t0_; asm volatile("s_memrealtime %0\n\ts_waitcnt lgkmcnt(0)" : "=s"(t0_)::"memory");
        _Pragma("unroll 1") for (int i = 0; i < 100000; ++i) { u64 t_; asm volatile("s_memrealtime %0\n\ts_waitcnt lgkmcnt(0)" : "=s"(t_)::"memory"); if ((u32)t_ - (u32)t0_ >= d) break; }
    }
}

__global__ void __launch_bounds__(512, 2)
fused_kernel(const u16* __restrict__ gx, const u16* __restrict__ gwqkv, const u16* __restrict__ gwo,
             const u16* __restrict__ gbk, const u16* __restrict__ gbv, const u16* __restrict__ gsk,
             const u16* __restrict__ ghk, const u16* __restrict__ gdv, u16* __restrict__ gout, char* __restrict__ ws) {
    __shared__ __attribute__((aligned(16))) char lds_[L_TOTAL];
    typedef u32 u32x16 __attribute__((ext_vector_type(16)));
    u32x16 ka; u32x4 kb;
    {
        auto kp = __builtin_amdgcn_kernarg_segment_ptr();
        asm volatile("s_load_dwordx16 %0, %2, 0x0\n\ts_load_dwordx4 %1, %2, 0x40\n\ts_waitcnt lgkmcnt(0)" : "=&s"(ka), "=&s"(kb) : "s"(kp) : "memory");
    }
    auto mkp = [](u32 lo, u32 hi) { return (char*)(((u64)hi << 32) | lo); };
    const Args a = {(const u16*)mkp(ka[0], ka[1]), (const u16*)mkp(ka[2], ka[3]), (const u16*)mkp(ka[4], ka[5]), (const u16*)mkp(ka[6], ka[7]), (const u16*)mkp(ka[8], ka[9]),
                    (const u16*)mkp(ka[10], ka[11]), (const u16*)mkp(ka[12], ka[13]), (const u16*)mkp(ka[14], ka[15]), (u16*)mkp(kb[0], kb[1]), mkp(kb[2], kb[3])};
    const int wave = __builtin_amdgcn_readfirstlane(threadIdx.x >> 6);
    if (wave < 4) body<0>(a, (LAS char*)lds_, wave); else body<1>(a, (LAS char*)lds_, wave);
}

extern "C" void kernel_launch(void* const* d_in, const int* in_sizes, int n_in,
                              void* d_out, int out_size, void* d_ws, size_t ws_size,
                              hipStream_t stream) {
    fused_kernel<<<256, 512, 0, stream>>>((const u16*)d_in[0], (const u16*)d_in[1], (const u16*)d_in[2],
                                          (const u16*)d_in[4], (const u16*)d_in[5], (const u16*)d_in[6],
                                          (const u16*)d_in[7], (const u16*)d_in[8], (u16*)d_out, (char*)d_ws);
}
```

```cpp
#define DIAG 17
#define CODE_LINES 312
#define ROT 2
#define PRO 2
#define NO_PROBE
#include <hip/hip_runtime.h>
#include <stdint.h>

typedef unsigned short u16;
typedef unsigned u32;
typedef unsigned long long u64;
typedef short s16x4 __attribute__((ext_vector_type(4)));
typedef short s16x8 __attribute__((ext_vector_type(8)));
typedef __bf16 bf16x2 __attribute__((ext_vector_type(2)));
typedef float f32x2 __attribute__((ext_vector_type(2)));
typedef float f32x4 __attribute__((ext_vector_type(4)));
typedef u32 u32x2 __attribute__((ext_vector_type(2)));
typedef u32 u32x4 __attribute__((ext_vector_type(4)));
#define LAS __attribute__((address_space(3)))
#define DEV __device__ __forceinline__
typedef __amdgpu_buffer_rsrc_t rsrc_t;

constexpr u32 WS_EPOCH = 0;
constexpr u32 WS_F1 = 32768;
constexpr u32 WS_F2 = 65536;
constexpr u32 WS_ST0 = 98304;
constexpr u32 WS_ST1 = 131072;
constexpr u32 WS_ST2 = WS_ST1 + 262144;
constexpr u32 WS_ST3 = WS_ST2 + 262144;
constexpr u32 WS_UT = 1u << 20;
constexpr u32 WS_O1 = WS_UT + (2u << 20);
constexpr u32 WS_O2 = WS_O1 + (2u << 20);
constexpr u32 WS_Y2 = WS_O2 + (2u << 20);
constexpr u32 WS_X2 = WS_Y2 + (2u << 20);
constexpr u32 WS_END = WS_X2 + (2u << 20);

constexpr u32 L_SLOT = 34816;
constexpr u32 L_KV = 4 * L_SLOT;
constexpr u32 L_K = L_KV, L_V = L_KV + 9216;
constexpr u32 L_SK = L_KV + 17408;
constexpr u32 L_WO = 0;
constexpr u32 L_CX = 65536;
constexpr u32 L_HC = L_CX;
constexpr u32 L_UR = L_CX + 69632;
constexpr u32 L_WX = L_CX + 40960;
constexpr u32 L_EX = 147456;
constexpr u32 L_TAB = L_EX + 12288;
constexpr u32 L_CS = L_TAB + 1024;
constexpr u32 L_FLG = L_TAB + 2048;
constexpr u32 L_DUMMY = L_FLG + 1024;
constexpr u32 L_TOTAL = 160 * 1024;
static_assert(L_DUMMY + 1024 <= L_TOTAL, "LDS map");

#define WO_POS 0
#define OUT_AUX 0
constexpr int SPIN_CAP = 1 << 14;
constexpr u32 SENT = 0xFFFFFFFFu;

#define WAIT_VM(n) asm volatile("s_waitcnt vmcnt(%0)" ::"i"(n) : "memory")
#define BAR() do { asm volatile("s_waitcnt lgkmcnt(0)" ::: "memory"); __builtin_amdgcn_s_barrier(); asm volatile("" ::: "memory"); } while (0)
#define RBAR() do { asm volatile("" ::: "memory"); __builtin_amdgcn_s_barrier(); asm volatile("" ::: "memory"); } while (0)
#define CFENCE() asm volatile("" ::: "memory")
#define SB() __builtin_amdgcn_sched_barrier(0)
template <int N> struct IC { static constexpr int value = N; };
template <int I, int N, class F> DEV void sfor(F&& f) { if constexpr (I < N) { f(IC<I>{}); sfor<I + 1, N>(f); } }

DEV f32x4 mfma32(s16x8 a, s16x8 b, f32x4 c) { return __builtin_amdgcn_mfma_f32_16x16x32_bf16(a, b, c, 0, 0, 0); }
DEV f32x4 mfma16(s16x4 a, s16x4 b, f32x4 c) { return __builtin_amdgcn_mfma_f32_16x16x16bf16_1k(a, b, c, 0, 0, 0); }
DEV float vmax(float a, float b) { float r; asm("v_max_f32 %0, %1, %2" : "=v"(r) : "v"(a), "v"(b)); return r; }
DEV u32 pk2(float lo, float hi) { f32x2 f = {lo, hi}; bf16x2 b = __builtin_convertvector(f, bf16x2); return __builtin_bit_cast(u32, b); }
DEV float bflo(u32 w) { return __uint_as_float(w << 16); }
DEV float bfhi(u32 w) { return __uint_as_float(w & 0xffff0000u); }
DEV float dot2(u32 a, u32 b, float c) { return __builtin_amdgcn_fdot2_f32_bf16(__builtin_bit_cast(bf16x2, a), __builtin_bit_cast(bf16x2, b), c, false); }
DEV u32 nosent(u32 w) { return min(w, 0xFFFFFFFEu); }
DEV float xsum(float v) {
    auto r = __builtin_amdgcn_permlane32_swap(__float_as_uint(v), __float_as_uint(v), false, false);
    v = __uint_as_float(r[0]) + __uint_as_float(r[1]);
    auto s = __builtin_amdgcn_permlane16_swap(__float_as_uint(v), __float_as_uint(v), false, false);
    return __uint_as_float(s[0]) + __uint_as_float(s[1]);
}
DEV float xmax(float v) {
    auto r = __builtin_amdgcn_permlane32_swap(__float_as_uint(v), __float_as_uint(v), false, false);
    v = vmax(__uint_as_float(r[0]), __uint_as_float(r[1]));
    auto s = __builtin_amdgcn_permlane16_swap(__float_as_uint(v), __float_as_uint(v), false, false);
    return vmax(__uint_as_float(s[0]), __uint_as_float(s[1]));
}
DEV float rowsum16(float v) {
    v += __builtin_bit_cast(float, __builtin_amdgcn_update_dpp(0, __builtin_bit_cast(int, v), 0xB1, 0xf, 0xf, true));
    v += __builtin_bit_cast(float, __builtin_amdgcn_update_dpp(0, __builtin_bit_cast(int, v), 0x4E, 0xf, 0xf, true));
    v += __builtin_bit_cast(float, __builtin_amdgcn_update_dpp(0, __builtin_bit_cast(int, v), 0x141, 0xf, 0xf, true));
    v += __builtin_bit_cast(float, __builtin_amdgcn_update_dpp(0, __builtin_bit_cast(int, v), 0x140, 0xf, 0xf, true));
    return v;
}
DEV float bcast_row0(float v) { return __builtin_bit_cast(float, __builtin_amdgcn_update_dpp(0, __builtin_bit_cast(int, v), 0x150, 0xf, 0xf, true)); }
template <int OFF> DEV void ald4o(u32x4& d, u32 voff, u32x4 rs) { asm volatile("buffer_load_dwordx4 %0, %1, %2, 0 offen offset:%3 sc1" : "=v"(d) : "v"(voff), "s"(rs), "i"(OFF) : "memory"); }
template <class T> DEV void pin(T& v) { asm volatile("" : "+v"(v)); }
DEV int opq(int v) { asm volatile("" : "+v"(v)); return v; }
DEV s16x8 cat8(s16x4 a, s16x4 b) { return __builtin_shufflevector(a, b, 0, 1, 2, 3, 4, 5, 6, 7); }
DEV s16x4 pack4(f32x4 v) { u32x2 w = {pk2(v[0], v[1]), pk2(v[2], v[3])}; return __builtin_bit_cast(s16x4, w); }
DEV u64 mix64(u64 z) { z += 0x9E3779B97F4A7C15ull; z = (z ^ (z >> 30)) * 0xBF58476D1CE4E5B9ull; z = (z ^ (z >> 27)) * 0x94D049BB133111EBull; return z ^ (z >> 31); }

struct ConvSched {
    int n[4], f[18], i[18], slot[64];
    constexpr ConvSched(int R) : n{}, f{}, i{}, slot{} {
        constexpr int LEAD = 9;
        const int N0[4] = {0, 3, 4, 7};
        for (int k = 0; k < 4; ++k) n[k] = N0[k] ^ R;
        int c = 0;
        for (int ff = 0; ff < 8; ++ff) for (int k = 3; k >= 0; --k) if (ff <= n[k]) { f[c] = ff; i[c] = k; ++c; }
        for (int k = 0; k < 64; ++k) slot[k] = 1000;
        for (int p = 71; p >= 0; --p) {
            const int e = p / 18, ff = f[p % 18], g = n[i[p % 18]] - ff, s = p - LEAD < -1 ? -1 : p - LEAD;
            slot[e * 16 + ff] = s; slot[e * 16 + 8 + g] = s;
        }
    }
};

#define STAMP(i) do { if constexpr ((i) == 0 || (i) == DIAG) { u64 t_; asm volatile("s_memrealtime %0\n\ts_waitcnt lgkmcnt(0)" : "=s"(t_)::"memory"); if ((i) == 0) dg0 = (u32)t_; else dg1 = (u32)t_; } } while (0)
#define CSTAMP(i) do { } while (0)

struct Args { const u16 *gx, *gwqkv, *gwo, *gbk, *gbv, *gsk, *ghk, *gdv; u16* gout; char* ws; };

template <int R>
DEV void body(const Args& A_, LAS char* const lds, const int wave, const u64 pc0) {
    const u16* gx = A_.gx; char* ws = A_.ws;
    u32 dg0 = 0, dg1 = 0;
    STAMP(0);
    const int b = blockIdx.x, tid = threadIdx.x, lane = tid & 63;
    const int hd = wave & 3;
    const int grp = (b & 7) * 4 + (b >> 6), mem = (b >> 3) & 7;
    const int T0 = grp * 64;
    const int rot = (mem + ROT * (grp & 3)) & 7;

    u64 epoch;
    asm volatile("s_load_dwordx2 %0, %1, 0x0" : "=s"(epoch) : "s"(ws));
    u32 dw;
    asm volatile("s_load_dword %0, %1, 0x0" : "=s"(dw) : "s"(A_.gdv + 2 * b));
    const rsrc_t r_x = __builtin_amdgcn_make_buffer_rsrc((void*)gx, 0, 2048 * 512 * 2, 0x00020000);
    const rsrc_t r_ws = __builtin_amdgcn_make_buffer_rsrc((void*)ws, 0, WS_END, 0x00020000);
    const rsrc_t r_out = __builtin_amdgcn_make_buffer_rsrc((void*)A_.gout, 0, 2048 * 512 * 2, 0x00020000);
    const u32 ldsbase = (u32)(size_t)lds;
    auto mkdesc = [](const void* p, u32 bytes) { const u64 a = (u64)p; return (u32x4){(u32)a, (u32)(a >> 32) & 0xffffu, bytes, 0x00020000u}; };
    const u32x4 d_x = mkdesc(gx, 2048 * 512 * 2), d_w = mkdesc(A_.gwqkv, 512 * 1536 * 2), d_wo = mkdesc(A_.gwo, 512 * 512 * 2);
    const u32x4 d_k = mkdesc(A_.gbk, 2048 * 512 * 2), d_v = mkdesc(A_.gbv, 2048 * 512 * 2), d_sk = mkdesc(A_.gsk, 512 * 4 * 2);
    const u32x4 d_h = mkdesc(A_.ghk, 512 * 2048 * 2), d_x2 = mkdesc(ws + WS_X2, 2u << 20), d_ws = mkdesc(ws, WS_END);
    const u32 wb1k = ldsbase + wave * 1024, hb1k = ldsbase + hd * 1024, hb512 = ldsbase + hd * 512;
#define DMA_ASM(pre, mods, post) asm volatile("s_add_u32 m0, %0, %4\n\t" pre "buffer_load_dwordx4 %1, %2, %3 offen " mods "lds" post \
                     :: "s"(sbase), "v"(voff), "s"(rs), "s"(soff), "i"(IMM) : "memory", "scc")
    const u32 xmlo = hd == 0 ? ~0xFFu : ~0u;
    auto dma = [&](auto kind, auto imm, u32x4 rs, u32 sbase, u32 voff, u32 soff) {
        constexpr int K = decltype(kind)::value; constexpr int IMM = decltype(imm)::value;
        if constexpr (K == 0) DMA_ASM("s_nop 0\n\t", "", "");
        else if constexpr (K == 1) DMA_ASM("s_mov_b32 exec_hi, 0\n\t", "", "\n\ts_mov_b32 exec_hi, -1");
        else if constexpr (K == 2) {
            const u32 ml = xmlo;
            asm volatile("s_add_u32 m0, %0, %4\n\ts_mov_b32 exec_lo, %5\n\tbuffer_load_dwordx4 %1, %2, %3 offen lds\n\ts_mov_b32 exec_lo, -1"
                         :: "s"(sbase), "v"(voff), "s"(rs), "s"(soff), "i"(IMM), "s"(ml) : "memory", "scc");
        }
        else if constexpr (K == 4) DMA_ASM("s_nop 0\n\t", "sc1 ", "");
        else DMA_ASM("s_mov_b32 exec_hi, 0\n\t", "sc1 ", "\n\ts_mov_b32 exec_hi, -1");
    };
    auto ald4 = [&](u32x4& d, u32x4 rs, u32 voff) { asm volatile("buffer_load_dwordx4 %0, %1, %2, 0 offen" : "=v"(d) : "v"(voff), "s"(rs) : "memory"); };
    auto ald4s = [&](u32x4& d, u32 voff) { asm volatile("buffer_load_dwordx4 %0, %1, %2, 0 offen sc1" : "=v"(d) : "v"(voff), "s"(d_ws) : "memory"); };
    auto ald2s = [&](u32x2& d, u32 voff) { asm volatile("buffer_load_dwordx2 %0, %1, %2, 0 offen sc1" : "=v"(d) : "v"(voff), "s"(d_ws) : "memory"); };

    const int pr = lane >> 3, pc = lane & 7;
    const int xr0 = 8 * hd + pr, xr2 = 64 + 4 * hd + pr;
    const u32 xv0 = (u32)((T0 - 16 + xr0) * 1024 + ((pc ^ ((xr0 >> 1) & 7)) << 4));
    const u32 xv2 = (u32)((T0 - 16 + xr2) * 1024 + ((pc ^ ((xr2 >> 1) & 7)) << 4));
    const int wfw = ((xr0 >> 1) & 1) | (((xr0 >> 3) & 1) << 1);
    const u32 wv0 = (u32)(xr0 * 3072 + mem * 128 + ((((pc >> 1) ^ wfw) * 2 + (pc & 1)) << 4));
    auto issue_piece = [&](auto sc, auto ic) {
        constexpr int s = decltype(sc)::value, i = decltype(ic)::value;
        const u32 kp = (u32)((s + rot) & 7);
        constexpr int sb = (s & 3) * L_SLOT;
        if constexpr (i == 0) dma(IC<2>{}, IC<sb>{}, d_x, hb1k, xv0, kp * 128);
        else if constexpr (i == 1) dma(IC<0>{}, IC<sb + 4096>{}, d_x, hb1k, xv0 + 32768, kp * 128);
        else if constexpr (i == 2) dma(IC<1>{}, IC<sb + 8192>{}, d_x, hb512, xv2, kp * 128);
        else dma(IC<0>{}, IC<sb + 10240 + (i - 3) * 4096>{}, d_w, hb1k, wv0, kp * (64 * 3072) + ((i - 3) >> 1) * 1024 + ((i - 3) & 1) * (32 * 3072));
    };
    auto issue_wo = [&](auto ic) {
        constexpr int i = decltype(ic)::value;
        const int l2 = opq(tid) & 63, pr = l2 >> 3, pc = l2 & 7;
        const u32 wov = (u32)((8 * hd + pr) * 1024 + mem * 128 + ((((pc >> 1) ^ (((pr >> 1) & 1) | ((hd & 1) << 1))) * 2 + (pc & 1)) << 4));
        const u32 j = (u32)((i + 2 * grp) & 7);
        dma(IC<0>{}, IC<L_WO + (i & 8) * 4096>{}, d_wo, hb1k + j * 4096, wov, ((i & 8) + j) * 32768);
    };
    auto issue_kv = [&](int i) {
        const int l2 = opq(tid) & 63, pr = l2 >> 3, pc = l2 & 7, p = hd + 4 * (i & 1);
        const u32 cb = (u32)(mem * 128);
        if (i < 2) dma(IC<0>{}, IC<L_K>{}, d_k, hb1k + (i & 1) * 4096, (u32)((T0 - 8 + 8 * p + pr) * 1024) + cb + ((pc ^ p) << 4), 0);
        else if (i < 4) dma(IC<0>{}, IC<L_V>{}, d_v, hb1k + (i & 1) * 4096, (u32)((T0 + 8 * p + pr) * 1024) + cb + ((pc ^ p) << 4), 0);
        else if (hd == 0) dma(IC<0>{}, IC<L_K + 8192>{}, d_k, ldsbase, (u32)((T0 - 8 + 64 + pr) * 1024) + cb + (pc << 4), 0);
        else dma(IC<0>{}, IC<L_SK>{}, d_sk, ldsbase, (u32)(mem * 512 + l2 * 16), 0);
    };

    const int srow = T0 + 8 * mem + 2 * hd;
    u32x4 xrow[2];
    sfor<0, 9>([&](auto ic) { if constexpr ((decltype(ic)::value & 1) != R) issue_piece(IC<0>{}, ic); });
    sfor<0, 9>([&](auto ic) { if constexpr ((decltype(ic)::value & 1) != R) issue_piece(IC<1>{}, ic); });
    {
        const u32 ln = (u32)((b >> 3) * 8 + wave);
        const u64 pa = (pc0 & ~127ull) + ln * 128, pb = pa + (ln + 256 < CODE_LINES ? 32768 : 0);
        u32 j0, j1;
        asm volatile("s_load_dword %0, %2, 0x0\n\ts_load_dword %1, %3, 0x0\n\ts_waitcnt lgkmcnt(0)" : "=&s"(j0), "=&s"(j1) : "s"(pa), "s"(pb));
    }
    asm volatile("s_waitcnt lgkmcnt(0)" : "+s"(epoch), "+s"(dw));
    const u64 tag = mix64(epoch);
    const u32 tl = (u32)tag, th = (u32)(tag >> 32);
    if (R == 1 && wave == 7 && lane < 32) *(LAS u32x4*)(lds + (lane >> 3) * L_SLOT + (lane & 7) * 16) = (u32x4){0x3F803F80u, 0x3F803F80u, 0x3F803F80u, 0x3F803F80u};

    auto trr = [&](u32 off) { return __builtin_amdgcn_ds_read_tr16_b64_v4i16((LAS s16x4*)(lds + off)); };
    auto ldsA = [&](u32 off) { return *(LAS s16x8*)(lds + off); };
    auto flag_ok = [&](const u32x2& f) { return __all(f[0] == tl && f[1] == th); };
    auto spin_flags = [&](u32x2& f, u32 off) {
        _Pragma("unroll 1") for (int spin = 0; spin < SPIN_CAP; ++spin) {
            if (__builtin_expect(flag_ok(f), 1)) break;
            __builtin_amdgcn_s_sleep(2);
            asm volatile("buffer_load_dwordx2 %0, %1, %2, 0 offen sc1\n\ts_waitcnt vmcnt(0)" : "=&v"(f) : "v"(off), "s"(d_ws) : "memory");
        }
    };

    s16x8 Wq[16], Wm[16];
    f32x4 accM[5], accQ[5];
    const f32x4 zero4 = {0.f, 0.f, 0.f, 0.f};
    const s16x8 ones8 = {0x3F80, 0x3F80, 0x3F80, 0x3F80, 0x3F80, 0x3F80, 0x3F80, 0x3F80};
    s16x8 Ab[3][5];
    auto qkv_step = [&](auto lc, int st, const s16x8 (&A)[5], auto fill) {
        sfor<0, 5>([&](auto mc) {
            constexpr int mt = decltype(mc)::value;
            const f32x4 cm = st ? accM[mt] : zero4;
            accM[mt] = R ? mfma32(A[mt], Wm[st], cm) : mfma32(Wm[st], A[mt], cm);
            if constexpr (R == 0 && (mt > 0 || decltype(lc)::value == 0)) accQ[mt] = mfma32(Wq[st], A[mt], st ? accQ[mt] : zero4);
            fill(mc);
        });
    };

    float m1[5], r1[5]; f32x4 m4[5], r4[5], csK, csQ;
    auto load_tab = [&](bool cs) {
        const int l2 = opq(tid) & 63;
        u32 ta = L_TAB + (R ? (l2 >> 4) * 16 : (l2 & 15) * 4);
        pin(ta);
        if (R == 0 && cs) { csK = *(LAS f32x4*)(lds + L_CS + hd * 128 + (l2 >> 4) * 16); csQ = *(LAS f32x4*)(lds + L_CS + hd * 128 + 64 + (l2 >> 4) * 16); }
#pragma unroll
        for (int mt = 0; mt < 5; ++mt) {
            if (R == 0) { m1[mt] = *(LAS float*)(lds + ta + mt * 64); r1[mt] = *(LAS float*)(lds + ta + 320 + mt * 64); }
            else { m4[mt] = *(LAS f32x4*)(lds + ta + mt * 64); r4[mt] = *(LAS f32x4*)(lds + ta + 320 + mt * 64); }
        }
    };

    u32x4 hr[4];
    u32x2 flg;
    {
        const int li = lane & 15, lq = lane >> 4, tq = li >> 2, tp = lane & 3;
        const u32 xa0 = (u32)(li * 128 + ((lq ^ (li >> 1)) << 4));
        const u32 wa = (u32)((8 * lq + tq) * 128 + ((hd ^ (((tq >> 1) & 1) | ((lq & 1) << 1))) << 5) + tp * 8);
        u32 xa, wb, kw[11], vw[8];
        u32x4 s0v, skw;
        auto pf_addr = [&](int st) {
            const u32 sb = (u32)((st >> 1) & 3) * L_SLOT;
            xa = sb + (xa0 ^ ((st & 1) * 64)); wb = sb + 10240 + (st & 1) * 4096 + wa;
            asm volatile("" : "+v"(xa), "+v"(wb));
        };
        auto pf_part = [&](int st, int mt) {
            if (mt == 0) Wm[st] = cat8(trr(wb + (1 + R) * 8192), trr(wb + (1 + R) * 8192 + 512));
            Ab[st & 1][mt] = ldsA(xa + mt * 2048);
            if (R == 0 && mt == 1) Wq[st] = cat8(trr(wb), trr(wb + 512));
        };
        STAMP(15);
        WAIT_VM(R ? 5 : 4);
        BAR();
        STAMP(16);
        pf_addr(0);
#pragma unroll
        for (int mt = 0; mt < 5; ++mt) pf_part(0, mt);
        sfor<0, 16>([&](auto stc) {
            constexpr int st = decltype(stc)::value;
            if constexpr ((st & 1) && st < 15) {
                constexpr int s1 = (st >> 1) + 1;
                if constexpr (R == 0) {
                    if constexpr (s1 == 1) WAIT_VM(7);
                    if constexpr (s1 == 2) WAIT_VM(1);
                    if constexpr (s1 == 5) WAIT_VM(0);
                } else {
                    constexpr int WN[8] = {0, 9, 9, 9, 9, 9, 10, 10};
                    WAIT_VM(WN[s1]);
                    if constexpr (s1 == 7) {
                        pin(s0v);
                        const int l2 = opq(tid) & 63, tr = hd * 20 + min(l2, 19), row = T0 - 16 + tr;
                        _Pragma("unroll 1") for (int spin = 0; spin < SPIN_CAP; ++spin) {
                            if (__builtin_expect(__all(row < 0 || (s0v[1] == tl && s0v[3] == th)), 1)) break;
                            __builtin_amdgcn_s_sleep(2);
                            ald4s(s0v, WS_ST0 + (u32)(max(row, 0) * 16));
                            WAIT_VM(0);
                            pin(s0v);
                        }
                        *(LAS u32*)(lds + L_TAB + tr * 4) = row < 0 ? 0u : s0v[0];
                        *(LAS u32*)(lds + L_TAB + 320 + tr * 4) = row < 0 ? 0x3F800000u : s0v[2];
                        asm volatile("s_waitcnt lgkmcnt(0)" ::: "memory");
                    }
                }
                RBAR();
                STAMP(16 + s1);
                if constexpr (s1 == 5 && R == 0) { if (tid == 0) __builtin_amdgcn_raw_buffer_store_b64((u32x2){tl, th}, r_ws, WS_F1 + (mem * 32 + grp) * 128, 0, 16); CFENCE(); }
                if constexpr (s1 == 5 && R == 1) { const int row = T0 - 16 + hd * 20 + min(opq(tid) & 63, 19); ald4s(s0v, WS_ST0 + (u32)(max(row, 0) * 16)); }
            }
            if (st < 15) pf_addr(st + 1);
            if constexpr (st == 14) load_tab(false);
            SB();
            qkv_step(IC<0>{}, st, Ab[st & 1], [&](auto mc) {
                constexpr int mt = decltype(mc)::value;
                if (st < 15) pf_part(st + 1, mt);
                if constexpr (R == 1) {
                    if constexpr (st == 0) { issue_piece(IC<2>{}, IC<2 * mt>{}); if constexpr (mt < 4) issue_piece(IC<2>{}, IC<2 * mt + 1>{}); }
                    if constexpr (st >= 1 && st <= 10 && ((st - 1) & 1) * 5 + mt < 9) issue_piece(IC<((st - 1) >> 1) + 3>{}, IC<((st - 1) & 1) * 5 + mt>{});
                    if constexpr (st == 11 && mt == 0) { const u32 ho = (u32)(b * 8192 + opq(tid) * 16); ald4(hr[2], d_h, ho); ald4(hr[3], d_h, ho + 16); }
                    if constexpr (st == 13 && mt == 0) { if (wave == 7) { const int l2 = opq(tid) & 63; ald2s(flg, WS_F1 + (u32)((l2 < 32 ? (b >> 5) * 32 + l2 : (l2 & 7) * 32 + grp) * 128)); } }
                    if constexpr (st >= 11 && st <= 14 && mt < 4) issue_wo(IC<(st - 11) * 4 + mt>{});
                } else {
                    if constexpr (st == 0) {
                        if constexpr (mt == 0) { const u32 xo = (u32)(srow * 1024 + (opq(tid) & 63) * 16); ald4(xrow[0], d_x, xo); ald4(xrow[1], d_x, xo + 1024); }
                        issue_kv(mt);
                    }
                    if constexpr (st == 11 && mt < 2) { const u32 ho = (u32)(b * 8192 + mt * 4096 + opq(tid) * 16); ald4(hr[2 * mt], d_h, ho); ald4(hr[2 * mt + 1], d_h, ho + 16); }
                }
                SB();
            });
            if constexpr (R == 0 && st == 2) {
                WAIT_VM(5);
                pin(xrow[0]); pin(xrow[1]);
                float s[2] = {0.f, 0.f}, q[2] = {0.f, 0.f};
#pragma unroll
                for (int i = 0; i < 4; ++i)
#pragma unroll
                    for (int k = 0; k < 2; ++k) { s[k] = dot2(xrow[k][i], 0x3F803F80u, s[k]); q[k] = dot2(xrow[k][i], xrow[k][i], q[k]); }
                auto ss = __builtin_amdgcn_permlane32_swap(__float_as_uint(s[0]), __float_as_uint(s[1]), false, false);
                auto qq = __builtin_amdgcn_permlane32_swap(__float_as_uint(q[0]), __float_as_uint(q[1]), false, false);
                float sv1 = rowsum16(__uint_as_float(ss[0]) + __uint_as_float(ss[1])), qv1 = rowsum16(__uint_as_float(qq[0]) + __uint_as_float(qq[1]));
                auto s2 = __builtin_amdgcn_permlane16_swap(__float_as_uint(sv1), __float_as_uint(sv1), false, false);
                auto q2 = __builtin_amdgcn_permlane16_swap(__float_as_uint(qv1), __float_as_uint(qv1), false, false);
                const float m = (__uint_as_float(s2[0]) + __uint_as_float(s2[1])) * (1.f / 512.f);
                const float r = __builtin_amdgcn_rsqf(fmaxf((__uint_as_float(q2[0]) + __uint_as_float(q2[1])) * (1.f / 512.f) - m * m, 0.f) + 1e-5f);
                const u32x4 sv = {__float_as_uint(m), tl, __float_as_uint(r), th};
                const int l2 = opq(tid) & 63;
                __builtin_amdgcn_raw_buffer_store_b128(sv, r_ws, (l2 & 31) == 0 ? WS_ST0 + (u32)((srow + (l2 >> 5)) * 16) : 0xFFFFFF00u, 0, 16);
                CFENCE();
                SB();
            }
            if constexpr (R == 0 && (st == 4 || st == 6)) {
                const int l2 = opq(tid) & 63, tg = l2 & 7, cp = 8 * hd + (l2 >> 3);
                if constexpr (st == 4) {
                    skw = *(LAS u32x4*)(lds + L_SK + cp * 16);
                    u32 ka = L_K + tg * 1024 + ((((cp >> 2) ^ tg) & 7) << 4) + (cp & 3) * 4;
                    u32 kb = L_K + tg * 1024 + ((((cp >> 2) ^ (tg + 1)) & 7) << 4) + (cp & 3) * 4;
                    pin(ka); pin(kb);
#pragma unroll
                    for (int i = 0; i < 11; ++i) kw[i] = *(LAS u32*)(lds + ((i + 5 < 8) ? ka : kb) + (i + 5) * 128);
#pragma unroll
                    for (int i = 0; i < 8; ++i) vw[i] = *(LAS u32*)(lds + ka + (L_V - L_K) + i * 128);
                    SB();
                }
                constexpr bool HI = st == 6;
                auto ex = [&](u32 w) { return HI ? bfhi(w) : bflo(w); };
                const float a0 = bflo(skw[2 * HI]), a1 = bfhi(skw[2 * HI]), a2 = bflo(skw[2 * HI + 1]), a3 = bfhi(skw[2 * HI + 1]);
                float ua[8];
#pragma unroll
                for (int i = 0; i < 8; ++i) ua[i] = (ex(kw[i]) * a3 + ex(kw[i + 1]) * a2 + ex(kw[i + 2]) * a1 + ex(kw[i + 3]) * a0) * ex(vw[i]);
                const u32x4 oa = {pk2(ua[7], ua[6]), pk2(ua[5], ua[4]), pk2(ua[3], ua[2]), pk2(ua[1], ua[0])};
                __builtin_amdgcn_raw_buffer_store_b128(oa, r_ws, WS_UT + (u32)((mem * 64 + 2 * cp) * 4096 + (2040 - T0 - 8 * tg) * 2), HI * 4096, 16);
                __builtin_amdgcn_raw_buffer_store_b128((u32x4){SENT, SENT, SENT, SENT}, r_ws, (u32)((4 * grp + hd) * 16384 + mem * 2048 + HI * 1024 + l2 * 16), WS_O1, 16);
                CFENCE();
                SB();
            }
        });
    }
    STAMP(R ? 1 : 53);

    u32x4 uu;
    float csV;
    auto attention = [&](auto lc) {
        constexpr int LAYER = decltype(lc)::value;
        constexpr int SBASE = LAYER ? 32 : 28;
        const int lane = opq(tid) & 63, li = lane & 15, lq = lane >> 4;
        u32 ex = L_EX + hd * 2048 + lane * 8;
        pin(ex);
        u32 f1ok = 1;
        if constexpr (R == 0) {
            if constexpr (LAYER == 0) {
#pragma unroll
                for (int i = 0; i < 4; ++i) { csK[i] = bcast_row0(accM[0][i]); csQ[i] = bcast_row0(accQ[0][i]); }
                if (li == 0) { *(LAS f32x4*)(lds + L_CS + hd * 128 + lq * 16) = csK; *(LAS f32x4*)(lds + L_CS + hd * 128 + 64 + lq * 16) = csQ; }
            }
            const int par = li & 1;
            const bool cur0 = (4 * lq + par) <= li, cur1 = (4 * lq + par + 2) <= li;
            s16x4 Kb[5], Qb[5];
#pragma unroll
            for (int mt = 0; mt < 5; ++mt) {
                Kb[mt] = pack4((accM[mt] - m1[mt] * csK) * r1[mt]);
                if (mt) Qb[mt] = pack4(accQ[mt] - m1[mt] * csQ);
            }
            f32x4 Sc[5], Sp[5];
#pragma unroll
            for (int mt = 1; mt < 5; ++mt) { Sc[mt] = mfma16(Kb[mt], Qb[mt], zero4); Sp[mt] = mfma16(Kb[mt - 1], Qb[mt], zero4); }
            float s0[5], s1[5], mx[5];
#pragma unroll
            for (int mt = 1; mt < 5; ++mt) {
                const float sp0 = Sp[mt][0], sp1 = Sp[mt][1], sp2 = Sp[mt][2], sp3 = Sp[mt][3], c0 = Sc[mt][0], c1 = Sc[mt][1], c2 = Sc[mt][2], c3 = Sc[mt][3];
                float p0 = par ? sp1 : sp0, p1 = par ? sp3 : sp2, q0 = par ? c1 : c0, q1 = par ? c3 : c2;
                asm volatile("" : "+v"(p0), "+v"(p1), "+v"(q0), "+v"(q1));
                if (mt == 1 && grp == 0) { p0 = -3.0e38f; p1 = -3.0e38f; }
                s0[mt] = cur0 ? q0 : p0; s1[mt] = cur1 ? q1 : p1;
                mx[mt] = vmax(s0[mt], s1[mt]);
            }
#pragma unroll
            for (int mt = 1; mt < 5; ++mt) { auto r = __builtin_amdgcn_permlane32_swap(__float_as_uint(mx[mt]), __float_as_uint(mx[mt]), false, false); mx[mt] = vmax(__uint_as_float(r[0]), __uint_as_float(r[1])); }
#pragma unroll
            for (int mt = 1; mt < 5; ++mt) { auto r = __builtin_amdgcn_permlane16_swap(__float_as_uint(mx[mt]), __float_as_uint(mx[mt]), false, false); mx[mt] = vmax(__uint_as_float(r[0]), __uint_as_float(r[1])); }
#pragma unroll
            for (int mt = 1; mt < 5; ++mt) {
                const float sc = r1[mt] * (0.25f * 1.44269504088896341f);
                const float e0 = __builtin_amdgcn_exp2f((s0[mt] - mx[mt]) * sc), e1 = __builtin_amdgcn_exp2f((s1[mt] - mx[mt]) * sc);
                *(LAS u32x2*)(lds + ex + (mt - 1) * 512) = (u32x2){pk2(e0, e0), pk2(e1, e1)};
            }
            STAMP(SBASE + 2);
            BAR();
            STAMP(SBASE + 1);
            if constexpr (LAYER == 0) f1ok = __builtin_amdgcn_readfirstlane(*(LAS u32*)(lds + L_FLG));
        } else {
            if constexpr (LAYER == 0) {
                auto s = __builtin_amdgcn_permlane16_swap(__float_as_uint(accM[0][0]), __float_as_uint(accM[0][0]), false, false);
                auto t = __builtin_amdgcn_permlane32_swap(s[0], s[0], false, false);
                csV = __uint_as_float(t[0]);
                if (wave == 7) { WAIT_VM(8); pin(flg); const u32 ok = flag_ok(flg); *(LAS u32*)(lds + L_FLG) = ok; }
            }
            const int par = li & 1;
            const bool cur0 = (4 * lq + par) <= li, cur1 = (4 * lq + par + 2) <= li;
            const u32 pm = par ? 0xffff0000u : 0x0000ffffu;
            u32 mC0 = cur0 ? pm : 0u, mP0 = cur0 ? 0u : pm, mC1 = cur1 ? pm : 0u, mP1 = cur1 ? 0u : pm;
            u32 oo = (LAYER ? WS_O2 : WS_O1) + (u32)((4 * grp + (lq & 1)) * 16384 + (4 * mem + hd) * 512 + (lq >> 1) * 256 + li * 16);
            asm volatile("" : "+v"(mC0), "+v"(mP0), "+v"(mC1), "+v"(mP1), "+v"(oo));
            s16x4 Vb[5];
#pragma unroll
            for (int mt = 0; mt < 5; ++mt) { Vb[mt] = pack4((accM[mt] - m4[mt] * csV) * r4[mt]); pin(Vb[mt]); }
            STAMP(SBASE);
            BAR();
            STAMP(SBASE + 1);
            u32x2 pw[4];
#pragma unroll
            for (int j = 0; j < 4; ++j) pw[j] = *(LAS u32x2*)(lds + ex + j * 512);
            if constexpr (LAYER == 0) {
                f1ok = __builtin_amdgcn_readfirstlane(*(LAS u32*)(lds + L_FLG));
                if (f1ok) ald4s(uu, WS_UT + (u32)(b * 8192 + opq(tid) * 16));
            }
            SB();
            f32x4 O[4], L[4];
#pragma unroll
            for (int j = 0; j < 4; ++j) {
                const s16x8 P = __builtin_bit_cast(s16x8, (u32x4){pw[j][0] & mC0, pw[j][1] & mC1, pw[j][0] & mP0, pw[j][1] & mP1});
                L[j] = mfma32(ones8, P, zero4);
                O[j] = mfma32(cat8(Vb[j + 1], Vb[j]), P, zero4);
            }
            u32 ow[4][2];
#pragma unroll
            for (int j = 0; j < 4; ++j) {
                const float inv = __builtin_amdgcn_rcpf(L[j][0]);
                ow[j][0] = nosent(pk2(O[j][0] * inv, O[j][1] * inv)); ow[j][1] = nosent(pk2(O[j][2] * inv, O[j][3] * inv));
            }
#pragma unroll
            for (int j = 0; j < 4; j += 2) {
                auto r0 = __builtin_amdgcn_permlane16_swap(ow[j][0], ow[j + 1][0], false, false);
                auto r1 = __builtin_amdgcn_permlane16_swap(ow[j][1], ow[j + 1][1], false, false);
                __builtin_amdgcn_raw_buffer_store_b128((u32x4){r0[0], r1[0], r0[1], r1[1]}, r_ws, oo, j * 16384, 16);
            }
            CFENCE();
        }
        if constexpr (LAYER == 0 && R == 0) { if (f1ok) ald4s(uu, WS_UT + (u32)(b * 8192 + opq(tid) * 16)); }
        SB();
        return f1ok;
    };
    const u32 f1ok = attention(IC<0>{});
    {
        const u32x4 sv = {SENT, SENT, SENT, SENT};
        const int t2 = opq(tid);
        const u32 tb = (u32)((4 * grp + hd) * 16384 + mem * 2048 + R * 1024 + (t2 & 63) * 16);
        __builtin_amdgcn_raw_buffer_store_b128(sv, r_ws, tb, WS_O2, 16);
        __builtin_amdgcn_raw_buffer_store_b128(sv, r_ws, tb, WS_X2, 16);
        __builtin_amdgcn_raw_buffer_store_b128(sv, r_ws, (u32)(b * 8192 + t2 * 16), WS_Y2, 16);
        CFENCE();
        SB();
    }
    STAMP(2);

    const int TI = 4 * grp + hd;
    u32x4 of[8];
    auto load_o = [&](u32 ws_o) {
        const int lane = opq(tid) & 63;
#pragma unroll
        for (int i = 0; i < 8; ++i) of[i] = __builtin_amdgcn_raw_buffer_load_b128(r_ws, ws_o + (u32)(TI * 16384 + R * 8192 + lane * 16) + (i & 3) * 1024, (i >> 2) * 4096, 16);
    };
    u32 xw[8], yw[8];
    u32 woa[4];
    auto resid = [&](int n) { return R ? zero4 : (f32x4){bflo(xw[2 * n]), bfhi(xw[2 * n]), bflo(xw[2 * n + 1]), bfhi(xw[2 * n + 1])}; };
    s16x8 F[8][4];
    auto wo_addr = [&]() {
        const int l2 = opq(tid) & 63, lq = l2 >> 4, tq = (l2 & 15) >> 2, tp = l2 & 3;
#pragma unroll
        for (int n = 0; n < 4; ++n) woa[n] = L_WO + (u32)(R * 32768 + (8 * lq + tq) * 128 + ((n ^ (((lq & 1) << 1) | (tq >> 1))) << 5) + tp * 8);
    };
    auto load_F = [&](int ks) {
#pragma unroll
        for (int n = 0; n < 4; ++n) F[ks][n] = cat8(trr(woa[n] + ks * 4096), trr(woa[n] + ks * 4096 + 512));
    };

    {
        const int t2 = opq(tid), lane = t2 & 63, li = lane & 15, lq = lane >> 4;
        constexpr u32 OFFS = 0xEDA85410u;
        {
            const int j = t2 & 255;
            {
                if constexpr (R == 0) { if (f1ok) WAIT_VM(4); else WAIT_VM(3); }
                else WAIT_VM(5);
                u32 cb = L_HC + R * 34816 + (j + 2) * 16;
                pin(cb);
#pragma unroll
                for (int ch = R; ch < 2; ++ch) {
                    pin(hr[2 * ch]); pin(hr[2 * ch + 1]);
                    const u32 d[8] = {hr[2 * ch][0], hr[2 * ch][1], hr[2 * ch][2], hr[2 * ch][3], hr[2 * ch + 1][0], hr[2 * ch + 1][1], hr[2 * ch + 1][2], hr[2 * ch + 1][3]};
                    u32 e[7];
#pragma unroll
                    for (int i = 0; i < 7; ++i) e[i] = __builtin_amdgcn_alignbit(d[i + 1], d[i], 16);
                    if (ch > R) { cb += 34816; pin(cb); }
#pragma unroll
                    for (int p = (R ? 4 : 0); p < ((ch && !R) ? 4 : 8); ++p) {
                        const u32 po = (272 * p + ((OFFS >> (4 * p)) & 15)) * 16;
                        const int h = p >> 1;
                        const u32x4 w = (p & 1) ? (u32x4){e[h], e[h + 1], e[h + 2], e[h + 3]} : (u32x4){d[h], d[h + 1], d[h + 2], d[h + 3]};
                        *(LAS u32x4*)(lds + cb + po) = w;
                    }
                }
            }
            if constexpr (R == 0) {
                if (hd == 0) {
                    u32 hh[4];
#pragma unroll
                    for (int i = 0; i < 4; ++i) { const u32 a = __builtin_amdgcn_readfirstlane(hr[0][i]), c = __builtin_amdgcn_readfirstlane(hr[2][i]); hh[i] = (lane & 16) ? c : a; }
                    if (lane < 32) {
                        const int p = lane & 7;
                        const u32 po = (272 * p + ((OFFS >> (4 * p)) & 15)) * 16;
                        const unsigned __int128 hv = ((unsigned __int128)(((u64)hh[3] << 32) | hh[2]) << 64) | (((u64)hh[1] << 32) | hh[0]);
                        const unsigned __int128 sv = (p == 0 || !(lane & 8)) ? (unsigned __int128)0 : (hv << (16 * (8 - p)));
                        const u32x4 w = {(u32)sv, (u32)(sv >> 32), (u32)(sv >> 64), (u32)(sv >> 96)};
                        *(LAS u32x4*)(lds + L_HC + (lane >> 4) * 34816 + po + ((lane >> 3) & 1) * 16) = w;
                    }
                }
            }
            if (hd == 1 && lane < 30) *(LAS u32x4*)(lds + L_UR + R * 4576 + 4096 + lane * 16) = (u32x4){0, 0, 0, 0};
            STAMP(3);
            if (!f1ok) {
                const u32 fo = WS_F1 + (u32)(((lane & 8) ? (lane & 7) * 32 + grp : (b >> 5) * 32 + 31 - hd * 8 - (lane & 7)) * 128);
                flg = (u32x2){0u, 0u};
                spin_flags(flg, fo);
                ald4s(uu, WS_UT + (u32)(b * 8192 + t2 * 16));
            }
            if (f1ok) WAIT_VM(R ? 5 : 3); else WAIT_VM(0);
            pin(uu);
            *(LAS u32x4*)(lds + L_UR + R * 4576 + j * 16) = uu;
        }
        BAR();
        STAMP(4);
        const int ch = hd >> 1, e0 = (hd & 1) * 4;
        const int p = (li + 1) & 7;
        u32 aA = L_HC + ch * 34816 + (272 * p + ((OFFS >> (4 * p)) & 15) + ((li + 1) >> 3) + lq + 4 * e0) * 16;
        u32 aB = L_UR + ch * 4576 + (254 - 224 - 2 * li + lq + 4 * e0) * 16;
        asm volatile("" : "+v"(aA), "+v"(aB));
        f32x4 y[4];
        u32 ooff = WS_O1 + (u32)(TI * 16384 + R * 8192 + lane * 16);
        {
            constexpr ConvSched S(R);
            s16x8 A[4][8], B[4][8];
            auto loads_at = [&](auto pc) {
                constexpr int p = decltype(pc)::value;
                sfor<0, 64>([&](auto ic) {
                    constexpr int i = decltype(ic)::value, e = i >> 4, f = i & 7;
                    if constexpr (S.slot[i] == p) {
                        if constexpr (i & 8) B[e][f] = ldsA(aB + 64 * e + 512 * (7 - f)); else A[e][f] = ldsA(aA + 64 * (8 * f + e));
                    }
                });
            };
            loads_at(IC<-1>{});
            SB();
            sfor<0, 72>([&](auto pc) {
                constexpr int p = decltype(pc)::value, e = p / 18, f = S.f[p % 18], i = S.i[p % 18], g = S.n[i] - f;
                y[i] = mfma32(A[e][f], B[e][g], (e == 0 && f == 0) ? zero4 : y[i]);
                loads_at(pc);
                if constexpr (p == 54) WAIT_VM(0);
                if constexpr (p >= 55 && p < 71 && (p & 1)) {
                    constexpr int i = (p - 55) >> 1;
                    ald4o<(i & 3) * 1024>(of[i], ooff + (i >> 2) * 4096, d_ws);
                }
                SB();
            });
        }
        STAMP(14);
        RBAR();
        if (R == 0 && tid == 0) __builtin_amdgcn_raw_buffer_store_b64((u32x2){tl, th}, r_ws, WS_F2 + (grp * 8 + mem) * 128, 0, 16);
        {
            constexpr int N0[4] = {0, 3, 4, 7};
            u32 ra = L_HC + hd * 8192 + lane * 16;
            asm volatile("" : "+v"(ra));
#pragma unroll
            for (int i = 0; i < 4; ++i) *(LAS f32x4*)(lds + ra + (N0[i] ^ R) * 1024) = y[i];
        }
        SB();
        if constexpr (R == 0) {
#pragma unroll
            for (int n = 0; n < 4; ++n) { const u32x2 w = __builtin_amdgcn_raw_buffer_load_b64(r_x, (u32)((TI * 16 + li) * 1024 + mem * 128 + lq * 8) + n * 32, 0, 0); xw[2 * n] = w[0]; xw[2 * n + 1] = w[1]; }
        }
        wo_addr();
        SB();
        STAMP(37);
        BAR();
        STAMP(38);
        {
            const float D0 = bflo(dw), D1 = bfhi(dw);
            u32 ra = L_HC + wave * 1024 + lane * 16;
            const int tb = 256 * wave + 16 * li + 4 * lq;
            u32 ru = L_UR + (2044 - tb) * 2;
            asm volatile("" : "+v"(ra), "+v"(ru));
            const f32x4 y0 = *(LAS f32x4*)(lds + ra), y1 = *(LAS f32x4*)(lds + ra + 8192), y2 = *(LAS f32x4*)(lds + ra + 16384), y3 = *(LAS f32x4*)(lds + ra + 24576);
            const u32x2 ua = *(LAS u32x2*)(lds + ru), ub = *(LAS u32x2*)(lds + ru + 4576);
            load_F(0); load_F(1);
            SB();
            const f32x4 ya = y0 + y1, yb = y2 + y3;
            const u32x4 w = {nosent(pk2(ya[0] + bfhi(ua[1]) * D0, yb[0] + bfhi(ub[1]) * D1)), nosent(pk2(ya[1] + bflo(ua[1]) * D0, yb[1] + bflo(ub[1]) * D1)),
                             nosent(pk2(ya[2] + bfhi(ua[0]) * D0, yb[2] + bfhi(ub[0]) * D1)), nosent(pk2(ya[3] + bflo(ua[0]) * D0, yb[3] + bflo(ub[0]) * D1))};
            __builtin_amdgcn_raw_buffer_store_b128(w, r_ws, WS_Y2 + (u32)(b * 8192 + tb * 4), 0, 16);
        }
        SB();
        STAMP(5);
        WAIT_VM(1);
        STAMP(40);

        u32 f2oth = 0;
        _Pragma("unroll 1") for (int attempt = 0; attempt < 64; ++attempt) {
            sfor<0, 8>([&](auto ic) { pin(of[decltype(ic)::value]); });
            u32 m0 = 0, m1 = 0;
#pragma unroll
            for (int ks = 0; ks < 8; ++ks) {
                load_F((ks + 2) & 7);
                if (R == 1 && ks == 4 && wave == 7) {
                    const int pb = 32 * mem + (lane & 31);
                    f2oth = WS_F2 + (u32)((lane < 32 ? ((pb & 7) * 4 + (pb >> 6)) * 8 + ((pb >> 3) & 7) : max(grp * 8 - 8, 0) + (lane & 15)) * 128);
                    ald2s(flg, f2oth);
                }
                SB();
#pragma unroll
                for (int n = 0; n < 4; ++n) accM[n] = mfma32(F[ks][n], __builtin_bit_cast(s16x8, of[ks]), ks ? accM[n] : resid(n));
                m0 = max(max(m0, of[ks][0]), of[ks][1]); m1 = max(max(m1, of[ks][2]), of[ks][3]);
                SB();
            }
            if (__builtin_expect(__all(max(m0, m1) != SENT), 1)) break;
            __builtin_amdgcn_s_sleep(4);
            WAIT_VM(0);
            sfor<0, 8>([&](auto ic) { constexpr int i = decltype(ic)::value; ald4o<(i & 3) * 1024>(of[i], ooff + (i >> 2) * 4096, d_ws); });
            WAIT_VM(0);
        }
        STAMP(41);
        if constexpr (R == 1) { if (wave == 7) { WAIT_VM(0); pin(flg); spin_flags(flg, f2oth); } }
    }

    auto load_y = [&]() {
        const int l2 = opq(tid) & 63;
#pragma unroll
        for (int i = 0; i < 8; ++i)
            yw[i] = __builtin_amdgcn_raw_buffer_load_b32(r_ws, WS_Y2 + (u32)((32 * mem + 2 * (l2 >> 4)) * 8192 + (TI * 16 + (l2 & 15)) * 4), (8 * (i >> 1) + (i & 1)) * 8192, 16);
    };
    auto pub_off = [&](u32 ws_st) {
        const int lane = opq(tid) & 63;
        return (lane & 16) ? 0xFFFFFF00u : ws_st + (u32)((TI * 8 + mem) * 256 + (lane & 15) * 16 + (lane >> 5) * 8);
    };
    auto pub_stat = [&](u32 off, u32 tg, float s, float q) {
        auto r = __builtin_amdgcn_permlane32_swap(__float_as_uint(s), __float_as_uint(q), false, false);
        const float v = __uint_as_float(r[0]) + __uint_as_float(r[1]);
        auto t = __builtin_amdgcn_permlane16_swap(__float_as_uint(v), __float_as_uint(v), false, false);
        __builtin_amdgcn_raw_buffer_store_b64((u32x2){__float_as_uint(__uint_as_float(t[0]) + __uint_as_float(t[1])), tg}, r_ws, off, 0, 16);
    };
    auto stat_off = [&](u32 ws_st, int ti) { const int l2 = opq(tid) & 63; return ws_st + (u32)(ti * 2048 + (l2 >> 4) * 512 + (l2 & 15) * 16); };
    auto fin_stat = [&](u32x4& a, u32x4& c, u32 off, float& mean, float& rstd) {
        _Pragma("unroll 1") for (int spin = 0; spin < SPIN_CAP; ++spin) {
            if (__builtin_expect(__all(((a[1] ^ tl) | (a[3] ^ th) | (c[1] ^ tl) | (c[3] ^ th)) == 0), 1)) break;
            __builtin_amdgcn_s_sleep(1);
            asm volatile("buffer_load_dwordx4 %0, %2, %3, 0 offen sc1\n\tbuffer_load_dwordx4 %1, %2, %3, 0 offen offset:256 sc1\n\ts_waitcnt vmcnt(0)"
                         : "=&v"(a), "=&v"(c) : "v"(off), "s"(d_ws) : "memory");
        }
        auto r = __builtin_amdgcn_permlane32_swap(__float_as_uint(__uint_as_float(a[0]) + __uint_as_float(c[0])), __float_as_uint(__uint_as_float(a[2]) + __uint_as_float(c[2])), false, false);
        const float v = __uint_as_float(r[0]) + __uint_as_float(r[1]);
        auto t = __builtin_amdgcn_permlane16_swap(__float_as_uint(v), __float_as_uint(v), false, false);
        const u32 v2 = __float_as_uint(__uint_as_float(t[0]) + __uint_as_float(t[1]));
        auto u = __builtin_amdgcn_permlane32_swap(v2, v2, false, false);
        const float ts = __uint_as_float(u[0]), tq = __uint_as_float(u[1]);
        mean = ts * (1.f / 512.f);
        rstd = __builtin_amdgcn_rsqf(fmaxf(tq * (1.f / 512.f) - mean * mean, 0.f) + 1e-5f);
    };
    auto regroup = [&](u32 (&w)[8]) {
#pragma unroll
        for (int j = 0; j < 2; ++j) {
            auto r0 = __builtin_amdgcn_permlane16_swap(w[4 * j], w[4 * j + 2], false, false);
            auto r1 = __builtin_amdgcn_permlane16_swap(w[4 * j + 1], w[4 * j + 3], false, false);
            w[4 * j] = r0[0]; w[4 * j + 1] = r1[0]; w[4 * j + 2] = r0[1]; w[4 * j + 3] = r1[1];
        }
    };
    auto wo_finish = [&](auto lc) {
        constexpr int LAYER = decltype(lc)::value;
        const int lane = opq(tid) & 63, li = lane & 15, lq = lane >> 4;
        u32 wx = L_WX + hd * 4096 + lane * 16;
        pin(wx);
        u32 po = 0, so = 0, tg = 0, oa = 0;
        if constexpr (R == 1) {
#pragma unroll
            for (int n = 0; n < 4; ++n) *(LAS f32x4*)(lds + wx + n * 1024) = accM[n];
        } else {
            po = pub_off(LAYER ? WS_ST3 : WS_ST1); so = stat_off(LAYER ? WS_ST3 : WS_ST1, TI); tg = lane < 32 ? tl : th;
            const int c = 2 * (lq & 1) + (lq >> 1);
            oa = LAYER ? (u32)((TI * 16 + li) * 1024 + mem * 128 + c * 16) : WS_X2 + (u32)((TI * 8 + mem) * 2048 + li * 128 + ((c ^ (li >> 1)) << 4));
            pin(po); pin(so); pin(tg); pin(oa);
        }
        BAR();
        STAMP(LAYER ? 25 : 42);
        if constexpr (R == 0) {
            f32x4 t[4];
#pragma unroll
            for (int n = 0; n < 4; ++n) t[n] = *(LAS f32x4*)(lds + wx + n * 1024);
            SB();
            float s = 0.f, q = 0.f, s2 = 0.f, q2 = 0.f;
#pragma unroll
            for (int n = 0; n < 4; ++n) {
                const f32x4 v = accM[n] + t[n];
                const u32 w0 = pk2(v[0], v[1]), w1 = pk2(v[2], v[3]);
                xw[2 * n] = w0; xw[2 * n + 1] = w1;
                s = dot2(w0, 0x3F803F80u, s); q = dot2(w0, w0, q); s2 = dot2(w1, 0x3F803F80u, s2); q2 = dot2(w1, w1, q2);
            }
            pub_stat(po, tg, s + s2, q + q2);
            CFENCE();
            if constexpr (!LAYER) load_y();
            regroup(xw);
            SB();
            STAMP(LAYER ? 46 : 43);
            WAIT_VM(0);
            STAMP(LAYER ? 26 : 44);
            if constexpr (!LAYER) {
#pragma unroll
                for (int i = 0; i < 8; ++i) pin(yw[i]);
            }
            u32x4 a, c;
            ald4s(a, so); ald4s(c, so + 256);
            if constexpr (!LAYER) {
                _Pragma("unroll 1") for (int spin = 0; spin < SPIN_CAP; ++spin) {
                    if (__builtin_expect(__all(max(max(max(yw[0], yw[1]), max(yw[2], yw[3])), max(max(yw[4], yw[5]), max(yw[6], yw[7]))) != SENT), 1)) break;
                    __builtin_amdgcn_s_sleep(1);
                    CFENCE();
                    load_y();
                }
                regroup(yw);
            }
            f32x2 xa[8], ya[8], xy[8];
#pragma unroll
            for (int i = 0; i < 8; ++i) {
                xa[i] = (f32x2){bflo(xw[i]), bfhi(xw[i])}; ya[i] = (f32x2){bflo(yw[i]), bfhi(yw[i])}; xy[i] = xa[i] * ya[i];
                pin(xa[i]); pin(ya[i]); pin(xy[i]);
            }
            WAIT_VM(0);
            pin(a); pin(c);
            float mean, rstd;
            fin_stat(a, c, so, mean, rstd);
            STAMP(LAYER ? 11 : 6);
            const float nm = -mean * rstd;
#pragma unroll
            for (int i = 0; i < 8; ++i) {
                const f32x2 o = xy[i] * rstd + (ya[i] * nm + xa[i]);
                xw[i] = pk2(o[0], o[1]);
                if (!LAYER) xw[i] = nosent(xw[i]);
            }
#pragma unroll
            for (int j = 0; j < 2; ++j) {
                const u32x4 o = {xw[4 * j], xw[4 * j + 1], xw[4 * j + 2], xw[4 * j + 3]};
                if constexpr (LAYER) __builtin_amdgcn_raw_buffer_store_b128(o, r_out, oa + j * 64, 0, OUT_AUX);
                else __builtin_amdgcn_raw_buffer_store_b128(o, r_ws, oa ^ (j * 64), 0, 16);
            }
            if constexpr (!LAYER) {
                float s = 0.f, q = 0.f, s2 = 0.f, q2 = 0.f;
#pragma unroll
                for (int i = 0; i < 8; i += 2) { s = dot2(xw[i], 0x3F803F80u, s); q = dot2(xw[i], xw[i], q); s2 = dot2(xw[i + 1], 0x3F803F80u, s2); q2 = dot2(xw[i + 1], xw[i + 1], q2); }
                pub_stat(po + (WS_ST2 - WS_ST1) * !(lane & 16), tg, s + s2, q + q2);
                CFENCE();
                regroup(xw);
#pragma unroll
                for (int i = 0; i < 8; ++i) { pin(xw[i]); pin(yw[i]); }
                STAMP(48);
                WAIT_VM(0);
            }
        }
    };
    wo_finish(IC<0>{});
    RBAR();
    STAMP(7);

    {
        const int l3 = opq(tid) & 63;
        const u32 g0 = (u32)((4 * grp - 1 + (hd >> 1)) * 16384 + (hd & 1) * 1024 + l3 * 16);
        const u32 g2 = (u32)((4 * grp + 3) * 16384 + hd * 512 + l3 * 16);
        auto x2_piece = [&](auto ic) {
            constexpr int i = decltype(ic)::value, s = i / 3, p = i % 3;
            const u32 kp = (u32)((s + rot) & 7);
            if constexpr (p == 0) dma(IC<4>{}, IC<L_CX + s * 10240>{}, d_x2, hb1k, g0, kp * 2048);
            else if constexpr (p == 1) dma(IC<4>{}, IC<L_CX + s * 10240 + 4096>{}, d_x2, hb1k, g0 + 32768, kp * 2048);
            else dma(IC<5>{}, IC<L_CX + s * 10240 + 8192>{}, d_x2, hb512, g2, kp * 2048);
        };
        u32 va = L_CX + hd * 1024 + l3 * 16, vq = L_CX + 8192 + hd * 512 + (l3 & 31) * 16 + (l3 >> 5) * 10240;
        pin(va); pin(vq);
        u32x4 vr[5];
        auto ver_read = [&](auto cc) {
            constexpr int c = decltype(cc)::value;
            u32 a = va + c * 20480, q = vq + c * 20480;
            pin(a); pin(q); CFENCE();
            vr[0] = *(LAS u32x4*)(lds + a); vr[1] = *(LAS u32x4*)(lds + a + 4096); vr[2] = *(LAS u32x4*)(lds + a + 10240); vr[3] = *(LAS u32x4*)(lds + a + 14336);
            vr[4] = *(LAS u32x4*)(lds + q);
        };
        auto ver_check = [&](auto cc) {
            constexpr int c = decltype(cc)::value;
            _Pragma("unroll 1") for (int spin = 0; spin < SPIN_CAP; ++spin) {
                u32 m0 = 0, m1 = 0;
#pragma unroll
                for (int i = 0; i < 5; ++i) { m0 = max(max(m0, vr[i][0]), vr[i][1]); m1 = max(max(m1, vr[i][2]), vr[i][3]); }
                if (__builtin_expect(__all(max(m0, m1) != SENT), 1)) break;
                __builtin_amdgcn_s_sleep(1);
                sfor<6 * c, 6 * c + 6>([&](auto ic) { x2_piece(ic); });
                WAIT_VM(0);
                ver_read(cc);
            }
        };
        const u32 xb0 = (u32)((l3 & 15) * 128 + (((l3 >> 4) ^ ((l3 & 15) >> 1)) << 4));
        if constexpr (R == 1) {
            STAMP(39);
            sfor<0, 12>([&](auto ic) { x2_piece(ic); });
            WAIT_VM(6);
            STAMP(8);
            ver_read(IC<0>{});
            ver_check(IC<0>{});
        } else {
            u32x4 sa, sc, ha, hc;
            const u32 so = stat_off(WS_ST2, 4 * grp + hd);
            const bool halo = hd == 0 && grp > 0;
            ald4s(sa, so); ald4s(sc, so + 256);
            if (halo) { ald4s(ha, so - 2048); ald4s(hc, so - 2048 + 256); }
            WAIT_VM(0);
            pin(sa); pin(sc);
            float mean, rstd;
            fin_stat(sa, sc, so, mean, rstd);
            if (l3 < 16) { *(LAS float*)(lds + L_TAB + 64 + hd * 64 + l3 * 4) = mean; *(LAS float*)(lds + L_TAB + 320 + 64 + hd * 64 + l3 * 4) = rstd; }
            if (halo) {
                pin(ha); pin(hc);
                fin_stat(ha, hc, so - 2048, mean, rstd);
                if (l3 < 16) { *(LAS float*)(lds + L_TAB + l3 * 4) = mean; *(LAS float*)(lds + L_TAB + 320 + l3 * 4) = rstd; }
            }
        }
        BAR();
        STAMP(13);
        u32 xa;
        auto pf_addr = [&](int st) { xa = L_CX + (st >> 1) * 10240 + (xb0 ^ ((st & 1) * 64)); pin(xa); };
        pf_addr(0);
#pragma unroll
        for (int mt = 0; mt < 5; ++mt) Ab[0][mt] = ldsA(xa + mt * 2048);
        sfor<0, 16>([&](auto stc) {
            constexpr int st = decltype(stc)::value;
            if constexpr (R == 1) {
                if constexpr (st == 2) { WAIT_VM(10); ver_read(IC<1>{}); }
                if constexpr (st == 3) ver_check(IC<1>{});
                if constexpr (st == 5) { WAIT_VM(6); ver_read(IC<2>{}); }
                if constexpr (st == 6) { ver_check(IC<2>{}); WAIT_VM(0); ver_read(IC<3>{}); }
                if constexpr (st == 7) ver_check(IC<3>{});
            }
            if constexpr (st == 3) { RBAR(); STAMP(49); }
            if constexpr (st == 7) { RBAR(); STAMP(50); CSTAMP(60); }
            if (st < 15) pf_addr(st + 1);
            if constexpr (st == 14) load_tab(true);
            SB();
            qkv_step(IC<1>{}, st, Ab[st & 1], [&](auto mc) {
                constexpr int mt = decltype(mc)::value;
                if (st < 15) Ab[(st + 1) & 1][mt] = ldsA(xa + mt * 2048);
                if constexpr (R == 1 && 12 + 5 * st + mt < 24) x2_piece(IC<12 + 5 * st + mt>{});
                SB();
            });
        });
    }
    STAMP(R ? 9 : 52);
    CSTAMP(61);
    attention(IC<1>{});
    STAMP(27);

    {
        wo_addr();
#pragma unroll
        for (int ks = 0; ks < 8; ++ks) load_F(ks);
        SB();
        WAIT_VM(0);
        RBAR();
        STAMP(10);
        load_o(WS_O2);
        SB();
        _Pragma("unroll 1") for (int spin = 0; spin < 64; ++spin) {
            u32 m0 = 0, m1 = 0;
#pragma unroll
            for (int ks = 0; ks < 8; ++ks) {
#pragma unroll
                for (int n = 0; n < 4; ++n) accM[n] = mfma32(F[ks][n], __builtin_bit_cast(s16x8, of[ks]), ks ? accM[n] : resid(n));
                m0 = max(max(m0, of[ks][0]), of[ks][1]); m1 = max(max(m1, of[ks][2]), of[ks][3]);
                SB();
            }
            if (__builtin_expect(__all(max(m0, m1) != SENT), 1)) break;
            _Pragma("unroll 1") for (int sp2 = 0; sp2 < SPIN_CAP; ++sp2) {
                CFENCE();
                __builtin_amdgcn_s_sleep(1);
                const u32 pv = __builtin_amdgcn_raw_buffer_load_b32(r_ws, WS_O2 + (u32)(TI * 16384 + R * 8192 + (opq(tid) & 15) * 512 + 508), 0, 16);
                if (__all(pv != SENT)) break;
            }
            CFENCE();
            load_o(WS_O2);
        }
        SB();
        STAMP(45);
    }
    wo_finish(IC<1>{});
    if constexpr (R == 1) {
        if (b == 0 && wave == 7) {
            const int l3 = opq(tid) & 63;
#pragma unroll
            for (int i = 0; i < 4; ++i) { flg = (u32x2){0u, 0u}; spin_flags(flg, WS_F1 + (u32)((i * 64 + l3) * 128)); }
            if (l3 == 0) __builtin_amdgcn_raw_buffer_store_b64(__builtin_bit_cast(u32x2, epoch + 1), r_ws, WS_EPOCH, 0, 16);
        }
    }
    if constexpr (R == 0) {
        WAIT_VM(0);
        STAMP(12);
        const u32 d = dg1 - dg0;
        u64 t0_; asm volatile("s_memrealtime %0\n\ts_waitcnt lgkmcnt(0)" : "=s"(t0_)::"memory");
        _Pragma("unroll 1") for (int i = 0; i < 100000; ++i) { u64 t_; asm volatile("s_memrealtime %0\n\ts_waitcnt lgkmcnt(0)" : "=s"(t_)::"memory"); if ((u32)t_ - (u32)t0_ >= d) break; }
    }
}

__global__ void __launch_bounds__(512, 2)
fused_kernel(const u16* __restrict__ gx, const u16* __restrict__ gwqkv, const u16* __restrict__ gwo,
             const u16* __restrict__ gbk, const u16* __restrict__ gbv, const u16* __restrict__ gsk,
             const u16* __restrict__ ghk, const u16* __restrict__ gdv, u16* __restrict__ gout, char* __restrict__ ws) {
    __shared__ __attribute__((aligned(16))) char lds_[L_TOTAL];
    typedef u32 u32x16 __attribute__((ext_vector_type(16)));
    u32x16 ka; u32x4 kb;
    {
        auto kp = __builtin_amdgcn_kernarg_segment_ptr();
        asm volatile("s_load_dwordx16 %0, %2, 0x0\n\ts_load_dwordx4 %1, %2, 0x40\n\ts_waitcnt lgkmcnt(0)" : "=&s"(ka), "=&s"(kb) : "s"(kp) : "memory");
    }
    auto mkp = [](u32 lo, u32 hi) { return (char*)(((u64)hi << 32) | lo); };
    const Args a = {(const u16*)mkp(ka[0], ka[1]), (const u16*)mkp(ka[2], ka[3]), (const u16*)mkp(ka[4], ka[5]), (const u16*)mkp(ka[6], ka[7]), (const u16*)mkp(ka[8], ka[9]),
                    (const u16*)mkp(ka[10], ka[11]), (const u16*)mkp(ka[12], ka[13]), (const u16*)mkp(ka[14], ka[15]), (u16*)mkp(kb[0], kb[1]), mkp(kb[2], kb[3])};
    const int wave = __builtin_amdgcn_readfirstlane(threadIdx.x >> 6);
    u64 pc0;
    asm volatile("s_getpc_b64 %0" : "=s"(pc0));
    if (wave < 4) body<0>(a, (LAS char*)lds_, wave, pc0); else body<1>(a, (LAS char*)lds_, wave, pc0);
}

extern "C" void kernel_launch(void* const* d_in, const int* in_sizes, int n_in,
                              void* d_out, int out_size, void* d_ws, size_t ws_size,
                              hipStream_t stream) {
    fused_kernel<<<256, 512, 0, stream>>>((const u16*)d_in[0], (const u16*)d_in[1], (const u16*)d_in[2],
                                          (const u16*)d_in[4], (const u16*)d_in[5], (const u16*)d_in[6],
                                          (const u16*)d_in[7], (const u16*)d_in[8], (u16*)d_out, (char*)d_ws);
}
```

```cpp
#define ROT 2
#define PRO 2
#define NO_PROBE
#include <hip/hip_runtime.h>
#include <stdint.h>

typedef unsigned short u16;
typedef unsigned u32;
typedef unsigned long long u64;
typedef short s16x4 __attribute__((ext_vector_type(4)));
typedef short s16x8 __attribute__((ext_vector_type(8)));
typedef __bf16 bf16x2 __attribute__((ext_vector_type(2)));
typedef float f32x2 __attribute__((ext_vector_type(2)));
typedef float f32x4 __attribute__((ext_vector_type(4)));
typedef u32 u32x2 __attribute__((ext_vector_type(2)));
typedef u32 u32x4 __attribute__((ext_vector_type(4)));
#define LAS __attribute__((address_space(3)))
#define DEV __device__ __forceinline__
typedef __amdgpu_buffer_rsrc_t rsrc_t;

constexpr u32 WS_EPOCH = 0;
constexpr u32 WS_F1 = 32768;
constexpr u32 WS_F2 = 65536;
constexpr u32 WS_ST0 = 98304;
constexpr u32 WS_ST1 = 131072;
constexpr u32 WS_ST2 = WS_ST1 + 262144;
constexpr u32 WS_ST3 = WS_ST2 + 262144;
constexpr u32 WS_UT = 1u << 20;
constexpr u32 WS_O1 = WS_UT + (2u << 20);
constexpr u32 WS_O2 = WS_O1 + (2u << 20);
constexpr u32 WS_Y2 = WS_O2 + (2u << 20);
constexpr u32 WS_X2 = WS_Y2 + (2u << 20);
constexpr u32 WS_END = WS_X2 + (2u << 20);

constexpr u32 L_SLOT = 34816;
constexpr u32 L_KV = 4 * L_SLOT;
constexpr u32 L_K = L_KV, L_V = L_KV + 9216;
constexpr u32 L_SK = L_KV + 17408;
constexpr u32 L_WO = 0;
constexpr u32 L_CX = 65536;
constexpr u32 L_HC = L_CX;
constexpr u32 L_UR = L_CX + 69632;
constexpr u32 L_WX = L_CX + 40960;
constexpr u32 L_EX = 147456;
constexpr u32 L_TAB = L_EX + 12288;
constexpr u32 L_CS = L_TAB + 1024;
constexpr u32 L_FLG = L_TAB + 2048;
constexpr u32 L_DUMMY = L_FLG + 1024;
constexpr u32 L_TOTAL = 160 * 1024;
static_assert(L_DUMMY + 1024 <= L_TOTAL, "LDS map");

#define WO_POS 0
#define OUT_AUX 0
constexpr int SPIN_CAP = 1 << 14;
constexpr u32 SENT = 0xFFFFFFFFu;

#define WAIT_VM(n) asm volatile("s_waitcnt vmcnt(%0)" ::"i"(n) : "memory")
#define BAR() do { asm volatile("s_waitcnt lgkmcnt(0)" ::: "memory"); __builtin_amdgcn_s_barrier(); asm volatile("" ::: "memory"); } while (0)
#define RBAR() do { asm volatile("" ::: "memory"); __builtin_amdgcn_s_barrier(); asm volatile("" ::: "memory"); } while (0)
#define CFENCE() asm volatile("" ::: "memory")
#define SB() __builtin_amdgcn_sched_barrier(0)
template <int N> struct IC { static constexpr int value = N; };
template <int I, int N, class F> DEV void sfor(F&& f) { if constexpr (I < N) { f(IC<I>{}); sfor<I + 1, N>(f); } }

DEV f32x4 mfma32(s16x8 a, s16x8 b, f32x4 c) { return __builtin_amdgcn_mfma_f32_16x16x32_bf16(a, b, c, 0, 0, 0); }
DEV f32x4 mfma16(s16x4 a, s16x4 b, f32x4 c) { return __builtin_amdgcn_mfma_f32_16x16x16bf16_1k(a, b, c, 0, 0, 0); }
DEV float vmax(float a, float b) { float r; asm("v_max_f32 %0, %1, %2" : "=v"(r) : "v"(a), "v"(b)); return r; }
DEV u32 pk2(float lo, float hi) { f32x2 f = {lo, hi}; bf16x2 b = __builtin_convertvector(f, bf16x2); return __builtin_bit_cast(u32, b); }
DEV float bflo(u32 w) { return __uint_as_float(w << 16); }
DEV float bfhi(u32 w) { return __uint_as_float(w & 0xffff0000u); }
DEV float dot2(u32 a, u32 b, float c) { return __builtin_amdgcn_fdot2_f32_bf16(__builtin_bit_cast(bf16x2, a), __builtin_bit_cast(bf16x2, b), c, false); }
DEV u32 nosent(u32 w) { return min(w, 0xFFFFFFFEu); }
DEV float xsum(float v) {
    auto r = __builtin_amdgcn_permlane32_swap(__float_as_uint(v), __float_as_uint(v), false, false);
    v = __uint_as_float(r[0]) + __uint_as_float(r[1]);
    auto s = __builtin_amdgcn_permlane16_swap(__float_as_uint(v), __float_as_uint(v), false, false);
    return __uint_as_float(s[0]) + __uint_as_float(s[1]);
}
DEV float xmax(float v) {
    auto r = __builtin_amdgcn_permlane32_swap(__float_as_uint(v), __float_as_uint(v), false, false);
    v = vmax(__uint_as_float(r[0]), __uint_as_float(r[1]));
    auto s = __builtin_amdgcn_permlane16_swap(__float_as_uint(v), __float_as_uint(v), false, false);
    return vmax(__uint_as_float(s[0]), __uint_as_float(s[1]));
}
DEV float rowsum16(float v) {
    v += __builtin_bit_cast(float, __builtin_amdgcn_update_dpp(0, __builtin_bit_cast(int, v), 0xB1, 0xf, 0xf, true));
    v += __builtin_bit_cast(float, __builtin_amdgcn_update_dpp(0, __builtin_bit_cast(int, v), 0x4E, 0xf, 0xf, true));
    v += __builtin_bit_cast(float, __builtin_amdgcn_update_dpp(0, __builtin_bit_cast(int, v), 0x141, 0xf, 0xf, true));
    v += __builtin_bit_cast(float, __builtin_amdgcn_update_dpp(0, __builtin_bit_cast(int, v), 0x140, 0xf, 0xf, true));
    return v;
}
DEV float bcast_row0(float v) { return __builtin_bit_cast(float, __builtin_amdgcn_update_dpp(0, __builtin_bit_cast(int, v), 0x150, 0xf, 0xf, true)); }
template <int OFF> DEV void ald4o(u32x4& d, u32 voff, u32x4 rs) { asm volatile("buffer_load_dwordx4 %0, %1, %2, 0 offen offset:%3 sc1" : "=v"(d) : "v"(voff), "s"(rs), "i"(OFF) : "memory"); }
template <class T> DEV void pin(T& v) { asm volatile("" : "+v"(v)); }
DEV int opq(int v) { asm volatile("" : "+v"(v)); return v; }
DEV s16x8 cat8(s16x4 a, s16x4 b) { return __builtin_shufflevector(a, b, 0, 1, 2, 3, 4, 5, 6, 7); }
DEV s16x4 pack4(f32x4 v) { u32x2 w = {pk2(v[0], v[1]), pk2(v[2], v[3])}; return __builtin_bit_cast(s16x4, w); }
DEV u64 mix64(u64 z) { z += 0x9E3779B97F4A7C15ull; z = (z ^ (z >> 30)) * 0xBF58476D1CE4E5B9ull; z = (z ^ (z >> 27)) * 0x94D049BB133111EBull; return z ^ (z >> 31); }

struct ConvSched {
    int n[4], f[18], i[18], slot[64];
    constexpr ConvSched(int R) : n{}, f{}, i{}, slot{} {
        constexpr int LEAD = 9;
        const int N0[4] = {0, 3, 4, 7};
        for (int k = 0; k < 4; ++k) n[k] = N0[k] ^ R;
        int c = 0;
        for (int ff = 0; ff < 8; ++ff) for (int k = 3; k >= 0; --k) if (ff <= n[k]) { f[c] = ff; i[c] = k; ++c; }
        for (int k = 0; k < 64; ++k) slot[k] = 1000;
        for (int p = 71; p >= 0; --p) {
            const int e = p / 18, ff = f[p % 18], g = n[i[p % 18]] - ff, s = p - LEAD < -1 ? -1 : p - LEAD;
            slot[e * 16 + ff] = s; slot[e * 16 + 8 + g] = s;
        }
    }
};

#define STAMP(i) do { } while (0)
#define CSTAMP(i) do { } while (0)

struct Args { const u16 *gx, *gwqkv, *gwo, *gbk, *gbv, *gsk, *ghk, *gdv; u16* gout; char* ws; };

template <int R>
DEV void body(const Args& A_, LAS char* const lds, const int wave) {
    const u16* gx = A_.gx; char* ws = A_.ws;
    STAMP(0);
    const int b = blockIdx.x, tid = threadIdx.x, lane = tid & 63;
    const int hd = wave & 3;
    const int grp = (b & 7) * 4 + (b >> 6), mem = (b >> 3) & 7;
    const int T0 = grp * 64;
    const int rot = (mem + ROT * (grp & 3)) & 7;

    u64 epoch;
    asm volatile("s_load_dwordx2 %0, %1, 0x0" : "=s"(epoch) : "s"(ws));
    u32 dw;
    asm volatile("s_load_dword %0, %1, 0x0" : "=s"(dw) : "s"(A_.gdv + 2 * b));
    const rsrc_t r_x = __builtin_amdgcn_make_buffer_rsrc((void*)gx, 0, 2048 * 512 * 2, 0x00020000);
    const rsrc_t r_ws = __builtin_amdgcn_make_buffer_rsrc((void*)ws, 0, WS_END, 0x00020000);
    const rsrc_t r_out = __builtin_amdgcn_make_buffer_rsrc((void*)A_.gout, 0, 2048 * 512 * 2, 0x00020000);
    const u32 ldsbase = (u32)(size_t)lds;
    auto mkdesc = [](const void* p, u32 bytes) { const u64 a = (u64)p; return (u32x4){(u32)a, (u32)(a >> 32) & 0xffffu, bytes, 0x00020000u}; };
    const u32x4 d_x = mkdesc(gx, 2048 * 512 * 2), d_w = mkdesc(A_.gwqkv, 512 * 1536 * 2), d_wo = mkdesc(A_.gwo, 512 * 512 * 2);
    const u32x4 d_k = mkdesc(A_.gbk, 2048 * 512 * 2), d_v = mkdesc(A_.gbv, 2048 * 512 * 2), d_sk = mkdesc(A_.gsk, 512 * 4 * 2);
    const u32x4 d_h = mkdesc(A_.ghk, 512 * 2048 * 2), d_x2 = mkdesc(ws + WS_X2, 2u << 20), d_ws = mkdesc(ws, WS_END);
    const u32 wb1k = ldsbase + wave * 1024, hb1k = ldsbase + hd * 1024, hb512 = ldsbase + hd * 512;
#define DMA_ASM(pre, mods, post) asm volatile("s_add_u32 m0, %0, %4\n\t" pre "buffer_load_dwordx4 %1, %2, %3 offen " mods "lds" post \
                     :: "s"(sbase), "v"(voff), "s"(rs), "s"(soff), "i"(IMM) : "memory", "scc")
    const u32 xmlo = hd == 0 ? ~0xFFu : ~0u;
    auto dma = [&](auto kind, auto imm, u32x4 rs, u32 sbase, u32 voff, u32 soff) {
        constexpr int K = decltype(kind)::value; constexpr int IMM = decltype(imm)::value;
        if constexpr (K == 0) DMA_ASM("s_nop 0\n\t", "", "");
        else if constexpr (K == 1) DMA_ASM("s_mov_b32 exec_hi, 0\n\t", "", "\n\ts_mov_b32 exec_hi, -1");
        else if constexpr (K == 2) {
            const u32 ml = xmlo;
            asm volatile("s_add_u32 m0, %0, %4\n\ts_mov_b32 exec_lo, %5\n\tbuffer_load_dwordx4 %1, %2, %3 offen lds\n\ts_mov_b32 exec_lo, -1"
                         :: "s"(sbase), "v"(voff), "s"(rs), "s"(soff), "i"(IMM), "s"(ml) : "memory", "scc");
        }
        else if constexpr (K == 4) DMA_ASM("s_nop 0\n\t", "sc1 ", "");
        else DMA_ASM("s_mov_b32 exec_hi, 0\n\t", "sc1 ", "\n\ts_mov_b32 exec_hi, -1");
    };
    auto ald4 = [&](u32x4& d, u32x4 rs, u32 voff) { asm volatile("buffer_load_dwordx4 %0, %1, %2, 0 offen" : "=v"(d) : "v"(voff), "s"(rs) : "memory"); };
    auto ald4s = [&](u32x4& d, u32 voff) { asm volatile("buffer_load_dwordx4 %0, %1, %2, 0 offen sc1" : "=v"(d) : "v"(voff), "s"(d_ws) : "memory"); };
    auto ald2s = [&](u32x2& d, u32 voff) { asm volatile("buffer_load_dwordx2 %0, %1, %2, 0 offen sc1" : "=v"(d) : "v"(voff), "s"(d_ws) : "memory"); };

    const int pr = lane >> 3, pc = lane & 7;
    const int xr0 = 8 * hd + pr, xr2 = 64 + 4 * hd + pr;
    const u32 xv0 = (u32)((T0 - 16 + xr0) * 1024 + ((pc ^ ((xr0 >> 1) & 7)) << 4));
    const u32 xv2 = (u32)((T0 - 16 + xr2) * 1024 + ((pc ^ ((xr2 >> 1) & 7)) << 4));
    const int wfw = ((xr0 >> 1) & 1) | (((xr0 >> 3) & 1) << 1);
    const u32 wv0 = (u32)(xr0 * 3072 + mem * 128 + ((((pc >> 1) ^ wfw) * 2 + (pc & 1)) << 4));
    auto issue_piece = [&](auto sc, auto ic) {
        constexpr int s = decltype(sc)::value, i = decltype(ic)::value;
        const u32 kp = (u32)((s + rot) & 7);
        constexpr int sb = (s & 3) * L_SLOT;
        if constexpr (i == 0) dma(IC<2>{}, IC<sb>{}, d_x, hb1k, xv0, kp * 128);
        else if constexpr (i == 1) dma(IC<0>{}, IC<sb + 4096>{}, d_x, hb1k, xv0 + 32768, kp * 128);
        else if constexpr (i == 2) dma(IC<1>{}, IC<sb + 8192>{}, d_x, hb512, xv2, kp * 128);
        else dma(IC<0>{}, IC<sb + 10240 + (i - 3) * 4096>{}, d_w, hb1k, wv0, kp * (64 * 3072) + ((i - 3) >> 1) * 1024 + ((i - 3) & 1) * (32 * 3072));
    };
    auto issue_wo = [&](auto ic) {
        constexpr int i = decltype(ic)::value;
        const int l2 = opq(tid) & 63, pr = l2 >> 3, pc = l2 & 7;
        const u32 wov = (u32)((8 * hd + pr) * 1024 + mem * 128 + ((((pc >> 1) ^ (((pr >> 1) & 1) | ((hd & 1) << 1))) * 2 + (pc & 1)) << 4));
        const u32 j = (u32)((i + 2 * grp) & 7);
        dma(IC<0>{}, IC<L_WO + (i & 8) * 4096>{}, d_wo, hb1k + j * 4096, wov, ((i & 8) + j) * 32768);
    };
    auto issue_kv = [&](int i) {
        const int l2 = opq(tid) & 63, pr = l2 >> 3, pc = l2 & 7, p = hd + 4 * (i & 1);
        const u32 cb = (u32)(mem * 128);
        if (i < 2) dma(IC<0>{}, IC<L_K>{}, d_k, hb1k + (i & 1) * 4096, (u32)((T0 - 8 + 8 * p + pr) * 1024) + cb + ((pc ^ p) << 4), 0);
        else if (i < 4) dma(IC<0>{}, IC<L_V>{}, d_v, hb1k + (i & 1) * 4096, (u32)((T0 + 8 * p + pr) * 1024) + cb + ((pc ^ p) << 4), 0);
        else if (hd == 0) dma(IC<0>{}, IC<L_K + 8192>{}, d_k, ldsbase, (u32)((T0 - 8 + 64 + pr) * 1024) + cb + (pc << 4), 0);
        else dma(IC<0>{}, IC<L_SK>{}, d_sk, ldsbase, (u32)(mem * 512 + l2 * 16), 0);
    };

    const int srow = T0 + 8 * mem + 2 * hd;
    u32x4 xrow[2];
    sfor<0, 9>([&](auto ic) { if constexpr ((decltype(ic)::value & 1) != R) issue_piece(IC<0>{}, ic); });
    sfor<0, 9>([&](auto ic) { if constexpr ((decltype(ic)::value & 1) != R) issue_piece(IC<1>{}, ic); });
    asm volatile("s_waitcnt lgkmcnt(0)" : "+s"(epoch), "+s"(dw));
    const u64 tag = mix64(epoch);
    const u32 tl = (u32)tag, th = (u32)(tag >> 32);
    if (R == 1 && wave == 7 && lane < 32) *(LAS u32x4*)(lds + (lane >> 3) * L_SLOT + (lane & 7) * 16) = (u32x4){0x3F803F80u, 0x3F803F80u, 0x3F803F80u, 0x3F803F80u};

    auto trr = [&](u32 off) { return __builtin_amdgcn_ds_read_tr16_b64_v4i16((LAS s16x4*)(lds + off)); };
    auto ldsA = [&](u32 off) { return *(LAS s16x8*)(lds + off); };
    auto flag_ok = [&](const u32x2& f) { return __all(f[0] == tl && f[1] == th); };
    auto spin_flags = [&](u32x2& f, u32 off) {
        _Pragma("unroll 1") for (int spin = 0; spin < SPIN_CAP; ++spin) {
            if (__builtin_expect(flag_ok(f), 1)) break;
            __builtin_amdgcn_s_sleep(2);
            asm volatile("buffer_load_dwordx2 %0, %1, %2, 0 offen sc1\n\ts_waitcnt vmcnt(0)" : "=&v"(f) : "v"(off), "s"(d_ws) : "memory");
        }
    };

    s16x8 Wq[16], Wm[16];
    f32x4 accM[5], accQ[5];
    const f32x4 zero4 = {0.f, 0.f, 0.f, 0.f};
    const s16x8 ones8 = {0x3F80, 0x3F80, 0x3F80, 0x3F80, 0x3F80, 0x3F80, 0x3F80, 0x3F80};
    s16x8 Ab[3][5];
    auto qkv_step = [&](auto lc, int st, const s16x8 (&A)[5], auto fill) {
        sfor<0, 5>([&](auto mc) {
            constexpr int mt = decltype(mc)::value;
            const f32x4 cm = st ? accM[mt] : zero4;
            accM[mt] = R ? mfma32(A[mt], Wm[st], cm) : mfma32(Wm[st], A[mt], cm);
            if constexpr (R == 0 && (mt > 0 || decltype(lc)::value == 0)) accQ[mt] = mfma32(Wq[st], A[mt], st ? accQ[mt] : zero4);
            fill(mc);
        });
    };

    float m1[5], r1[5]; f32x4 m4[5], r4[5], csK, csQ;
    auto load_tab = [&](bool cs) {
        const int l2 = opq(tid) & 63;
        u32 ta = L_TAB + (R ? (l2 >> 4) * 16 : (l2 & 15) * 4);
        pin(ta);
        if (R == 0 && cs) { csK = *(LAS f32x4*)(lds + L_CS + hd * 128 + (l2 >> 4) * 16); csQ = *(LAS f32x4*)(lds + L_CS + hd * 128 + 64 + (l2 >> 4) * 16); }
#pragma unroll
        for (int mt = 0; mt < 5; ++mt) {
            if (R == 0) { m1[mt] = *(LAS float*)(lds + ta + mt * 64); r1[mt] = *(LAS float*)(lds + ta + 320 + mt * 64); }
            else { m4[mt] = *(LAS f32x4*)(lds + ta + mt * 64); r4[mt] = *(LAS f32x4*)(lds + ta + 320 + mt * 64); }
        }
    };

    u32x4 hr[4];
    u32x2 flg;
    {
        const int li = lane & 15, lq = lane >> 4, tq = li >> 2, tp = lane & 3;
        const u32 xa0 = (u32)(li * 128 + ((lq ^ (li >> 1)) << 4));
        const u32 wa = (u32)((8 * lq + tq) * 128 + ((hd ^ (((tq >> 1) & 1) | ((lq & 1) << 1))) << 5) + tp * 8);
        u32 xa, wb, kw[11], vw[8];
        u32x4 s0v, skw;
        auto pf_addr = [&](int st) {
            const u32 sb = (u32)((st >> 1) & 3) * L_SLOT;
            xa = sb + (xa0 ^ ((st & 1) * 64)); wb = sb + 10240 + (st & 1) * 4096 + wa;
            asm volatile("" : "+v"(xa), "+v"(wb));
        };
        auto pf_part = [&](int st, int mt) {
            if (mt == 0) Wm[st] = cat8(trr(wb + (1 + R) * 8192), trr(wb + (1 + R) * 8192 + 512));
            Ab[st & 1][mt] = ldsA(xa + mt * 2048);
            if (R == 0 && mt == 1) Wq[st] = cat8(trr(wb), trr(wb + 512));
        };
        STAMP(15);
        WAIT_VM(R ? 5 : 4);
        BAR();
        STAMP(16);
        pf_addr(0);
#pragma unroll
        for (int mt = 0; mt < 5; ++mt) pf_part(0, mt);
        sfor<0, 16>([&](auto stc) {
            constexpr int st = decltype(stc)::value;
            if constexpr ((st & 1) && st < 15) {
                constexpr int s1 = (st >> 1) + 1;
                if constexpr (R == 0) {
                    if constexpr (s1 == 1) WAIT_VM(7);
                    if constexpr (s1 == 2) WAIT_VM(1);
                    if constexpr (s1 == 5) WAIT_VM(0);
                } else {
                    constexpr int WN[8] = {0, 9, 9, 9, 9, 9, 10, 10};
                    WAIT_VM(WN[s1]);
                    if constexpr (s1 == 7) {
                        pin(s0v);
                        const int l2 = opq(tid) & 63, tr = hd * 20 + min(l2, 19), row = T0 - 16 + tr;
                        _Pragma("unroll 1") for (int spin = 0; spin < SPIN_CAP; ++spin) {
                            if (__builtin_expect(__all(row < 0 || (s0v[1] == tl && s0v[3] == th)), 1)) break;
                            __builtin_amdgcn_s_sleep(2);
                            ald4s(s0v, WS_ST0 + (u32)(max(row, 0) * 16));
                            WAIT_VM(0);
                            pin(s0v);
                        }
                        *(LAS u32*)(lds + L_TAB + tr * 4) = row < 0 ? 0u : s0v[0];
                        *(LAS u32*)(lds + L_TAB + 320 + tr * 4) = row < 0 ? 0x3F800000u : s0v[2];
                        asm volatile("s_waitcnt lgkmcnt(0)" ::: "memory");
                    }
                }
                RBAR();
                STAMP(16 + s1);
                if constexpr (s1 == 5 && R == 0) { if (tid == 0) __builtin_amdgcn_raw_buffer_store_b64((u32x2){tl, th}, r_ws, WS_F1 + (mem * 32 + grp) * 128, 0, 16); CFENCE(); }
                if constexpr (s1 == 5 && R == 1) { const int row = T0 - 16 + hd * 20 + min(opq(tid) & 63, 19); ald4s(s0v, WS_ST0 + (u32)(max(row, 0) * 16)); }
            }
            if (st < 15) pf_addr(st + 1);
            if constexpr (st == 14) load_tab(false);
            SB();
            qkv_step(IC<0>{}, st, Ab[st & 1], [&](auto mc) {
                constexpr int mt = decltype(mc)::value;
                if (st < 15) pf_part(st + 1, mt);
                if constexpr (R == 1) {
                    if constexpr (st == 0) { issue_piece(IC<2>{}, IC<2 * mt>{}); if constexpr (mt < 4) issue_piece(IC<2>{}, IC<2 * mt + 1>{}); }
                    if constexpr (st >= 1 && st <= 10 && ((st - 1) & 1) * 5 + mt < 9) issue_piece(IC<((st - 1) >> 1) + 3>{}, IC<((st - 1) & 1) * 5 + mt>{});
                    if constexpr (st == 11 && mt == 0) { const u32 ho = (u32)(b * 8192 + opq(tid) * 16); ald4(hr[2], d_h, ho); ald4(hr[3], d_h, ho + 16); }
                    if constexpr (st == 13 && mt == 0) { if (wave == 7) { const int l2 = opq(tid) & 63; ald2s(flg, WS_F1 + (u32)((l2 < 32 ? (b >> 5) * 32 + l2 : (l2 & 7) * 32 + grp) * 128)); } }
                    if constexpr (st >= 11 && st <= 14 && mt < 4) issue_wo(IC<(st - 11) * 4 + mt>{});
                } else {
                    if constexpr (st == 0) {
                        if constexpr (mt == 0) { const u32 xo = (u32)(srow * 1024 + (opq(tid) & 63) * 16); ald4(xrow[0], d_x, xo); ald4(xrow[1], d_x, xo + 1024); }
                        issue_kv(mt);
                    }
                    if constexpr (st == 11 && mt < 2) { const u32 ho = (u32)(b * 8192 + mt * 4096 + opq(tid) * 16); ald4(hr[2 * mt], d_h, ho); ald4(hr[2 * mt + 1], d_h, ho + 16); }
                }
                SB();
            });
            if constexpr (R == 0 && st == 2) {
                WAIT_VM(5);
                pin(xrow[0]); pin(xrow[1]);
                float s[2] = {0.f, 0.f}, q[2] = {0.f, 0.f};
#pragma unroll
                for (int i = 0; i < 4; ++i)
#pragma unroll
                    for (int k = 0; k < 2; ++k) { s[k] = dot2(xrow[k][i], 0x3F803F80u, s[k]); q[k] = dot2(xrow[k][i], xrow[k][i], q[k]); }
                auto ss = __builtin_amdgcn_permlane32_swap(__float_as_uint(s[0]), __float_as_uint(s[1]), false, false);
                auto qq = __builtin_amdgcn_permlane32_swap(__float_as_uint(q[0]), __float_as_uint(q[1]), false, false);
                float sv1 = rowsum16(__uint_as_float(ss[0]) + __uint_as_float(ss[1])), qv1 = rowsum16(__uint_as_float(qq[0]) + __uint_as_float(qq[1]));
                auto s2 = __builtin_amdgcn_permlane16_swap(__float_as_uint(sv1), __float_as_uint(sv1), false, false);
                auto q2 = __builtin_amdgcn_permlane16_swap(__float_as_uint(qv1), __float_as_uint(qv1), false, false);
                const float m = (__uint_as_float(s2[0]) + __uint_as_float(s2[1])) * (1.f / 512.f);
                const float r = __builtin_amdgcn_rsqf(fmaxf((__uint_as_float(q2[0]) + __uint_as_float(q2[1])) * (1.f / 512.f) - m * m, 0.f) + 1e-5f);
                const u32x4 sv = {__float_as_uint(m), tl, __float_as_uint(r), th};
                const int l2 = opq(tid) & 63;
                __builtin_amdgcn_raw_buffer_store_b128(sv, r_ws, (l2 & 31) == 0 ? WS_ST0 + (u32)((srow + (l2 >> 5)) * 16) : 0xFFFFFF00u, 0, 16);
                CFENCE();
                SB();
            }
            if constexpr (R == 0 && (st == 4 || st == 6)) {
                const int l2 = opq(tid) & 63, tg = l2 & 7, cp = 8 * hd + (l2 >> 3);
                if constexpr (st == 4) {
                    skw = *(LAS u32x4*)(lds + L_SK + cp * 16);
                    u32 ka = L_K + tg * 1024 + ((((cp >> 2) ^ tg) & 7) << 4) + (cp & 3) * 4;
                    u32 kb = L_K + tg * 1024 + ((((cp >> 2) ^ (tg + 1)) & 7) << 4) + (cp & 3) * 4;
                    pin(ka); pin(kb);
#pragma unroll
                    for (int i = 0; i < 11; ++i) kw[i] = *(LAS u32*)(lds + ((i + 5 < 8) ? ka : kb) + (i + 5) * 128);
#pragma unroll
                    for (int i = 0; i < 8; ++i) vw[i] = *(LAS u32*)(lds + ka + (L_V - L_K) + i * 128);
                    SB();
                }
                constexpr bool HI = st == 6;
                auto ex = [&](u32 w) { return HI ? bfhi(w) : bflo(w); };
                const float a0 = bflo(skw[2 * HI]), a1 = bfhi(skw[2 * HI]), a2 = bflo(skw[2 * HI + 1]), a3 = bfhi(skw[2 * HI + 1]);
                float ua[8];
#pragma unroll
                for (int i = 0; i < 8; ++i) ua[i] = (ex(kw[i]) * a3 + ex(kw[i + 1]) * a2 + ex(kw[i + 2]) * a1 + ex(kw[i + 3]) * a0) * ex(vw[i]);
                const u32x4 oa = {pk2(ua[7], ua[6]), pk2(ua[5], ua[4]), pk2(ua[3], ua[2]), pk2(ua[1], ua[0])};
                __builtin_amdgcn_raw_buffer_store_b128(oa, r_ws, WS_UT + (u32)((mem * 64 + 2 * cp) * 4096 + (2040 - T0 - 8 * tg) * 2), HI * 4096, 16);
                __builtin_amdgcn_raw_buffer_store_b128((u32x4){SENT, SENT, SENT, SENT}, r_ws, (u32)((4 * grp + hd) * 16384 + mem * 2048 + HI * 1024 + l2 * 16), WS_O1, 16);
                CFENCE();
                SB();
            }
        });
    }
    STAMP(R ? 1 : 53);

    u32x4 uu;
    float csV;
    auto attention = [&](auto lc) {
        constexpr int LAYER = decltype(lc)::value;
        constexpr int SBASE = LAYER ? 32 : 28;
        const int lane = opq(tid) & 63, li = lane & 15, lq = lane >> 4;
        u32 ex = L_EX + hd * 2048 + lane * 8;
        pin(ex);
        u32 f1ok = 1;
        if constexpr (R == 0) {
            if constexpr (LAYER == 0) {
#pragma unroll
                for (int i = 0; i < 4; ++i) { csK[i] = bcast_row0(accM[0][i]); csQ[i] = bcast_row0(accQ[0][i]); }
                if (li == 0) { *(LAS f32x4*)(lds + L_CS + hd * 128 + lq * 16) = csK; *(LAS f32x4*)(lds + L_CS + hd * 128 + 64 + lq * 16) = csQ; }
            }
            const int par = li & 1;
            const bool cur0 = (4 * lq + par) <= li, cur1 = (4 * lq + par + 2) <= li;
            s16x4 Kb[5], Qb[5];
#pragma unroll
            for (int mt = 0; mt < 5; ++mt) {
                Kb[mt] = pack4((accM[mt] - m1[mt] * csK) * r1[mt]);
                if (mt) Qb[mt] = pack4(accQ[mt] - m1[mt] * csQ);
            }
            f32x4 Sc[5], Sp[5];
#pragma unroll
            for (int mt = 1; mt < 5; ++mt) { Sc[mt] = mfma16(Kb[mt], Qb[mt], zero4); Sp[mt] = mfma16(Kb[mt - 1], Qb[mt], zero4); }
            float s0[5], s1[5], mx[5];
#pragma unroll
            for (int mt = 1; mt < 5; ++mt) {
                const float sp0 = Sp[mt][0], sp1 = Sp[mt][1], sp2 = Sp[mt][2], sp3 = Sp[mt][3], c0 = Sc[mt][0], c1 = Sc[mt][1], c2 = Sc[mt][2], c3 = Sc[mt][3];
                float p0 = par ? sp1 : sp0, p1 = par ? sp3 : sp2, q0 = par ? c1 : c0, q1 = par ? c3 : c2;
                asm volatile("" : "+v"(p0), "+v"(p1), "+v"(q0), "+v"(q1));
                if (mt == 1 && grp == 0) { p0 = -3.0e38f; p1 = -3.0e38f; }
                s0[mt] = cur0 ? q0 : p0; s1[mt] = cur1 ? q1 : p1;
                mx[mt] = vmax(s0[mt], s1[mt]);
            }
#pragma unroll
            for (int mt = 1; mt < 5; ++mt) { auto r = __builtin_amdgcn_permlane32_swap(__float_as_uint(mx[mt]), __float_as_uint(mx[mt]), false, false); mx[mt] = vmax(__uint_as_float(r[0]), __uint_as_float(r[1])); }
#pragma unroll
            for (int mt = 1; mt < 5; ++mt) { auto r = __builtin_amdgcn_permlane16_swap(__float_as_uint(mx[mt]), __float_as_uint(mx[mt]), false, false); mx[mt] = vmax(__uint_as_float(r[0]), __uint_as_float(r[1])); }
#pragma unroll
            for (int mt = 1; mt < 5; ++mt) {
                const float sc = r1[mt] * (0.25f * 1.44269504088896341f);
                const float e0 = __builtin_amdgcn_exp2f((s0[mt] - mx[mt]) * sc), e1 = __builtin_amdgcn_exp2f((s1[mt] - mx[mt]) * sc);
                *(LAS u32x2*)(lds + ex + (mt - 1) * 512) = (u32x2){pk2(e0, e0), pk2(e1, e1)};
            }
            STAMP(SBASE + 2);
            BAR();
            STAMP(SBASE + 1);
            if constexpr (LAYER == 0) f1ok = __builtin_amdgcn_readfirstlane(*(LAS u32*)(lds + L_FLG));
        } else {
            if constexpr (LAYER == 0) {
                auto s = __builtin_amdgcn_permlane16_swap(__float_as_uint(accM[0][0]), __float_as_uint(accM[0][0]), false, false);
                auto t = __builtin_amdgcn_permlane32_swap(s[0], s[0], false, false);
                csV = __uint_as_float(t[0]);
                if (wave == 7) { WAIT_VM(8); pin(flg); const u32 ok = flag_ok(flg); *(LAS u32*)(lds + L_FLG) = ok; }
            }
            const int par = li & 1;
            const bool cur0 = (4 * lq + par) <= li, cur1 = (4 * lq + par + 2) <= li;
            const u32 pm = par ? 0xffff0000u : 0x0000ffffu;
            u32 mC0 = cur0 ? pm : 0u, mP0 = cur0 ? 0u : pm, mC1 = cur1 ? pm : 0u, mP1 = cur1 ? 0u : pm;
            u32 oo = (LAYER ? WS_O2 : WS_O1) + (u32)((4 * grp + (lq & 1)) * 16384 + (4 * mem + hd) * 512 + (lq >> 1) * 256 + li * 16);
            asm volatile("" : "+v"(mC0), "+v"(mP0), "+v"(mC1), "+v"(mP1), "+v"(oo));
            s16x4 Vb[5];
#pragma unroll
            for (int mt = 0; mt < 5; ++mt) { Vb[mt] = pack4((accM[mt] - m4[mt] * csV) * r4[mt]); pin(Vb[mt]); }
            STAMP(SBASE);
            BAR();
            STAMP(SBASE + 1);
            u32x2 pw[4];
#pragma unroll
            for (int j = 0; j < 4; ++j) pw[j] = *(LAS u32x2*)(lds + ex + j * 512);
            if constexpr (LAYER == 0) {
                f1ok = __builtin_amdgcn_readfirstlane(*(LAS u32*)(lds + L_FLG));
                if (f1ok) ald4s(uu, WS_UT + (u32)(b * 8192 + opq(tid) * 16));
            }
            SB();
            f32x4 O[4], L[4];
#pragma unroll
            for (int j = 0; j < 4; ++j) {
                const s16x8 P = __builtin_bit_cast(s16x8, (u32x4){pw[j][0] & mC0, pw[j][1] & mC1, pw[j][0] & mP0, pw[j][1] & mP1});
                L[j] = mfma32(ones8, P, zero4);
                O[j] = mfma32(cat8(Vb[j + 1], Vb[j]), P, zero4);
            }
            u32 ow[4][2];
#pragma unroll
            for (int j = 0; j < 4; ++j) {
                const float inv = __builtin_amdgcn_rcpf(L[j][0]);
                ow[j][0] = nosent(pk2(O[j][0] * inv, O[j][1] * inv)); ow[j][1] = nosent(pk2(O[j][2] * inv, O[j][3] * inv));
            }
#pragma unroll
            for (int j = 0; j < 4; j += 2) {
                auto r0 = __builtin_amdgcn_permlane16_swap(ow[j][0], ow[j + 1][0], false, false);
                auto r1 = __builtin_amdgcn_permlane16_swap(ow[j][1], ow[j + 1][1], false, false);
                __builtin_amdgcn_raw_buffer_store_b128((u32x4){r0[0], r1[0], r0[1], r1[1]}, r_ws, oo, j * 16384, 16);
            }
            CFENCE();
        }
        if constexpr (LAYER == 0 && R == 0) { if (f1ok) ald4s(uu, WS_UT + (u32)(b * 8192 + opq(tid) * 16)); }
        SB();
        return f1ok;
    };
    const u32 f1ok = attention(IC<0>{});
    {
        const u32x4 sv = {SENT, SENT, SENT, SENT};
        const int t2 = opq(tid);
        const u32 tb = (u32)((4 * grp + hd) * 16384 + mem * 2048 + R * 1024 + (t2 & 63) * 16);
        __builtin_amdgcn_raw_buffer_store_b128(sv, r_ws, tb, WS_O2, 16);
        __builtin_amdgcn_raw_buffer_store_b128(sv, r_ws, tb, WS_X2, 16);
        __builtin_amdgcn_raw_buffer_store_b128(sv, r_ws, (u32)(b * 8192 + t2 * 16), WS_Y2, 16);
        CFENCE();
        SB();
    }
    STAMP(2);

    const int TI = 4 * grp + hd;
    u32x4 of[8];
    auto load_o = [&](u32 ws_o) {
        const int lane = opq(tid) & 63;
#pragma unroll
        for (int i = 0; i < 8; ++i) of[i] = __builtin_amdgcn_raw_buffer_load_b128(r_ws, ws_o + (u32)(TI * 16384 + R * 8192 + lane * 16) + (i & 3) * 1024, (i >> 2) * 4096, 16);
    };
    u32 xw[8], yw[8];
    u32 woa[4];
    auto resid = [&](int n) { return R ? zero4 : (f32x4){bflo(xw[2 * n]), bfhi(xw[2 * n]), bflo(xw[2 * n + 1]), bfhi(xw[2 * n + 1])}; };
    s16x8 F[8][4];
    auto wo_addr = [&]() {
        const int l2 = opq(tid) & 63, lq = l2 >> 4, tq = (l2 & 15) >> 2, tp = l2 & 3;
#pragma unroll
        for (int n = 0; n < 4; ++n) woa[n] = L_WO + (u32)(R * 32768 + (8 * lq + tq) * 128 + ((n ^ (((lq & 1) << 1) | (tq >> 1))) << 5) + tp * 8);
    };
    auto load_F = [&](int ks) {
#pragma unroll
        for (int n = 0; n < 4; ++n) F[ks][n] = cat8(trr(woa[n] + ks * 4096), trr(woa[n] + ks * 4096 + 512));
    };

    {
        const int t2 = opq(tid), lane = t2 & 63, li = lane & 15, lq = lane >> 4;
        constexpr u32 OFFS = 0xEDA85410u;
        {
            const int j = t2 & 255;
            {
                if constexpr (R == 0) { if (f1ok) WAIT_VM(4); else WAIT_VM(3); }
                else WAIT_VM(5);
                u32 cb = L_HC + R * 34816 + (j + 2) * 16;
                pin(cb);
#pragma unroll
                for (int ch = R; ch < 2; ++ch) {
                    pin(hr[2 * ch]); pin(hr[2 * ch + 1]);
                    const u32 d[8] = {hr[2 * ch][0], hr[2 * ch][1], hr[2 * ch][2], hr[2 * ch][3], hr[2 * ch + 1][0], hr[2 * ch + 1][1], hr[2 * ch + 1][2], hr[2 * ch + 1][3]};
                    u32 e[7];
#pragma unroll
                    for (int i = 0; i < 7; ++i) e[i] = __builtin_amdgcn_alignbit(d[i + 1], d[i], 16);
                    if (ch > R) { cb += 34816; pin(cb); }
#pragma unroll
                    for (int p = (R ? 4 : 0); p < ((ch && !R) ? 4 : 8); ++p) {
                        const u32 po = (272 * p + ((OFFS >> (4 * p)) & 15)) * 16;
                        const int h = p >> 1;
                        const u32x4 w = (p & 1) ? (u32x4){e[h], e[h + 1], e[h + 2], e[h + 3]} : (u32x4){d[h], d[h + 1], d[h + 2], d[h + 3]};
                        *(LAS u32x4*)(lds + cb + po) = w;
                    }
                }
            }
            if constexpr (R == 0) {
                if (hd == 0) {
                    u32 hh[4];
#pragma unroll
                    for (int i = 0; i < 4; ++i) { const u32 a = __builtin_amdgcn_readfirstlane(hr[0][i]), c = __builtin_amdgcn_readfirstlane(hr[2][i]); hh[i] = (lane & 16) ? c : a; }
                    if (lane < 32) {
                        const int p = lane & 7;
                        const u32 po = (272 * p + ((OFFS >> (4 * p)) & 15)) * 16;
                        const unsigned __int128 hv = ((unsigned __int128)(((u64)hh[3] << 32) | hh[2]) << 64) | (((u64)hh[1] << 32) | hh[0]);
                        const unsigned __int128 sv = (p == 0 || !(lane & 8)) ? (unsigned __int128)0 : (hv << (16 * (8 - p)));
                        const u32x4 w = {(u32)sv, (u32)(sv >> 32), (u32)(sv >> 64), (u32)(sv >> 96)};
                        *(LAS u32x4*)(lds + L_HC + (lane >> 4) * 34816 + po + ((lane >> 3) & 1) * 16) = w;
                    }
                }
            }
            if (hd == 1 && lane < 30) *(LAS u32x4*)(lds + L_UR + R * 4576 + 4096 + lane * 16) = (u32x4){0, 0, 0, 0};
            STAMP(3);
            if (!f1ok) {
                const u32 fo = WS_F1 + (u32)(((lane & 8) ? (lane & 7) * 32 + grp : (b >> 5) * 32 + 31 - hd * 8 - (lane & 7)) * 128);
                flg = (u32x2){0u, 0u};
                spin_flags(flg, fo);
                ald4s(uu, WS_UT + (u32)(b * 8192 + t2 * 16));
            }
            if (f1ok) WAIT_VM(R ? 5 : 3); else WAIT_VM(0);
            pin(uu);
            *(LAS u32x4*)(lds + L_UR + R * 4576 + j * 16) = uu;
        }
        BAR();
        STAMP(4);
        const int ch = hd >> 1, e0 = (hd & 1) * 4;
        const int p = (li + 1) & 7;
        u32 aA = L_HC + ch * 34816 + (272 * p + ((OFFS >> (4 * p)) & 15) + ((li + 1) >> 3) + lq + 4 * e0) * 16;
        u32 aB = L_UR + ch * 4576 + (254 - 224 - 2 * li + lq + 4 * e0) * 16;
        asm volatile("" : "+v"(aA), "+v"(aB));
        f32x4 y[4];
        u32 ooff = WS_O1 + (u32)(TI * 16384 + R * 8192 + lane * 16);
        {
            constexpr ConvSched S(R);
            s16x8 A[4][8], B[4][8];
            auto loads_at = [&](auto pc) {
                constexpr int p = decltype(pc)::value;
                sfor<0, 64>([&](auto ic) {
                    constexpr int i = decltype(ic)::value, e = i >> 4, f = i & 7;
                    if constexpr (S.slot[i] == p) {
                        if constexpr (i & 8) B[e][f] = ldsA(aB + 64 * e + 512 * (7 - f)); else A[e][f] = ldsA(aA + 64 * (8 * f + e));
                    }
                });
            };
            loads_at(IC<-1>{});
            SB();
            sfor<0, 72>([&](auto pc) {
                constexpr int p = decltype(pc)::value, e = p / 18, f = S.f[p % 18], i = S.i[p % 18], g = S.n[i] - f;
                y[i] = mfma32(A[e][f], B[e][g], (e == 0 && f == 0) ? zero4 : y[i]);
                loads_at(pc);
                if constexpr (p == 54) WAIT_VM(0);
                if constexpr (p >= 55 && p < 71 && (p & 1)) {
                    constexpr int i = (p - 55) >> 1;
                    ald4o<(i & 3) * 1024>(of[i], ooff + (i >> 2) * 4096, d_ws);
                }
                SB();
            });
        }
        STAMP(14);
        RBAR();
        if (R == 0 && tid == 0) __builtin_amdgcn_raw_buffer_store_b64((u32x2){tl, th}, r_ws, WS_F2 + (grp * 8 + mem) * 128, 0, 16);
        {
            constexpr int N0[4] = {0, 3, 4, 7};
            u32 ra = L_HC + hd * 8192 + lane * 16;
            asm volatile("" : "+v"(ra));
#pragma unroll
            for (int i = 0; i < 4; ++i) *(LAS f32x4*)(lds + ra + (N0[i] ^ R) * 1024) = y[i];
        }
        SB();
        if constexpr (R == 0) {
#pragma unroll
            for (int n = 0; n < 4; ++n) { const u32x2 w = __builtin_amdgcn_raw_buffer_load_b64(r_x, (u32)((TI * 16 + li) * 1024 + mem * 128 + lq * 8) + n * 32, 0, 0); xw[2 * n] = w[0]; xw[2 * n + 1] = w[1]; }
        }
        wo_addr();
        SB();
        STAMP(37);
        BAR();
        STAMP(38);
        {
            const float D0 = bflo(dw), D1 = bfhi(dw);
            u32 ra = L_HC + wave * 1024 + lane * 16;
            const int tb = 256 * wave + 16 * li + 4 * lq;
            u32 ru = L_UR + (2044 - tb) * 2;
            asm volatile("" : "+v"(ra), "+v"(ru));
            const f32x4 y0 = *(LAS f32x4*)(lds + ra), y1 = *(LAS f32x4*)(lds + ra + 8192), y2 = *(LAS f32x4*)(lds + ra + 16384), y3 = *(LAS f32x4*)(lds + ra + 24576);
            const u32x2 ua = *(LAS u32x2*)(lds + ru), ub = *(LAS u32x2*)(lds + ru + 4576);
            load_F(0); load_F(1);
            SB();
            const f32x4 ya = y0 + y1, yb = y2 + y3;
            const u32x4 w = {nosent(pk2(ya[0] + bfhi(ua[1]) * D0, yb[0] + bfhi(ub[1]) * D1)), nosent(pk2(ya[1] + bflo(ua[1]) * D0, yb[1] + bflo(ub[1]) * D1)),
                             nosent(pk2(ya[2] + bfhi(ua[0]) * D0, yb[2] + bfhi(ub[0]) * D1)), nosent(pk2(ya[3] + bflo(ua[0]) * D0, yb[3] + bflo(ub[0]) * D1))};
            __builtin_amdgcn_raw_buffer_store_b128(w, r_ws, WS_Y2 + (u32)(b * 8192 + tb * 4), 0, 16);
        }
        SB();
        STAMP(5);
        WAIT_VM(1);
        STAMP(40);

        u32 f2oth = 0;
        _Pragma("unroll 1") for (int attempt = 0; attempt < 64; ++attempt) {
            sfor<0, 8>([&](auto ic) { pin(of[decltype(ic)::value]); });
            u32 m0 = 0, m1 = 0;
#pragma unroll
            for (int ks = 0; ks < 8; ++ks) {
                load_F((ks + 2) & 7);
                if (R == 1 && ks == 4 && wave == 7) {
                    const int pb = 32 * mem + (lane & 31);
                    f2oth = WS_F2 + (u32)((lane < 32 ? ((pb & 7) * 4 + (pb >> 6)) * 8 + ((pb >> 3) & 7) : max(grp * 8 - 8, 0) + (lane & 15)) * 128);
                    ald2s(flg, f2oth);
                }
                SB();
#pragma unroll
                for (int n = 0; n < 4; ++n) accM[n] = mfma32(F[ks][n], __builtin_bit_cast(s16x8, of[ks]), ks ? accM[n] : resid(n));
                m0 = max(max(m0, of[ks][0]), of[ks][1]); m1 = max(max(m1, of[ks][2]), of[ks][3]);
                SB();
            }
            if (__builtin_expect(__all(max(m0, m1) != SENT), 1)) break;
            __builtin_amdgcn_s_sleep(4);
            WAIT_VM(0);
            sfor<0, 8>([&](auto ic) { constexpr int i = decltype(ic)::value; ald4o<(i & 3) * 1024>(of[i], ooff + (i >> 2) * 4096, d_ws); });
            WAIT_VM(0);
        }
        STAMP(41);
        if constexpr (R == 1) { if (wave == 7) { WAIT_VM(0); pin(flg); spin_flags(flg, f2oth); } }
    }

    auto load_y = [&]() {
        const int l2 = opq(tid) & 63;
#pragma unroll
        for (int i = 0; i < 8; ++i)
            yw[i] = __builtin_amdgcn_raw_buffer_load_b32(r_ws, WS_Y2 + (u32)((32 * mem + 2 * (l2 >> 4)) * 8192 + (TI * 16 + (l2 & 15)) * 4), (8 * (i >> 1) + (i & 1)) * 8192, 16);
    };
    auto pub_off = [&](u32 ws_st) {
        const int lane = opq(tid) & 63;
        return (lane & 16) ? 0xFFFFFF00u : ws_st + (u32)((TI * 8 + mem) * 256 + (lane & 15) * 16 + (lane >> 5) * 8);
    };
    auto pub_stat = [&](u32 off, u32 tg, float s, float q) {
        auto r = __builtin_amdgcn_permlane32_swap(__float_as_uint(s), __float_as_uint(q), false, false);
        const float v = __uint_as_float(r[0]) + __uint_as_float(r[1]);
        auto t = __builtin_amdgcn_permlane16_swap(__float_as_uint(v), __float_as_uint(v), false, false);
        __builtin_amdgcn_raw_buffer_store_b64((u32x2){__float_as_uint(__uint_as_float(t[0]) + __uint_as_float(t[1])), tg}, r_ws, off, 0, 16);
    };
    auto stat_off = [&](u32 ws_st, int ti) { const int l2 = opq(tid) & 63; return ws_st + (u32)(ti * 2048 + (l2 >> 4) * 512 + (l2 & 15) * 16); };
    auto fin_stat = [&](u32x4& a, u32x4& c, u32 off, float& mean, float& rstd) {
        _Pragma("unroll 1") for (int spin = 0; spin < SPIN_CAP; ++spin) {
            if (__builtin_expect(__all(((a[1] ^ tl) | (a[3] ^ th) | (c[1] ^ tl) | (c[3] ^ th)) == 0), 1)) break;
            __builtin_amdgcn_s_sleep(1);
            asm volatile("buffer_load_dwordx4 %0, %2, %3, 0 offen sc1\n\tbuffer_load_dwordx4 %1, %2, %3, 0 offen offset:256 sc1\n\ts_waitcnt vmcnt(0)"
                         : "=&v"(a), "=&v"(c) : "v"(off), "s"(d_ws) : "memory");
        }
        auto r = __builtin_amdgcn_permlane32_swap(__float_as_uint(__uint_as_float(a[0]) + __uint_as_float(c[0])), __float_as_uint(__uint_as_float(a[2]) + __uint_as_float(c[2])), false, false);
        const float v = __uint_as_float(r[0]) + __uint_as_float(r[1]);
        auto t = __builtin_amdgcn_permlane16_swap(__float_as_uint(v), __float_as_uint(v), false, false);
        const u32 v2 = __float_as_uint(__uint_as_float(t[0]) + __uint_as_float(t[1]));
        auto u = __builtin_amdgcn_permlane32_swap(v2, v2, false, false);
        const float ts = __uint_as_float(u[0]), tq = __uint_as_float(u[1]);
        mean = ts * (1.f / 512.f);
        rstd = __builtin_amdgcn_rsqf(fmaxf(tq * (1.f / 512.f) - mean * mean, 0.f) + 1e-5f);
    };
    auto regroup = [&](u32 (&w)[8]) {
#pragma unroll
        for (int j = 0; j < 2; ++j) {
            auto r0 = __builtin_amdgcn_permlane16_swap(w[4 * j], w[4 * j + 2], false, false);
            auto r1 = __builtin_amdgcn_permlane16_swap(w[4 * j + 1], w[4 * j + 3], false, false);
            w[4 * j] = r0[0]; w[4 * j + 1] = r1[0]; w[4 * j + 2] = r0[1]; w[4 * j + 3] = r1[1];
        }
    };
    auto wo_finish = [&](auto lc) {
        constexpr int LAYER = decltype(lc)::value;
        const int lane = opq(tid) & 63, li = lane & 15, lq = lane >> 4;
        u32 wx = L_WX + hd * 4096 + lane * 16;
        pin(wx);
        u32 po = 0, so = 0, tg = 0, oa = 0;
        if constexpr (R == 1) {
#pragma unroll
            for (int n = 0; n < 4; ++n) *(LAS f32x4*)(lds + wx + n * 1024) = accM[n];
        } else {
            po = pub_off(LAYER ? WS_ST3 : WS_ST1); so = stat_off(LAYER ? WS_ST3 : WS_ST1, TI); tg = lane < 32 ? tl : th;
            const int c = 2 * (lq & 1) + (lq >> 1);
            oa = LAYER ? (u32)((TI * 16 + li) * 1024 + mem * 128 + c * 16) : WS_X2 + (u32)((TI * 8 + mem) * 2048 + li * 128 + ((c ^ (li >> 1)) << 4));
            pin(po); pin(so); pin(tg); pin(oa);
        }
        BAR();
        STAMP(LAYER ? 25 : 42);
        if constexpr (R == 0) {
            f32x4 t[4];
#pragma unroll
            for (int n = 0; n < 4; ++n) t[n] = *(LAS f32x4*)(lds + wx + n * 1024);
            SB();
            float s = 0.f, q = 0.f, s2 = 0.f, q2 = 0.f;
#pragma unroll
            for (int n = 0; n < 4; ++n) {
                const f32x4 v = accM[n] + t[n];
                const u32 w0 = pk2(v[0], v[1]), w1 = pk2(v[2], v[3]);
                xw[2 * n] = w0; xw[2 * n + 1] = w1;
                s = dot2(w0, 0x3F803F80u, s); q = dot2(w0, w0, q); s2 = dot2(w1, 0x3F803F80u, s2); q2 = dot2(w1, w1, q2);
            }
            pub_stat(po, tg, s + s2, q + q2);
            CFENCE();
            if constexpr (!LAYER) load_y();
            regroup(xw);
            SB();
            STAMP(LAYER ? 46 : 43);
            WAIT_VM(0);
            STAMP(LAYER ? 26 : 44);
            if constexpr (!LAYER) {
#pragma unroll
                for (int i = 0; i < 8; ++i) pin(yw[i]);
            }
            u32x4 a, c;
            ald4s(a, so); ald4s(c, so + 256);
            if constexpr (!LAYER) {
                _Pragma("unroll 1") for (int spin = 0; spin < SPIN_CAP; ++spin) {
                    if (__builtin_expect(__all(max(max(max(yw[0], yw[1]), max(yw[2], yw[3])), max(max(yw[4], yw[5]), max(yw[6], yw[7]))) != SENT), 1)) break;
                    __builtin_amdgcn_s_sleep(1);
                    CFENCE();
                    load_y();
                }
                regroup(yw);
            }
            f32x2 xa[8], ya[8], xy[8];
#pragma unroll
            for (int i = 0; i < 8; ++i) {
                xa[i] = (f32x2){bflo(xw[i]), bfhi(xw[i])}; ya[i] = (f32x2){bflo(yw[i]), bfhi(yw[i])}; xy[i] = xa[i] * ya[i];
                pin(xa[i]); pin(ya[i]); pin(xy[i]);
            }
            WAIT_VM(0);
            pin(a); pin(c);
            float mean, rstd;
            fin_stat(a, c, so, mean, rstd);
            STAMP(LAYER ? 11 : 6);
            const float nm = -mean * rstd;
#pragma unroll
            for (int i = 0; i < 8; ++i) {
                const f32x2 o = xy[i] * rstd + (ya[i] * nm + xa[i]);
                xw[i] = pk2(o[0], o[1]);
                if (!LAYER) xw[i] = nosent(xw[i]);
            }
#pragma unroll
            for (int j = 0; j < 2; ++j) {
                const u32x4 o = {xw[4 * j], xw[4 * j + 1], xw[4 * j + 2], xw[4 * j + 3]};
                if constexpr (LAYER) __builtin_amdgcn_raw_buffer_store_b128(o, r_out, oa + j * 64, 0, OUT_AUX);
                else __builtin_amdgcn_raw_buffer_store_b128(o, r_ws, oa ^ (j * 64), 0, 16);
            }
            if constexpr (!LAYER) {
                float s = 0.f, q = 0.f, s2 = 0.f, q2 = 0.f;
#pragma unroll
                for (int i = 0; i < 8; i += 2) { s = dot2(xw[i], 0x3F803F80u, s); q = dot2(xw[i], xw[i], q); s2 = dot2(xw[i + 1], 0x3F803F80u, s2); q2 = dot2(xw[i + 1], xw[i + 1], q2); }
                pub_stat(po + (WS_ST2 - WS_ST1) * !(lane & 16), tg, s + s2, q + q2);
                CFENCE();
                regroup(xw);
#pragma unroll
                for (int i = 0; i < 8; ++i) { pin(xw[i]); pin(yw[i]); }
                STAMP(48);
                WAIT_VM(0);
            }
        }
    };
    wo_finish(IC<0>{});
    RBAR();
    STAMP(7);

    {
        const int l3 = opq(tid) & 63;
        const u32 g0 = (u32)((4 * grp - 1 + (hd >> 1)) * 16384 + (hd & 1) * 1024 + l3 * 16);
        const u32 g2 = (u32)((4 * grp + 3) * 16384 + hd * 512 + l3 * 16);
        auto x2_piece = [&](auto ic) {
            constexpr int i = decltype(ic)::value, s = i / 3, p = i % 3;
            const u32 kp = (u32)((s + rot) & 7);
            if constexpr (p == 0) dma(IC<4>{}, IC<L_CX + s * 10240>{}, d_x2, hb1k, g0, kp * 2048);
            else if constexpr (p == 1) dma(IC<4>{}, IC<L_CX + s * 10240 + 4096>{}, d_x2, hb1k, g0 + 32768, kp * 2048);
            else dma(IC<5>{}, IC<L_CX + s * 10240 + 8192>{}, d_x2, hb512, g2, kp * 2048);
        };
        u32 va = L_CX + hd * 1024 + l3 * 16, vq = L_CX + 8192 + hd * 512 + (l3 & 31) * 16 + (l3 >> 5) * 10240;
        pin(va); pin(vq);
        u32x4 vr[5];
        auto ver_read = [&](auto cc) {
            constexpr int c = decltype(cc)::value;
            u32 a = va + c * 20480, q = vq + c * 20480;
            pin(a); pin(q); CFENCE();
            vr[0] = *(LAS u32x4*)(lds + a); vr[1] = *(LAS u32x4*)(lds + a + 4096); vr[2] = *(LAS u32x4*)(lds + a + 10240); vr[3] = *(LAS u32x4*)(lds + a + 14336);
            vr[4] = *(LAS u32x4*)(lds + q);
        };
        auto ver_check = [&](auto cc) {
            constexpr int c = decltype(cc)::value;
            _Pragma("unroll 1") for (int spin = 0; spin < SPIN_CAP; ++spin) {
                u32 m0 = 0, m1 = 0;
#pragma unroll
                for (int i = 0; i < 5; ++i) { m0 = max(max(m0, vr[i][0]), vr[i][1]); m1 = max(max(m1, vr[i][2]), vr[i][3]); }
                if (__builtin_expect(__all(max(m0, m1) != SENT), 1)) break;
                __builtin_amdgcn_s_sleep(1);
                sfor<6 * c, 6 * c + 6>([&](auto ic) { x2_piece(ic); });
                WAIT_VM(0);
                ver_read(cc);
            }
        };
        const u32 xb0 = (u32)((l3 & 15) * 128 + (((l3 >> 4) ^ ((l3 & 15) >> 1)) << 4));
        if constexpr (R == 1) {
            STAMP(39);
            sfor<0, 12>([&](auto ic) { x2_piece(ic); });
            WAIT_VM(6);
            STAMP(8);
            ver_read(IC<0>{});
            ver_check(IC<0>{});
        } else {
            u32x4 sa, sc, ha, hc;
            const u32 so = stat_off(WS_ST2, 4 * grp + hd);
            const bool halo = hd == 0 && grp > 0;
            ald4s(sa, so); ald4s(sc, so + 256);
            if (halo) { ald4s(ha, so - 2048); ald4s(hc, so - 2048 + 256); }
            WAIT_VM(0);
            pin(sa); pin(sc);
            float mean, rstd;
            fin_stat(sa, sc, so, mean, rstd);
            if (l3 < 16) { *(LAS float*)(lds + L_TAB + 64 + hd * 64 + l3 * 4) = mean; *(LAS float*)(lds + L_TAB + 320 + 64 + hd * 64 + l3 * 4) = rstd; }
            if (halo) {
                pin(ha); pin(hc);
                fin_stat(ha, hc, so - 2048, mean, rstd);
                if (l3 < 16) { *(LAS float*)(lds + L_TAB + l3 * 4) = mean; *(LAS float*)(lds + L_TAB + 320 + l3 * 4) = rstd; }
            }
        }
        BAR();
        STAMP(13);
        u32 xa;
        auto pf_addr = [&](int st) { xa = L_CX + (st >> 1) * 10240 + (xb0 ^ ((st & 1) * 64)); pin(xa); };
        pf_addr(0);
#pragma unroll
        for (int mt = 0; mt < 5; ++mt) Ab[0][mt] = ldsA(xa + mt * 2048);
        sfor<0, 16>([&](auto stc) {
            constexpr int st = decltype(stc)::value;
            if constexpr (R == 1) {
                if constexpr (st == 2) { WAIT_VM(10); ver_read(IC<1>{}); }
                if constexpr (st == 3) ver_check(IC<1>{});
                if constexpr (st == 5) { WAIT_VM(6); ver_read(IC<2>{}); }
                if constexpr (st == 6) { ver_check(IC<2>{}); WAIT_VM(0); ver_read(IC<3>{}); }
                if constexpr (st == 7) ver_check(IC<3>{});
            }
            if constexpr (st == 3) { RBAR(); STAMP(49); }
            if constexpr (st == 7) { RBAR(); STAMP(50); CSTAMP(60); }
            if (st < 15) pf_addr(st + 1);
            if constexpr (st == 14) load_tab(true);
            SB();
            qkv_step(IC<1>{}, st, Ab[st & 1], [&](auto mc) {
                constexpr int mt = decltype(mc)::value;
                if (st < 15) Ab[(st + 1) & 1][mt] = ldsA(xa + mt * 2048);
                if constexpr (R == 1 && 12 + 5 * st + mt < 24) x2_piece(IC<12 + 5 * st + mt>{});
                SB();
            });
        });
    }
    STAMP(R ? 9 : 52);
    CSTAMP(61);
    attention(IC<1>{});
    STAMP(27);

    {
        wo_addr();
#pragma unroll
        for (int ks = 0; ks < 8; ++ks) load_F(ks);
        SB();
        WAIT_VM(0);
        RBAR();
        STAMP(10);
        load_o(WS_O2);
        SB();
        _Pragma("unroll 1") for (int spin = 0; spin < 64; ++spin) {
            u32 m0 = 0, m1 = 0;
#pragma unroll
            for (int ks = 0; ks < 8; ++ks) {
#pragma unroll
                for (int n = 0; n < 4; ++n) accM[n] = mfma32(F[ks][n], __builtin_bit_cast(s16x8, of[ks]), ks ? accM[n] : resid(n));
                m0 = max(max(m0, of[ks][0]), of[ks][1]); m1 = max(max(m1, of[ks][2]), of[ks][3]);
                SB();
            }
            if (__builtin_expect(__all(max(m0, m1) != SENT), 1)) break;
            _Pragma("unroll 1") for (int sp2 = 0; sp2 < SPIN_CAP; ++sp2) {
                CFENCE();
                __builtin_amdgcn_s_sleep(1);
                const u32 pv = __builtin_amdgcn_raw_buffer_load_b32(r_ws, WS_O2 + (u32)(TI * 16384 + R * 8192 + (opq(tid) & 15) * 512 + 508), 0, 16);
                if (__all(pv != SENT)) break;
            }
            CFENCE();
            load_o(WS_O2);
        }
        SB();
        STAMP(45);
    }
    wo_finish(IC<1>{});
    if constexpr (R == 1) {
        if (b == 0 && wave == 7) {
            const int l3 = opq(tid) & 63;
#pragma unroll
            for (int i = 0; i < 4; ++i) { flg = (u32x2){0u, 0u}; spin_flags(flg, WS_F1 + (u32)((i * 64 + l3) * 128)); }
            if (l3 == 0) __builtin_amdgcn_raw_buffer_store_b64(__builtin_bit_cast(u32x2, epoch + 1), r_ws, WS_EPOCH, 0, 16);
        }
    }
}

__global__ void __launch_bounds__(512, 2)
fused_kernel(const u16* __restrict__ gx, const u16* __restrict__ gwqkv, const u16* __restrict__ gwo,
             const u16* __restrict__ gbk, const u16* __restrict__ gbv, const u16* __restrict__ gsk,
             const u16* __restrict__ ghk, const u16* __restrict__ gdv, u16* __restrict__ gout, char* __restrict__ ws) {
    __shared__ __attribute__((aligned(16))) char lds_[L_TOTAL];
    typedef u32 u32x16 __attribute__((ext_vector_type(16)));
    u32x16 ka; u32x4 kb;
    {
        auto kp = __builtin_amdgcn_kernarg_segment_ptr();
        asm volatile("s_load_dwordx16 %0, %2, 0x0\n\ts_load_dwordx4 %1, %2, 0x40\n\ts_waitcnt lgkmcnt(0)" : "=&s"(ka), "=&s"(kb) : "s"(kp) : "memory");
    }
    auto mkp = [](u32 lo, u32 hi) { return (char*)(((u64)hi << 32) | lo); };
    const Args a = {(const u16*)mkp(ka[0], ka[1]), (const u16*)mkp(ka[2], ka[3]), (const u16*)mkp(ka[4], ka[5]), (const u16*)mkp(ka[6], ka[7]), (const u16*)mkp(ka[8], ka[9]),
                    (const u16*)mkp(ka[10], ka[11]), (const u16*)mkp(ka[12], ka[13]), (const u16*)mkp(ka[14], ka[15]), (u16*)mkp(kb[0], kb[1]), mkp(kb[2], kb[3])};
    const int wave = __builtin_amdgcn_readfirstlane(threadIdx.x >> 6);
    if (wave < 4) body<0>(a, (LAS char*)lds_, wave); else body<1>(a, (LAS char*)lds_, wave);
}

extern "C" void kernel_launch(void* const* d_in, const int* in_sizes, int n_in,
                              void* d_out, int out_size, void* d_ws, size_t ws_size,
                              hipStream_t stream) {
    fused_kernel<<<256, 512, 0, stream>>>((const u16*)d_in[0], (const u16*)d_in[1], (const u16*)d_in[2],
                                          (const u16*)d_in[4], (const u16*)d_in[5], (const u16*)d_in[6],
                                          (const u16*)d_in[7], (const u16*)d_in[8], (u16*)d_out, (char*)d_ws);
}
```

```cpp
#define ROT 2
#define PRO 2
#define UA 3
#define UB 5
#define F1B 4
#define WOPF 12
#define WOLATE
#define NO_PROBE
#include <hip/hip_runtime.h>
#include <stdint.h>

typedef unsigned short u16;
typedef unsigned u32;
typedef unsigned long long u64;
typedef short s16x4 __attribute__((ext_vector_type(4)));
typedef short s16x8 __attribute__((ext_vector_type(8)));
typedef __bf16 bf16x2 __attribute__((ext_vector_type(2)));
typedef float f32x2 __attribute__((ext_vector_type(2)));
typedef float f32x4 __attribute__((ext_vector_type(4)));
typedef u32 u32x2 __attribute__((ext_vector_type(2)));
typedef u32 u32x4 __attribute__((ext_vector_type(4)));
#define LAS __attribute__((address_space(3)))
#define DEV __device__ __forceinline__
typedef __amdgpu_buffer_rsrc_t rsrc_t;

constexpr u32 WS_EPOCH = 0;
constexpr u32 WS_F1 = 32768;
constexpr u32 WS_F2 = 65536;
constexpr u32 WS_ST0 = 98304;
constexpr u32 WS_ST1 = 131072;
constexpr u32 WS_ST2 = WS_ST1 + 262144;
constexpr u32 WS_ST3 = WS_ST2 + 262144;
constexpr u32 WS_UT = 1u << 20;
constexpr u32 WS_O1 = WS_UT + (2u << 20);
constexpr u32 WS_O2 = WS_O1 + (2u << 20);
constexpr u32 WS_Y2 = WS_O2 + (2u << 20);
constexpr u32 WS_X2 = WS_Y2 + (2u << 20);
constexpr u32 WS_END = WS_X2 + (2u << 20);

constexpr u32 L_SLOT = 34816;
constexpr u32 L_KV = 4 * L_SLOT;
constexpr u32 L_K = L_KV, L_V = L_KV + 9216;
constexpr u32 L_SK = L_KV + 17408;
constexpr u32 L_WO = 0;
constexpr u32 L_CX = 65536;
constexpr u32 L_HC = L_CX;
constexpr u32 L_UR = L_CX + 69632;
constexpr u32 L_WX = L_CX + 40960;
constexpr u32 L_EX = 147456;
constexpr u32 L_TAB = L_EX + 12288;
constexpr u32 L_CS = L_TAB + 1024;
constexpr u32 L_FLG = L_TAB + 2048;
constexpr u32 L_DUMMY = L_FLG + 1024;
constexpr u32 L_TOTAL = 160 * 1024;
static_assert(L_DUMMY + 1024 <= L_TOTAL, "LDS map");

#define WO_POS 0
#define OUT_AUX 0
constexpr int SPIN_CAP = 1 << 14;
constexpr u32 SENT = 0xFFFFFFFFu;

#define WAIT_VM(n) asm volatile("s_waitcnt vmcnt(%0)" ::"i"(n) : "memory")
#define BAR() do { asm volatile("s_waitcnt lgkmcnt(0)" ::: "memory"); __builtin_amdgcn_s_barrier(); asm volatile("" ::: "memory"); } while (0)
#define RBAR() do { asm volatile("" ::: "memory"); __builtin_amdgcn_s_barrier(); asm volatile("" ::: "memory"); } while (0)
#define CFENCE() asm volatile("" ::: "memory")
#define SB() __builtin_amdgcn_sched_barrier(0)
template <int N> struct IC { static constexpr int value = N; };
template <int I, int N, class F> DEV void sfor(F&& f) { if constexpr (I < N) { f(IC<I>{}); sfor<I + 1, N>(f); } }

DEV f32x4 mfma32(s16x8 a, s16x8 b, f32x4 c) { return __builtin_amdgcn_mfma_f32_16x16x32_bf16(a, b, c, 0, 0, 0); }
DEV f32x4 mfma16(s16x4 a, s16x4 b, f32x4 c) { return __builtin_amdgcn_mfma_f32_16x16x16bf16_1k(a, b, c, 0, 0, 0); }
DEV float vmax(float a, float b) { float r; asm("v_max_f32 %0, %1, %2" : "=v"(r) : "v"(a), "v"(b)); return r; }
DEV u32 pk2(float lo, float hi) { f32x2 f = {lo, hi}; bf16x2 b = __builtin_convertvector(f, bf16x2); return __builtin_bit_cast(u32, b); }
DEV float bflo(u32 w) { return __uint_as_float(w << 16); }
DEV float bfhi(u32 w) { return __uint_as_float(w & 0xffff0000u); }
DEV float dot2(u32 a, u32 b, float c) { return __builtin_amdgcn_fdot2_f32_bf16(__builtin_bit_cast(bf16x2, a), __builtin_bit_cast(bf16x2, b), c, false); }
DEV u32 nosent(u32 w) { return min(w, 0xFFFFFFFEu); }
DEV float xsum(float v) {
    auto r = __builtin_amdgcn_permlane32_swap(__float_as_uint(v), __float_as_uint(v), false, false);
    v = __uint_as_float(r[0]) + __uint_as_float(r[1]);
    auto s = __builtin_amdgcn_permlane16_swap(__float_as_uint(v), __float_as_uint(v), false, false);
    return __uint_as_float(s[0]) + __uint_as_float(s[1]);
}
DEV float xmax(float v) {
    auto r = __builtin_amdgcn_permlane32_swap(__float_as_uint(v), __float_as_uint(v), false, false);
    v = vmax(__uint_as_float(r[0]), __uint_as_float(r[1]));
    auto s = __builtin_amdgcn_permlane16_swap(__float_as_uint(v), __float_as_uint(v), false, false);
    return vmax(__uint_as_float(s[0]), __uint_as_float(s[1]));
}
DEV float rowsum16(float v) {
    v += __builtin_bit_cast(float, __builtin_amdgcn_update_dpp(0, __builtin_bit_cast(int, v), 0xB1, 0xf, 0xf, true));
    v += __builtin_bit_cast(float, __builtin_amdgcn_update_dpp(0, __builtin_bit_cast(int, v), 0x4E, 0xf, 0xf, true));
    v += __builtin_bit_cast(float, __builtin_amdgcn_update_dpp(0, __builtin_bit_cast(int, v), 0x141, 0xf, 0xf, true));
    v += __builtin_bit_cast(float, __builtin_amdgcn_update_dpp(0, __builtin_bit_cast(int, v), 0x140, 0xf, 0xf, true));
    return v;
}
DEV float bcast_row0(float v) { return __builtin_bit_cast(float, __builtin_amdgcn_update_dpp(0, __builtin_bit_cast(int, v), 0x150, 0xf, 0xf, true)); }
template <int OFF> DEV void ald4o(u32x4& d, u32 voff, u32x4 rs) { asm volatile("buffer_load_dwordx4 %0, %1, %2, 0 offen offset:%3 sc1" : "=v"(d) : "v"(voff), "s"(rs), "i"(OFF) : "memory"); }
template <class T> DEV void pin(T& v) { asm volatile("" : "+v"(v)); }
DEV int opq(int v) { asm volatile("" : "+v"(v)); return v; }
DEV s16x8 cat8(s16x4 a, s16x4 b) { return __builtin_shufflevector(a, b, 0, 1, 2, 3, 4, 5, 6, 7); }
DEV s16x4 pack4(f32x4 v) { u32x2 w = {pk2(v[0], v[1]), pk2(v[2], v[3])}; return __builtin_bit_cast(s16x4, w); }
DEV u64 mix64(u64 z) { z += 0x9E3779B97F4A7C15ull; z = (z ^ (z >> 30)) * 0xBF58476D1CE4E5B9ull; z = (z ^ (z >> 27)) * 0x94D049BB133111EBull; return z ^ (z >> 31); }

struct ConvSched {
    int n[4], f[18], i[18], slot[64];
    constexpr ConvSched(int R) : n{}, f{}, i{}, slot{} {
        constexpr int LEAD = 9;
        const int N0[4] = {0, 3, 4, 7};
        for (int k = 0; k < 4; ++k) n[k] = N0[k] ^ R;
        int c = 0;
        for (int ff = 0; ff < 8; ++ff) for (int k = 3; k >= 0; --k) if (ff <= n[k]) { f[c] = ff; i[c] = k; ++c; }
        for (int k = 0; k < 64; ++k) slot[k] = 1000;
        for (int p = 71; p >= 0; --p) {
            const int e = p / 18, ff = f[p % 18], g = n[i[p % 18]] - ff, s = p - LEAD < -1 ? -1 : p - LEAD;
            slot[e * 16 + ff] = s; slot[e * 16 + 8 + g] = s;
        }
    }
};

#define STAMP(i) do { } while (0)
#define CSTAMP(i) do { } while (0)

struct Args { const u16 *gx, *gwqkv, *gwo, *gbk, *gbv, *gsk, *ghk, *gdv; u16* gout; char* ws; };

template <int R>
DEV void body(const Args& A_, LAS char* const lds, const int wave) {
    const u16* gx = A_.gx; char* ws = A_.ws;
    STAMP(0);
    const int b = blockIdx.x, tid = threadIdx.x, lane = tid & 63;
    const int hd = wave & 3;
    const int grp = (b & 7) * 4 + (b >> 6), mem = (b >> 3) & 7;
    const int T0 = grp * 64;
    const int rot = (mem + ROT * (grp & 3)) & 7;

    u64 epoch;
    asm volatile("s_load_dwordx2 %0, %1, 0x0" : "=s"(epoch) : "s"(ws));
    u32 dw;
    asm volatile("s_load_dword %0, %1, 0x0" : "=s"(dw) : "s"(A_.gdv + 2 * b));
    const rsrc_t r_x = __builtin_amdgcn_make_buffer_rsrc((void*)gx, 0, 2048 * 512 * 2, 0x00020000);
    const rsrc_t r_ws = __builtin_amdgcn_make_buffer_rsrc((void*)ws, 0, WS_END, 0x00020000);
    const rsrc_t r_out = __builtin_amdgcn_make_buffer_rsrc((void*)A_.gout, 0, 2048 * 512 * 2, 0x00020000);
    const u32 ldsbase = (u32)(size_t)lds;
    auto mkdesc = [](const void* p, u32 bytes) { const u64 a = (u64)p; return (u32x4){(u32)a, (u32)(a >> 32) & 0xffffu, bytes, 0x00020000u}; };
    const u32x4 d_x = mkdesc(gx, 2048 * 512 * 2), d_w = mkdesc(A_.gwqkv, 512 * 1536 * 2), d_wo = mkdesc(A_.gwo, 512 * 512 * 2);
    const u32x4 d_k = mkdesc(A_.gbk, 2048 * 512 * 2), d_v = mkdesc(A_.gbv, 2048 * 512 * 2), d_sk = mkdesc(A_.gsk, 512 * 4 * 2);
    const u32x4 d_h = mkdesc(A_.ghk, 512 * 2048 * 2), d_x2 = mkdesc(ws + WS_X2, 2u << 20), d_ws = mkdesc(ws, WS_END);
    const u32 wb1k = ldsbase + wave * 1024, hb1k = ldsbase + hd * 1024, hb512 = ldsbase + hd * 512;
#define DMA_ASM(pre, mods, post) asm volatile("s_add_u32 m0, %0, %4\n\t" pre "buffer_load_dwordx4 %1, %2, %3 offen " mods "lds" post \
                     :: "s"(sbase), "v"(voff), "s"(rs), "s"(soff), "i"(IMM) : "memory", "scc")
    const u32 xmlo = hd == 0 ? ~0xFFu : ~0u;
    auto dma = [&](auto kind, auto imm, u32x4 rs, u32 sbase, u32 voff, u32 soff) {
        constexpr int K = decltype(kind)::value; constexpr int IMM = decltype(imm)::value;
        if constexpr (K == 0) DMA_ASM("s_nop 0\n\t", "", "");
        else if constexpr (K == 1) DMA_ASM("s_mov_b32 exec_hi, 0\n\t", "", "\n\ts_mov_b32 exec_hi, -1");
        else if constexpr (K == 2) {
            const u32 ml = xmlo;
            asm volatile("s_add_u32 m0, %0, %4\n\ts_mov_b32 exec_lo, %5\n\tbuffer_load_dwordx4 %1, %2, %3 offen lds\n\ts_mov_b32 exec_lo, -1"
                         :: "s"(sbase), "v"(voff), "s"(rs), "s"(soff), "i"(IMM), "s"(ml) : "memory", "scc");
        }
        else if constexpr (K == 4) DMA_ASM("s_nop 0\n\t", "sc1 ", "");
        else DMA_ASM("s_mov_b32 exec_hi, 0\n\t", "sc1 ", "\n\ts_mov_b32 exec_hi, -1");
    };
    auto ald4 = [&](u32x4& d, u32x4 rs, u32 voff) { asm volatile("buffer_load_dwordx4 %0, %1, %2, 0 offen" : "=v"(d) : "v"(voff), "s"(rs) : "memory"); };
    auto ald4s = [&](u32x4& d, u32 voff) { asm volatile("buffer_load_dwordx4 %0, %1, %2, 0 offen sc1" : "=v"(d) : "v"(voff), "s"(d_ws) : "memory"); };
    auto ald2s = [&](u32x2& d, u32 voff) { asm volatile("buffer_load_dwordx2 %0, %1, %2, 0 offen sc1" : "=v"(d) : "v"(voff), "s"(d_ws) : "memory"); };

    const int pr = lane >> 3, pc = lane & 7;
    const int xr0 = 8 * hd + pr, xr2 = 64 + 4 * hd + pr;
    const u32 xv0 = (u32)((T0 - 16 + xr0) * 1024 + ((pc ^ ((xr0 >> 1) & 7)) << 4));
    const u32 xv2 = (u32)((T0 - 16 + xr2) * 1024 + ((pc ^ ((xr2 >> 1) & 7)) << 4));
    const int wfw = ((xr0 >> 1) & 1) | (((xr0 >> 3) & 1) << 1);
    const u32 wv0 = (u32)(xr0 * 3072 + mem * 128 + ((((pc >> 1) ^ wfw) * 2 + (pc & 1)) << 4));
    auto issue_piece = [&](auto sc, auto ic) {
        constexpr int s = decltype(sc)::value, i = decltype(ic)::value;
        const u32 kp = (u32)((s + rot) & 7);
        constexpr int sb = (s & 3) * L_SLOT;
        if constexpr (i == 0) dma(IC<2>{}, IC<sb>{}, d_x, hb1k, xv0, kp * 128);
        else if constexpr (i == 1) dma(IC<0>{}, IC<sb + 4096>{}, d_x, hb1k, xv0 + 32768, kp * 128);
        else if constexpr (i == 2) dma(IC<1>{}, IC<sb + 8192>{}, d_x, hb512, xv2, kp * 128);
        else dma(IC<0>{}, IC<sb + 10240 + (i - 3) * 4096>{}, d_w, hb1k, wv0, kp * (64 * 3072) + ((i - 3) >> 1) * 1024 + ((i - 3) & 1) * (32 * 3072));
    };
    auto issue_wo = [&](auto ic) {
        constexpr int i = decltype(ic)::value;
        const int l2 = opq(tid) & 63, pr = l2 >> 3, pc = l2 & 7;
        const u32 wov = (u32)((8 * hd + pr) * 1024 + mem * 128 + ((((pc >> 1) ^ (((pr >> 1) & 1) | ((hd & 1) << 1))) * 2 + (pc & 1)) << 4));
        const u32 j = (u32)((i + 2 * grp) & 7);
        dma(IC<0>{}, IC<L_WO + (i & 8) * 4096>{}, d_wo, hb1k + j * 4096, wov, ((i & 8) + j) * 32768);
    };
    auto issue_kv = [&](int i) {
        const int l2 = opq(tid) & 63, pr = l2 >> 3, pc = l2 & 7, p = hd + 4 * (i & 1);
        const u32 cb = (u32)(mem * 128);
        if (i < 2) dma(IC<0>{}, IC<L_K>{}, d_k, hb1k + (i & 1) * 4096, (u32)((T0 - 8 + 8 * p + pr) * 1024) + cb + ((pc ^ p) << 4), 0);
        else if (i < 4) dma(IC<0>{}, IC<L_V>{}, d_v, hb1k + (i & 1) * 4096, (u32)((T0 + 8 * p + pr) * 1024) + cb + ((pc ^ p) << 4), 0);
        else if (hd == 0) dma(IC<0>{}, IC<L_K + 8192>{}, d_k, ldsbase, (u32)((T0 - 8 + 64 + pr) * 1024) + cb + (pc << 4), 0);
        else dma(IC<0>{}, IC<L_SK>{}, d_sk, ldsbase, (u32)(mem * 512 + l2 * 16), 0);
    };

    const int srow = T0 + 8 * mem + 2 * hd;
    u32x4 xrow[2];
    sfor<0, 9>([&](auto ic) { if constexpr ((decltype(ic)::value & 1) != R) issue_piece(IC<0>{}, ic); });
    sfor<0, 9>([&](auto ic) { if constexpr ((decltype(ic)::value & 1) != R) issue_piece(IC<1>{}, ic); });
    asm volatile("s_waitcnt lgkmcnt(0)" : "+s"(epoch), "+s"(dw));
    const u64 tag = mix64(epoch);
    const u32 tl = (u32)tag, th = (u32)(tag >> 32);
    if (R == 1 && wave == 7 && lane < 32) *(LAS u32x4*)(lds + (lane >> 3) * L_SLOT + (lane & 7) * 16) = (u32x4){0x3F803F80u, 0x3F803F80u, 0x3F803F80u, 0x3F803F80u};

    auto trr = [&](u32 off) { return __builtin_amdgcn_ds_read_tr16_b64_v4i16((LAS s16x4*)(lds + off)); };
    auto ldsA = [&](u32 off) { return *(LAS s16x8*)(lds + off); };
    auto flag_ok = [&](const u32x2& f) { return __all(f[0] == tl && f[1] == th); };
    auto spin_flags = [&](u32x2& f, u32 off) {
        _Pragma("unroll 1") for (int spin = 0; spin < SPIN_CAP; ++spin) {
            if (__builtin_expect(flag_ok(f), 1)) break;
            __builtin_amdgcn_s_sleep(2);
            asm volatile("buffer_load_dwordx2 %0, %1, %2, 0 offen sc1\n\ts_waitcnt vmcnt(0)" : "=&v"(f) : "v"(off), "s"(d_ws) : "memory");
        }
    };

    s16x8 Wq[16], Wm[16];
    f32x4 accM[5], accQ[5];
    const f32x4 zero4 = {0.f, 0.f, 0.f, 0.f};
    const s16x8 ones8 = {0x3F80, 0x3F80, 0x3F80, 0x3F80, 0x3F80, 0x3F80, 0x3F80, 0x3F80};
    s16x8 Ab[3][5];
    auto qkv_step = [&](auto lc, int st, const s16x8 (&A)[5], auto fill) {
        sfor<0, 5>([&](auto mc) {
            constexpr int mt = decltype(mc)::value;
            const f32x4 cm = st ? accM[mt] : zero4;
            accM[mt] = R ? mfma32(A[mt], Wm[st], cm) : mfma32(Wm[st], A[mt], cm);
            if constexpr (R == 0 && (mt > 0 || decltype(lc)::value == 0)) accQ[mt] = mfma32(Wq[st], A[mt], st ? accQ[mt] : zero4);
            fill(mc);
        });
    };

    float m1[5], r1[5]; f32x4 m4[5], r4[5], csK, csQ;
    auto load_tab = [&](bool cs) {
        const int l2 = opq(tid) & 63;
        u32 ta = L_TAB + (R ? (l2 >> 4) * 16 : (l2 & 15) * 4);
        pin(ta);
        if (R == 0 && cs) { csK = *(LAS f32x4*)(lds + L_CS + hd * 128 + (l2 >> 4) * 16); csQ = *(LAS f32x4*)(lds + L_CS + hd * 128 + 64 + (l2 >> 4) * 16); }
#pragma unroll
        for (int mt = 0; mt < 5; ++mt) {
            if (R == 0) { m1[mt] = *(LAS float*)(lds + ta + mt * 64); r1[mt] = *(LAS float*)(lds + ta + 320 + mt * 64); }
            else { m4[mt] = *(LAS f32x4*)(lds + ta + mt * 64); r4[mt] = *(LAS f32x4*)(lds + ta + 320 + mt * 64); }
        }
    };

    u32x4 hr[4];
    u32x2 flg;
    {
        const int li = lane & 15, lq = lane >> 4, tq = li >> 2, tp = lane & 3;
        const u32 xa0 = (u32)(li * 128 + ((lq ^ (li >> 1)) << 4));
        const u32 wa = (u32)((8 * lq + tq) * 128 + ((hd ^ (((tq >> 1) & 1) | ((lq & 1) << 1))) << 5) + tp * 8);
        u32 xa, wb, kw[11], vw[8];
        u32x4 s0v, skw;
        auto pf_addr = [&](int st) {
            const u32 sb = (u32)((st >> 1) & 3) * L_SLOT;
            xa = sb + (xa0 ^ ((st & 1) * 64)); wb = sb + 10240 + (st & 1) * 4096 + wa;
            asm volatile("" : "+v"(xa), "+v"(wb));
        };
        auto pf_part = [&](int st, int mt) {
            if (mt == 0) Wm[st] = cat8(trr(wb + (1 + R) * 8192), trr(wb + (1 + R) * 8192 + 512));
            Ab[st & 1][mt] = ldsA(xa + mt * 2048);
            if (R == 0 && mt == 1) Wq[st] = cat8(trr(wb), trr(wb + 512));
        };
        STAMP(15);
        WAIT_VM(R ? 5 : 4);
        BAR();
        STAMP(16);
        pf_addr(0);
#pragma unroll
        for (int mt = 0; mt < 5; ++mt) pf_part(0, mt);
        sfor<0, 16>([&](auto stc) {
            constexpr int st = decltype(stc)::value;
            if constexpr ((st & 1) && st < 15) {
                constexpr int s1 = (st >> 1) + 1;
                if constexpr (R == 0) {
                    if constexpr (s1 == 1) WAIT_VM(7);
                    if constexpr (s1 == 2) WAIT_VM(1);
                    if constexpr (s1 == F1B) WAIT_VM(0);
                } else {

                    constexpr int WN[8] = {0, 9, 9, 9, 9, 9, 10, 2};
                    WAIT_VM(WN[s1]);
                    if constexpr (s1 == 7) {
                        pin(s0v);
                        const int l2 = opq(tid) & 63, tr = hd * 20 + min(l2, 19), row = T0 - 16 + tr;
                        _Pragma("unroll 1") for (int spin = 0; spin < SPIN_CAP; ++spin) {
                            if (__builtin_expect(__all(row < 0 || (s0v[1] == tl && s0v[3] == th)), 1)) break;
                            __builtin_amdgcn_s_sleep(2);
                            ald4s(s0v, WS_ST0 + (u32)(max(row, 0) * 16));
                            WAIT_VM(0);
                            pin(s0v);
                        }
                        *(LAS u32*)(lds + L_TAB + tr * 4) = row < 0 ? 0u : s0v[0];
                        *(LAS u32*)(lds + L_TAB + 320 + tr * 4) = row < 0 ? 0x3F800000u : s0v[2];
                        asm volatile("s_waitcnt lgkmcnt(0)" ::: "memory");
                    }
                }
                RBAR();
                STAMP(16 + s1);
                if constexpr (s1 == F1B && R == 0) { if (tid == 0) __builtin_amdgcn_raw_buffer_store_b64((u32x2){tl, th}, r_ws, WS_F1 + (mem * 32 + grp) * 128, 0, 16); CFENCE(); }
                if constexpr (s1 == 5 && R == 1) { const int row = T0 - 16 + hd * 20 + min(opq(tid) & 63, 19); ald4s(s0v, WS_ST0 + (u32)(max(row, 0) * 16)); }
            }
            if (st < 15) pf_addr(st + 1);
            if constexpr (st == 14) load_tab(false);
            SB();
            qkv_step(IC<0>{}, st, Ab[st & 1], [&](auto mc) {
                constexpr int mt = decltype(mc)::value;
                if (st < 15) pf_part(st + 1, mt);
                if constexpr (R == 1) {
                    if constexpr (st == 0) { issue_piece(IC<2>{}, IC<2 * mt>{}); if constexpr (mt < 4) issue_piece(IC<2>{}, IC<2 * mt + 1>{}); }
                    if constexpr (st >= 1 && st <= 10 && ((st - 1) & 1) * 5 + mt < 9) issue_piece(IC<((st - 1) >> 1) + 3>{}, IC<((st - 1) & 1) * 5 + mt>{});
                    if constexpr (st == 11 && mt == 0) { const u32 ho = (u32)(b * 8192 + opq(tid) * 16); ald4(hr[2], d_h, ho); ald4(hr[3], d_h, ho + 16); }
                    if constexpr (st == 13 && mt == 0) { if (wave == 7) { const int l2 = opq(tid) & 63; ald2s(flg, WS_F1 + (u32)((l2 < 32 ? (b >> 5) * 32 + l2 : (l2 & 7) * 32 + grp) * 128)); } }

                } else {
                    if constexpr (st == 0) {
                        if constexpr (mt == 0) { const u32 xo = (u32)(srow * 1024 + (opq(tid) & 63) * 16); ald4(xrow[0], d_x, xo); ald4(xrow[1], d_x, xo + 1024); }
                        issue_kv(mt);
                    }
                    if constexpr (st == WOPF && mt < 4) {
                        const int l2 = opq(tid) & 63;
                        dma(IC<0>{}, IC<L_DUMMY>{}, d_wo, ldsbase, (u32)((l2 >> 3) * 1024 + (l2 & 7) * 16), (u32)(((grp & 3) * 128 + hd * 32 + mt * 8) * 1024 + mem * 128));
                    }
                    if constexpr (st == 11 && mt < 2) { const u32 ho = (u32)(b * 8192 + mt * 4096 + opq(tid) * 16); ald4(hr[2 * mt], d_h, ho); ald4(hr[2 * mt + 1], d_h, ho + 16); }
                }
                SB();
            });
            if constexpr (R == 0 && st == 2) {
                WAIT_VM(5);
                pin(xrow[0]); pin(xrow[1]);
                float s[2] = {0.f, 0.f}, q[2] = {0.f, 0.f};
#pragma unroll
                for (int i = 0; i < 4; ++i)
#pragma unroll
                    for (int k = 0; k < 2; ++k) { s[k] = dot2(xrow[k][i], 0x3F803F80u, s[k]); q[k] = dot2(xrow[k][i], xrow[k][i], q[k]); }
                auto ss = __builtin_amdgcn_permlane32_swap(__float_as_uint(s[0]), __float_as_uint(s[1]), false, false);
                auto qq = __builtin_amdgcn_permlane32_swap(__float_as_uint(q[0]), __float_as_uint(q[1]), false, false);
                float sv1 = rowsum16(__uint_as_float(ss[0]) + __uint_as_float(ss[1])), qv1 = rowsum16(__uint_as_float(qq[0]) + __uint_as_float(qq[1]));
                auto s2 = __builtin_amdgcn_permlane16_swap(__float_as_uint(sv1), __float_as_uint(sv1), false, false);
                auto q2 = __builtin_amdgcn_permlane16_swap(__float_as_uint(qv1), __float_as_uint(qv1), false, false);
                const float m = (__uint_as_float(s2[0]) + __uint_as_float(s2[1])) * (1.f / 512.f);
                const float r = __builtin_amdgcn_rsqf(fmaxf((__uint_as_float(q2[0]) + __uint_as_float(q2[1])) * (1.f / 512.f) - m * m, 0.f) + 1e-5f);
                const u32x4 sv = {__float_as_uint(m), tl, __float_as_uint(r), th};
                const int l2 = opq(tid) & 63;
                __builtin_amdgcn_raw_buffer_store_b128(sv, r_ws, (l2 & 31) == 0 ? WS_ST0 + (u32)((srow + (l2 >> 5)) * 16) : 0xFFFFFF00u, 0, 16);
                CFENCE();
                SB();
            }
            if constexpr (R == 0 && (st == UA || st == UB)) {
                const int l2 = opq(tid) & 63, tg = l2 & 7, cp = 8 * hd + (l2 >> 3);
                if constexpr (st == UA) {
                    skw = *(LAS u32x4*)(lds + L_SK + cp * 16);
                    u32 ka = L_K + tg * 1024 + ((((cp >> 2) ^ tg) & 7) << 4) + (cp & 3) * 4;
                    u32 kb = L_K + tg * 1024 + ((((cp >> 2) ^ (tg + 1)) & 7) << 4) + (cp & 3) * 4;
                    pin(ka); pin(kb);
#pragma unroll
                    for (int i = 0; i < 11; ++i) kw[i] = *(LAS u32*)(lds + ((i + 5 < 8) ? ka : kb) + (i + 5) * 128);
#pragma unroll
                    for (int i = 0; i < 8; ++i) vw[i] = *(LAS u32*)(lds + ka + (L_V - L_K) + i * 128);
                    SB();
                }
                constexpr bool HI = st == UB;
                auto ex = [&](u32 w) { return HI ? bfhi(w) : bflo(w); };
                const float a0 = bflo(skw[2 * HI]), a1 = bfhi(skw[2 * HI]), a2 = bflo(skw[2 * HI + 1]), a3 = bfhi(skw[2 * HI + 1]);
                float ua[8];
#pragma unroll
                for (int i = 0; i < 8; ++i) ua[i] = (ex(kw[i]) * a3 + ex(kw[i + 1]) * a2 + ex(kw[i + 2]) * a1 + ex(kw[i + 3]) * a0) * ex(vw[i]);
                const u32x4 oa = {pk2(ua[7], ua[6]), pk2(ua[5], ua[4]), pk2(ua[3], ua[2]), pk2(ua[1], ua[0])};
                __builtin_amdgcn_raw_buffer_store_b128(oa, r_ws, WS_UT + (u32)((mem * 64 + 2 * cp) * 4096 + (2040 - T0 - 8 * tg) * 2), HI * 4096, 16);
                __builtin_amdgcn_raw_buffer_store_b128((u32x4){SENT, SENT, SENT, SENT}, r_ws, (u32)((4 * grp + hd) * 16384 + mem * 2048 + HI * 1024 + l2 * 16), WS_O1, 16);
                CFENCE();
                SB();
            }
        });
    }
    STAMP(R ? 1 : 53);

    u32x4 uu;
    float csV;
    auto attention = [&](auto lc) {
        constexpr int LAYER = decltype(lc)::value;
        constexpr int SBASE = LAYER ? 32 : 28;
        const int lane = opq(tid) & 63, li = lane & 15, lq = lane >> 4;
        u32 ex = L_EX + hd * 2048 + lane * 8;
        pin(ex);
        u32 f1ok = 1;
        if constexpr (R == 0) {
            if constexpr (LAYER == 0) {
#pragma unroll
                for (int i = 0; i < 4; ++i) { csK[i] = bcast_row0(accM[0][i]); csQ[i] = bcast_row0(accQ[0][i]); }
                if (li == 0) { *(LAS f32x4*)(lds + L_CS + hd * 128 + lq * 16) = csK; *(LAS f32x4*)(lds + L_CS + hd * 128 + 64 + lq * 16) = csQ; }
            }
            const int par = li & 1;
            const bool cur0 = (4 * lq + par) <= li, cur1 = (4 * lq + par + 2) <= li;
            s16x4 Kb[5], Qb[5];
#pragma unroll
            for (int mt = 0; mt < 5; ++mt) {
                Kb[mt] = pack4((accM[mt] - m1[mt] * csK) * r1[mt]);
                if (mt) Qb[mt] = pack4(accQ[mt] - m1[mt] * csQ);
            }
            f32x4 Sc[5], Sp[5];
#pragma unroll
            for (int mt = 1; mt < 5; ++mt) { Sc[mt] = mfma16(Kb[mt], Qb[mt], zero4); Sp[mt] = mfma16(Kb[mt - 1], Qb[mt], zero4); }
            float s0[5], s1[5], mx[5];
#pragma unroll
            for (int mt = 1; mt < 5; ++mt) {
                const float sp0 = Sp[mt][0], sp1 = Sp[mt][1], sp2 = Sp[mt][2], sp3 = Sp[mt][3], c0 = Sc[mt][0], c1 = Sc[mt][1], c2 = Sc[mt][2], c3 = Sc[mt][3];
                float p0 = par ? sp1 : sp0, p1 = par ? sp3 : sp2, q0 = par ? c1 : c0, q1 = par ? c3 : c2;
                asm volatile("" : "+v"(p0), "+v"(p1), "+v"(q0), "+v"(q1));
                if (mt == 1 && grp == 0) { p0 = -3.0e38f; p1 = -3.0e38f; }
                s0[mt] = cur0 ? q0 : p0; s1[mt] = cur1 ? q1 : p1;
                mx[mt] = vmax(s0[mt], s1[mt]);
            }
#pragma unroll
            for (int mt = 1; mt < 5; ++mt) { auto r = __builtin_amdgcn_permlane32_swap(__float_as_uint(mx[mt]), __float_as_uint(mx[mt]), false, false); mx[mt] = vmax(__uint_as_float(r[0]), __uint_as_float(r[1])); }
#pragma unroll
            for (int mt = 1; mt < 5; ++mt) { auto r = __builtin_amdgcn_permlane16_swap(__float_as_uint(mx[mt]), __float_as_uint(mx[mt]), false, false); mx[mt] = vmax(__uint_as_float(r[0]), __uint_as_float(r[1])); }
#pragma unroll
            for (int mt = 1; mt < 5; ++mt) {
                const float sc = r1[mt] * (0.25f * 1.44269504088896341f);
                const float e0 = __builtin_amdgcn_exp2f((s0[mt] - mx[mt]) * sc), e1 = __builtin_amdgcn_exp2f((s1[mt] - mx[mt]) * sc);
                *(LAS u32x2*)(lds + ex + (mt - 1) * 512) = (u32x2){pk2(e0, e0), pk2(e1, e1)};
            }
            STAMP(SBASE + 2);
            BAR();
            STAMP(SBASE + 1);
            if constexpr (LAYER == 0) f1ok = __builtin_amdgcn_readfirstlane(*(LAS u32*)(lds + L_FLG));
        } else {
            if constexpr (LAYER == 0) {
                auto s = __builtin_amdgcn_permlane16_swap(__float_as_uint(accM[0][0]), __float_as_uint(accM[0][0]), false, false);
                auto t = __builtin_amdgcn_permlane32_swap(s[0], s[0], false, false);
                csV = __uint_as_float(t[0]);

                if (wave == 7) { WAIT_VM(0); pin(flg);
                 const u32 ok = flag_ok(flg); *(LAS u32*)(lds + L_FLG) = ok; }
            }
            const int par = li & 1;
            const bool cur0 = (4 * lq + par) <= li, cur1 = (4 * lq + par + 2) <= li;
            const u32 pm = par ? 0xffff0000u : 0x0000ffffu;
            u32 mC0 = cur0 ? pm : 0u, mP0 = cur0 ? 0u : pm, mC1 = cur1 ? pm : 0u, mP1 = cur1 ? 0u : pm;
            u32 oo = (LAYER ? WS_O2 : WS_O1) + (u32)((4 * grp + (lq & 1)) * 16384 + (4 * mem + hd) * 512 + (lq >> 1) * 256 + li * 16);
            asm volatile("" : "+v"(mC0), "+v"(mP0), "+v"(mC1), "+v"(mP1), "+v"(oo));
            s16x4 Vb[5];
#pragma unroll
            for (int mt = 0; mt < 5; ++mt) { Vb[mt] = pack4((accM[mt] - m4[mt] * csV) * r4[mt]); pin(Vb[mt]); }
            STAMP(SBASE);
            BAR();
            STAMP(SBASE + 1);
            u32x2 pw[4];
#pragma unroll
            for (int j = 0; j < 4; ++j) pw[j] = *(LAS u32x2*)(lds + ex + j * 512);
            if constexpr (LAYER == 0) {
                f1ok = __builtin_amdgcn_readfirstlane(*(LAS u32*)(lds + L_FLG));
                if (f1ok) ald4s(uu, WS_UT + (u32)(b * 8192 + opq(tid) * 16));
            }
            SB();
            f32x4 O[4], L[4];
#pragma unroll
            for (int j = 0; j < 4; ++j) {
                const s16x8 P = __builtin_bit_cast(s16x8, (u32x4){pw[j][0] & mC0, pw[j][1] & mC1, pw[j][0] & mP0, pw[j][1] & mP1});
                L[j] = mfma32(ones8, P, zero4);
                O[j] = mfma32(cat8(Vb[j + 1], Vb[j]), P, zero4);
            }
            u32 ow[4][2];
#pragma unroll
            for (int j = 0; j < 4; ++j) {
                const float inv = __builtin_amdgcn_rcpf(L[j][0]);
                ow[j][0] = nosent(pk2(O[j][0] * inv, O[j][1] * inv)); ow[j][1] = nosent(pk2(O[j][2] * inv, O[j][3] * inv));
            }
#pragma unroll
            for (int j = 0; j < 4; j += 2) {
                auto r0 = __builtin_amdgcn_permlane16_swap(ow[j][0], ow[j + 1][0], false, false);
                auto r1 = __builtin_amdgcn_permlane16_swap(ow[j][1], ow[j + 1][1], false, false);
                __builtin_amdgcn_raw_buffer_store_b128((u32x4){r0[0], r1[0], r0[1], r1[1]}, r_ws, oo, j * 16384, 16);
            }
            CFENCE();
        }
        if constexpr (LAYER == 0 && R == 0) { if (f1ok) ald4s(uu, WS_UT + (u32)(b * 8192 + opq(tid) * 16)); }
        SB();
        return f1ok;
    };
    const u32 f1ok = attention(IC<0>{});
    {
        const u32x4 sv = {SENT, SENT, SENT, SENT};
        const int t2 = opq(tid);
        const u32 tb = (u32)((4 * grp + hd) * 16384 + mem * 2048 + R * 1024 + (t2 & 63) * 16);
        __builtin_amdgcn_raw_buffer_store_b128(sv, r_ws, tb, WS_O2, 16);
        __builtin_amdgcn_raw_buffer_store_b128(sv, r_ws, tb, WS_X2, 16);
        __builtin_amdgcn_raw_buffer_store_b128(sv, r_ws, (u32)(b * 8192 + t2 * 16), WS_Y2, 16);
        CFENCE();
        SB();
    }
    STAMP(2);

    const int TI = 4 * grp + hd;
    u32x4 of[8];
    auto load_o = [&](u32 ws_o) {
        const int lane = opq(tid) & 63;
#pragma unroll
        for (int i = 0; i < 8; ++i) of[i] = __builtin_amdgcn_raw_buffer_load_b128(r_ws, ws_o + (u32)(TI * 16384 + R * 8192 + lane * 16) + (i & 3) * 1024, (i >> 2) * 4096, 16);
    };
    u32 xw[8], yw[8];
    u32 woa[4];
    auto resid = [&](int n) { return R ? zero4 : (f32x4){bflo(xw[2 * n]), bfhi(xw[2 * n]), bflo(xw[2 * n + 1]), bfhi(xw[2 * n + 1])}; };
    s16x8 F[8][4];
    auto wo_addr = [&]() {
        const int l2 = opq(tid) & 63, lq = l2 >> 4, tq = (l2 & 15) >> 2, tp = l2 & 3;
#pragma unroll
        for (int n = 0; n < 4; ++n) woa[n] = L_WO + (u32)(R * 32768 + (8 * lq + tq) * 128 + ((n ^ (((lq & 1) << 1) | (tq >> 1))) << 5) + tp * 8);
    };
    auto load_F = [&](int ks) {
#pragma unroll
        for (int n = 0; n < 4; ++n) F[ks][n] = cat8(trr(woa[n] + ks * 4096), trr(woa[n] + ks * 4096 + 512));
    };

    {
        const int t2 = opq(tid), lane = t2 & 63, li = lane & 15, lq = lane >> 4;
        constexpr u32 OFFS = 0xEDA85410u;
        {
            const int j = t2 & 255;
            {
                if constexpr (R == 0) { if (f1ok) WAIT_VM(4); else WAIT_VM(3); }
                else WAIT_VM(5);
                u32 cb = L_HC + R * 34816 + (j + 2) * 16;
                pin(cb);
#pragma unroll
                for (int ch = R; ch < 2; ++ch) {
                    pin(hr[2 * ch]); pin(hr[2 * ch + 1]);
                    const u32 d[8] = {hr[2 * ch][0], hr[2 * ch][1], hr[2 * ch][2], hr[2 * ch][3], hr[2 * ch + 1][0], hr[2 * ch + 1][1], hr[2 * ch + 1][2], hr[2 * ch + 1][3]};
                    u32 e[7];
#pragma unroll
                    for (int i = 0; i < 7; ++i) e[i] = __builtin_amdgcn_alignbit(d[i + 1], d[i], 16);
                    if (ch > R) { cb += 34816; pin(cb); }
#pragma unroll
                    for (int p = (R ? 4 : 0); p < ((ch && !R) ? 4 : 8); ++p) {
                        const u32 po = (272 * p + ((OFFS >> (4 * p)) & 15)) * 16;
                        const int h = p >> 1;
                        const u32x4 w = (p & 1) ? (u32x4){e[h], e[h + 1], e[h + 2], e[h + 3]} : (u32x4){d[h], d[h + 1], d[h + 2], d[h + 3]};
                        *(LAS u32x4*)(lds + cb + po) = w;
                    }
                }
            }
            if constexpr (R == 0) {
                if (hd == 0) {
                    u32 hh[4];
#pragma unroll
                    for (int i = 0; i < 4; ++i) { const u32 a = __builtin_amdgcn_readfirstlane(hr[0][i]), c = __builtin_amdgcn_readfirstlane(hr[2][i]); hh[i] = (lane & 16) ? c : a; }
                    if (lane < 32) {
                        const int p = lane & 7;
                        const u32 po = (272 * p + ((OFFS >> (4 * p)) & 15)) * 16;
                        const unsigned __int128 hv = ((unsigned __int128)(((u64)hh[3] << 32) | hh[2]) << 64) | (((u64)hh[1] << 32) | hh[0]);
                        const unsigned __int128 sv = (p == 0 || !(lane & 8)) ? (unsigned __int128)0 : (hv << (16 * (8 - p)));
                        const u32x4 w = {(u32)sv, (u32)(sv >> 32), (u32)(sv >> 64), (u32)(sv >> 96)};
                        *(LAS u32x4*)(lds + L_HC + (lane >> 4) * 34816 + po + ((lane >> 3) & 1) * 16) = w;
                    }
                }
            }
            if (hd == 1 && lane < 30) *(LAS u32x4*)(lds + L_UR + R * 4576 + 4096 + lane * 16) = (u32x4){0, 0, 0, 0};
            STAMP(3);
            if (!f1ok) {
                const u32 fo = WS_F1 + (u32)(((lane & 8) ? (lane & 7) * 32 + grp : (b >> 5) * 32 + 31 - hd * 8 - (lane & 7)) * 128);
                flg = (u32x2){0u, 0u};
                spin_flags(flg, fo);
                ald4s(uu, WS_UT + (u32)(b * 8192 + t2 * 16));
            }
            if (f1ok) WAIT_VM(R ? 5 : 3); else WAIT_VM(0);
            pin(uu);
            *(LAS u32x4*)(lds + L_UR + R * 4576 + j * 16) = uu;
        }
        BAR();
        STAMP(4);
        const int ch = hd >> 1, e0 = (hd & 1) * 4;
        const int p = (li + 1) & 7;
        u32 aA = L_HC + ch * 34816 + (272 * p + ((OFFS >> (4 * p)) & 15) + ((li + 1) >> 3) + lq + 4 * e0) * 16;
        u32 aB = L_UR + ch * 4576 + (254 - 224 - 2 * li + lq + 4 * e0) * 16;
        asm volatile("" : "+v"(aA), "+v"(aB));
        f32x4 y[4];
        u32 ooff = WS_O1 + (u32)(TI * 16384 + R * 8192 + lane * 16);
        {
            constexpr ConvSched S(R);
            s16x8 A[4][8], B[4][8];
            auto loads_at = [&](auto pc) {
                constexpr int p = decltype(pc)::value;
                sfor<0, 64>([&](auto ic) {
                    constexpr int i = decltype(ic)::value, e = i >> 4, f = i & 7;
                    if constexpr (S.slot[i] == p) {
                        if constexpr (i & 8) B[e][f] = ldsA(aB + 64 * e + 512 * (7 - f)); else A[e][f] = ldsA(aA + 64 * (8 * f + e));
                    }
                });
            };
            loads_at(IC<-1>{});
            SB();
            sfor<0, 72>([&](auto pc) {
                constexpr int p = decltype(pc)::value, e = p / 18, f = S.f[p % 18], i = S.i[p % 18], g = S.n[i] - f;
                y[i] = mfma32(A[e][f], B[e][g], (e == 0 && f == 0) ? zero4 : y[i]);
                loads_at(pc);
                if constexpr (p < 24 && (p % 3) == 1) issue_wo(IC<R * 8 + p / 3>{});
                if constexpr (p == 54) WAIT_VM(0);
                if constexpr (p >= 55 && p < 71 && (p & 1)) {
                    constexpr int i = (p - 55) >> 1;
                    ald4o<(i & 3) * 1024>(of[i], ooff + (i >> 2) * 4096, d_ws);
                }
                SB();
            });
        }
        STAMP(14);
        RBAR();
        if (R == 0 && tid == 0) __builtin_amdgcn_raw_buffer_store_b64((u32x2){tl, th}, r_ws, WS_F2 + (grp * 8 + mem) * 128, 0, 16);
        {
            constexpr int N0[4] = {0, 3, 4, 7};
            u32 ra = L_HC + hd * 8192 + lane * 16;
            asm volatile("" : "+v"(ra));
#pragma unroll
            for (int i = 0; i < 4; ++i) *(LAS f32x4*)(lds + ra + (N0[i] ^ R) * 1024) = y[i];
        }
        SB();
        if constexpr (R == 0) {
#pragma unroll
            for (int n = 0; n < 4; ++n) { const u32x2 w = __builtin_amdgcn_raw_buffer_load_b64(r_x, (u32)((TI * 16 + li) * 1024 + mem * 128 + lq * 8) + n * 32, 0, 0); xw[2 * n] = w[0]; xw[2 * n + 1] = w[1]; }
        }
        wo_addr();
        SB();
        STAMP(37);
        BAR();
        STAMP(38);
        {
            const float D0 = bflo(dw), D1 = bfhi(dw);
            u32 ra = L_HC + wave * 1024 + lane * 16;
            const int tb = 256 * wave + 16 * li + 4 * lq;
            u32 ru = L_UR + (2044 - tb) * 2;
            asm volatile("" : "+v"(ra), "+v"(ru));
            const f32x4 y0 = *(LAS f32x4*)(lds + ra), y1 = *(LAS f32x4*)(lds + ra + 8192), y2 = *(LAS f32x4*)(lds + ra + 16384), y3 = *(LAS f32x4*)(lds + ra + 24576);
            const u32x2 ua = *(LAS u32x2*)(lds + ru), ub = *(LAS u32x2*)(lds + ru + 4576);
            load_F(0); load_F(1);
            SB();
            const f32x4 ya = y0 + y1, yb = y2 + y3;
            const u32x4 w = {nosent(pk2(ya[0] + bfhi(ua[1]) * D0, yb[0] + bfhi(ub[1]) * D1)), nosent(pk2(ya[1] + bflo(ua[1]) * D0, yb[1] + bflo(ub[1]) * D1)),
                             nosent(pk2(ya[2] + bfhi(ua[0]) * D0, yb[2] + bfhi(ub[0]) * D1)), nosent(pk2(ya[3] + bflo(ua[0]) * D0, yb[3] + bflo(ub[0]) * D1))};
            __builtin_amdgcn_raw_buffer_store_b128(w, r_ws, WS_Y2 + (u32)(b * 8192 + tb * 4), 0, 16);
        }
        SB();
        STAMP(5);
        WAIT_VM(1);
        STAMP(40);

        u32 f2oth = 0;
        _Pragma("unroll 1") for (int attempt = 0; attempt < 64; ++attempt) {
            sfor<0, 8>([&](auto ic) { pin(of[decltype(ic)::value]); });
            u32 m0 = 0, m1 = 0;
#pragma unroll
            for (int ks = 0; ks < 8; ++ks) {
                load_F((ks + 2) & 7);
                if (R == 1 && ks == 4 && wave == 7) {
                    const int pb = 32 * mem + (lane & 31);
                    f2oth = WS_F2 + (u32)((lane < 32 ? ((pb & 7) * 4 + (pb >> 6)) * 8 + ((pb >> 3) & 7) : max(grp * 8 - 8, 0) + (lane & 15)) * 128);
                    ald2s(flg, f2oth);
                }
                SB();
#pragma unroll
                for (int n = 0; n < 4; ++n) accM[n] = mfma32(F[ks][n], __builtin_bit_cast(s16x8, of[ks]), ks ? accM[n] : resid(n));
                m0 = max(max(m0, of[ks][0]), of[ks][1]); m1 = max(max(m1, of[ks][2]), of[ks][3]);
                SB();
            }
            if (__builtin_expect(__all(max(m0, m1) != SENT), 1)) break;
            __builtin_amdgcn_s_sleep(4);
            WAIT_VM(0);
            sfor<0, 8>([&](auto ic) { constexpr int i = decltype(ic)::value; ald4o<(i & 3) * 1024>(of[i], ooff + (i >> 2) * 4096, d_ws); });
            WAIT_VM(0);
        }
        STAMP(41);
        if constexpr (R == 1) { if (wave == 7) { WAIT_VM(0); pin(flg); spin_flags(flg, f2oth); } }
    }

    auto load_y = [&]() {
        const int l2 = opq(tid) & 63;
#pragma unroll
        for (int i = 0; i < 8; ++i)
            yw[i] = __builtin_amdgcn_raw_buffer_load_b32(r_ws, WS_Y2 + (u32)((32 * mem + 2 * (l2 >> 4)) * 8192 + (TI * 16 + (l2 & 15)) * 4), (8 * (i >> 1) + (i & 1)) * 8192, 16);
    };
    auto pub_off = [&](u32 ws_st) {
        const int lane = opq(tid) & 63;
        return (lane & 16) ? 0xFFFFFF00u : ws_st + (u32)((TI * 8 + mem) * 256 + (lane & 15) * 16 + (lane >> 5) * 8);
    };
    auto pub_stat = [&](u32 off, u32 tg, float s, float q) {
        auto r = __builtin_amdgcn_permlane32_swap(__float_as_uint(s), __float_as_uint(q), false, false);
        const float v = __uint_as_float(r[0]) + __uint_as_float(r[1]);
        auto t = __builtin_amdgcn_permlane16_swap(__float_as_uint(v), __float_as_uint(v), false, false);
        __builtin_amdgcn_raw_buffer_store_b64((u32x2){__float_as_uint(__uint_as_float(t[0]) + __uint_as_float(t[1])), tg}, r_ws, off, 0, 16);
    };
    auto stat_off = [&](u32 ws_st, int ti) { const int l2 = opq(tid) & 63; return ws_st + (u32)(ti * 2048 + (l2 >> 4) * 512 + (l2 & 15) * 16); };
    auto fin_stat = [&](u32x4& a, u32x4& c, u32 off, float& mean, float& rstd) {
        _Pragma("unroll 1") for (int spin = 0; spin < SPIN_CAP; ++spin) {
            if (__builtin_expect(__all(((a[1] ^ tl) | (a[3] ^ th) | (c[1] ^ tl) | (c[3] ^ th)) == 0), 1)) break;
            __builtin_amdgcn_s_sleep(1);
            asm volatile("buffer_load_dwordx4 %0, %2, %3, 0 offen sc1\n\tbuffer_load_dwordx4 %1, %2, %3, 0 offen offset:256 sc1\n\ts_waitcnt vmcnt(0)"
                         : "=&v"(a), "=&v"(c) : "v"(off), "s"(d_ws) : "memory");
        }
        auto r = __builtin_amdgcn_permlane32_swap(__float_as_uint(__uint_as_float(a[0]) + __uint_as_float(c[0])), __float_as_uint(__uint_as_float(a[2]) + __uint_as_float(c[2])), false, false);
        const float v = __uint_as_float(r[0]) + __uint_as_float(r[1]);
        auto t = __builtin_amdgcn_permlane16_swap(__float_as_uint(v), __float_as_uint(v), false, false);
        const u32 v2 = __float_as_uint(__uint_as_float(t[0]) + __uint_as_float(t[1]));
        auto u = __builtin_amdgcn_permlane32_swap(v2, v2, false, false);
        const float ts = __uint_as_float(u[0]), tq = __uint_as_float(u[1]);
        mean = ts * (1.f / 512.f);
        rstd = __builtin_amdgcn_rsqf(fmaxf(tq * (1.f / 512.f) - mean * mean, 0.f) + 1e-5f);
    };
    auto regroup = [&](u32 (&w)[8]) {
#pragma unroll
        for (int j = 0; j < 2; ++j) {
            auto r0 = __builtin_amdgcn_permlane16_swap(w[4 * j], w[4 * j + 2], false, false);
            auto r1 = __builtin_amdgcn_permlane16_swap(w[4 * j + 1], w[4 * j + 3], false, false);
            w[4 * j] = r0[0]; w[4 * j + 1] = r1[0]; w[4 * j + 2] = r0[1]; w[4 * j + 3] = r1[1];
        }
    };
    auto wo_finish = [&](auto lc) {
        constexpr int LAYER = decltype(lc)::value;
        const int lane = opq(tid) & 63, li = lane & 15, lq = lane >> 4;
        u32 wx = L_WX + hd * 4096 + lane * 16;
        pin(wx);
        u32 po = 0, so = 0, tg = 0, oa = 0;
        if constexpr (R == 1) {
#pragma unroll
            for (int n = 0; n < 4; ++n) *(LAS f32x4*)(lds + wx + n * 1024) = accM[n];
        } else {
            po = pub_off(LAYER ? WS_ST3 : WS_ST1); so = stat_off(LAYER ? WS_ST3 : WS_ST1, TI); tg = lane < 32 ? tl : th;
            const int c = 2 * (lq & 1) + (lq >> 1);
            oa = LAYER ? (u32)((TI * 16 + li) * 1024 + mem * 128 + c * 16) : WS_X2 + (u32)((TI * 8 + mem) * 2048 + li * 128 + ((c ^ (li >> 1)) << 4));
            pin(po); pin(so); pin(tg); pin(oa);
        }
        BAR();
        STAMP(LAYER ? 25 : 42);
        if constexpr (R == 0) {
            f32x4 t[4];
#pragma unroll
            for (int n = 0; n < 4; ++n) t[n] = *(LAS f32x4*)(lds + wx + n * 1024);
            SB();
            float s = 0.f, q = 0.f, s2 = 0.f, q2 = 0.f;
#pragma unroll
            for (int n = 0; n < 4; ++n) {
                const f32x4 v = accM[n] + t[n];
                const u32 w0 = pk2(v[0], v[1]), w1 = pk2(v[2], v[3]);
                xw[2 * n] = w0; xw[2 * n + 1] = w1;
                s = dot2(w0, 0x3F803F80u, s); q = dot2(w0, w0, q); s2 = dot2(w1, 0x3F803F80u, s2); q2 = dot2(w1, w1, q2);
            }
            pub_stat(po, tg, s + s2, q + q2);
            CFENCE();
            if constexpr (!LAYER) load_y();
            regroup(xw);
            SB();
            STAMP(LAYER ? 46 : 43);
            WAIT_VM(0);
            STAMP(LAYER ? 26 : 44);
            if constexpr (!LAYER) {
#pragma unroll
                for (int i = 0; i < 8; ++i) pin(yw[i]);
            }
            u32x4 a, c;
            ald4s(a, so); ald4s(c, so + 256);
            if constexpr (!LAYER) {
                _Pragma("unroll 1") for (int spin = 0; spin < SPIN_CAP; ++spin) {
                    if (__builtin_expect(__all(max(max(max(yw[0], yw[1]), max(yw[2], yw[3])), max(max(yw[4], yw[5]), max(yw[6], yw[7]))) != SENT), 1)) break;
                    __builtin_amdgcn_s_sleep(1);
                    CFENCE();
                    load_y();
                }
                regroup(yw);
            }
            f32x2 xa[8], ya[8], xy[8];
#pragma unroll
            for (int i = 0; i < 8; ++i) {
                xa[i] = (f32x2){bflo(xw[i]), bfhi(xw[i])}; ya[i] = (f32x2){bflo(yw[i]), bfhi(yw[i])}; xy[i] = xa[i] * ya[i];
                pin(xa[i]); pin(ya[i]); pin(xy[i]);
            }
            WAIT_VM(0);
            pin(a); pin(c);
            float mean, rstd;
            fin_stat(a, c, so, mean, rstd);
            STAMP(LAYER ? 11 : 6);
            const float nm = -mean * rstd;
#pragma unroll
            for (int i = 0; i < 8; ++i) {
                const f32x2 o = xy[i] * rstd + (ya[i] * nm + xa[i]);
                xw[i] = pk2(o[0], o[1]);
                if (!LAYER) xw[i] = nosent(xw[i]);
            }
#pragma unroll
            for (int j = 0; j < 2; ++j) {
                const u32x4 o = {xw[4 * j], xw[4 * j + 1], xw[4 * j + 2], xw[4 * j + 3]};
                if constexpr (LAYER) __builtin_amdgcn_raw_buffer_store_b128(o, r_out, oa + j * 64, 0, OUT_AUX);
                else __builtin_amdgcn_raw_buffer_store_b128(o, r_ws, oa ^ (j * 64), 0, 16);
            }
            if constexpr (!LAYER) {
                float s = 0.f, q = 0.f, s2 = 0.f, q2 = 0.f;
#pragma unroll
                for (int i = 0; i < 8; i += 2) { s = dot2(xw[i], 0x3F803F80u, s); q = dot2(xw[i], xw[i], q); s2 = dot2(xw[i + 1], 0x3F803F80u, s2); q2 = dot2(xw[i + 1], xw[i + 1], q2); }
                pub_stat(po + (WS_ST2 - WS_ST1) * !(lane & 16), tg, s + s2, q + q2);
                CFENCE();
                regroup(xw);
#pragma unroll
                for (int i = 0; i < 8; ++i) { pin(xw[i]); pin(yw[i]); }
                STAMP(48);
                WAIT_VM(0);
            }
        }
    };
    wo_finish(IC<0>{});
    RBAR();
    STAMP(7);

    {
        const int l3 = opq(tid) & 63;
        const u32 g0 = (u32)((4 * grp - 1 + (hd >> 1)) * 16384 + (hd & 1) * 1024 + l3 * 16);
        const u32 g2 = (u32)((4 * grp + 3) * 16384 + hd * 512 + l3 * 16);
        auto x2_piece = [&](auto ic) {
            constexpr int i = decltype(ic)::value, s = i / 3, p = i % 3;
            const u32 kp = (u32)((s + rot) & 7);
            if constexpr (p == 0) dma(IC<4>{}, IC<L_CX + s * 10240>{}, d_x2, hb1k, g0, kp * 2048);
            else if constexpr (p == 1) dma(IC<4>{}, IC<L_CX + s * 10240 + 4096>{}, d_x2, hb1k, g0 + 32768, kp * 2048);
            else dma(IC<5>{}, IC<L_CX + s * 10240 + 8192>{}, d_x2, hb512, g2, kp * 2048);
        };
        u32 va = L_CX + hd * 1024 + l3 * 16, vq = L_CX + 8192 + hd * 512 + (l3 & 31) * 16 + (l3 >> 5) * 10240;
        pin(va); pin(vq);
        u32x4 vr[5];
        auto ver_read = [&](auto cc) {
            constexpr int c = decltype(cc)::value;
            u32 a = va + c * 20480, q = vq + c * 20480;
            pin(a); pin(q); CFENCE();
            vr[0] = *(LAS u32x4*)(lds + a); vr[1] = *(LAS u32x4*)(lds + a + 4096); vr[2] = *(LAS u32x4*)(lds + a + 10240); vr[3] = *(LAS u32x4*)(lds + a + 14336);
            vr[4] = *(LAS u32x4*)(lds + q);
        };
        auto ver_check = [&](auto cc) {
            constexpr int c = decltype(cc)::value;
            _Pragma("unroll 1") for (int spin = 0; spin < SPIN_CAP; ++spin) {
                u32 m0 = 0, m1 = 0;
#pragma unroll
                for (int i = 0; i < 5; ++i) { m0 = max(max(m0, vr[i][0]), vr[i][1]); m1 = max(max(m1, vr[i][2]), vr[i][3]); }
                if (__builtin_expect(__all(max(m0, m1) != SENT), 1)) break;
                __builtin_amdgcn_s_sleep(1);
                sfor<6 * c, 6 * c + 6>([&](auto ic) { x2_piece(ic); });
                WAIT_VM(0);
                ver_read(cc);
            }
        };
        const u32 xb0 = (u32)((l3 & 15) * 128 + (((l3 >> 4) ^ ((l3 & 15) >> 1)) << 4));
        if constexpr (R == 1) {
            STAMP(39);
            sfor<0, 12>([&](auto ic) { x2_piece(ic); });
            WAIT_VM(6);
            STAMP(8);
            ver_read(IC<0>{});
            ver_check(IC<0>{});
        } else {
            u32x4 sa, sc, ha, hc;
            const u32 so = stat_off(WS_ST2, 4 * grp + hd);
            const bool halo = hd == 0 && grp > 0;
            ald4s(sa, so); ald4s(sc, so + 256);
            if (halo) { ald4s(ha, so - 2048); ald4s(hc, so - 2048 + 256); }
            WAIT_VM(0);
            pin(sa); pin(sc);
            float mean, rstd;
            fin_stat(sa, sc, so, mean, rstd);
            if (l3 < 16) { *(LAS float*)(lds + L_TAB + 64 + hd * 64 + l3 * 4) = mean; *(LAS float*)(lds + L_TAB + 320 + 64 + hd * 64 + l3 * 4) = rstd; }
            if (halo) {
                pin(ha); pin(hc);
                fin_stat(ha, hc, so - 2048, mean, rstd);
                if (l3 < 16) { *(LAS float*)(lds + L_TAB + l3 * 4) = mean; *(LAS float*)(lds + L_TAB + 320 + l3 * 4) = rstd; }
            }
        }
        BAR();
        STAMP(13);
        u32 xa;
        auto pf_addr = [&](int st) { xa = L_CX + (st >> 1) * 10240 + (xb0 ^ ((st & 1) * 64)); pin(xa); };
        pf_addr(0);
#pragma unroll
        for (int mt = 0; mt < 5; ++mt) Ab[0][mt] = ldsA(xa + mt * 2048);
        sfor<0, 16>([&](auto stc) {
            constexpr int st = decltype(stc)::value;
            if constexpr (R == 1) {
                if constexpr (st == 2) { WAIT_VM(10); ver_read(IC<1>{}); }
                if constexpr (st == 3) ver_check(IC<1>{});
                if constexpr (st == 5) { WAIT_VM(6); ver_read(IC<2>{}); }
                if constexpr (st == 6) { ver_check(IC<2>{}); WAIT_VM(0); ver_read(IC<3>{}); }
                if constexpr (st == 7) ver_check(IC<3>{});
            }
            if constexpr (st == 3) { RBAR(); STAMP(49); }
            if constexpr (st == 7) { RBAR(); STAMP(50); CSTAMP(60); }
            if (st < 15) pf_addr(st + 1);
            if constexpr (st == 14) load_tab(true);
            SB();
            qkv_step(IC<1>{}, st, Ab[st & 1], [&](auto mc) {
                constexpr int mt = decltype(mc)::value;
                if (st < 15) Ab[(st + 1) & 1][mt] = ldsA(xa + mt * 2048);
                if constexpr (R == 1 && 12 + 5 * st + mt < 24) x2_piece(IC<12 + 5 * st + mt>{});
                SB();
            });
        });
    }
    STAMP(R ? 9 : 52);
    CSTAMP(61);
    attention(IC<1>{});
    STAMP(27);

    {
        wo_addr();
#pragma unroll
        for (int ks = 0; ks < 8; ++ks) load_F(ks);
        SB();
        WAIT_VM(0);
        RBAR();
        STAMP(10);
        load_o(WS_O2);
        SB();
        _Pragma("unroll 1") for (int spin = 0; spin < 64; ++spin) {
            u32 m0 = 0, m1 = 0;
#pragma unroll
            for (int ks = 0; ks < 8; ++ks) {
#pragma unroll
                for (int n = 0; n < 4; ++n) accM[n] = mfma32(F[ks][n], __builtin_bit_cast(s16x8, of[ks]), ks ? accM[n] : resid(n));
                m0 = max(max(m0, of[ks][0]), of[ks][1]); m1 = max(max(m1, of[ks][2]), of[ks][3]);
                SB();
            }
            if (__builtin_expect(__all(max(m0, m1) != SENT), 1)) break;
            _Pragma("unroll 1") for (int sp2 = 0; sp2 < SPIN_CAP; ++sp2) {
                CFENCE();
                __builtin_amdgcn_s_sleep(1);
                const u32 pv = __builtin_amdgcn_raw_buffer_load_b32(r_ws, WS_O2 + (u32)(TI * 16384 + R * 8192 + (opq(tid) & 15) * 512 + 508), 0, 16);
                if (__all(pv != SENT)) break;
            }
            CFENCE();
            load_o(WS_O2);
        }
        SB();
        STAMP(45);
    }
    wo_finish(IC<1>{});
    if constexpr (R == 1) {
        if (b == 0 && wave == 7) {
            const int l3 = opq(tid) & 63;
#pragma unroll
            for (int i = 0; i < 4; ++i) { flg = (u32x2){0u, 0u}; spin_flags(flg, WS_F1 + (u32)((i * 64 + l3) * 128)); }
            if (l3 == 0) __builtin_amdgcn_raw_buffer_store_b64(__builtin_bit_cast(u32x2, epoch + 1), r_ws, WS_EPOCH, 0, 16);
        }
    }
}

__global__ void __launch_bounds__(512, 2)
fused_kernel(const u16* __restrict__ gx, const u16* __restrict__ gwqkv, const u16* __restrict__ gwo,
             const u16* __restrict__ gbk, const u16* __restrict__ gbv, const u16* __restrict__ gsk,
             const u16* __restrict__ ghk, const u16* __restrict__ gdv, u16* __restrict__ gout, char* __restrict__ ws) {
    __shared__ __attribute__((aligned(16))) char lds_[L_TOTAL];
    typedef u32 u32x16 __attribute__((ext_vector_type(16)));
    u32x16 ka; u32x4 kb;
    {
        auto kp = __builtin_amdgcn_kernarg_segment_ptr();
        asm volatile("s_load_dwordx16 %0, %2, 0x0\n\ts_load_dwordx4 %1, %2, 0x40\n\ts_waitcnt lgkmcnt(0)" : "=&s"(ka), "=&s"(kb) : "s"(kp) : "memory");
    }
    auto mkp = [](u32 lo, u32 hi) { return (char*)(((u64)hi << 32) | lo); };
    const Args a = {(const u16*)mkp(ka[0], ka[1]), (const u16*)mkp(ka[2], ka[3]), (const u16*)mkp(ka[4], ka[5]), (const u16*)mkp(ka[6], ka[7]), (const u16*)mkp(ka[8], ka[9]),
                    (const u16*)mkp(ka[10], ka[11]), (const u16*)mkp(ka[12], ka[13]), (const u16*)mkp(ka[14], ka[15]), (u16*)mkp(kb[0], kb[1]), mkp(kb[2], kb[3])};
    const int wave = __builtin_amdgcn_readfirstlane(threadIdx.x >> 6);
    if (wave < 4) body<0>(a, (LAS char*)lds_, wave); else body<1>(a, (LAS char*)lds_, wave);
}

extern "C" void kernel_launch(void* const* d_in, const int* in_sizes, int n_in,
                              void* d_out, int out_size, void* d_ws, size_t ws_size,
                              hipStream_t stream) {
    fused_kernel<<<256, 512, 0, stream>>>((const u16*)d_in[0], (const u16*)d_in[1], (const u16*)d_in[2],
                                          (const u16*)d_in[4], (const u16*)d_in[5], (const u16*)d_in[6],
                                          (const u16*)d_in[7], (const u16*)d_in[8], (u16*)d_out, (char*)d_ws);
}
```

```cpp
#define ROT 2
#define PRO 2
#define NO_PROBE
#include <hip/hip_runtime.h>
#include <stdint.h>

typedef unsigned short u16;
typedef unsigned u32;
typedef unsigned long long u64;
typedef short s16x4 __attribute__((ext_vector_type(4)));
typedef short s16x8 __attribute__((ext_vector_type(8)));
typedef __bf16 bf16x2 __attribute__((ext_vector_type(2)));
typedef float f32x2 __attribute__((ext_vector_type(2)));
typedef float f32x4 __attribute__((ext_vector_type(4)));
typedef u32 u32x2 __attribute__((ext_vector_type(2)));
typedef u32 u32x4 __attribute__((ext_vector_type(4)));
#define LAS __attribute__((address_space(3)))
#define DEV __device__ __forceinline__
typedef __amdgpu_buffer_rsrc_t rsrc_t;

constexpr u32 WS_EPOCH = 0;
constexpr u32 WS_F1 = 32768;
constexpr u32 WS_F2 = 65536;
constexpr u32 WS_ST0 = 98304;
constexpr u32 WS_ST1 = 131072;
constexpr u32 WS_ST2 = WS_ST1 + 262144;
constexpr u32 WS_ST3 = WS_ST2 + 262144;
constexpr u32 WS_UT = 1u << 20;
constexpr u32 WS_O1 = WS_UT + (2u << 20);
constexpr u32 WS_O2 = WS_O1 + (2u << 20);
constexpr u32 WS_Y2 = WS_O2 + (2u << 20);
constexpr u32 WS_X2 = WS_Y2 + (2u << 20);
constexpr u32 WS_END = WS_X2 + (2u << 20);

constexpr u32 L_SLOT = 34816;
constexpr u32 L_KV = 4 * L_SLOT;
constexpr u32 L_K = L_KV, L_V = L_KV + 9216;
constexpr u32 L_SK = L_KV + 17408;
constexpr u32 L_WO = 0;
constexpr u32 L_CX = 65536;
constexpr u32 L_HC = L_CX;
constexpr u32 L_UR = L_CX + 69632;
constexpr u32 L_WX = L_CX + 40960;
constexpr u32 L_EX = 147456;
constexpr u32 L_TAB = L_EX + 12288;
constexpr u32 L_CS = L_TAB + 1024;
constexpr u32 L_FLG = L_TAB + 2048;
constexpr u32 L_DUMMY = L_FLG + 1024;
constexpr u32 L_TOTAL = 160 * 1024;
static_assert(L_DUMMY + 1024 <= L_TOTAL, "LDS map");

#define WO_POS 0
#define OUT_AUX 0
constexpr int SPIN_CAP = 1 << 14;
constexpr u32 SENT = 0xFFFFFFFFu;

#define WAIT_VM(n) asm volatile("s_waitcnt vmcnt(%0)" ::"i"(n) : "memory")
#define BAR() do { asm volatile("s_waitcnt lgkmcnt(0)" ::: "memory"); __builtin_amdgcn_s_barrier(); asm volatile("" ::: "memory"); } while (0)
#define RBAR() do { asm volatile("" ::: "memory"); __builtin_amdgcn_s_barrier(); asm volatile("" ::: "memory"); } while (0)
#define CFENCE() asm volatile("" ::: "memory")
#define SB() __builtin_amdgcn_sched_barrier(0)
template <int N> struct IC { static constexpr int value = N; };
template <int I, int N, class F> DEV void sfor(F&& f) { if constexpr (I < N) { f(IC<I>{}); sfor<I + 1, N>(f); } }

DEV f32x4 mfma32(s16x8 a, s16x8 b, f32x4 c) { return __builtin_amdgcn_mfma_f32_16x16x32_bf16(a, b, c, 0, 0, 0); }
DEV f32x4 mfma16(s16x4 a, s16x4 b, f32x4 c) { return __builtin_amdgcn_mfma_f32_16x16x16bf16_1k(a, b, c, 0, 0, 0); }
DEV float vmax(float a, float b) { float r; asm("v_max_f32 %0, %1, %2" : "=v"(r) : "v"(a), "v"(b)); return r; }
DEV u32 pk2(float lo, float hi) { f32x2 f = {lo, hi}; bf16x2 b = __builtin_convertvector(f, bf16x2); return __builtin_bit_cast(u32, b); }
DEV float bflo(u32 w) { return __uint_as_float(w << 16); }
DEV float bfhi(u32 w) { return __uint_as_float(w & 0xffff0000u); }
DEV float dot2(u32 a, u32 b, float c) { return __builtin_amdgcn_fdot2_f32_bf16(__builtin_bit_cast(bf16x2, a), __builtin_bit_cast(bf16x2, b), c, false); }
DEV u32 nosent(u32 w) { return min(w, 0xFFFFFFFEu); }
DEV float xsum(float v) {
    auto r = __builtin_amdgcn_permlane32_swap(__float_as_uint(v), __float_as_uint(v), false, false);
    v = __uint_as_float(r[0]) + __uint_as_float(r[1]);
    auto s = __builtin_amdgcn_permlane16_swap(__float_as_uint(v), __float_as_uint(v), false, false);
    return __uint_as_float(s[0]) + __uint_as_float(s[1]);
}
DEV float xmax(float v) {
    auto r = __builtin_amdgcn_permlane32_swap(__float_as_uint(v), __float_as_uint(v), false, false);
    v = vmax(__uint_as_float(r[0]), __uint_as_float(r[1]));
    auto s = __builtin_amdgcn_permlane16_swap(__float_as_uint(v), __float_as_uint(v), false, false);
    return vmax(__uint_as_float(s[0]), __uint_as_float(s[1]));
}
DEV float rowsum16(float v) {
    v += __builtin_bit_cast(float, __builtin_amdgcn_update_dpp(0, __builtin_bit_cast(int, v), 0xB1, 0xf, 0xf, true));
    v += __builtin_bit_cast(float, __builtin_amdgcn_update_dpp(0, __builtin_bit_cast(int, v), 0x4E, 0xf, 0xf, true));
    v += __builtin_bit_cast(float, __builtin_amdgcn_update_dpp(0, __builtin_bit_cast(int, v), 0x141, 0xf, 0xf, true));
    v += __builtin_bit_cast(float, __builtin_amdgcn_update_dpp(0, __builtin_bit_cast(int, v), 0x140, 0xf, 0xf, true));
    return v;
}
DEV float bcast_row0(float v) { return __builtin_bit_cast(float, __builtin_amdgcn_update_dpp(0, __builtin_bit_cast(int, v), 0x150, 0xf, 0xf, true)); }
template <int OFF> DEV void ald4o(u32x4& d, u32 voff, u32x4 rs) { asm volatile("buffer_load_dwordx4 %0, %1, %2, 0 offen offset:%3 sc1" : "=v"(d) : "v"(voff), "s"(rs), "i"(OFF) : "memory"); }
template <class T> DEV void pin(T& v) { asm volatile("" : "+v"(v)); }
DEV int opq(int v) { asm volatile("" : "+v"(v)); return v; }
DEV s16x8 cat8(s16x4 a, s16x4 b) { return __builtin_shufflevector(a, b, 0, 1, 2, 3, 4, 5, 6, 7); }
DEV s16x4 pack4(f32x4 v) { u32x2 w = {pk2(v[0], v[1]), pk2(v[2], v[3])}; return __builtin_bit_cast(s16x4, w); }
DEV u64 mix64(u64 z) { z += 0x9E3779B97F4A7C15ull; z = (z ^ (z >> 30)) * 0xBF58476D1CE4E5B9ull; z = (z ^ (z >> 27)) * 0x94D049BB133111EBull; return z ^ (z >> 31); }

struct ConvSched {
    int n[4], f[18], i[18], slot[64];
    constexpr ConvSched(int R) : n{}, f{}, i{}, slot{} {
        constexpr int LEAD = 9;
        const int N0[4] = {0, 3, 4, 7};
        for (int k = 0; k < 4; ++k) n[k] = N0[k] ^ R;
        int c = 0;
        for (int ff = 0; ff < 8; ++ff) for (int k = 3; k >= 0; --k) if (ff <= n[k]) { f[c] = ff; i[c] = k; ++c; }
        for (int k = 0; k < 64; ++k) slot[k] = 1000;
        for (int p = 71; p >= 0; --p) {
            const int e = p / 18, ff = f[p % 18], g = n[i[p % 18]] - ff, s = p - LEAD < -1 ? -1 : p - LEAD;
            slot[e * 16 + ff] = s; slot[e * 16 + 8 + g] = s;
        }
    }
};

#define STAMP(i) do { } while (0)
#define CSTAMP(i) do { } while (0)

struct Args { const u16 *gx, *gwqkv, *gwo, *gbk, *gbv, *gsk, *ghk, *gdv; u16* gout; char* ws; };

template <int R>
DEV void body(const Args& A_, LAS char* const lds, const int wave) {
    const u16* gx = A_.gx; char* ws = A_.ws;
    STAMP(0);
    const int b = blockIdx.x, tid = threadIdx.x, lane = tid & 63;
    const int hd = wave & 3;
    const int grp = (b & 7) * 4 + (b >> 6), mem = (b >> 3) & 7;
    const int T0 = grp * 64;
    const int rot = (mem + ROT * (grp & 3)) & 7;

    u64 epoch;
    asm volatile("s_load_dwordx2 %0, %1, 0x0" : "=s"(epoch) : "s"(ws));
    u32 dw;
    asm volatile("s_load_dword %0, %1, 0x0" : "=s"(dw) : "s"(A_.gdv + 2 * b));
    const rsrc_t r_x = __builtin_amdgcn_make_buffer_rsrc((void*)gx, 0, 2048 * 512 * 2, 0x00020000);
    const rsrc_t r_ws = __builtin_amdgcn_make_buffer_rsrc((void*)ws, 0, WS_END, 0x00020000);
    const rsrc_t r_out = __builtin_amdgcn_make_buffer_rsrc((void*)A_.gout, 0, 2048 * 512 * 2, 0x00020000);
    const u32 ldsbase = (u32)(size_t)lds;
    auto mkdesc = [](const void* p, u32 bytes) { const u64 a = (u64)p; return (u32x4){(u32)a, (u32)(a >> 32) & 0xffffu, bytes, 0x00020000u}; };
    const u32x4 d_x = mkdesc(gx, 2048 * 512 * 2), d_w = mkdesc(A_.gwqkv, 512 * 1536 * 2), d_wo = mkdesc(A_.gwo, 512 * 512 * 2);
    const u32x4 d_k = mkdesc(A_.gbk, 2048 * 512 * 2), d_v = mkdesc(A_.gbv, 2048 * 512 * 2), d_sk = mkdesc(A_.gsk, 512 * 4 * 2);
    const u32x4 d_h = mkdesc(A_.ghk, 512 * 2048 * 2), d_x2 = mkdesc(ws + WS_X2, 2u << 20), d_ws = mkdesc(ws, WS_END);
    const u32 wb1k = ldsbase + wave * 1024, hb1k = ldsbase + hd * 1024, hb512 = ldsbase + hd * 512;
#define DMA_ASM(pre, mods, post) asm volatile("s_add_u32 m0, %0, %4\n\t" pre "buffer_load_dwordx4 %1, %2, %3 offen " mods "lds" post \
                     :: "s"(sbase), "v"(voff), "s"(rs), "s"(soff), "i"(IMM) : "memory", "scc")
    const u32 xmlo = hd == 0 ? ~0xFFu : ~0u;
    auto dma = [&](auto kind, auto imm, u32x4 rs, u32 sbase, u32 voff, u32 soff) {
        constexpr int K = decltype(kind)::value; constexpr int IMM = decltype(imm)::value;
        if constexpr (K == 0) DMA_ASM("s_nop 0\n\t", "", "");
        else if constexpr (K == 1) DMA_ASM("s_mov_b32 exec_hi, 0\n\t", "", "\n\ts_mov_b32 exec_hi, -1");
        else if constexpr (K == 2) {
            const u32 ml = xmlo;
            asm volatile("s_add_u32 m0, %0, %4\n\ts_mov_b32 exec_lo, %5\n\tbuffer_load_dwordx4 %1, %2, %3 offen lds\n\ts_mov_b32 exec_lo, -1"
                         :: "s"(sbase), "v"(voff), "s"(rs), "s"(soff), "i"(IMM), "s"(ml) : "memory", "scc");
        }
        else if constexpr (K == 4) DMA_ASM("s_nop 0\n\t", "sc1 ", "");
        else DMA_ASM("s_mov_b32 exec_hi, 0\n\t", "sc1 ", "\n\ts_mov_b32 exec_hi, -1");
    };
    auto ald4 = [&](u32x4& d, u32x4 rs, u32 voff) { asm volatile("buffer_load_dwordx4 %0, %1, %2, 0 offen" : "=v"(d) : "v"(voff), "s"(rs) : "memory"); };
    auto ald4s = [&](u32x4& d, u32 voff) { asm volatile("buffer_load_dwordx4 %0, %1, %2, 0 offen sc1" : "=v"(d) : "v"(voff), "s"(d_ws) : "memory"); };
    auto ald2s = [&](u32x2& d, u32 voff) { asm volatile("buffer_load_dwordx2 %0, %1, %2, 0 offen sc1" : "=v"(d) : "v"(voff), "s"(d_ws) : "memory"); };

    const int pr = lane >> 3, pc = lane & 7;
    const int xr0 = 8 * hd + pr, xr2 = 64 + 4 * hd + pr;
    const u32 xv0 = (u32)((T0 - 16 + xr0) * 1024 + ((pc ^ ((xr0 >> 1) & 7)) << 4));
    const u32 xv2 = (u32)((T0 - 16 + xr2) * 1024 + ((pc ^ ((xr2 >> 1) & 7)) << 4));
    const int wfw = ((xr0 >> 1) & 1) | (((xr0 >> 3) & 1) << 1);
    const u32 wv0 = (u32)(xr0 * 3072 + mem * 128 + ((((pc >> 1) ^ wfw) * 2 + (pc & 1)) << 4));
    auto issue_piece = [&](auto sc, auto ic) {
        constexpr int s = decltype(sc)::value, i = decltype(ic)::value;
        const u32 kp = (u32)((s + rot) & 7);
        constexpr int sb = (s & 3) * L_SLOT;
        if constexpr (i == 0) dma(IC<2>{}, IC<sb>{}, d_x, hb1k, xv0, kp * 128);
        else if constexpr (i == 1) dma(IC<0>{}, IC<sb + 4096>{}, d_x, hb1k, xv0 + 32768, kp * 128);
        else if constexpr (i == 2) dma(IC<1>{}, IC<sb + 8192>{}, d_x, hb512, xv2, kp * 128);
        else dma(IC<0>{}, IC<sb + 10240 + (i - 3) * 4096>{}, d_w, hb1k, wv0, kp * (64 * 3072) + ((i - 3) >> 1) * 1024 + ((i - 3) & 1) * (32 * 3072));
    };
    auto issue_wo = [&](auto ic) {
        constexpr int i = decltype(ic)::value;
        const int l2 = opq(tid) & 63, pr = l2 >> 3, pc = l2 & 7;
        const u32 wov = (u32)((8 * hd + pr) * 1024 + mem * 128 + ((((pc >> 1) ^ (((pr >> 1) & 1) | ((hd & 1) << 1))) * 2 + (pc & 1)) << 4));
        const u32 j = (u32)((i + 2 * grp) & 7);
        dma(IC<0>{}, IC<L_WO + (i & 8) * 4096>{}, d_wo, hb1k + j * 4096, wov, ((i & 8) + j) * 32768);
    };
    auto issue_kv = [&](int i) {
        const int l2 = opq(tid) & 63, pr = l2 >> 3, pc = l2 & 7, p = hd + 4 * (i & 1);
        const u32 cb = (u32)(mem * 128);
        if (i < 2) dma(IC<0>{}, IC<L_K>{}, d_k, hb1k + (i & 1) * 4096, (u32)((T0 - 8 + 8 * p + pr) * 1024) + cb + ((pc ^ p) << 4), 0);
        else if (i < 4) dma(IC<0>{}, IC<L_V>{}, d_v, hb1k + (i & 1) * 4096, (u32)((T0 + 8 * p + pr) * 1024) + cb + ((pc ^ p) << 4), 0);
        else if (hd == 0) dma(IC<0>{}, IC<L_K + 8192>{}, d_k, ldsbase, (u32)((T0 - 8 + 64 + pr) * 1024) + cb + (pc << 4), 0);
        else dma(IC<0>{}, IC<L_SK>{}, d_sk, ldsbase, (u32)(mem * 512 + l2 * 16), 0);
    };

    const int srow = T0 + 8 * mem + 2 * hd;
    u32x4 xrow[2];
    sfor<0, 9>([&](auto ic) { if constexpr ((decltype(ic)::value & 1) != R) issue_piece(IC<0>{}, ic); });
    sfor<0, 9>([&](auto ic) { if constexpr ((decltype(ic)::value & 1) != R) issue_piece(IC<1>{}, ic); });
    asm volatile("s_waitcnt lgkmcnt(0)" : "+s"(epoch), "+s"(dw));
    const u64 tag = mix64(epoch);
    const u32 tl = (u32)tag, th = (u32)(tag >> 32);
    if (R == 1 && wave == 7 && lane < 32) *(LAS u32x4*)(lds + (lane >> 3) * L_SLOT + (lane & 7) * 16) = (u32x4){0x3F803F80u, 0x3F803F80u, 0x3F803F80u, 0x3F803F80u};

    auto trr = [&](u32 off) { return __builtin_amdgcn_ds_read_tr16_b64_v4i16((LAS s16x4*)(lds + off)); };
    auto ldsA = [&](u32 off) { return *(LAS s16x8*)(lds + off); };
    auto flag_ok = [&](const u32x2& f) { return __all(f[0] == tl && f[1] == th); };
    auto spin_flags = [&](u32x2& f, u32 off) {
        _Pragma("unroll 1") for (int spin = 0; spin < SPIN_CAP; ++spin) {
            if (__builtin_expect(flag_ok(f), 1)) break;
            __builtin_amdgcn_s_sleep(2);
            asm volatile("buffer_load_dwordx2 %0, %1, %2, 0 offen sc1\n\ts_waitcnt vmcnt(0)" : "=&v"(f) : "v"(off), "s"(d_ws) : "memory");
        }
    };

    s16x8 Wq[16], Wm[16];
    f32x4 accM[5], accQ[5];
    const f32x4 zero4 = {0.f, 0.f, 0.f, 0.f};
    const s16x8 ones8 = {0x3F80, 0x3F80, 0x3F80, 0x3F80, 0x3F80, 0x3F80, 0x3F80, 0x3F80};
    s16x8 Ab[3][5];
    auto qkv_step = [&](auto lc, int st, const s16x8 (&A)[5], auto fill) {
        sfor<0, 5>([&](auto mc) {
            constexpr int mt = decltype(mc)::value;
            const f32x4 cm = st ? accM[mt] : zero4;
            accM[mt] = R ? mfma32(A[mt], Wm[st], cm) : mfma32(Wm[st], A[mt], cm);
            if constexpr (R == 0 && (mt > 0 || decltype(lc)::value == 0)) accQ[mt] = mfma32(Wq[st], A[mt], st ? accQ[mt] : zero4);
            fill(mc);
        });
    };

    float m1[5], r1[5]; f32x4 m4[5], r4[5], csK, csQ;
    auto load_tab = [&](bool cs) {
        const int l2 = opq(tid) & 63;
        u32 ta = L_TAB + (R ? (l2 >> 4) * 16 : (l2 & 15) * 4);
        pin(ta);
        if (R == 0 && cs) { csK = *(LAS f32x4*)(lds + L_CS + hd * 128 + (l2 >> 4) * 16); csQ = *(LAS f32x4*)(lds + L_CS + hd * 128 + 64 + (l2 >> 4) * 16); }
#pragma unroll
        for (int mt = 0; mt < 5; ++mt) {
            if (R == 0) { m1[mt] = *(LAS float*)(lds + ta + mt * 64); r1[mt] = *(LAS float*)(lds + ta + 320 + mt * 64); }
            else { m4[mt] = *(LAS f32x4*)(lds + ta + mt * 64); r4[mt] = *(LAS f32x4*)(lds + ta + 320 + mt * 64); }
        }
    };

    u32x4 hr[4];
    u32x2 flg;
    {
        const int li = lane & 15, lq = lane >> 4, tq = li >> 2, tp = lane & 3;
        const u32 xa0 = (u32)(li * 128 + ((lq ^ (li >> 1)) << 4));
        const u32 wa = (u32)((8 * lq + tq) * 128 + ((hd ^ (((tq >> 1) & 1) | ((lq & 1) << 1))) << 5) + tp * 8);
        u32 xa, wb, kw[11], vw[8];
        u32x4 s0v, skw;
        auto pf_addr = [&](int st) {
            const u32 sb = (u32)((st >> 1) & 3) * L_SLOT;
            xa = sb + (xa0 ^ ((st & 1) * 64)); wb = sb + 10240 + (st & 1) * 4096 + wa;
            asm volatile("" : "+v"(xa), "+v"(wb));
        };
        auto pf_part = [&](int st, int mt) {
            if (mt == 0) Wm[st] = cat8(trr(wb + (1 + R) * 8192), trr(wb + (1 + R) * 8192 + 512));
            Ab[st & 1][mt] = ldsA(xa + mt * 2048);
            if (R == 0 && mt == 1) Wq[st] = cat8(trr(wb), trr(wb + 512));
        };
        STAMP(15);
        WAIT_VM(R ? 5 : 4);
        BAR();
        STAMP(16);
        pf_addr(0);
#pragma unroll
        for (int mt = 0; mt < 5; ++mt) pf_part(0, mt);
        sfor<0, 16>([&](auto stc) {
            constexpr int st = decltype(stc)::value;
            if constexpr ((st & 1) && st < 15) {
                constexpr int s1 = (st >> 1) + 1;
                if constexpr (R == 0) {
                    if constexpr (s1 == 1) WAIT_VM(7);
                    if constexpr (s1 == 2) WAIT_VM(1);
                    if constexpr (s1 == 5) WAIT_VM(0);
                } else {
                    constexpr int WN[8] = {0, 9, 9, 9, 9, 9, 10, 10};
                    WAIT_VM(WN[s1]);
                    if constexpr (s1 == 7) {
                        pin(s0v);
                        const int l2 = opq(tid) & 63, tr = hd * 20 + min(l2, 19), row = T0 - 16 + tr;
                        _Pragma("unroll 1") for (int spin = 0; spin < SPIN_CAP; ++spin) {
                            if (__builtin_expect(__all(row < 0 || (s0v[1] == tl && s0v[3] == th)), 1)) break;
                            __builtin_amdgcn_s_sleep(2);
                            ald4s(s0v, WS_ST0 + (u32)(max(row, 0) * 16));
                            WAIT_VM(0);
                            pin(s0v);
                        }
                        *(LAS u32*)(lds + L_TAB + tr * 4) = row < 0 ? 0u : s0v[0];
                        *(LAS u32*)(lds + L_TAB + 320 + tr * 4) = row < 0 ? 0x3F800000u : s0v[2];
                        asm volatile("s_waitcnt lgkmcnt(0)" ::: "memory");
                    }
                }
                RBAR();
                STAMP(16 + s1);
                if constexpr (s1 == 5 && R == 0) { if (tid == 0) __builtin_amdgcn_raw_buffer_store_b64((u32x2){tl, th}, r_ws, WS_F1 + (mem * 32 + grp) * 128, 0, 16); CFENCE(); }
                if constexpr (s1 == 5 && R == 1) { const int row = T0 - 16 + hd * 20 + min(opq(tid) & 63, 19); ald4s(s0v, WS_ST0 + (u32)(max(row, 0) * 16)); }
            }
            if (st < 15) pf_addr(st + 1);
            if constexpr (st == 14) load_tab(false);
            SB();
            qkv_step(IC<0>{}, st, Ab[st & 1], [&](auto mc) {
                constexpr int mt = decltype(mc)::value;
                if (st < 15) pf_part(st + 1, mt);
                if constexpr (R == 1) {
                    if constexpr (st == 0) { issue_piece(IC<2>{}, IC<2 * mt>{}); if constexpr (mt < 4) issue_piece(IC<2>{}, IC<2 * mt + 1>{}); }
                    if constexpr (st >= 1 && st <= 10 && ((st - 1) & 1) * 5 + mt < 9) issue_piece(IC<((st - 1) >> 1) + 3>{}, IC<((st - 1) & 1) * 5 + mt>{});
                    if constexpr (st == 11 && mt == 0) { const u32 ho = (u32)(b * 8192 + opq(tid) * 16); ald4(hr[2], d_h, ho); ald4(hr[3], d_h, ho + 16); }
                    if constexpr (st == 13 && mt == 0) { if (wave == 7) { const int l2 = opq(tid) & 63; ald2s(flg, WS_F1 + (u32)((l2 < 32 ? (b >> 5) * 32 + l2 : (l2 & 7) * 32 + grp) * 128)); } }
                    if constexpr (st >= 11 && st <= 14 && mt < 4) issue_wo(IC<(st - 11) * 4 + mt>{});
                } else {
                    if constexpr (st == 0) {
                        if constexpr (mt == 0) { const u32 xo = (u32)(srow * 1024 + (opq(tid) & 63) * 16); ald4(xrow[0], d_x, xo); ald4(xrow[1], d_x, xo + 1024); }
                        issue_kv(mt);
                    }
                    if constexpr (st == 11 && mt < 2) { const u32 ho = (u32)(b * 8192 + mt * 4096 + opq(tid) * 16); ald4(hr[2 * mt], d_h, ho); ald4(hr[2 * mt + 1], d_h, ho + 16); }
                }
                SB();
            });
            if constexpr (R == 0 && st == 2) {
                WAIT_VM(5);
                pin(xrow[0]); pin(xrow[1]);
                float s[2] = {0.f, 0.f}, q[2] = {0.f, 0.f};
#pragma unroll
                for (int i = 0; i < 4; ++i)
#pragma unroll
                    for (int k = 0; k < 2; ++k) { s[k] = dot2(xrow[k][i], 0x3F803F80u, s[k]); q[k] = dot2(xrow[k][i], xrow[k][i], q[k]); }
                auto ss = __builtin_amdgcn_permlane32_swap(__float_as_uint(s[0]), __float_as_uint(s[1]), false, false);
                auto qq = __builtin_amdgcn_permlane32_swap(__float_as_uint(q[0]), __float_as_uint(q[1]), false, false);
                float sv1 = rowsum16(__uint_as_float(ss[0]) + __uint_as_float(ss[1])), qv1 = rowsum16(__uint_as_float(qq[0]) + __uint_as_float(qq[1]));
                auto s2 = __builtin_amdgcn_permlane16_swap(__float_as_uint(sv1), __float_as_uint(sv1), false, false);
                auto q2 = __builtin_amdgcn_permlane16_swap(__float_as_uint(qv1), __float_as_uint(qv1), false, false);
                const float m = (__uint_as_float(s2[0]) + __uint_as_float(s2[1])) * (1.f / 512.f);
                const float r = __builtin_amdgcn_rsqf(fmaxf((__uint_as_float(q2[0]) + __uint_as_float(q2[1])) * (1.f / 512.f) - m * m, 0.f) + 1e-5f);
                const u32x4 sv = {__float_as_uint(m), tl, __float_as_uint(r), th};
                const int l2 = opq(tid) & 63;
                __builtin_amdgcn_raw_buffer_store_b128(sv, r_ws, (l2 & 31) == 0 ? WS_ST0 + (u32)((srow + (l2 >> 5)) * 16) : 0xFFFFFF00u, 0, 16);
                CFENCE();
                SB();
            }
            if constexpr (R == 0 && (st == 4 || st == 6)) {
                const int l2 = opq(tid) & 63, tg = l2 & 7, cp = 8 * hd + (l2 >> 3);
                if constexpr (st == 4) {
                    skw = *(LAS u32x4*)(lds + L_SK + cp * 16);
                    u32 ka = L_K + tg * 1024 + ((((cp >> 2) ^ tg) & 7) << 4) + (cp & 3) * 4;
                    u32 kb = L_K + tg * 1024 + ((((cp >> 2) ^ (tg + 1)) & 7) << 4) + (cp & 3) * 4;
                    pin(ka); pin(kb);
#pragma unroll
                    for (int i = 0; i < 11; ++i) kw[i] = *(LAS u32*)(lds + ((i + 5 < 8) ? ka : kb) + (i + 5) * 128);
#pragma unroll
                    for (int i = 0; i < 8; ++i) vw[i] = *(LAS u32*)(lds + ka + (L_V - L_K) + i * 128);
                    SB();
                }
                constexpr bool HI = st == 6;
                auto ex = [&](u32 w) { return HI ? bfhi(w) : bflo(w); };
                const float a0 = bflo(skw[2 * HI]), a1 = bfhi(skw[2 * HI]), a2 = bflo(skw[2 * HI + 1]), a3 = bfhi(skw[2 * HI + 1]);
                float ua[8];
#pragma unroll
                for (int i = 0; i < 8; ++i) ua[i] = (ex(kw[i]) * a3 + ex(kw[i + 1]) * a2 + ex(kw[i + 2]) * a1 + ex(kw[i + 3]) * a0) * ex(vw[i]);
                const u32x4 oa = {pk2(ua[7], ua[6]), pk2(ua[5], ua[4]), pk2(ua[3], ua[2]), pk2(ua[1], ua[0])};
                __builtin_amdgcn_raw_buffer_store_b128(oa, r_ws, WS_UT + (u32)((mem * 64 + 2 * cp) * 4096 + (2040 - T0 - 8 * tg) * 2), HI * 4096, 16);
                __builtin_amdgcn_raw_buffer_store_b128((u32x4){SENT, SENT, SENT, SENT}, r_ws, (u32)((4 * grp + hd) * 16384 + mem * 2048 + HI * 1024 + l2 * 16), WS_O1, 16);
                CFENCE();
                SB();
            }
        });
    }
    STAMP(R ? 1 : 53);

    u32x4 uu;
    float csV;
    auto attention = [&](auto lc) {
        constexpr int LAYER = decltype(lc)::value;
        constexpr int SBASE = LAYER ? 32 : 28;
        const int lane = opq(tid) & 63, li = lane & 15, lq = lane >> 4;
        u32 ex = L_EX + hd * 2048 + lane * 8;
        pin(ex);
        u32 f1ok = 1;
        if constexpr (R == 0) {
            if constexpr (LAYER == 0) {
#pragma unroll
                for (int i = 0; i < 4; ++i) { csK[i] = bcast_row0(accM[0][i]); csQ[i] = bcast_row0(accQ[0][i]); }
                if (li == 0) { *(LAS f32x4*)(lds + L_CS + hd * 128 + lq * 16) = csK; *(LAS f32x4*)(lds + L_CS + hd * 128 + 64 + lq * 16) = csQ; }
            }
            const int par = li & 1;
            const bool cur0 = (4 * lq + par) <= li, cur1 = (4 * lq + par + 2) <= li;
            s16x4 Kb[5], Qb[5];
#pragma unroll
            for (int mt = 0; mt < 5; ++mt) {
                Kb[mt] = pack4((accM[mt] - m1[mt] * csK) * r1[mt]);
                if (mt) Qb[mt] = pack4(accQ[mt] - m1[mt] * csQ);
            }
            f32x4 Sc[5], Sp[5];
#pragma unroll
            for (int mt = 1; mt < 5; ++mt) { Sc[mt] = mfma16(Kb[mt], Qb[mt], zero4); Sp[mt] = mfma16(Kb[mt - 1], Qb[mt], zero4); }
            float s0[5], s1[5], mx[5];
#pragma unroll
            for (int mt = 1; mt < 5; ++mt) {
                const float sp0 = Sp[mt][0], sp1 = Sp[mt][1], sp2 = Sp[mt][2], sp3 = Sp[mt][3], c0 = Sc[mt][0], c1 = Sc[mt][1], c2 = Sc[mt][2], c3 = Sc[mt][3];
                float p0 = par ? sp1 : sp0, p1 = par ? sp3 : sp2, q0 = par ? c1 : c0, q1 = par ? c3 : c2;
                asm volatile("" : "+v"(p0), "+v"(p1), "+v"(q0), "+v"(q1));
                if (mt == 1 && grp == 0) { p0 = -3.0e38f; p1 = -3.0e38f; }
                s0[mt] = cur0 ? q0 : p0; s1[mt] = cur1 ? q1 : p1;
                mx[mt] = vmax(s0[mt], s1[mt]);
            }
#pragma unroll
            for (int mt = 1; mt < 5; ++mt) { auto r = __builtin_amdgcn_permlane32_swap(__float_as_uint(mx[mt]), __float_as_uint(mx[mt]), false, false); mx[mt] = vmax(__uint_as_float(r[0]), __uint_as_float(r[1])); }
#pragma unroll
            for (int mt = 1; mt < 5; ++mt) { auto r = __builtin_amdgcn_permlane16_swap(__float_as_uint(mx[mt]), __float_as_uint(mx[mt]), false, false); mx[mt] = vmax(__uint_as_float(r[0]), __uint_as_float(r[1])); }
#pragma unroll
            for (int mt = 1; mt < 5; ++mt) {
                const float sc = r1[mt] * (0.25f * 1.44269504088896341f);
                const float e0 = __builtin_amdgcn_exp2f((s0[mt] - mx[mt]) * sc), e1 = __builtin_amdgcn_exp2f((s1[mt] - mx[mt]) * sc);
                *(LAS u32x2*)(lds + ex + (mt - 1) * 512) = (u32x2){pk2(e0, e0), pk2(e1, e1)};
            }
            STAMP(SBASE + 2);
            BAR();
            STAMP(SBASE + 1);
            if constexpr (LAYER == 0) f1ok = __builtin_amdgcn_readfirstlane(*(LAS u32*)(lds + L_FLG));
        } else {
            if constexpr (LAYER == 0) {
                auto s = __builtin_amdgcn_permlane16_swap(__float_as_uint(accM[0][0]), __float_as_uint(accM[0][0]), false, false);
                auto t = __builtin_amdgcn_permlane32_swap(s[0], s[0], false, false);
                csV = __uint_as_float(t[0]);
                if (wave == 7) { WAIT_VM(8); pin(flg); const u32 ok = flag_ok(flg); *(LAS u32*)(lds + L_FLG) = ok; }
            }
            const int par = li & 1;
            const bool cur0 = (4 * lq + par) <= li, cur1 = (4 * lq + par + 2) <= li;
            const u32 pm = par ? 0xffff0000u : 0x0000ffffu;
            u32 mC0 = cur0 ? pm : 0u, mP0 = cur0 ? 0u : pm, mC1 = cur1 ? pm : 0u, mP1 = cur1 ? 0u : pm;
            u32 oo = (LAYER ? WS_O2 : WS_O1) + (u32)((4 * grp + (lq & 1)) * 16384 + (4 * mem + hd) * 512 + (lq >> 1) * 256 + li * 16);
            asm volatile("" : "+v"(mC0), "+v"(mP0), "+v"(mC1), "+v"(mP1), "+v"(oo));
            s16x4 Vb[5];
#pragma unroll
            for (int mt = 0; mt < 5; ++mt) { Vb[mt] = pack4((accM[mt] - m4[mt] * csV) * r4[mt]); pin(Vb[mt]); }
            STAMP(SBASE);
            BAR();
            STAMP(SBASE + 1);
            u32x2 pw[4];
#pragma unroll
            for (int j = 0; j < 4; ++j) pw[j] = *(LAS u32x2*)(lds + ex + j * 512);
            if constexpr (LAYER == 0) {
                f1ok = __builtin_amdgcn_readfirstlane(*(LAS u32*)(lds + L_FLG));
                if (f1ok) ald4s(uu, WS_UT + (u32)(b * 8192 + opq(tid) * 16));
            }
            SB();
            f32x4 O[4], L[4];
#pragma unroll
            for (int j = 0; j < 4; ++j) {
                const s16x8 P = __builtin_bit_cast(s16x8, (u32x4){pw[j][0] & mC0, pw[j][1] & mC1, pw[j][0] & mP0, pw[j][1] & mP1});
                L[j] = mfma32(ones8, P, zero4);
                O[j] = mfma32(cat8(Vb[j + 1], Vb[j]), P, zero4);
            }
            u32 ow[4][2];
#pragma unroll
            for (int j = 0; j < 4; ++j) {
                const float inv = __builtin_amdgcn_rcpf(L[j][0]);
                ow[j][0] = nosent(pk2(O[j][0] * inv, O[j][1] * inv)); ow[j][1] = nosent(pk2(O[j][2] * inv, O[j][3] * inv));
            }
#pragma unroll
            for (int j = 0; j < 4; j += 2) {
                auto r0 = __builtin_amdgcn_permlane16_swap(ow[j][0], ow[j + 1][0], false, false);
                auto r1 = __builtin_amdgcn_permlane16_swap(ow[j][1], ow[j + 1][1], false, false);
                __builtin_amdgcn_raw_buffer_store_b128((u32x4){r0[0], r1[0], r0[1], r1[1]}, r_ws, oo, j * 16384, 16);
            }
            CFENCE();
        }
        if constexpr (LAYER == 0 && R == 0) { if (f1ok) ald4s(uu, WS_UT + (u32)(b * 8192 + opq(tid) * 16)); }
        SB();
        return f1ok;
    };
    const u32 f1ok = attention(IC<0>{});
    {
        const u32x4 sv = {SENT, SENT, SENT, SENT};
        const int t2 = opq(tid);
        const u32 tb = (u32)((4 * grp + hd) * 16384 + mem * 2048 + R * 1024 + (t2 & 63) * 16);
        __builtin_amdgcn_raw_buffer_store_b128(sv, r_ws, tb, WS_O2, 16);
        __builtin_amdgcn_raw_buffer_store_b128(sv, r_ws, tb, WS_X2, 16);
        __builtin_amdgcn_raw_buffer_store_b128(sv, r_ws, (u32)(b * 8192 + t2 * 16), WS_Y2, 16);
        CFENCE();
        SB();
    }
    STAMP(2);

    const int TI = 4 * grp + hd;
    u32x4 of[8];
    auto load_o = [&](u32 ws_o) {
        const int lane = opq(tid) & 63;
#pragma unroll
        for (int i = 0; i < 8; ++i) of[i] = __builtin_amdgcn_raw_buffer_load_b128(r_ws, ws_o + (u32)(TI * 16384 + R * 8192 + lane * 16) + (i & 3) * 1024, (i >> 2) * 4096, 16);
    };
    u32 xw[8], yw[8];
    u32 woa[4];
    auto resid = [&](int n) { return R ? zero4 : (f32x4){bflo(xw[2 * n]), bfhi(xw[2 * n]), bflo(xw[2 * n + 1]), bfhi(xw[2 * n + 1])}; };
    s16x8 F[8][4];
    auto wo_addr = [&]() {
        const int l2 = opq(tid) & 63, lq = l2 >> 4, tq = (l2 & 15) >> 2, tp = l2 & 3;
#pragma unroll
        for (int n = 0; n < 4; ++n) woa[n] = L_WO + (u32)(R * 32768 + (8 * lq + tq) * 128 + ((n ^ (((lq & 1) << 1) | (tq >> 1))) << 5) + tp * 8);
    };
    auto load_F = [&](int ks) {
#pragma unroll
        for (int n = 0; n < 4; ++n) F[ks][n] = cat8(trr(woa[n] + ks * 4096), trr(woa[n] + ks * 4096 + 512));
    };

    {
        const int t2 = opq(tid), lane = t2 & 63, li = lane & 15, lq = lane >> 4;
        constexpr u32 OFFS = 0xEDA85410u;
        {
            const int j = t2 & 255;
            {
                if constexpr (R == 0) { if (f1ok) WAIT_VM(4); else WAIT_VM(3); }
                else WAIT_VM(5);
                u32 cb = L_HC + R * 34816 + (j + 2) * 16;
                pin(cb);
#pragma unroll
                for (int ch = R; ch < 2; ++ch) {
                    pin(hr[2 * ch]); pin(hr[2 * ch + 1]);
                    const u32 d[8] = {hr[2 * ch][0], hr[2 * ch][1], hr[2 * ch][2], hr[2 * ch][3], hr[2 * ch + 1][0], hr[2 * ch + 1][1], hr[2 * ch + 1][2], hr[2 * ch + 1][3]};
                    u32 e[7];
#pragma unroll
                    for (int i = 0; i < 7; ++i) e[i] = __builtin_amdgcn_alignbit(d[i + 1], d[i], 16);
                    if (ch > R) { cb += 34816; pin(cb); }
#pragma unroll
                    for (int p = (R ? 4 : 0); p < ((ch && !R) ? 4 : 8); ++p) {
                        const u32 po = (272 * p + ((OFFS >> (4 * p)) & 15)) * 16;
                        const int h = p >> 1;
                        const u32x4 w = (p & 1) ? (u32x4){e[h], e[h + 1], e[h + 2], e[h + 3]} : (u32x4){d[h], d[h + 1], d[h + 2], d[h + 3]};
                        *(LAS u32x4*)(lds + cb + po) = w;
                    }
                }
            }
            if constexpr (R == 0) {
                if (hd == 0) {
                    u32 hh[4];
#pragma unroll
                    for (int i = 0; i < 4; ++i) { const u32 a = __builtin_amdgcn_readfirstlane(hr[0][i]), c = __builtin_amdgcn_readfirstlane(hr[2][i]); hh[i] = (lane & 16) ? c : a; }
                    if (lane < 32) {
                        const int p = lane & 7;
                        const u32 po = (272 * p + ((OFFS >> (4 * p)) & 15)) * 16;
                        const unsigned __int128 hv = ((unsigned __int128)(((u64)hh[3] << 32) | hh[2]) << 64) | (((u64)hh[1] << 32) | hh[0]);
                        const unsigned __int128 sv = (p == 0 || !(lane & 8)) ? (unsigned __int128)0 : (hv << (16 * (8 - p)));
                        const u32x4 w = {(u32)sv, (u32)(sv >> 32), (u32)(sv >> 64), (u32)(sv >> 96)};
                        *(LAS u32x4*)(lds + L_HC + (lane >> 4) * 34816 + po + ((lane >> 3) & 1) * 16) = w;
                    }
                }
            }
            if (hd == 1 && lane < 30) *(LAS u32x4*)(lds + L_UR + R * 4576 + 4096 + lane * 16) = (u32x4){0, 0, 0, 0};
            STAMP(3);
            if (!f1ok) {
                const u32 fo = WS_F1 + (u32)(((lane & 8) ? (lane & 7) * 32 + grp : (b >> 5) * 32 + 31 - hd * 8 - (lane & 7)) * 128);
                flg = (u32x2){0u, 0u};
                spin_flags(flg, fo);
                ald4s(uu, WS_UT + (u32)(b * 8192 + t2 * 16));
            }
            if (f1ok) WAIT_VM(R ? 5 : 3); else WAIT_VM(0);
            pin(uu);
            *(LAS u32x4*)(lds + L_UR + R * 4576 + j * 16) = uu;
        }
        BAR();
        STAMP(4);
        const int ch = hd >> 1, e0 = (hd & 1) * 4;
        const int p = (li + 1) & 7;
        u32 aA = L_HC + ch * 34816 + (272 * p + ((OFFS >> (4 * p)) & 15) + ((li + 1) >> 3) + lq + 4 * e0) * 16;
        u32 aB = L_UR + ch * 4576 + (254 - 224 - 2 * li + lq + 4 * e0) * 16;
        asm volatile("" : "+v"(aA), "+v"(aB));
        f32x4 y[4];
        u32 ooff = WS_O1 + (u32)(TI * 16384 + R * 8192 + lane * 16);
        {
            constexpr ConvSched S(R);
            s16x8 A[4][8], B[4][8];
            auto loads_at = [&](auto pc) {
                constexpr int p = decltype(pc)::value;
                sfor<0, 64>([&](auto ic) {
                    constexpr int i = decltype(ic)::value, e = i >> 4, f = i & 7;
                    if constexpr (S.slot[i] == p) {
                        if constexpr (i & 8) B[e][f] = ldsA(aB + 64 * e + 512 * (7 - f)); else A[e][f] = ldsA(aA + 64 * (8 * f + e));
                    }
                });
            };
            loads_at(IC<-1>{});
            SB();
            sfor<0, 72>([&](auto pc) {
                constexpr int p = decltype(pc)::value, e = p / 18, f = S.f[p % 18], i = S.i[p % 18], g = S.n[i] - f;
                y[i] = mfma32(A[e][f], B[e][g], (e == 0 && f == 0) ? zero4 : y[i]);
                loads_at(pc);
                if constexpr (p == 54) WAIT_VM(0);
                if constexpr (p >= 55 && p < 71 && (p & 1)) {
                    constexpr int i = (p - 55) >> 1;
                    ald4o<(i & 3) * 1024>(of[i], ooff + (i >> 2) * 4096, d_ws);
                }
                SB();
            });
        }
        STAMP(14);
        RBAR();
        if (R == 0 && tid == 0) __builtin_amdgcn_raw_buffer_store_b64((u32x2){tl, th}, r_ws, WS_F2 + (grp * 8 + mem) * 128, 0, 16);
        {
            constexpr int N0[4] = {0, 3, 4, 7};
            u32 ra = L_HC + hd * 8192 + lane * 16;
            asm volatile("" : "+v"(ra));
#pragma unroll
            for (int i = 0; i < 4; ++i) *(LAS f32x4*)(lds + ra + (N0[i] ^ R) * 1024) = y[i];
        }
        SB();
        if constexpr (R == 0) {
#pragma unroll
            for (int n = 0; n < 4; ++n) { const u32x2 w = __builtin_amdgcn_raw_buffer_load_b64(r_x, (u32)((TI * 16 + li) * 1024 + mem * 128 + lq * 8) + n * 32, 0, 0); xw[2 * n] = w[0]; xw[2 * n + 1] = w[1]; }
        }
        wo_addr();
        SB();
        STAMP(37);
        BAR();
        STAMP(38);
        {
            const float D0 = bflo(dw), D1 = bfhi(dw);
            u32 ra = L_HC + wave * 1024 + lane * 16;
            const int tb = 256 * wave + 16 * li + 4 * lq;
            u32 ru = L_UR + (2044 - tb) * 2;
            asm volatile("" : "+v"(ra), "+v"(ru));
            const f32x4 y0 = *(LAS f32x4*)(lds + ra), y1 = *(LAS f32x4*)(lds + ra + 8192), y2 = *(LAS f32x4*)(lds + ra + 16384), y3 = *(LAS f32x4*)(lds + ra + 24576);
            const u32x2 ua = *(LAS u32x2*)(lds + ru), ub = *(LAS u32x2*)(lds + ru + 4576);
            load_F(0); load_F(1);
            SB();
            const f32x4 ya = y0 + y1, yb = y2 + y3;
            const u32x4 w = {nosent(pk2(ya[0] + bfhi(ua[1]) * D0, yb[0] + bfhi(ub[1]) * D1)), nosent(pk2(ya[1] + bflo(ua[1]) * D0, yb[1] + bflo(ub[1]) * D1)),
                             nosent(pk2(ya[2] + bfhi(ua[0]) * D0, yb[2] + bfhi(ub[0]) * D1)), nosent(pk2(ya[3] + bflo(ua[0]) * D0, yb[3] + bflo(ub[0]) * D1))};
            __builtin_amdgcn_raw_buffer_store_b128(w, r_ws, WS_Y2 + (u32)(b * 8192 + tb * 4), 0, 16);
        }
        SB();
        STAMP(5);
        WAIT_VM(1);
        STAMP(40);

        u32 f2oth = 0;
        _Pragma("unroll 1") for (int attempt = 0; attempt < 64; ++attempt) {
            sfor<0, 8>([&](auto ic) { pin(of[decltype(ic)::value]); });
            u32 m0 = 0, m1 = 0;
#pragma unroll
            for (int ks = 0; ks < 8; ++ks) {
                load_F((ks + 2) & 7);
                if (R == 1 && ks == 4 && wave == 7) {
                    const int pb = 32 * mem + (lane & 31);
                    f2oth = WS_F2 + (u32)((lane < 32 ? ((pb & 7) * 4 + (pb >> 6)) * 8 + ((pb >> 3) & 7) : max(grp * 8 - 8, 0) + (lane & 15)) * 128);
                    ald2s(flg, f2oth);
                }
                SB();
#pragma unroll
                for (int n = 0; n < 4; ++n) accM[n] = mfma32(F[ks][n], __builtin_bit_cast(s16x8, of[ks]), ks ? accM[n] : resid(n));
                m0 = max(max(m0, of[ks][0]), of[ks][1]); m1 = max(max(m1, of[ks][2]), of[ks][3]);
                SB();
            }
            if (__builtin_expect(__all(max(m0, m1) != SENT), 1)) break;
            __builtin_amdgcn_s_sleep(4);
            WAIT_VM(0);
            sfor<0, 8>([&](auto ic) { constexpr int i = decltype(ic)::value; ald4o<(i & 3) * 1024>(of[i], ooff + (i >> 2) * 4096, d_ws); });
            WAIT_VM(0);
        }
        STAMP(41);
        if constexpr (R == 1) { if (wave == 7) { WAIT_VM(0); pin(flg); spin_flags(flg, f2oth); } }
    }

    auto load_y = [&]() {
        const int l2 = opq(tid) & 63;
#pragma unroll
        for (int i = 0; i < 8; ++i)
            yw[i] = __builtin_amdgcn_raw_buffer_load_b32(r_ws, WS_Y2 + (u32)((32 * mem + 2 * (l2 >> 4)) * 8192 + (TI * 16 + (l2 & 15)) * 4), (8 * (i >> 1) + (i & 1)) * 8192, 16);
    };
    auto pub_off = [&](u32 ws_st) {
        const int lane = opq(tid) & 63;
        return (lane & 16) ? 0xFFFFFF00u : ws_st + (u32)((TI * 8 + mem) * 256 + (lane & 15) * 16 + (lane >> 5) * 8);
    };
    auto pub_stat = [&](u32 off, u32 tg, float s, float q) {
        auto r = __builtin_amdgcn_permlane32_swap(__float_as_uint(s), __float_as_uint(q), false, false);
        const float v = __uint_as_float(r[0]) + __uint_as_float(r[1]);
        auto t = __builtin_amdgcn_permlane16_swap(__float_as_uint(v), __float_as_uint(v), false, false);
        __builtin_amdgcn_raw_buffer_store_b64((u32x2){__float_as_uint(__uint_as_float(t[0]) + __uint_as_float(t[1])), tg}, r_ws, off, 0, 16);
    };
    auto stat_off = [&](u32 ws_st, int ti) { const int l2 = opq(tid) & 63; return ws_st + (u32)(ti * 2048 + (l2 >> 4) * 512 + (l2 & 15) * 16); };
    auto fin_stat = [&](u32x4& a, u32x4& c, u32 off, float& mean, float& rstd) {
        _Pragma("unroll 1") for (int spin = 0; spin < SPIN_CAP; ++spin) {
            if (__builtin_expect(__all(((a[1] ^ tl) | (a[3] ^ th) | (c[1] ^ tl) | (c[3] ^ th)) == 0), 1)) break;
            __builtin_amdgcn_s_sleep(1);
            asm volatile("buffer_load_dwordx4 %0, %2, %3, 0 offen sc1\n\tbuffer_load_dwordx4 %1, %2, %3, 0 offen offset:256 sc1\n\ts_waitcnt vmcnt(0)"
                         : "=&v"(a), "=&v"(c) : "v"(off), "s"(d_ws) : "memory");
        }
        auto r = __builtin_amdgcn_permlane32_swap(__float_as_uint(__uint_as_float(a[0]) + __uint_as_float(c[0])), __float_as_uint(__uint_as_float(a[2]) + __uint_as_float(c[2])), false, false);
        const float v = __uint_as_float(r[0]) + __uint_as_float(r[1]);
        auto t = __builtin_amdgcn_permlane16_swap(__float_as_uint(v), __float_as_uint(v), false, false);
        const u32 v2 = __float_as_uint(__uint_as_float(t[0]) + __uint_as_float(t[1]));
        auto u = __builtin_amdgcn_permlane32_swap(v2, v2, false, false);
        const float ts = __uint_as_float(u[0]), tq = __uint_as_float(u[1]);
        mean = ts * (1.f / 512.f);
        rstd = __builtin_amdgcn_rsqf(fmaxf(tq * (1.f / 512.f) - mean * mean, 0.f) + 1e-5f);
    };
    auto regroup = [&](u32 (&w)[8]) {
#pragma unroll
        for (int j = 0; j < 2; ++j) {
            auto r0 = __builtin_amdgcn_permlane16_swap(w[4 * j], w[4 * j + 2], false, false);
            auto r1 = __builtin_amdgcn_permlane16_swap(w[4 * j + 1], w[4 * j + 3], false, false);
            w[4 * j] = r0[0]; w[4 * j + 1] = r1[0]; w[4 * j + 2] = r0[1]; w[4 * j + 3] = r1[1];
        }
    };
    auto wo_finish = [&](auto lc) {
        constexpr int LAYER = decltype(lc)::value;
        const int lane = opq(tid) & 63, li = lane & 15, lq = lane >> 4;
        u32 wx = L_WX + hd * 4096 + lane * 16;
        pin(wx);
        u32 po = 0, so = 0, tg = 0, oa = 0;
        if constexpr (R == 1) {
#pragma unroll
            for (int n = 0; n < 4; ++n) *(LAS f32x4*)(lds + wx + n * 1024) = accM[n];
        } else {
            po = pub_off(LAYER ? WS_ST3 : WS_ST1); so = stat_off(LAYER ? WS_ST3 : WS_ST1, TI); tg = lane < 32 ? tl : th;
            const int c = 2 * (lq & 1) + (lq >> 1);
            oa = LAYER ? (u32)((TI * 16 + li) * 1024 + mem * 128 + c * 16) : WS_X2 + (u32)((TI * 8 + mem) * 2048 + li * 128 + ((c ^ (li >> 1)) << 4));
            pin(po); pin(so); pin(tg); pin(oa);
        }
        BAR();
        STAMP(LAYER ? 25 : 42);
        if constexpr (R == 0) {
            f32x4 t[4];
#pragma unroll
            for (int n = 0; n < 4; ++n) t[n] = *(LAS f32x4*)(lds + wx + n * 1024);
            SB();
            float s = 0.f, q = 0.f, s2 = 0.f, q2 = 0.f;
#pragma unroll
            for (int n = 0; n < 4; ++n) {
                const f32x4 v = accM[n] + t[n];
                const u32 w0 = pk2(v[0], v[1]), w1 = pk2(v[2], v[3]);
                xw[2 * n] = w0; xw[2 * n + 1] = w1;
                s = dot2(w0, 0x3F803F80u, s); q = dot2(w0, w0, q); s2 = dot2(w1, 0x3F803F80u, s2); q2 = dot2(w1, w1, q2);
            }
            pub_stat(po, tg, s + s2, q + q2);
            CFENCE();
            if constexpr (!LAYER) load_y();
            regroup(xw);
            SB();
            STAMP(LAYER ? 46 : 43);
            WAIT_VM(0);
            STAMP(LAYER ? 26 : 44);
            if constexpr (!LAYER) {
#pragma unroll
                for (int i = 0; i < 8; ++i) pin(yw[i]);
            }
            u32x4 a, c;
            ald4s(a, so); ald4s(c, so + 256);
            if constexpr (!LAYER) {
                _Pragma("unroll 1") for (int spin = 0; spin < SPIN_CAP; ++spin) {
                    if (__builtin_expect(__all(max(max(max(yw[0], yw[1]), max(yw[2], yw[3])), max(max(yw[4], yw[5]), max(yw[6], yw[7]))) != SENT), 1)) break;
                    __builtin_amdgcn_s_sleep(1);
                    CFENCE();
                    load_y();
                }
                regroup(yw);
            }
            f32x2 xa[8], ya[8], xy[8];
#pragma unroll
            for (int i = 0; i < 8; ++i) {
                xa[i] = (f32x2){bflo(xw[i]), bfhi(xw[i])}; ya[i] = (f32x2){bflo(yw[i]), bfhi(yw[i])}; xy[i] = xa[i] * ya[i];
                pin(xa[i]); pin(ya[i]); pin(xy[i]);
            }
            WAIT_VM(0);
            pin(a); pin(c);
            float mean, rstd;
            fin_stat(a, c, so, mean, rstd);
            STAMP(LAYER ? 11 : 6);
            const float nm = -mean * rstd;
#pragma unroll
            for (int i = 0; i < 8; ++i) {
                const f32x2 o = xy[i] * rstd + (ya[i] * nm + xa[i]);
                xw[i] = pk2(o[0], o[1]);
                if (!LAYER) xw[i] = nosent(xw[i]);
            }
#pragma unroll
            for (int j = 0; j < 2; ++j) {
                const u32x4 o = {xw[4 * j], xw[4 * j + 1], xw[4 * j + 2], xw[4 * j + 3]};
                if constexpr (LAYER) __builtin_amdgcn_raw_buffer_store_b128(o, r_out, oa + j * 64, 0, OUT_AUX);
                else __builtin_amdgcn_raw_buffer_store_b128(o, r_ws, oa ^ (j * 64), 0, 16);
            }
            if constexpr (!LAYER) {
                float s = 0.f, q = 0.f, s2 = 0.f, q2 = 0.f;
#pragma unroll
                for (int i = 0; i < 8; i += 2) { s = dot2(xw[i], 0x3F803F80u, s); q = dot2(xw[i], xw[i], q); s2 = dot2(xw[i + 1], 0x3F803F80u, s2); q2 = dot2(xw[i + 1], xw[i + 1], q2); }
                pub_stat(po + (WS_ST2 - WS_ST1) * !(lane & 16), tg, s + s2, q + q2);
                CFENCE();
                regroup(xw);
#pragma unroll
                for (int i = 0; i < 8; ++i) { pin(xw[i]); pin(yw[i]); }
                STAMP(48);
                WAIT_VM(0);
            }
        }
    };
    wo_finish(IC<0>{});
    if constexpr (R == 1) {
        if (b == 0 && wave == 7) {
            const u32 fo = WS_F1 + (u32)((opq(tid) & 63) * 128);
            u32x2 ef[4];
#pragma unroll
            for (int i = 0; i < 4; ++i) ald2s(ef[i], fo + i * 8192);
            WAIT_VM(0);
#pragma unroll
            for (int i = 0; i < 4; ++i) { pin(ef[i]); spin_flags(ef[i], fo + i * 8192); }
            if (fo == WS_F1) __builtin_amdgcn_raw_buffer_store_b64(__builtin_bit_cast(u32x2, epoch + 1), r_ws, WS_EPOCH, 0, 16);
            WAIT_VM(0);
        }
    }
    RBAR();
    STAMP(7);

    {
        const int l3 = opq(tid) & 63;
        const u32 g0 = (u32)((4 * grp - 1 + (hd >> 1)) * 16384 + (hd & 1) * 1024 + l3 * 16);
        const u32 g2 = (u32)((4 * grp + 3) * 16384 + hd * 512 + l3 * 16);
        auto x2_piece = [&](auto ic) {
            constexpr int i = decltype(ic)::value, s = i / 3, p = i % 3;
            const u32 kp = (u32)((s + rot) & 7);
            if constexpr (p == 0) dma(IC<4>{}, IC<L_CX + s * 10240>{}, d_x2, hb1k, g0, kp * 2048);
            else if constexpr (p == 1) dma(IC<4>{}, IC<L_CX + s * 10240 + 4096>{}, d_x2, hb1k, g0 + 32768, kp * 2048);
            else dma(IC<5>{}, IC<L_CX + s * 10240 + 8192>{}, d_x2, hb512, g2, kp * 2048);
        };
        u32 va = L_CX + hd * 1024 + l3 * 16, vq = L_CX + 8192 + hd * 512 + (l3 & 31) * 16 + (l3 >> 5) * 10240;
        pin(va); pin(vq);
        u32x4 vr[5];
        auto ver_read = [&](auto cc) {
            constexpr int c = decltype(cc)::value;
            u32 a = va + c * 20480, q = vq + c * 20480;
            pin(a); pin(q); CFENCE();
            vr[0] = *(LAS u32x4*)(lds + a); vr[1] = *(LAS u32x4*)(lds + a + 4096); vr[2] = *(LAS u32x4*)(lds + a + 10240); vr[3] = *(LAS u32x4*)(lds + a + 14336);
            vr[4] = *(LAS u32x4*)(lds + q);
        };
        auto ver_check = [&](auto cc) {
            constexpr int c = decltype(cc)::value;
            _Pragma("unroll 1") for (int spin = 0; spin < SPIN_CAP; ++spin) {
                u32 m0 = 0, m1 = 0;
#pragma unroll
                for (int i = 0; i < 5; ++i) { m0 = max(max(m0, vr[i][0]), vr[i][1]); m1 = max(max(m1, vr[i][2]), vr[i][3]); }
                if (__builtin_expect(__all(max(m0, m1) != SENT), 1)) break;
                __builtin_amdgcn_s_sleep(1);
                sfor<6 * c, 6 * c + 6>([&](auto ic) { x2_piece(ic); });
                WAIT_VM(0);
                ver_read(cc);
            }
        };
        const u32 xb0 = (u32)((l3 & 15) * 128 + (((l3 >> 4) ^ ((l3 & 15) >> 1)) << 4));
        if constexpr (R == 1) {
            STAMP(39);
            sfor<0, 12>([&](auto ic) { x2_piece(ic); });
            WAIT_VM(6);
            STAMP(8);
            ver_read(IC<0>{});
            ver_check(IC<0>{});
        } else {
            u32x4 sa, sc, ha, hc;
            const u32 so = stat_off(WS_ST2, 4 * grp + hd);
            const bool halo = hd == 0 && grp > 0;
            ald4s(sa, so); ald4s(sc, so + 256);
            if (halo) { ald4s(ha, so - 2048); ald4s(hc, so - 2048 + 256); }
            WAIT_VM(0);
            pin(sa); pin(sc);
            float mean, rstd;
            fin_stat(sa, sc, so, mean, rstd);
            if (l3 < 16) { *(LAS float*)(lds + L_TAB + 64 + hd * 64 + l3 * 4) = mean; *(LAS float*)(lds + L_TAB + 320 + 64 + hd * 64 + l3 * 4) = rstd; }
            if (halo) {
                pin(ha); pin(hc);
                fin_stat(ha, hc, so - 2048, mean, rstd);
                if (l3 < 16) { *(LAS float*)(lds + L_TAB + l3 * 4) = mean; *(LAS float*)(lds + L_TAB + 320 + l3 * 4) = rstd; }
            }
        }
        BAR();
        STAMP(13);
        u32 xa;
        auto pf_addr = [&](int st) { xa = L_CX + (st >> 1) * 10240 + (xb0 ^ ((st & 1) * 64)); pin(xa); };
        pf_addr(0);
#pragma unroll
        for (int mt = 0; mt < 5; ++mt) Ab[0][mt] = ldsA(xa + mt * 2048);
        sfor<0, 16>([&](auto stc) {
            constexpr int st = decltype(stc)::value;
            if constexpr (R == 1) {
                if constexpr (st == 2) { WAIT_VM(10); ver_read(IC<1>{}); }
                if constexpr (st == 3) ver_check(IC<1>{});
                if constexpr (st == 5) { WAIT_VM(6); ver_read(IC<2>{}); }
                if constexpr (st == 6) { ver_check(IC<2>{}); WAIT_VM(0); ver_read(IC<3>{}); }
                if constexpr (st == 7) ver_check(IC<3>{});
            }
            if constexpr (st == 3) { RBAR(); STAMP(49); }
            if constexpr (st == 7) { RBAR(); STAMP(50); CSTAMP(60); }
            if (st < 15) pf_addr(st + 1);
            if constexpr (st == 14) load_tab(true);
            SB();
            qkv_step(IC<1>{}, st, Ab[st & 1], [&](auto mc) {
                constexpr int mt = decltype(mc)::value;
                if (st < 15) Ab[(st + 1) & 1][mt] = ldsA(xa + mt * 2048);
                if constexpr (R == 1 && 12 + 5 * st + mt < 24) x2_piece(IC<12 + 5 * st + mt>{});
                SB();
            });
        });
    }
    STAMP(R ? 9 : 52);
    CSTAMP(61);
    attention(IC<1>{});
    STAMP(27);

    {
        wo_addr();
#pragma unroll
        for (int ks = 0; ks < 8; ++ks) load_F(ks);
        SB();
        WAIT_VM(0);
        RBAR();
        STAMP(10);
        load_o(WS_O2);
        SB();
        _Pragma("unroll 1") for (int spin = 0; spin < 64; ++spin) {
            u32 m0 = 0, m1 = 0;
#pragma unroll
            for (int ks = 0; ks < 8; ++ks) {
#pragma unroll
                for (int n = 0; n < 4; ++n) accM[n] = mfma32(F[ks][n], __builtin_bit_cast(s16x8, of[ks]), ks ? accM[n] : resid(n));
                m0 = max(max(m0, of[ks][0]), of[ks][1]); m1 = max(max(m1, of[ks][2]), of[ks][3]);
                SB();
            }
            if (__builtin_expect(__all(max(m0, m1) != SENT), 1)) break;
            _Pragma("unroll 1") for (int sp2 = 0; sp2 < SPIN_CAP; ++sp2) {
                CFENCE();
                __builtin_amdgcn_s_sleep(1);
                const u32 pv = __builtin_amdgcn_raw_buffer_load_b32(r_ws, WS_O2 + (u32)(TI * 16384 + R * 8192 + (opq(tid) & 15) * 512 + 508), 0, 16);
                if (__all(pv != SENT)) break;
            }
            CFENCE();
            load_o(WS_O2);
        }
        SB();
        STAMP(45);
    }
    wo_finish(IC<1>{});
}

__global__ void __launch_bounds__(512, 2)
fused_kernel(const u16* __restrict__ gx, const u16* __restrict__ gwqkv, const u16* __restrict__ gwo,
             const u16* __restrict__ gbk, const u16* __restrict__ gbv, const u16* __restrict__ gsk,
             const u16* __restrict__ ghk, const u16* __restrict__ gdv, u16* __restrict__ gout, char* __restrict__ ws) {
    __shared__ __attribute__((aligned(16))) char lds_[L_TOTAL];
    typedef u32 u32x16 __attribute__((ext_vector_type(16)));
    u32x16 ka; u32x4 kb;
    {
        auto kp = __builtin_amdgcn_kernarg_segment_ptr();
        asm volatile("s_load_dwordx16 %0, %2, 0x0\n\ts_load_dwordx4 %1, %2, 0x40\n\ts_waitcnt lgkmcnt(0)" : "=&s"(ka), "=&s"(kb) : "s"(kp) : "memory");
    }
    auto mkp = [](u32 lo, u32 hi) { return (char*)(((u64)hi << 32) | lo); };
    const Args a = {(const u16*)mkp(ka[0], ka[1]), (const u16*)mkp(ka[2], ka[3]), (const u16*)mkp(ka[4], ka[5]), (const u16*)mkp(ka[6], ka[7]), (const u16*)mkp(ka[8], ka[9]),
                    (const u16*)mkp(ka[10], ka[11]), (const u16*)mkp(ka[12], ka[13]), (const u16*)mkp(ka[14], ka[15]), (u16*)mkp(kb[0], kb[1]), mkp(kb[2], kb[3])};
    const int wave = __builtin_amdgcn_readfirstlane(threadIdx.x >> 6);
    if (wave < 4) body<0>(a, (LAS char*)lds_, wave); else body<1>(a, (LAS char*)lds_, wave);
}

extern "C" void kernel_launch(void* const* d_in, const int* in_sizes, int n_in,
                              void* d_out, int out_size, void* d_ws, size_t ws_size,
                              hipStream_t stream) {
    fused_kernel<<<256, 512, 0, stream>>>((const u16*)d_in[0], (const u16*)d_in[1], (const u16*)d_in[2],
                                          (const u16*)d_in[4], (const u16*)d_in[5], (const u16*)d_in[6],
                                          (const u16*)d_in[7], (const u16*)d_in[8], (u16*)d_out, (char*)d_ws);
}
```

```cpp
#include <hip/hip_runtime.h>
#include <stdint.h>

typedef unsigned short u16;
typedef unsigned u32;
typedef unsigned long long u64;
typedef short s16x4 __attribute__((ext_vector_type(4)));
typedef short s16x8 __attribute__((ext_vector_type(8)));
typedef __bf16 bf16x2 __attribute__((ext_vector_type(2)));
typedef float f32x2 __attribute__((ext_vector_type(2)));
typedef float f32x4 __attribute__((ext_vector_type(4)));
typedef u32 u32x2 __attribute__((ext_vector_type(2)));
typedef u32 u32x4 __attribute__((ext_vector_type(4)));
#define LAS __attribute__((address_space(3)))
#define DEV __device__ __forceinline__
typedef __amdgpu_buffer_rsrc_t rsrc_t;

constexpr u32 WS_EPOCH = 0;
constexpr u32 WS_F1 = 32768;
constexpr u32 WS_F2 = 65536;
constexpr u32 WS_ST0 = 98304;
constexpr u32 WS_ST1 = 131072;
constexpr u32 WS_ST2 = WS_ST1 + 262144;
constexpr u32 WS_ST3 = WS_ST2 + 262144;
constexpr u32 WS_UT = 1u << 20;
constexpr u32 WS_O1 = WS_UT + (2u << 20);
constexpr u32 WS_O2 = WS_O1 + (2u << 20);
constexpr u32 WS_Y2 = WS_O2 + (2u << 20);
constexpr u32 WS_X2 = WS_Y2 + (2u << 20);
constexpr u32 WS_END = WS_X2 + (2u << 20);

constexpr u32 L_SLOT = 34816;
constexpr u32 L_KV = 4 * L_SLOT;
constexpr u32 L_K = L_KV, L_V = L_KV + 9216;
constexpr u32 L_SK = L_KV + 17408;
constexpr u32 L_WO = 0;
constexpr u32 L_CX = 65536;
constexpr u32 L_HC = L_CX;
constexpr u32 L_UR = L_CX + 69632;
constexpr u32 L_WX = L_CX + 40960;
constexpr u32 L_EX = 147456;
constexpr u32 L_TAB = L_EX + 12288;
constexpr u32 L_CS = L_TAB + 1024;
constexpr u32 L_FLG = L_TAB + 2048;
constexpr u32 L_TOTAL = 160 * 1024;
static_assert(L_FLG + 64 <= L_TOTAL, "LDS map");

constexpr int SPIN_CAP = 1 << 14;
constexpr u32 SENT = 0xFFFFFFFFu;

#define WAIT_VM(n) asm volatile("s_waitcnt vmcnt(%0)" ::"i"(n) : "memory")
#define BAR() do { asm volatile("s_waitcnt lgkmcnt(0)" ::: "memory"); __builtin_amdgcn_s_barrier(); asm volatile("" ::: "memory"); } while (0)
#define RBAR() do { asm volatile("" ::: "memory"); __builtin_amdgcn_s_barrier(); asm volatile("" ::: "memory"); } while (0)
#define CFENCE() asm volatile("" ::: "memory")
#define SB() __builtin_amdgcn_sched_barrier(0)
template <int N> struct IC { static constexpr int value = N; };
template <int I, int N, class F> DEV void sfor(F&& f) { if constexpr (I < N) { f(IC<I>{}); sfor<I + 1, N>(f); } }

DEV f32x4 mfma32(s16x8 a, s16x8 b, f32x4 c) { return __builtin_amdgcn_mfma_f32_16x16x32_bf16(a, b, c, 0, 0, 0); }
DEV f32x4 mfma16(s16x4 a, s16x4 b, f32x4 c) { return __builtin_amdgcn_mfma_f32_16x16x16bf16_1k(a, b, c, 0, 0, 0); }
DEV float vmax(float a, float b) { float r; asm("v_max_f32 %0, %1, %2" : "=v"(r) : "v"(a), "v"(b)); return r; }
DEV u32 pk2(float lo, float hi) { f32x2 f = {lo, hi}; bf16x2 b = __builtin_convertvector(f, bf16x2); return __builtin_bit_cast(u32, b); }
DEV float bflo(u32 w) { return __uint_as_float(w << 16); }
DEV float bfhi(u32 w) { return __uint_as_float(w & 0xffff0000u); }
DEV float dot2(u32 a, u32 b, float c) { return __builtin_amdgcn_fdot2_f32_bf16(__builtin_bit_cast(bf16x2, a), __builtin_bit_cast(bf16x2, b), c, false); }
DEV u32 nosent(u32 w) { return min(w, 0xFFFFFFFEu); }
DEV float rowsum16(float v) {
    v += __builtin_bit_cast(float, __builtin_amdgcn_update_dpp(0, __builtin_bit_cast(int, v), 0xB1, 0xf, 0xf, true));
    v += __builtin_bit_cast(float, __builtin_amdgcn_update_dpp(0, __builtin_bit_cast(int, v), 0x4E, 0xf, 0xf, true));
    v += __builtin_bit_cast(float, __builtin_amdgcn_update_dpp(0, __builtin_bit_cast(int, v), 0x141, 0xf, 0xf, true));
    v += __builtin_bit_cast(float, __builtin_amdgcn_update_dpp(0, __builtin_bit_cast(int, v), 0x140, 0xf, 0xf, true));
    return v;
}
DEV float bcast_row0(float v) { return __builtin_bit_cast(float, __builtin_amdgcn_update_dpp(0, __builtin_bit_cast(int, v), 0x150, 0xf, 0xf, true)); }
template <int OFF> DEV void ald4o(u32x4& d, u32 voff, u32x4 rs) { asm volatile("buffer_load_dwordx4 %0, %1, %2, 0 offen offset:%3 sc1" : "=v"(d) : "v"(voff), "s"(rs), "i"(OFF) : "memory"); }
template <class T> DEV void pin(T& v) { asm volatile("" : "+v"(v)); }
DEV int opq(int v) { asm volatile("" : "+v"(v)); return v; }
DEV s16x8 cat8(s16x4 a, s16x4 b) { return __builtin_shufflevector(a, b, 0, 1, 2, 3, 4, 5, 6, 7); }
DEV s16x4 pack4(f32x4 v) { u32x2 w = {pk2(v[0], v[1]), pk2(v[2], v[3])}; return __builtin_bit_cast(s16x4, w); }
DEV u64 mix64(u64 z) { z += 0x9E3779B97F4A7C15ull; z = (z ^ (z >> 30)) * 0xBF58476D1CE4E5B9ull; z = (z ^ (z >> 27)) * 0x94D049BB133111EBull; return z ^ (z >> 31); }

struct ConvSched {
    int n[4], f[18], i[18], slot[64];
    constexpr ConvSched(int R) : n{}, f{}, i{}, slot{} {
        constexpr int LEAD = 9;
        const int N0[4] = {0, 3, 4, 7};
        for (int k = 0; k < 4; ++k) n[k] = N0[k] ^ R;
        int c = 0;
        for (int ff = 0; ff < 8; ++ff) for (int k = 3; k >= 0; --k) if (ff <= n[k]) { f[c] = ff; i[c] = k; ++c; }
        for (int k = 0; k < 64; ++k) slot[k] = 1000;
        for (int p = 71; p >= 0; --p) {
            const int e = p / 18, ff = f[p % 18], g = n[i[p % 18]] - ff, s = p - LEAD < -1 ? -1 : p - LEAD;
            slot[e * 16 + ff] = s; slot[e * 16 + 8 + g] = s;
        }
    }
};

struct Args { const u16 *gx, *gwqkv, *gwo, *gbk, *gbv, *gsk, *ghk, *gdv; u16* gout; char* ws; };

template <int R>
DEV void body(const Args& A_, LAS char* const lds, const int wave) {
    const u16* gx = A_.gx; char* ws = A_.ws;
    const int b = blockIdx.x, tid = threadIdx.x, lane = tid & 63;
    const int hd = wave & 3;
    const int grp = (b & 7) * 4 + (b >> 6), mem = (b >> 3) & 7;
    const int T0 = grp * 64;
    const int rot = (mem + 2 * (grp & 3)) & 7;

    u64 epoch;
    asm volatile("s_load_dwordx2 %0, %1, 0x0" : "=s"(epoch) : "s"(ws));
    u32 dw;
    asm volatile("s_load_dword %0, %1, 0x0" : "=s"(dw) : "s"(A_.gdv + 2 * b));
    const rsrc_t r_x = __builtin_amdgcn_make_buffer_rsrc((void*)gx, 0, 2048 * 512 * 2, 0x00020000);
    const rsrc_t r_ws = __builtin_amdgcn_make_buffer_rsrc((void*)ws, 0, WS_END, 0x00020000);
    const rsrc_t r_out = __builtin_amdgcn_make_buffer_rsrc((void*)A_.gout, 0, 2048 * 512 * 2, 0x00020000);
    const u32 ldsbase = (u32)(size_t)lds;
    auto mkdesc = [](const void* p, u32 bytes) { const u64 a = (u64)p; return (u32x4){(u32)a, (u32)(a >> 32) & 0xffffu, bytes, 0x00020000u}; };
    const u32x4 d_x = mkdesc(gx, 2048 * 512 * 2), d_w = mkdesc(A_.gwqkv, 512 * 1536 * 2), d_wo = mkdesc(A_.gwo, 512 * 512 * 2);
    const u32x4 d_k = mkdesc(A_.gbk, 2048 * 512 * 2), d_v = mkdesc(A_.gbv, 2048 * 512 * 2), d_sk = mkdesc(A_.gsk, 512 * 4 * 2);
    const u32x4 d_h = mkdesc(A_.ghk, 512 * 2048 * 2), d_x2 = mkdesc(ws + WS_X2, 2u << 20), d_ws = mkdesc(ws, WS_END);
    const u32 wb1k = ldsbase + wave * 1024, hb1k = ldsbase + hd * 1024, hb512 = ldsbase + hd * 512;
#define DMA_ASM(pre, mods, post) asm volatile("s_add_u32 m0, %0, %4\n\t" pre "buffer_load_dwordx4 %1, %2, %3 offen " mods "lds" post \
                     :: "s"(sbase), "v"(voff), "s"(rs), "s"(soff), "i"(IMM) : "memory", "scc")
    const u32 xmlo = hd == 0 ? ~0xFFu : ~0u;
    auto dma = [&](auto kind, auto imm, u32x4 rs, u32 sbase, u32 voff, u32 soff) {
        constexpr int K = decltype(kind)::value; constexpr int IMM = decltype(imm)::value;
        if constexpr (K == 0) DMA_ASM("s_nop 0\n\t", "", "");
        else if constexpr (K == 1) DMA_ASM("s_mov_b32 exec_hi, 0\n\t", "", "\n\ts_mov_b32 exec_hi, -1");
        else if constexpr (K == 2) {
            const u32 ml = xmlo;
            asm volatile("s_add_u32 m0, %0, %4\n\ts_mov_b32 exec_lo, %5\n\tbuffer_load_dwordx4 %1, %2, %3 offen lds\n\ts_mov_b32 exec_lo, -1"
                         :: "s"(sbase), "v"(voff), "s"(rs), "s"(soff), "i"(IMM), "s"(ml) : "memory", "scc");
        }
        else if constexpr (K == 4) DMA_ASM("s_nop 0\n\t", "sc1 ", "");
        else DMA_ASM("s_mov_b32 exec_hi, 0\n\t", "sc1 ", "\n\ts_mov_b32 exec_hi, -1");
    };
    auto ald4 = [&](u32x4& d, u32x4 rs, u32 voff) { asm volatile("buffer_load_dwordx4 %0, %1, %2, 0 offen" : "=v"(d) : "v"(voff), "s"(rs) : "memory"); };
    auto ald4s = [&](u32x4& d, u32 voff) { asm volatile("buffer_load_dwordx4 %0, %1, %2, 0 offen sc1" : "=v"(d) : "v"(voff), "s"(d_ws) : "memory"); };
    auto ald2s = [&](u32x2& d, u32 voff) { asm volatile("buffer_load_dwordx2 %0, %1, %2, 0 offen sc1" : "=v"(d) : "v"(voff), "s"(d_ws) : "memory"); };

    const int pr = lane >> 3, pc = lane & 7;
    const int xr0 = 8 * hd + pr, xr2 = 64 + 4 * hd + pr;
    const u32 xv0 = (u32)((T0 - 16 + xr0) * 1024 + ((pc ^ ((xr0 >> 1) & 7)) << 4));
    const u32 xv2 = (u32)((T0 - 16 + xr2) * 1024 + ((pc ^ ((xr2 >> 1) & 7)) << 4));
    const int wfw = ((xr0 >> 1) & 1) | (((xr0 >> 3) & 1) << 1);
    const u32 wv0 = (u32)(xr0 * 3072 + mem * 128 + ((((pc >> 1) ^ wfw) * 2 + (pc & 1)) << 4));
    auto issue_piece = [&](auto sc, auto ic) {
        constexpr int s = decltype(sc)::value, i = decltype(ic)::value;
        const u32 kp = (u32)((s + rot) & 7);
        constexpr int sb = (s & 3) * L_SLOT;
        if constexpr (i == 0) dma(IC<2>{}, IC<sb>{}, d_x, hb1k, xv0, kp * 128);
        else if constexpr (i == 1) dma(IC<0>{}, IC<sb + 4096>{}, d_x, hb1k, xv0 + 32768, kp * 128);
        else if constexpr (i == 2) dma(IC<1>{}, IC<sb + 8192>{}, d_x, hb512, xv2, kp * 128);
        else dma(IC<0>{}, IC<sb + 10240 + (i - 3) * 4096>{}, d_w, hb1k, wv0, kp * (64 * 3072) + ((i - 3) >> 1) * 1024 + ((i - 3) & 1) * (32 * 3072));
    };
    auto issue_wo = [&](auto ic) {
        constexpr int i = decltype(ic)::value;
        const int l2 = opq(tid) & 63, pr = l2 >> 3, pc = l2 & 7;
        const u32 wov = (u32)((8 * hd + pr) * 1024 + mem * 128 + ((((pc >> 1) ^ (((pr >> 1) & 1) | ((hd & 1) << 1))) * 2 + (pc & 1)) << 4));
        const u32 j = (u32)((i + 2 * grp) & 7);
        dma(IC<0>{}, IC<L_WO + (i & 8) * 4096>{}, d_wo, hb1k + j * 4096, wov, ((i & 8) + j) * 32768);
    };
    auto issue_kv = [&](int i) {
        const int l2 = opq(tid) & 63, pr = l2 >> 3, pc = l2 & 7, p = hd + 4 * (i & 1);
        const u32 cb = (u32)(mem * 128);
        if (i < 2) dma(IC<0>{}, IC<L_K>{}, d_k, hb1k + (i & 1) * 4096, (u32)((T0 - 8 + 8 * p + pr) * 1024) + cb + ((pc ^ p) << 4), 0);
        else if (i < 4) dma(IC<0>{}, IC<L_V>{}, d_v, hb1k + (i & 1) * 4096, (u32)((T0 + 8 * p + pr) * 1024) + cb + ((pc ^ p) << 4), 0);
        else if (hd == 0) dma(IC<0>{}, IC<L_K + 8192>{}, d_k, ldsbase, (u32)((T0 - 8 + 64 + pr) * 1024) + cb + (pc << 4), 0);
        else dma(IC<0>{}, IC<L_SK>{}, d_sk, ldsbase, (u32)(mem * 512 + l2 * 16), 0);
    };

    const int srow = T0 + 8 * mem + 2 * hd;
    u32x4 xrow[2];
    sfor<0, 9>([&](auto ic) { if constexpr ((decltype(ic)::value & 1) != R) issue_piece(IC<0>{}, ic); });
    sfor<0, 9>([&](auto ic) { if constexpr ((decltype(ic)::value & 1) != R) issue_piece(IC<1>{}, ic); });
    asm volatile("s_waitcnt lgkmcnt(0)" : "+s"(epoch), "+s"(dw));
    const u64 tag = mix64(epoch);
    const u32 tl = (u32)tag, th = (u32)(tag >> 32);
    if (R == 1 && wave == 7 && lane < 32) *(LAS u32x4*)(lds + (lane >> 3) * L_SLOT + (lane & 7) * 16) = (u32x4){0x3F803F80u, 0x3F803F80u, 0x3F803F80u, 0x3F803F80u};

    auto trr = [&](u32 off) { return __builtin_amdgcn_ds_read_tr16_b64_v4i16((LAS s16x4*)(lds + off)); };
    auto ldsA = [&](u32 off) { return *(LAS s16x8*)(lds + off); };
    auto flag_ok = [&](const u32x2& f) { return __all(f[0] == tl && f[1] == th); };
    auto spin_flags = [&](u32x2& f, u32 off) {
        _Pragma("unroll 1") for (int spin = 0; spin < SPIN_CAP; ++spin) {
            if (__builtin_expect(flag_ok(f), 1)) break;
            __builtin_amdgcn_s_sleep(2);
            asm volatile("buffer_load_dwordx2 %0, %1, %2, 0 offen sc1\n\ts_waitcnt vmcnt(0)" : "=&v"(f) : "v"(off), "s"(d_ws) : "memory");
        }
    };

    s16x8 Wq[16], Wm[16];
    f32x4 accM[5], accQ[5];
    const f32x4 zero4 = {0.f, 0.f, 0.f, 0.f};
    const s16x8 ones8 = {0x3F80, 0x3F80, 0x3F80, 0x3F80, 0x3F80, 0x3F80, 0x3F80, 0x3F80};
    s16x8 Ab[3][5];
    auto qkv_step = [&](auto lc, int st, const s16x8 (&A)[5], auto fill) {
        sfor<0, 5>([&](auto mc) {
            constexpr int mt = decltype(mc)::value;
            const f32x4 cm = st ? accM[mt] : zero4;
            accM[mt] = R ? mfma32(A[mt], Wm[st], cm) : mfma32(Wm[st], A[mt], cm);
            if constexpr (R == 0 && (mt > 0 || decltype(lc)::value == 0)) accQ[mt] = mfma32(Wq[st], A[mt], st ? accQ[mt] : zero4);
            fill(mc);
        });
    };

    float m1[5], r1[5]; f32x4 m4[5], r4[5], csK, csQ;
    auto load_tab = [&](bool cs) {
        const int l2 = opq(tid) & 63;
        u32 ta = L_TAB + (R ? (l2 >> 4) * 16 : (l2 & 15) * 4);
        pin(ta);
        if (R == 0 && cs) { csK = *(LAS f32x4*)(lds + L_CS + hd * 128 + (l2 >> 4) * 16); csQ = *(LAS f32x4*)(lds + L_CS + hd * 128 + 64 + (l2 >> 4) * 16); }
#pragma unroll
        for (int mt = 0; mt < 5; ++mt) {
            if (R == 0) { m1[mt] = *(LAS float*)(lds + ta + mt * 64); r1[mt] = *(LAS float*)(lds + ta + 320 + mt * 64); }
            else { m4[mt] = *(LAS f32x4*)(lds + ta + mt * 64); r4[mt] = *(LAS f32x4*)(lds + ta + 320 + mt * 64); }
        }
    };

    u32x4 hr[4];
    u32x2 flg;
    {
        const int li = lane & 15, lq = lane >> 4, tq = li >> 2, tp = lane & 3;
        const u32 xa0 = (u32)(li * 128 + ((lq ^ (li >> 1)) << 4));
        const u32 wa = (u32)((8 * lq + tq) * 128 + ((hd ^ (((tq >> 1) & 1) | ((lq & 1) << 1))) << 5) + tp * 8);
        u32 xa, wb, kw[11], vw[8];
        u32x4 s0v, skw;
        auto pf_addr = [&](int st) {
            const u32 sb = (u32)((st >> 1) & 3) * L_SLOT;
            xa = sb + (xa0 ^ ((st & 1) * 64)); wb = sb + 10240 + (st & 1) * 4096 + wa;
            asm volatile("" : "+v"(xa), "+v"(wb));
        };
        auto pf_part = [&](int st, int mt) {
            if (mt == 0) Wm[st] = cat8(trr(wb + (1 + R) * 8192), trr(wb + (1 + R) * 8192 + 512));
            Ab[st & 1][mt] = ldsA(xa + mt * 2048);
            if (R == 0 && mt == 1) Wq[st] = cat8(trr(wb), trr(wb + 512));
        };
        WAIT_VM(R ? 5 : 4);
        BAR();
        pf_addr(0);
#pragma unroll
        for (int mt = 0; mt < 5; ++mt) pf_part(0, mt);
        sfor<0, 16>([&](auto stc) {
            constexpr int st = decltype(stc)::value;
            if constexpr ((st & 1) && st < 15) {
                constexpr int s1 = (st >> 1) + 1;
                if constexpr (R == 0) {
                    if constexpr (s1 == 1) WAIT_VM(7);
                    if constexpr (s1 == 2) WAIT_VM(1);
                    if constexpr (s1 == 5) WAIT_VM(0);
                } else {
                    constexpr int WN[8] = {0, 9, 9, 9, 9, 9, 10, 10};
                    WAIT_VM(WN[s1]);
                    if constexpr (s1 == 7) {
                        pin(s0v);
                        const int l2 = opq(tid) & 63, tr = hd * 20 + min(l2, 19), row = T0 - 16 + tr;
                        _Pragma("unroll 1") for (int spin = 0; spin < SPIN_CAP; ++spin) {
                            if (__builtin_expect(__all(row < 0 || (s0v[1] == tl && s0v[3] == th)), 1)) break;
                            __builtin_amdgcn_s_sleep(2);
                            ald4s(s0v, WS_ST0 + (u32)(max(row, 0) * 16));
                            WAIT_VM(0);
                            pin(s0v);
                        }
                        *(LAS u32*)(lds + L_TAB + tr * 4) = row < 0 ? 0u : s0v[0];
                        *(LAS u32*)(lds + L_TAB + 320 + tr * 4) = row < 0 ? 0x3F800000u : s0v[2];
                        asm volatile("s_waitcnt lgkmcnt(0)" ::: "memory");
                    }
                }
                RBAR();
                if constexpr (s1 == 5 && R == 0) { if (tid == 0) __builtin_amdgcn_raw_buffer_store_b64((u32x2){tl, th}, r_ws, WS_F1 + (mem * 32 + grp) * 128, 0, 16); CFENCE(); }
                if constexpr (s1 == 5 && R == 1) { const int row = T0 - 16 + hd * 20 + min(opq(tid) & 63, 19); ald4s(s0v, WS_ST0 + (u32)(max(row, 0) * 16)); }
            }
            if (st < 15) pf_addr(st + 1);
            if constexpr (st == 14) load_tab(false);
            SB();
            qkv_step(IC<0>{}, st, Ab[st & 1], [&](auto mc) {
                constexpr int mt = decltype(mc)::value;
                if (st < 15) pf_part(st + 1, mt);
                if constexpr (R == 1) {
                    if constexpr (st == 0) { issue_piece(IC<2>{}, IC<2 * mt>{}); if constexpr (mt < 4) issue_piece(IC<2>{}, IC<2 * mt + 1>{}); }
                    if constexpr (st >= 1 && st <= 10 && ((st - 1) & 1) * 5 + mt < 9) issue_piece(IC<((st - 1) >> 1) + 3>{}, IC<((st - 1) & 1) * 5 + mt>{});
                    if constexpr (st == 11 && mt == 0) { const u32 ho = (u32)(b * 8192 + opq(tid) * 16); ald4(hr[2], d_h, ho); ald4(hr[3], d_h, ho + 16); }
                    if constexpr (st == 13 && mt == 0) { if (wave == 7) { const int l2 = opq(tid) & 63; ald2s(flg, WS_F1 + (u32)((l2 < 32 ? (b >> 5) * 32 + l2 : (l2 & 7) * 32 + grp) * 128)); } }
                    if constexpr (st >= 11 && st <= 14 && mt < 4) issue_wo(IC<(st - 11) * 4 + mt>{});
                } else {
                    if constexpr (st == 0) {
                        if constexpr (mt == 0) { const u32 xo = (u32)(srow * 1024 + (opq(tid) & 63) * 16); ald4(xrow[0], d_x, xo); ald4(xrow[1], d_x, xo + 1024); }
                        issue_kv(mt);
                    }
                    if constexpr (st == 11 && mt < 2) { const u32 ho = (u32)(b * 8192 + mt * 4096 + opq(tid) * 16); ald4(hr[2 * mt], d_h, ho); ald4(hr[2 * mt + 1], d_h, ho + 16); }
                }
                SB();
            });
            if constexpr (R == 0 && st == 2) {
                WAIT_VM(5);
                pin(xrow[0]); pin(xrow[1]);
                float s[2] = {0.f, 0.f}, q[2] = {0.f, 0.f};
#pragma unroll
                for (int i = 0; i < 4; ++i)
#pragma unroll
                    for (int k = 0; k < 2; ++k) { s[k] = dot2(xrow[k][i], 0x3F803F80u, s[k]); q[k] = dot2(xrow[k][i], xrow[k][i], q[k]); }
                auto ss = __builtin_amdgcn_permlane32_swap(__float_as_uint(s[0]), __float_as_uint(s[1]), false, false);
                auto qq = __builtin_amdgcn_permlane32_swap(__float_as_uint(q[0]), __float_as_uint(q[1]), false, false);
                float sv1 = rowsum16(__uint_as_float(ss[0]) + __uint_as_float(ss[1])), qv1 = rowsum16(__uint_as_float(qq[0]) + __uint_as_float(qq[1]));
                auto s2 = __builtin_amdgcn_permlane16_swap(__float_as_uint(sv1), __float_as_uint(sv1), false, false);
                auto q2 = __builtin_amdgcn_permlane16_swap(__float_as_uint(qv1), __float_as_uint(qv1), false, false);
                const float m = (__uint_as_float(s2[0]) + __uint_as_float(s2[1])) * (1.f / 512.f);
                const float r = __builtin_amdgcn_rsqf(fmaxf((__uint_as_float(q2[0]) + __uint_as_float(q2[1])) * (1.f / 512.f) - m * m, 0.f) + 1e-5f);
                const u32x4 sv = {__float_as_uint(m), tl, __float_as_uint(r), th};
                const int l2 = opq(tid) & 63;
                __builtin_amdgcn_raw_buffer_store_b128(sv, r_ws, (l2 & 31) == 0 ? WS_ST0 + (u32)((srow + (l2 >> 5)) * 16) : 0xFFFFFF00u, 0, 16);
                CFENCE();
                SB();
            }
            if constexpr (R == 0 && (st == 4 || st == 6)) {
                const int l2 = opq(tid) & 63, tg = l2 & 7, cp = 8 * hd + (l2 >> 3);
                if constexpr (st == 4) {
                    skw = *(LAS u32x4*)(lds + L_SK + cp * 16);
                    u32 ka = L_K + tg * 1024 + ((((cp >> 2) ^ tg) & 7) << 4) + (cp & 3) * 4;
                    u32 kb = L_K + tg * 1024 + ((((cp >> 2) ^ (tg + 1)) & 7) << 4) + (cp & 3) * 4;
                    pin(ka); pin(kb);
#pragma unroll
                    for (int i = 0; i < 11; ++i) kw[i] = *(LAS u32*)(lds + ((i + 5 < 8) ? ka : kb) + (i + 5) * 128);
#pragma unroll
                    for (int i = 0; i < 8; ++i) vw[i] = *(LAS u32*)(lds + ka + (L_V - L_K) + i * 128);
                    SB();
                }
                constexpr bool HI = st == 6;
                auto ex = [&](u32 w) { return HI ? bfhi(w) : bflo(w); };
                const float a0 = bflo(skw[2 * HI]), a1 = bfhi(skw[2 * HI]), a2 = bflo(skw[2 * HI + 1]), a3 = bfhi(skw[2 * HI + 1]);
                float ua[8];
#pragma unroll
                for (int i = 0; i < 8; ++i) ua[i] = (ex(kw[i]) * a3 + ex(kw[i + 1]) * a2 + ex(kw[i + 2]) * a1 + ex(kw[i + 3]) * a0) * ex(vw[i]);
                const u32x4 oa = {pk2(ua[7], ua[6]), pk2(ua[5], ua[4]), pk2(ua[3], ua[2]), pk2(ua[1], ua[0])};
                __builtin_amdgcn_raw_buffer_store_b128(oa, r_ws, WS_UT + (u32)((mem * 64 + 2 * cp) * 4096 + (2040 - T0 - 8 * tg) * 2), HI * 4096, 16);
                __builtin_amdgcn_raw_buffer_store_b128((u32x4){SENT, SENT, SENT, SENT}, r_ws, (u32)((4 * grp + hd) * 16384 + mem * 2048 + HI * 1024 + l2 * 16), WS_O1, 16);
                CFENCE();
                SB();
            }
        });
    }

    u32x4 uu;
    float csV;
    auto attention = [&](auto lc) {
        constexpr int LAYER = decltype(lc)::value;
        const int lane = opq(tid) & 63, li = lane & 15, lq = lane >> 4;
        u32 ex = L_EX + hd * 2048 + lane * 8;
        pin(ex);
        u32 f1ok = 1;
        if constexpr (R == 0) {
            if constexpr (LAYER == 0) {
#pragma unroll
                for (int i = 0; i < 4; ++i) { csK[i] = bcast_row0(accM[0][i]); csQ[i] = bcast_row0(accQ[0][i]); }
                if (li == 0) { *(LAS f32x4*)(lds + L_CS + hd * 128 + lq * 16) = csK; *(LAS f32x4*)(lds + L_CS + hd * 128 + 64 + lq * 16) = csQ; }
            }
            const int par = li & 1;
            const bool cur0 = (4 * lq + par) <= li, cur1 = (4 * lq + par + 2) <= li;
            s16x4 Kb[5], Qb[5];
#pragma unroll
            for (int mt = 0; mt < 5; ++mt) {
                Kb[mt] = pack4((accM[mt] - m1[mt] * csK) * r1[mt]);
                if (mt) Qb[mt] = pack4(accQ[mt] - m1[mt] * csQ);
            }
            f32x4 Sc[5], Sp[5];
#pragma unroll
            for (int mt = 1; mt < 5; ++mt) { Sc[mt] = mfma16(Kb[mt], Qb[mt], zero4); Sp[mt] = mfma16(Kb[mt - 1], Qb[mt], zero4); }
            float s0[5], s1[5], mx[5];
#pragma unroll
            for (int mt = 1; mt < 5; ++mt) {
                const float sp0 = Sp[mt][0], sp1 = Sp[mt][1], sp2 = Sp[mt][2], sp3 = Sp[mt][3], c0 = Sc[mt][0], c1 = Sc[mt][1], c2 = Sc[mt][2], c3 = Sc[mt][3];
                float p0 = par ? sp1 : sp0, p1 = par ? sp3 : sp2, q0 = par ? c1 : c0, q1 = par ? c3 : c2;
                asm volatile("" : "+v"(p0), "+v"(p1), "+v"(q0), "+v"(q1));
                if (mt == 1 && grp == 0) { p0 = -3.0e38f; p1 = -3.0e38f; }
                s0[mt] = cur0 ? q0 : p0; s1[mt] = cur1 ? q1 : p1;
                mx[mt] = vmax(s0[mt], s1[mt]);
            }
#pragma unroll
            for (int mt = 1; mt < 5; ++mt) { auto r = __builtin_amdgcn_permlane32_swap(__float_as_uint(mx[mt]), __float_as_uint(mx[mt]), false, false); mx[mt] = vmax(__uint_as_float(r[0]), __uint_as_float(r[1])); }
#pragma unroll
            for (int mt = 1; mt < 5; ++mt) { auto r = __builtin_amdgcn_permlane16_swap(__float_as_uint(mx[mt]), __float_as_uint(mx[mt]), false, false); mx[mt] = vmax(__uint_as_float(r[0]), __uint_as_float(r[1])); }
#pragma unroll
            for (int mt = 1; mt < 5; ++mt) {
                const float sc = r1[mt] * (0.25f * 1.44269504088896341f);
                const float e0 = __builtin_amdgcn_exp2f((s0[mt] - mx[mt]) * sc), e1 = __builtin_amdgcn_exp2f((s1[mt] - mx[mt]) * sc);
                *(LAS u32x2*)(lds + ex + (mt - 1) * 512) = (u32x2){pk2(e0, e0), pk2(e1, e1)};
            }
            BAR();
            if constexpr (LAYER == 0) f1ok = __builtin_amdgcn_readfirstlane(*(LAS u32*)(lds + L_FLG));
        } else {
            if constexpr (LAYER == 0) {
                auto s = __builtin_amdgcn_permlane16_swap(__float_as_uint(accM[0][0]), __float_as_uint(accM[0][0]), false, false);
                auto t = __builtin_amdgcn_permlane32_swap(s[0], s[0], false, false);
                csV = __uint_as_float(t[0]);
                if (wave == 7) { WAIT_VM(8); pin(flg); const u32 ok = flag_ok(flg); *(LAS u32*)(lds + L_FLG) = ok; }
            }
            const int par = li & 1;
            const bool cur0 = (4 * lq + par) <= li, cur1 = (4 * lq + par + 2) <= li;
            const u32 pm = par ? 0xffff0000u : 0x0000ffffu;
            u32 mC0 = cur0 ? pm : 0u, mP0 = cur0 ? 0u : pm, mC1 = cur1 ? pm : 0u, mP1 = cur1 ? 0u : pm;
            u32 oo = (LAYER ? WS_O2 : WS_O1) + (u32)((4 * grp + (lq & 1)) * 16384 + (4 * mem + hd) * 512 + (lq >> 1) * 256 + li * 16);
            asm volatile("" : "+v"(mC0), "+v"(mP0), "+v"(mC1), "+v"(mP1), "+v"(oo));
            s16x4 Vb[5];
#pragma unroll
            for (int mt = 0; mt < 5; ++mt) { Vb[mt] = pack4((accM[mt] - m4[mt] * csV) * r4[mt]); pin(Vb[mt]); }
            BAR();
            u32x2 pw[4];
#pragma unroll
            for (int j = 0; j < 4; ++j) pw[j] = *(LAS u32x2*)(lds + ex + j * 512);
            if constexpr (LAYER == 0) {
                f1ok = __builtin_amdgcn_readfirstlane(*(LAS u32*)(lds + L_FLG));
                if (f1ok) ald4s(uu, WS_UT + (u32)(b * 8192 + opq(tid) * 16));
            }
            SB();
            f32x4 O[4], L[4];
#pragma unroll
            for (int j = 0; j < 4; ++j) {
                const s16x8 P = __builtin_bit_cast(s16x8, (u32x4){pw[j][0] & mC0, pw[j][1] & mC1, pw[j][0] & mP0, pw[j][1] & mP1});
                L[j] = mfma32(ones8, P, zero4);
                O[j] = mfma32(cat8(Vb[j + 1], Vb[j]), P, zero4);
            }
            u32 ow[4][2];
#pragma unroll
            for (int j = 0; j < 4; ++j) {
                const float inv = __builtin_amdgcn_rcpf(L[j][0]);
                ow[j][0] = nosent(pk2(O[j][0] * inv, O[j][1] * inv)); ow[j][1] = nosent(pk2(O[j][2] * inv, O[j][3] * inv));
            }
#pragma unroll
            for (int j = 0; j < 4; j += 2) {
                auto r0 = __builtin_amdgcn_permlane16_swap(ow[j][0], ow[j + 1][0], false, false);
                auto r1 = __builtin_amdgcn_permlane16_swap(ow[j][1], ow[j + 1][1], false, false);
                __builtin_amdgcn_raw_buffer_store_b128((u32x4){r0[0], r1[0], r0[1], r1[1]}, r_ws, oo, j * 16384, 16);
            }
            CFENCE();
        }
        if constexpr (LAYER == 0 && R == 0) { if (f1ok) ald4s(uu, WS_UT + (u32)(b * 8192 + opq(tid) * 16)); }
        SB();
        return f1ok;
    };
    const u32 f1ok = attention(IC<0>{});
    {
        const u32x4 sv = {SENT, SENT, SENT, SENT};
        const int t2 = opq(tid);
        const u32 tb = (u32)((4 * grp + hd) * 16384 + mem * 2048 + R * 1024 + (t2 & 63) * 16);
        __builtin_amdgcn_raw_buffer_store_b128(sv, r_ws, tb, WS_O2, 16);
        __builtin_amdgcn_raw_buffer_store_b128(sv, r_ws, tb, WS_X2, 16);
        __builtin_amdgcn_raw_buffer_store_b128(sv, r_ws, (u32)(b * 8192 + t2 * 16), WS_Y2, 16);
        CFENCE();
        SB();
    }

    const int TI = 4 * grp + hd;
    u32x4 of[8];
    auto load_o = [&](u32 ws_o) {
        const int lane = opq(tid) & 63;
#pragma unroll
        for (int i = 0; i < 8; ++i) of[i] = __builtin_amdgcn_raw_buffer_load_b128(r_ws, ws_o + (u32)(TI * 16384 + R * 8192 + lane * 16) + (i & 3) * 1024, (i >> 2) * 4096, 16);
    };
    u32 xw[8], yw[8];
    u32 woa[4];
    auto resid = [&](int n) { return R ? zero4 : (f32x4){bflo(xw[2 * n]), bfhi(xw[2 * n]), bflo(xw[2 * n + 1]), bfhi(xw[2 * n + 1])}; };
    s16x8 F[8][4];
    auto wo_addr = [&]() {
        const int l2 = opq(tid) & 63, lq = l2 >> 4, tq = (l2 & 15) >> 2, tp = l2 & 3;
#pragma unroll
        for (int n = 0; n < 4; ++n) woa[n] = L_WO + (u32)(R * 32768 + (8 * lq + tq) * 128 + ((n ^ (((lq & 1) << 1) | (tq >> 1))) << 5) + tp * 8);
    };
    auto load_F = [&](int ks) {
#pragma unroll
        for (int n = 0; n < 4; ++n) F[ks][n] = cat8(trr(woa[n] + ks * 4096), trr(woa[n] + ks * 4096 + 512));
    };

    {
        const int t2 = opq(tid), lane = t2 & 63, li = lane & 15, lq = lane >> 4;
        constexpr u32 OFFS = 0xEDA85410u;
        {
            const int j = t2 & 255;
            {
                if constexpr (R == 0) { if (f1ok) WAIT_VM(4); else WAIT_VM(3); }
                else WAIT_VM(5);
                u32 cb = L_HC + R * 34816 + (j + 2) * 16;
                pin(cb);
#pragma unroll
                for (int ch = R; ch < 2; ++ch) {
                    pin(hr[2 * ch]); pin(hr[2 * ch + 1]);
                    const u32 d[8] = {hr[2 * ch][0], hr[2 * ch][1], hr[2 * ch][2], hr[2 * ch][3], hr[2 * ch + 1][0], hr[2 * ch + 1][1], hr[2 * ch + 1][2], hr[2 * ch + 1][3]};
                    u32 e[7];
#pragma unroll
                    for (int i = 0; i < 7; ++i) e[i] = __builtin_amdgcn_alignbit(d[i + 1], d[i], 16);
                    if (ch > R) { cb += 34816; pin(cb); }
#pragma unroll
                    for (int p = (R ? 4 : 0); p < ((ch && !R) ? 4 : 8); ++p) {
                        const u32 po = (272 * p + ((OFFS >> (4 * p)) & 15)) * 16;
                        const int h = p >> 1;
                        const u32x4 w = (p & 1) ? (u32x4){e[h], e[h + 1], e[h + 2], e[h + 3]} : (u32x4){d[h], d[h + 1], d[h + 2], d[h + 3]};
                        *(LAS u32x4*)(lds + cb + po) = w;
                    }
                }
            }
            if constexpr (R == 0) {
                if (hd == 0) {
                    u32 hh[4];
#pragma unroll
                    for (int i = 0; i < 4; ++i) { const u32 a = __builtin_amdgcn_readfirstlane(hr[0][i]), c = __builtin_amdgcn_readfirstlane(hr[2][i]); hh[i] = (lane & 16) ? c : a; }
                    if (lane < 32) {
                        const int p = lane & 7;
                        const u32 po = (272 * p + ((OFFS >> (4 * p)) & 15)) * 16;
                        const unsigned __int128 hv = ((unsigned __int128)(((u64)hh[3] << 32) | hh[2]) << 64) | (((u64)hh[1] << 32) | hh[0]);
                        const unsigned __int128 sv = (p == 0 || !(lane & 8)) ? (unsigned __int128)0 : (hv << (16 * (8 - p)));
                        const u32x4 w = {(u32)sv, (u32)(sv >> 32), (u32)(sv >> 64), (u32)(sv >> 96)};
                        *(LAS u32x4*)(lds + L_HC + (lane >> 4) * 34816 + po + ((lane >> 3) & 1) * 16) = w;
                    }
                }
            }
            if (hd == 1 && lane < 30) *(LAS u32x4*)(lds + L_UR + R * 4576 + 4096 + lane * 16) = (u32x4){0, 0, 0, 0};
            if (!f1ok) {
                const u32 fo = WS_F1 + (u32)(((lane & 8) ? (lane & 7) * 32 + grp : (b >> 5) * 32 + 31 - hd * 8 - (lane & 7)) * 128);
                flg = (u32x2){0u, 0u};
                spin_flags(flg, fo);
                ald4s(uu, WS_UT + (u32)(b * 8192 + t2 * 16));
            }
            if (f1ok) WAIT_VM(R ? 5 : 3); else WAIT_VM(0);
            pin(uu);
            *(LAS u32x4*)(lds + L_UR + R * 4576 + j * 16) = uu;
        }
        BAR();
        const int ch = hd >> 1, e0 = (hd & 1) * 4;
        const int p = (li + 1) & 7;
        u32 aA = L_HC + ch * 34816 + (272 * p + ((OFFS >> (4 * p)) & 15) + ((li + 1) >> 3) + lq + 4 * e0) * 16;
        u32 aB = L_UR + ch * 4576 + (254 - 224 - 2 * li + lq + 4 * e0) * 16;
        asm volatile("" : "+v"(aA), "+v"(aB));
        f32x4 y[4];
        u32 ooff = WS_O1 + (u32)(TI * 16384 + R * 8192 + lane * 16);
        {
            constexpr ConvSched S(R);
            s16x8 A[4][8], B[4][8];
            auto loads_at = [&](auto pc) {
                constexpr int p = decltype(pc)::value;
                sfor<0, 64>([&](auto ic) {
                    constexpr int i = decltype(ic)::value, e = i >> 4, f = i & 7;
                    if constexpr (S.slot[i] == p) {
                        if constexpr (i & 8) B[e][f] = ldsA(aB + 64 * e + 512 * (7 - f)); else A[e][f] = ldsA(aA + 64 * (8 * f + e));
                    }
                });
            };
            loads_at(IC<-1>{});
            SB();
            sfor<0, 72>([&](auto pc) {
                constexpr int p = decltype(pc)::value, e = p / 18, f = S.f[p % 18], i = S.i[p % 18], g = S.n[i] - f;
                y[i] = mfma32(A[e][f], B[e][g], (e == 0 && f == 0) ? zero4 : y[i]);
                loads_at(pc);
                if constexpr (p == 54) WAIT_VM(0);
                if constexpr (p >= 55 && p < 71 && (p & 1)) {
                    constexpr int i = (p - 55) >> 1;
                    ald4o<(i & 3) * 1024>(of[i], ooff + (i >> 2) * 4096, d_ws);
                }
                SB();
            });
        }
        RBAR();
        if (R == 0 && tid == 0) __builtin_amdgcn_raw_buffer_store_b64((u32x2){tl, th}, r_ws, WS_F2 + (grp * 8 + mem) * 128, 0, 16);
        {
            constexpr int N0[4] = {0, 3, 4, 7};
            u32 ra = L_HC + hd * 8192 + lane * 16;
            asm volatile("" : "+v"(ra));
#pragma unroll
            for (int i = 0; i < 4; ++i) *(LAS f32x4*)(lds + ra + (N0[i] ^ R) * 1024) = y[i];
        }
        SB();
        if constexpr (R == 0) {
#pragma unroll
            for (int n = 0; n < 4; ++n) { const u32x2 w = __builtin_amdgcn_raw_buffer_load_b64(r_x, (u32)((TI * 16 + li) * 1024 + mem * 128 + lq * 8) + n * 32, 0, 0); xw[2 * n] = w[0]; xw[2 * n + 1] = w[1]; }
        }
        wo_addr();
        SB();
        BAR();
        {
            const float D0 = bflo(dw), D1 = bfhi(dw);
            u32 ra = L_HC + wave * 1024 + lane * 16;
            const int tb = 256 * wave + 16 * li + 4 * lq;
            u32 ru = L_UR + (2044 - tb) * 2;
            asm volatile("" : "+v"(ra), "+v"(ru));
            const f32x4 y0 = *(LAS f32x4*)(lds + ra), y1 = *(LAS f32x4*)(lds + ra + 8192), y2 = *(LAS f32x4*)(lds + ra + 16384), y3 = *(LAS f32x4*)(lds + ra + 24576);
            const u32x2 ua = *(LAS u32x2*)(lds + ru), ub = *(LAS u32x2*)(lds + ru + 4576);
            load_F(0); load_F(1);
            SB();
            const f32x4 ya = y0 + y1, yb = y2 + y3;
            const u32x4 w = {nosent(pk2(ya[0] + bfhi(ua[1]) * D0, yb[0] + bfhi(ub[1]) * D1)), nosent(pk2(ya[1] + bflo(ua[1]) * D0, yb[1] + bflo(ub[1]) * D1)),
                             nosent(pk2(ya[2] + bfhi(ua[0]) * D0, yb[2] + bfhi(ub[0]) * D1)), nosent(pk2(ya[3] + bflo(ua[0]) * D0, yb[3] + bflo(ub[0]) * D1))};
            __builtin_amdgcn_raw_buffer_store_b128(w, r_ws, WS_Y2 + (u32)(b * 8192 + tb * 4), 0, 16);
        }
        SB();
        WAIT_VM(1);

        u32 f2oth = 0;
        _Pragma("unroll 1") for (int attempt = 0; attempt < 64; ++attempt) {
            sfor<0, 8>([&](auto ic) { pin(of[decltype(ic)::value]); });
            u32 m0 = 0, m1 = 0;
#pragma unroll
            for (int ks = 0; ks < 8; ++ks) {
                load_F((ks + 2) & 7);
                if (R == 1 && ks == 4 && wave == 7) {
                    const int pb = 32 * mem + (lane & 31);
                    f2oth = WS_F2 + (u32)((lane < 32 ? ((pb & 7) * 4 + (pb >> 6)) * 8 + ((pb >> 3) & 7) : max(grp * 8 - 8, 0) + (lane & 15)) * 128);
                    ald2s(flg, f2oth);
                }
                SB();
#pragma unroll
                for (int n = 0; n < 4; ++n) accM[n] = mfma32(F[ks][n], __builtin_bit_cast(s16x8, of[ks]), ks ? accM[n] : resid(n));
                m0 = max(max(m0, of[ks][0]), of[ks][1]); m1 = max(max(m1, of[ks][2]), of[ks][3]);
                SB();
            }
            if (__builtin_expect(__all(max(m0, m1) != SENT), 1)) break;
            __builtin_amdgcn_s_sleep(4);
            WAIT_VM(0);
            sfor<0, 8>([&](auto ic) { constexpr int i = decltype(ic)::value; ald4o<(i & 3) * 1024>(of[i], ooff + (i >> 2) * 4096, d_ws); });
            WAIT_VM(0);
        }
        if constexpr (R == 1) { if (wave == 7) { WAIT_VM(0); pin(flg); spin_flags(flg, f2oth); } }
    }

    auto load_y = [&]() {
        const int l2 = opq(tid) & 63;
#pragma unroll
        for (int i = 0; i < 8; ++i)
            yw[i] = __builtin_amdgcn_raw_buffer_load_b32(r_ws, WS_Y2 + (u32)((32 * mem + 2 * (l2 >> 4)) * 8192 + (TI * 16 + (l2 & 15)) * 4), (8 * (i >> 1) + (i & 1)) * 8192, 16);
    };
    auto pub_off = [&](u32 ws_st) {
        const int lane = opq(tid) & 63;
        return (lane & 16) ? 0xFFFFFF00u : ws_st + (u32)((TI * 8 + mem) * 256 + (lane & 15) * 16 + (lane >> 5) * 8);
    };
    auto pub_stat = [&](u32 off, u32 tg, float s, float q) {
        auto r = __builtin_amdgcn_permlane32_swap(__float_as_uint(s), __float_as_uint(q), false, false);
        const float v = __uint_as_float(r[0]) + __uint_as_float(r[1]);
        auto t = __builtin_amdgcn_permlane16_swap(__float_as_uint(v), __float_as_uint(v), false, false);
        __builtin_amdgcn_raw_buffer_store_b64((u32x2){__float_as_uint(__uint_as_float(t[0]) + __uint_as_float(t[1])), tg}, r_ws, off, 0, 16);
    };
    auto stat_off = [&](u32 ws_st, int ti) { const int l2 = opq(tid) & 63; return ws_st + (u32)(ti * 2048 + (l2 >> 4) * 512 + (l2 & 15) * 16); };
    auto fin_stat = [&](u32x4& a, u32x4& c, u32 off, float& mean, float& rstd) {
        _Pragma("unroll 1") for (int spin = 0; spin < SPIN_CAP; ++spin) {
            if (__builtin_expect(__all(((a[1] ^ tl) | (a[3] ^ th) | (c[1] ^ tl) | (c[3] ^ th)) == 0), 1)) break;
            __builtin_amdgcn_s_sleep(1);
            asm volatile("buffer_load_dwordx4 %0, %2, %3, 0 offen sc1\n\tbuffer_load_dwordx4 %1, %2, %3, 0 offen offset:256 sc1\n\ts_waitcnt vmcnt(0)"
                         : "=&v"(a), "=&v"(c) : "v"(off), "s"(d_ws) : "memory");
        }
        auto r = __builtin_amdgcn_permlane32_swap(__float_as_uint(__uint_as_float(a[0]) + __uint_as_float(c[0])), __float_as_uint(__uint_as_float(a[2]) + __uint_as_float(c[2])), false, false);
        const float v = __uint_as_float(r[0]) + __uint_as_float(r[1]);
        auto t = __builtin_amdgcn_permlane16_swap(__float_as_uint(v), __float_as_uint(v), false, false);
        const u32 v2 = __float_as_uint(__uint_as_float(t[0]) + __uint_as_float(t[1]));
        auto u = __builtin_amdgcn_permlane32_swap(v2, v2, false, false);
        const float ts = __uint_as_float(u[0]), tq = __uint_as_float(u[1]);
        mean = ts * (1.f / 512.f);
        rstd = __builtin_amdgcn_rsqf(fmaxf(tq * (1.f / 512.f) - mean * mean, 0.f) + 1e-5f);
    };
    auto regroup = [&](u32 (&w)[8]) {
#pragma unroll
        for (int j = 0; j < 2; ++j) {
            auto r0 = __builtin_amdgcn_permlane16_swap(w[4 * j], w[4 * j + 2], false, false);
            auto r1 = __builtin_amdgcn_permlane16_swap(w[4 * j + 1], w[4 * j + 3], false, false);
            w[4 * j] = r0[0]; w[4 * j + 1] = r1[0]; w[4 * j + 2] = r0[1]; w[4 * j + 3] = r1[1];
        }
    };
    auto wo_finish = [&](auto lc) {
        constexpr int LAYER = decltype(lc)::value;
        const int lane = opq(tid) & 63, li = lane & 15, lq = lane >> 4;
        u32 wx = L_WX + hd * 4096 + lane * 16;
        pin(wx);
        u32 po = 0, so = 0, tg = 0, oa = 0;
        if constexpr (R == 1) {
#pragma unroll
            for (int n = 0; n < 4; ++n) *(LAS f32x4*)(lds + wx + n * 1024) = accM[n];
        } else {
            po = pub_off(LAYER ? WS_ST3 : WS_ST1); so = stat_off(LAYER ? WS_ST3 : WS_ST1, TI); tg = lane < 32 ? tl : th;
            const int c = 2 * (lq & 1) + (lq >> 1);
            oa = LAYER ? (u32)((TI * 16 + li) * 1024 + mem * 128 + c * 16) : WS_X2 + (u32)((TI * 8 + mem) * 2048 + li * 128 + ((c ^ (li >> 1)) << 4));
            pin(po); pin(so); pin(tg); pin(oa);
        }
        BAR();
        if constexpr (R == 0) {
            f32x4 t[4];
#pragma unroll
            for (int n = 0; n < 4; ++n) t[n] = *(LAS f32x4*)(lds + wx + n * 1024);
            SB();
            float s = 0.f, q = 0.f, s2 = 0.f, q2 = 0.f;
#pragma unroll
            for (int n = 0; n < 4; ++n) {
                const f32x4 v = accM[n] + t[n];
                const u32 w0 = pk2(v[0], v[1]), w1 = pk2(v[2], v[3]);
                xw[2 * n] = w0; xw[2 * n + 1] = w1;
                s = dot2(w0, 0x3F803F80u, s); q = dot2(w0, w0, q); s2 = dot2(w1, 0x3F803F80u, s2); q2 = dot2(w1, w1, q2);
            }
            pub_stat(po, tg, s + s2, q + q2);
            CFENCE();
            if constexpr (!LAYER) load_y();
            regroup(xw);
            SB();
            WAIT_VM(0);
            if constexpr (!LAYER) {
#pragma unroll
                for (int i = 0; i < 8; ++i) pin(yw[i]);
            }
            u32x4 a, c;
            ald4s(a, so); ald4s(c, so + 256);
            if constexpr (!LAYER) {
                _Pragma("unroll 1") for (int spin = 0; spin < SPIN_CAP; ++spin) {
                    if (__builtin_expect(__all(max(max(max(yw[0], yw[1]), max(yw[2], yw[3])), max(max(yw[4], yw[5]), max(yw[6], yw[7]))) != SENT), 1)) break;
                    __builtin_amdgcn_s_sleep(1);
                    CFENCE();
                    load_y();
                }
                regroup(yw);
            }
            f32x2 xa[8], ya[8], xy[8];
#pragma unroll
            for (int i = 0; i < 8; ++i) {
                xa[i] = (f32x2){bflo(xw[i]), bfhi(xw[i])}; ya[i] = (f32x2){bflo(yw[i]), bfhi(yw[i])}; xy[i] = xa[i] * ya[i];
                pin(xa[i]); pin(ya[i]); pin(xy[i]);
            }
            WAIT_VM(0);
            pin(a); pin(c);
            float mean, rstd;
            fin_stat(a, c, so, mean, rstd);
            const float nm = -mean * rstd;
#pragma unroll
            for (int i = 0; i < 8; ++i) {
                const f32x2 o = xy[i] * rstd + (ya[i] * nm + xa[i]);
                xw[i] = pk2(o[0], o[1]);
                if (!LAYER) xw[i] = nosent(xw[i]);
            }
#pragma unroll
            for (int j = 0; j < 2; ++j) {
                const u32x4 o = {xw[4 * j], xw[4 * j + 1], xw[4 * j + 2], xw[4 * j + 3]};
                if constexpr (LAYER) __builtin_amdgcn_raw_buffer_store_b128(o, r_out, oa + j * 64, 0, 0);
                else __builtin_amdgcn_raw_buffer_store_b128(o, r_ws, oa ^ (j * 64), 0, 16);
            }
            if constexpr (!LAYER) {
                float s = 0.f, q = 0.f, s2 = 0.f, q2 = 0.f;
#pragma unroll
                for (int i = 0; i < 8; i += 2) { s = dot2(xw[i], 0x3F803F80u, s); q = dot2(xw[i], xw[i], q); s2 = dot2(xw[i + 1], 0x3F803F80u, s2); q2 = dot2(xw[i + 1], xw[i + 1], q2); }
                pub_stat(po + (WS_ST2 - WS_ST1) * !(lane & 16), tg, s + s2, q + q2);
                CFENCE();
                regroup(xw);
#pragma unroll
                for (int i = 0; i < 8; ++i) { pin(xw[i]); pin(yw[i]); }
                WAIT_VM(0);
            }
        }
    };
    wo_finish(IC<0>{});
    if constexpr (R == 1) {
        if (b == 0 && wave == 7) {
            const u32 fo = WS_F1 + (u32)((opq(tid) & 63) * 128);
            u32x2 ef[4];
#pragma unroll
            for (int i = 0; i < 4; ++i) ald2s(ef[i], fo + i * 8192);
            WAIT_VM(0);
#pragma unroll
            for (int i = 0; i < 4; ++i) { pin(ef[i]); spin_flags(ef[i], fo + i * 8192); }
            if (fo == WS_F1) __builtin_amdgcn_raw_buffer_store_b64(__builtin_bit_cast(u32x2, epoch + 1), r_ws, WS_EPOCH, 0, 16);
            WAIT_VM(0);
        }
    }
    RBAR();

    {
        const int l3 = opq(tid) & 63;
        const u32 g0 = (u32)((4 * grp - 1 + (hd >> 1)) * 16384 + (hd & 1) * 1024 + l3 * 16);
        const u32 g2 = (u32)((4 * grp + 3) * 16384 + hd * 512 + l3 * 16);
        auto x2_piece = [&](auto ic) {
            constexpr int i = decltype(ic)::value, s = i / 3, p = i % 3;
            const u32 kp = (u32)((s + rot) & 7);
            if constexpr (p == 0) dma(IC<4>{}, IC<L_CX + s * 10240>{}, d_x2, hb1k, g0, kp * 2048);
            else if constexpr (p == 1) dma(IC<4>{}, IC<L_CX + s * 10240 + 4096>{}, d_x2, hb1k, g0 + 32768, kp * 2048);
            else dma(IC<5>{}, IC<L_CX + s * 10240 + 8192>{}, d_x2, hb512, g2, kp * 2048);
        };
        u32 va = L_CX + hd * 1024 + l3 * 16, vq = L_CX + 8192 + hd * 512 + (l3 & 31) * 16 + (l3 >> 5) * 10240;
        pin(va); pin(vq);
        u32x4 vr[5];
        auto ver_read = [&](auto cc) {
            constexpr int c = decltype(cc)::value;
            u32 a = va + c * 20480, q = vq + c * 20480;
            pin(a); pin(q); CFENCE();
            vr[0] = *(LAS u32x4*)(lds + a); vr[1] = *(LAS u32x4*)(lds + a + 4096); vr[2] = *(LAS u32x4*)(lds + a + 10240); vr[3] = *(LAS u32x4*)(lds + a + 14336);
            vr[4] = *(LAS u32x4*)(lds + q);
        };
        auto ver_check = [&](auto cc) {
            constexpr int c = decltype(cc)::value;
            _Pragma("unroll 1") for (int spin = 0; spin < SPIN_CAP; ++spin) {
                u32 m0 = 0, m1 = 0;
#pragma unroll
                for (int i = 0; i < 5; ++i) { m0 = max(max(m0, vr[i][0]), vr[i][1]); m1 = max(max(m1, vr[i][2]), vr[i][3]); }
                if (__builtin_expect(__all(max(m0, m1) != SENT), 1)) break;
                __builtin_amdgcn_s_sleep(1);
                sfor<6 * c, 6 * c + 6>([&](auto ic) { x2_piece(ic); });
                WAIT_VM(0);
                ver_read(cc);
            }
        };
        const u32 xb0 = (u32)((l3 & 15) * 128 + (((l3 >> 4) ^ ((l3 & 15) >> 1)) << 4));
        if constexpr (R == 1) {
            sfor<0, 12>([&](auto ic) { x2_piece(ic); });
            WAIT_VM(6);
            ver_read(IC<0>{});
            ver_check(IC<0>{});
        } else {
            u32x4 sa, sc, ha, hc;
            const u32 so = stat_off(WS_ST2, 4 * grp + hd);
            const bool halo = hd == 0 && grp > 0;
            ald4s(sa, so); ald4s(sc, so + 256);
            if (halo) { ald4s(ha, so - 2048); ald4s(hc, so - 2048 + 256); }
            WAIT_VM(0);
            pin(sa); pin(sc);
            float mean, rstd;
            fin_stat(sa, sc, so, mean, rstd);
            if (l3 < 16) { *(LAS float*)(lds + L_TAB + 64 + hd * 64 + l3 * 4) = mean; *(LAS float*)(lds + L_TAB + 320 + 64 + hd * 64 + l3 * 4) = rstd; }
            if (halo) {
                pin(ha); pin(hc);
                fin_stat(ha, hc, so - 2048, mean, rstd);
                if (l3 < 16) { *(LAS float*)(lds + L_TAB + l3 * 4) = mean; *(LAS float*)(lds + L_TAB + 320 + l3 * 4) = rstd; }
            }
        }
        BAR();
        u32 xa;
        auto pf_addr = [&](int st) { xa = L_CX + (st >> 1) * 10240 + (xb0 ^ ((st & 1) * 64)); pin(xa); };
        pf_addr(0);
#pragma unroll
        for (int mt = 0; mt < 5; ++mt) Ab[0][mt] = ldsA(xa + mt * 2048);
        sfor<0, 16>([&](auto stc) {
            constexpr int st = decltype(stc)::value;
            if constexpr (R == 1) {
                if constexpr (st == 2) { WAIT_VM(10); ver_read(IC<1>{}); }
                if constexpr (st == 3) ver_check(IC<1>{});
                if constexpr (st == 5) { WAIT_VM(6); ver_read(IC<2>{}); }
                if constexpr (st == 6) { ver_check(IC<2>{}); WAIT_VM(0); ver_read(IC<3>{}); }
                if constexpr (st == 7) ver_check(IC<3>{});
            }
            if constexpr (st == 3) { RBAR(); }
            if constexpr (st == 7) { RBAR(); }
            if (st < 15) pf_addr(st + 1);
            if constexpr (st == 14) load_tab(true);
            SB();
            qkv_step(IC<1>{}, st, Ab[st & 1], [&](auto mc) {
                constexpr int mt = decltype(mc)::value;
                if (st < 15) Ab[(st + 1) & 1][mt] = ldsA(xa + mt * 2048);
                if constexpr (R == 1 && 12 + 5 * st + mt < 24) x2_piece(IC<12 + 5 * st + mt>{});
                SB();
            });
        });
    }
    attention(IC<1>{});

    {
        wo_addr();
#pragma unroll
        for (int ks = 0; ks < 8; ++ks) load_F(ks);
        SB();
        WAIT_VM(0);
        RBAR();
        load_o(WS_O2);
        SB();
        _Pragma("unroll 1") for (int spin = 0; spin < 64; ++spin) {
            u32 m0 = 0, m1 = 0;
#pragma unroll
            for (int ks = 0; ks < 8; ++ks) {
#pragma unroll
                for (int n = 0; n < 4; ++n) accM[n] = mfma32(F[ks][n], __builtin_bit_cast(s16x8, of[ks]), ks ? accM[n] : resid(n));
                m0 = max(max(m0, of[ks][0]), of[ks][1]); m1 = max(max(m1, of[ks][2]), of[ks][3]);
                SB();
            }
            if (__builtin_expect(__all(max(m0, m1) != SENT), 1)) break;
            _Pragma("unroll 1") for (int sp2 = 0; sp2 < SPIN_CAP; ++sp2) {
                CFENCE();
                __builtin_amdgcn_s_sleep(1);
                const u32 pv = __builtin_amdgcn_raw_buffer_load_b32(r_ws, WS_O2 + (u32)(TI * 16384 + R * 8192 + (opq(tid) & 15) * 512 + 508), 0, 16);
                if (__all(pv != SENT)) break;
            }
            CFENCE();
            load_o(WS_O2);
        }
        SB();
    }
    wo_finish(IC<1>{});
}

__global__ void __launch_bounds__(512, 2)
fused_kernel(const u16* __restrict__ gx, const u16* __restrict__ gwqkv, const u16* __restrict__ gwo,
             const u16* __restrict__ gbk, const u16* __restrict__ gbv, const u16* __restrict__ gsk,
             const u16* __restrict__ ghk, const u16* __restrict__ gdv, u16* __restrict__ gout, char* __restrict__ ws) {
    __shared__ __attribute__((aligned(16))) char lds_[L_TOTAL];
    typedef u32 u32x16 __attribute__((ext_vector_type(16)));
    u32x16 ka; u32x4 kb;
    {
        auto kp = __builtin_amdgcn_kernarg_segment_ptr();
        asm volatile("s_load_dwordx16 %0, %2, 0x0\n\ts_load_dwordx4 %1, %2, 0x40\n\ts_waitcnt lgkmcnt(0)" : "=&s"(ka), "=&s"(kb) : "s"(kp) : "memory");
    }
    auto mkp = [](u32 lo, u32 hi) { return (char*)(((u64)hi << 32) | lo); };
    const Args a = {(const u16*)mkp(ka[0], ka[1]), (const u16*)mkp(ka[2], ka[3]), (const u16*)mkp(ka[4], ka[5]), (const u16*)mkp(ka[6], ka[7]), (const u16*)mkp(ka[8], ka[9]),
                    (const u16*)mkp(ka[10], ka[11]), (const u16*)mkp(ka[12], ka[13]), (const u16*)mkp(ka[14], ka[15]), (u16*)mkp(kb[0], kb[1]), mkp(kb[2], kb[3])};
    const int wave = __builtin_amdgcn_readfirstlane(threadIdx.x >> 6);
    if (wave < 4) body<0>(a, (LAS char*)lds_, wave); else body<1>(a, (LAS char*)lds_, wave);
}

extern "C" void kernel_launch(void* const* d_in, const int* in_sizes, int n_in,
                              void* d_out, int out_size, void* d_ws, size_t ws_size,
                              hipStream_t stream) {
    fused_kernel<<<256, 512, 0, stream>>>((const u16*)d_in[0], (const u16*)d_in[1], (const u16*)d_in[2],
                                          (const u16*)d_in[4], (const u16*)d_in[5], (const u16*)d_in[6],
                                          (const u16*)d_in[7], (const u16*)d_in[8], (u16*)d_out, (char*)d_ws);
}
```

```cpp
#include <hip/hip_runtime.h>
#include <stdint.h>

typedef unsigned short u16;
typedef unsigned u32;
typedef unsigned long long u64;
typedef short s16x4 __attribute__((ext_vector_type(4)));
typedef short s16x8 __attribute__((ext_vector_type(8)));
typedef __bf16 bf16x2 __attribute__((ext_vector_type(2)));
typedef float f32x2 __attribute__((ext_vector_type(2)));
typedef float f32x4 __attribute__((ext_vector_type(4)));
typedef u32 u32x2 __attribute__((ext_vector_type(2)));
typedef u32 u32x4 __attribute__((ext_vector_type(4)));
#define LAS __attribute__((address_space(3)))
#define DEV __device__ __forceinline__
typedef __amdgpu_buffer_rsrc_t rsrc_t;

constexpr u32 WS_EPOCH = 0;
constexpr u32 WS_F1 = 32768;
constexpr u32 WS_F2 = 65536;
constexpr u32 WS_ST0 = 98304;
constexpr u32 WS_ST1 = 131072;
constexpr u32 WS_ST2 = WS_ST1 + 262144;
constexpr u32 WS_ST3 = WS_ST2 + 262144;
constexpr u32 WS_UT = 1u << 20;
constexpr u32 WS_O1 = WS_UT + (2u << 20);
constexpr u32 WS_O2 = WS_O1 + (2u << 20);
constexpr u32 WS_Y2 = WS_O2 + (2u << 20);
constexpr u32 WS_X2 = WS_Y2 + (2u << 20);
constexpr u32 WS_END = WS_X2 + (2u << 20);

constexpr u32 L_SLOT = 34816;
constexpr u32 L_KV = 4 * L_SLOT;
constexpr u32 L_K = L_KV, L_V = L_KV + 9216;
constexpr u32 L_SK = L_KV + 17408;
constexpr u32 L_WO = 0;
constexpr u32 L_CX = 65536;
constexpr u32 L_HC = L_CX;
constexpr u32 L_UR = L_CX + 69632;
constexpr u32 L_WX = L_CX + 40960;
constexpr u32 L_EX = 147456;
constexpr u32 L_TAB = L_EX + 12288;
constexpr u32 L_CS = L_TAB + 1024;
constexpr u32 L_FLG = L_TAB + 2048;
constexpr u32 L_TOTAL = 160 * 1024;
static_assert(L_FLG + 64 <= L_TOTAL, "LDS map");

constexpr int SPIN_CAP = 1 << 14;
constexpr u32 SENT = 0xFFFFFFFFu;

#define WAIT_VM(n) asm volatile("s_waitcnt vmcnt(%0)" ::"i"(n) : "memory")
#define BAR() do { asm volatile("s_waitcnt lgkmcnt(0)" ::: "memory"); __builtin_amdgcn_s_barrier(); asm volatile("" ::: "memory"); } while (0)
#define RBAR() do { asm volatile("" ::: "memory"); __builtin_amdgcn_s_barrier(); asm volatile("" ::: "memory"); } while (0)
#define CFENCE() asm volatile("" ::: "memory")
#define SB() __builtin_amdgcn_sched_barrier(0)
template <int N> struct IC { static constexpr int value = N; };
template <int I, int N, class F> DEV void sfor(F&& f) { if constexpr (I < N) { f(IC<I>{}); sfor<I + 1, N>(f); } }

DEV f32x4 mfma32(s16x8 a, s16x8 b, f32x4 c) { return __builtin_amdgcn_mfma_f32_16x16x32_bf16(a, b, c, 0, 0, 0); }
DEV f32x4 mfma16(s16x4 a, s16x4 b, f32x4 c) { return __builtin_amdgcn_mfma_f32_16x16x16bf16_1k(a, b, c, 0, 0, 0); }
DEV float vmax(float a, float b) { float r; asm("v_max_f32 %0, %1, %2" : "=v"(r) : "v"(a), "v"(b)); return r; }
DEV u32 pk2(float lo, float hi) { f32x2 f = {lo, hi}; bf16x2 b = __builtin_convertvector(f, bf16x2); return __builtin_bit_cast(u32, b); }
DEV float bflo(u32 w) { return __uint_as_float(w << 16); }
DEV float bfhi(u32 w) { return __uint_as_float(w & 0xffff0000u); }
DEV float dot2(u32 a, u32 b, float c) { return __builtin_amdgcn_fdot2_f32_bf16(__builtin_bit_cast(bf16x2, a), __builtin_bit_cast(bf16x2, b), c, false); }
DEV u32 nosent(u32 w) { return min(w, 0xFFFFFFFEu); }
DEV float rowsum16(float v) {
    v += __builtin_bit_cast(float, __builtin_amdgcn_update_dpp(0, __builtin_bit_cast(int, v), 0xB1, 0xf, 0xf, true));
    v += __builtin_bit_cast(float, __builtin_amdgcn_update_dpp(0, __builtin_bit_cast(int, v), 0x4E, 0xf, 0xf, true));
    v += __builtin_bit_cast(float, __builtin_amdgcn_update_dpp(0, __builtin_bit_cast(int, v), 0x141, 0xf, 0xf, true));
    v += __builtin_bit_cast(float, __builtin_amdgcn_update_dpp(0, __builtin_bit_cast(int, v), 0x140, 0xf, 0xf, true));
    return v;
}
DEV float bcast_row0(float v) { return __builtin_bit_cast(float, __builtin_amdgcn_update_dpp(0, __builtin_bit_cast(int, v), 0x150, 0xf, 0xf, true)); }
template <int OFF> DEV void ald4o(u32x4& d, u32 voff, u32x4 rs) { asm volatile("buffer_load_dwordx4 %0, %1, %2, 0 offen offset:%3 sc1" : "=v"(d) : "v"(voff), "s"(rs), "i"(OFF) : "memory"); }
template <class T> DEV void pin(T& v) { asm volatile("" : "+v"(v)); }
DEV int opq(int v) { asm volatile("" : "+v"(v)); return v; }
DEV s16x8 cat8(s16x4 a, s16x4 b) { return __builtin_shufflevector(a, b, 0, 1, 2, 3, 4, 5, 6, 7); }
DEV s16x4 pack4(f32x4 v) { u32x2 w = {pk2(v[0], v[1]), pk2(v[2], v[3])}; return __builtin_bit_cast(s16x4, w); }
DEV u64 mix64(u64 z) { z += 0x9E3779B97F4A7C15ull; z = (z ^ (z >> 30)) * 0xBF58476D1CE4E5B9ull; z = (z ^ (z >> 27)) * 0x94D049BB133111EBull; return z ^ (z >> 31); }

struct ConvSched {
    int n[4], f[18], i[18], slot[64];
    constexpr ConvSched(int R) : n{}, f{}, i{}, slot{} {
        constexpr int LEAD = 9;
        const int N0[4] = {0, 3, 4, 7};
        for (int k = 0; k < 4; ++k) n[k] = N0[k] ^ R;
        int c = 0;
        for (int ff = 0; ff < 8; ++ff) for (int k = 3; k >= 0; --k) if (ff <= n[k]) { f[c] = ff; i[c] = k; ++c; }
        for (int k = 0; k < 64; ++k) slot[k] = 1000;
        for (int p = 71; p >= 0; --p) {
            const int e = p / 18, ff = f[p % 18], g = n[i[p % 18]] - ff, s = p - LEAD < -1 ? -1 : p - LEAD;
            slot[e * 16 + ff] = s; slot[e * 16 + 8 + g] = s;
        }
    }
};

struct Args { const u16 *gx, *gwqkv, *gwo, *gbk, *gbv, *gsk, *ghk, *gdv; u16* gout; char* ws; };

template <int R>
DEV void body(const Args& A_, LAS char* const lds, const int wave) {
    const u16* gx = A_.gx; char* ws = A_.ws;
    const int b = blockIdx.x, tid = threadIdx.x, lane = tid & 63;
    const int hd = wave & 3;
    const int grp = (b & 7) * 4 + (b >> 6), mem = (b >> 3) & 7;
    const int T0 = grp * 64;
    const int rot = (mem + 2 * (grp & 3)) & 7;

    u64 epoch;
    asm volatile("s_load_dwordx2 %0, %1, 0x0" : "=s"(epoch) : "s"(ws));
    u32 dw;
    asm volatile("s_load_dword %0, %1, 0x0" : "=s"(dw) : "s"(A_.gdv + 2 * b));
    const rsrc_t r_x = __builtin_amdgcn_make_buffer_rsrc((void*)gx, 0, 2048 * 512 * 2, 0x00020000);
    const rsrc_t r_ws = __builtin_amdgcn_make_buffer_rsrc((void*)ws, 0, WS_END, 0x00020000);
    const rsrc_t r_out = __builtin_amdgcn_make_buffer_rsrc((void*)A_.gout, 0, 2048 * 512 * 2, 0x00020000);
    const u32 ldsbase = (u32)(size_t)lds;
    auto mkdesc = [](const void* p, u32 bytes) { const u64 a = (u64)p; return (u32x4){(u32)a, (u32)(a >> 32) & 0xffffu, bytes, 0x00020000u}; };
    const u32x4 d_x = mkdesc(gx, 2048 * 512 * 2), d_w = mkdesc(A_.gwqkv, 512 * 1536 * 2), d_wo = mkdesc(A_.gwo, 512 * 512 * 2);
    const u32x4 d_k = mkdesc(A_.gbk, 2048 * 512 * 2), d_v = mkdesc(A_.gbv, 2048 * 512 * 2), d_sk = mkdesc(A_.gsk, 512 * 4 * 2);
    const u32x4 d_h = mkdesc(A_.ghk, 512 * 2048 * 2), d_x2 = mkdesc(ws + WS_X2, 2u << 20), d_ws = mkdesc(ws, WS_END);
    const u32 hb1k = ldsbase + hd * 1024, hb512 = ldsbase + hd * 512;
#define DMA_ASM(pre, mods, post) asm volatile("s_add_u32 m0, %0, %4\n\t" pre "buffer_load_dwordx4 %1, %2, %3 offen " mods "lds" post \
                     :: "s"(sbase), "v"(voff), "s"(rs), "s"(soff), "i"(IMM) : "memory", "scc")
    const u32 xmlo = hd == 0 ? ~0xFFu : ~0u;
    auto dma = [&](auto kind, auto imm, u32x4 rs, u32 sbase, u32 voff, u32 soff) {
        constexpr int K = decltype(kind)::value; constexpr int IMM = decltype(imm)::value;
        if constexpr (K == 0) DMA_ASM("s_nop 0\n\t", "", "");
        else if constexpr (K == 1) DMA_ASM("s_mov_b32 exec_hi, 0\n\t", "", "\n\ts_mov_b32 exec_hi, -1");
        else if constexpr (K == 2) {
            const u32 ml = xmlo;
            asm volatile("s_add_u32 m0, %0, %4\n\ts_mov_b32 exec_lo, %5\n\tbuffer_load_dwordx4 %1, %2, %3 offen lds\n\ts_mov_b32 exec_lo, -1"
                         :: "s"(sbase), "v"(voff), "s"(rs), "s"(soff), "i"(IMM), "s"(ml) : "memory", "scc");
        }
        else if constexpr (K == 4) DMA_ASM("s_nop 0\n\t", "sc1 ", "");
        else DMA_ASM("s_mov_b32 exec_hi, 0\n\t", "sc1 ", "\n\ts_mov_b32 exec_hi, -1");
    };
    auto ald4 = [&](u32x4& d, u32x4 rs, u32 voff) { asm volatile("buffer_load_dwordx4 %0, %1, %2, 0 offen" : "=v"(d) : "v"(voff), "s"(rs) : "memory"); };
    auto ald4s = [&](u32x4& d, u32 voff) { asm volatile("buffer_load_dwordx4 %0, %1, %2, 0 offen sc1" : "=v"(d) : "v"(voff), "s"(d_ws) : "memory"); };
    auto ald2s = [&](u32x2& d, u32 voff) { asm volatile("buffer_load_dwordx2 %0, %1, %2, 0 offen sc1" : "=v"(d) : "v"(voff), "s"(d_ws) : "memory"); };

    const int pr = lane >> 3, pc = lane & 7;
    const int xr0 = 8 * hd + pr, xr2 = 64 + 4 * hd + pr;
    const u32 xv0 = (u32)((T0 - 16 + xr0) * 1024 + ((pc ^ ((xr0 >> 1) & 7)) << 4));
    const u32 xv2 = (u32)((T0 - 16 + xr2) * 1024 + ((pc ^ ((xr2 >> 1) & 7)) << 4));
    const int wfw = ((xr0 >> 1) & 1) | (((xr0 >> 3) & 1) << 1);
    const u32 wv0 = (u32)(xr0 * 3072 + mem * 128 + ((((pc >> 1) ^ wfw) * 2 + (pc & 1)) << 4));
    auto issue_piece = [&](auto sc, auto ic) {
        constexpr int s = decltype(sc)::value, i = decltype(ic)::value;
        const u32 kp = (u32)((s + rot) & 7);
        constexpr int sb = (s & 3) * L_SLOT;
        if constexpr (i == 0) dma(IC<2>{}, IC<sb>{}, d_x, hb1k, xv0, kp * 128);
        else if constexpr (i == 1) dma(IC<0>{}, IC<sb + 4096>{}, d_x, hb1k, xv0 + 32768, kp * 128);
        else if constexpr (i == 2) dma(IC<1>{}, IC<sb + 8192>{}, d_x, hb512, xv2, kp * 128);
        else dma(IC<0>{}, IC<sb + 10240 + (i - 3) * 4096>{}, d_w, hb1k, wv0, kp * (64 * 3072) + ((i - 3) >> 1) * 1024 + ((i - 3) & 1) * (32 * 3072));
    };
    u32 wov = 0;
    auto issue_wo = [&](auto ic) {
        constexpr int i = decltype(ic)::value;
        if constexpr (i == 0) {
            const int l2 = opq(tid) & 63, pr = l2 >> 3, pc = l2 & 7;
            wov = (u32)((8 * hd + pr) * 1024 + mem * 128 + ((((pc >> 1) ^ (((pr >> 1) & 1) | ((hd & 1) << 1))) * 2 + (pc & 1)) << 4));
            pin(wov);
        }
        const u32 j = (u32)((i + 2 * grp) & 7);
        dma(IC<0>{}, IC<L_WO + (i & 8) * 4096>{}, d_wo, hb1k + j * 4096, wov, ((i & 8) + j) * 32768);
    };
    auto issue_kv = [&](int i) {
        const int l2 = opq(tid) & 63, pr = l2 >> 3, pc = l2 & 7, p = hd + 4 * (i & 1);
        const u32 cb = (u32)(mem * 128);
        if (i < 2) dma(IC<0>{}, IC<L_K>{}, d_k, hb1k + (i & 1) * 4096, (u32)((T0 - 8 + 8 * p + pr) * 1024) + cb + ((pc ^ p) << 4), 0);
        else if (i < 4) dma(IC<0>{}, IC<L_V>{}, d_v, hb1k + (i & 1) * 4096, (u32)((T0 + 8 * p + pr) * 1024) + cb + ((pc ^ p) << 4), 0);
        else if (hd == 0) dma(IC<0>{}, IC<L_K + 8192>{}, d_k, ldsbase, (u32)((T0 - 8 + 64 + pr) * 1024) + cb + (pc << 4), 0);
        else dma(IC<0>{}, IC<L_SK>{}, d_sk, ldsbase, (u32)(mem * 512 + l2 * 16), 0);
    };

    const int srow = T0 + 8 * mem + 2 * hd;
    u32x4 xrow[2];
    sfor<0, 9>([&](auto ic) { if constexpr ((decltype(ic)::value & 1) != R) issue_piece(IC<0>{}, ic); });
    sfor<0, 9>([&](auto ic) { if constexpr ((decltype(ic)::value & 1) != R) issue_piece(IC<1>{}, ic); });
    asm volatile("s_waitcnt lgkmcnt(0)" : "+s"(epoch), "+s"(dw));
    const u64 tag = mix64(epoch);
    const u32 tl = (u32)tag, th = (u32)(tag >> 32);
    if (R == 1 && wave == 7 && lane < 32) *(LAS u32x4*)(lds + (lane >> 3) * L_SLOT + (lane & 7) * 16) = (u32x4){0x3F803F80u, 0x3F803F80u, 0x3F803F80u, 0x3F803F80u};

    auto trr = [&](u32 off) { return __builtin_amdgcn_ds_read_tr16_b64_v4i16((LAS s16x4*)(lds + off)); };
    auto ldsA = [&](u32 off) { return *(LAS s16x8*)(lds + off); };
    auto flag_ok = [&](const u32x2& f) { return __all(f[0] == tl && f[1] == th); };
    auto spin_flags = [&](u32x2& f, u32 off) {
        _Pragma("unroll 1") for (int spin = 0; spin < SPIN_CAP; ++spin) {
            if (__builtin_expect(flag_ok(f), 1)) break;
            __builtin_amdgcn_s_sleep(2);
            asm volatile("buffer_load_dwordx2 %0, %1, %2, 0 offen sc1\n\ts_waitcnt vmcnt(0)" : "=&v"(f) : "v"(off), "s"(d_ws) : "memory");
        }
    };

    s16x8 Wq[16], Wm[16];
    f32x4 accM[5], accQ[5];
    const f32x4 zero4 = {0.f, 0.f, 0.f, 0.f};
    const s16x8 ones8 = {0x3F80, 0x3F80, 0x3F80, 0x3F80, 0x3F80, 0x3F80, 0x3F80, 0x3F80};
    s16x8 Ab[3][5];
    auto qkv_step = [&](auto lc, int st, const s16x8 (&A)[5], auto fill) {
        sfor<0, 5>([&](auto mc) {
            constexpr int mt = decltype(mc)::value;
            const f32x4 cm = st ? accM[mt] : zero4;
            accM[mt] = R ? mfma32(A[mt], Wm[st], cm) : mfma32(Wm[st], A[mt], cm);
            if constexpr (R == 0 && (mt > 0 || decltype(lc)::value == 0)) accQ[mt] = mfma32(Wq[st], A[mt], st ? accQ[mt] : zero4);
            fill(mc);
        });
    };

    float m1[5], r1[5]; f32x4 m4[5], r4[5], csK, csQ;
    auto load_tab = [&](bool cs) {
        const int l2 = opq(tid) & 63;
        u32 ta = L_TAB + (R ? (l2 >> 4) * 16 : (l2 & 15) * 4);
        pin(ta);
        if (R == 0 && cs) { csK = *(LAS f32x4*)(lds + L_CS + hd * 128 + (l2 >> 4) * 16); csQ = *(LAS f32x4*)(lds + L_CS + hd * 128 + 64 + (l2 >> 4) * 16); }
#pragma unroll
        for (int mt = 0; mt < 5; ++mt) {
            if (R == 0) { m1[mt] = *(LAS float*)(lds + ta + mt * 64); r1[mt] = *(LAS float*)(lds + ta + 320 + mt * 64); }
            else { m4[mt] = *(LAS f32x4*)(lds + ta + mt * 64); r4[mt] = *(LAS f32x4*)(lds + ta + 320 + mt * 64); }
        }
    };

    u32x4 hr[4];
    u32x2 flg;
    {
        const int li = lane & 15, lq = lane >> 4, tq = li >> 2, tp = lane & 3;
        const u32 xa0 = (u32)(li * 128 + ((lq ^ (li >> 1)) << 4));
        const u32 wa = (u32)((8 * lq + tq) * 128 + ((hd ^ (((tq >> 1) & 1) | ((lq & 1) << 1))) << 5) + tp * 8);
        u32 xa, wb, kw[11], vw[8];
        u32x4 s0v, skw;
        auto pf_addr = [&](int st) {
            const u32 sb = (u32)((st >> 1) & 3) * L_SLOT;
            xa = sb + (xa0 ^ ((st & 1) * 64)); wb = sb + 10240 + (st & 1) * 4096 + wa;
            asm volatile("" : "+v"(xa), "+v"(wb));
        };
        auto pf_part = [&](int st, int mt) {
            if (mt == 0) Wm[st] = cat8(trr(wb + (1 + R) * 8192), trr(wb + (1 + R) * 8192 + 512));
            Ab[st & 1][mt] = ldsA(xa + mt * 2048);
            if (R == 0 && mt == 1) Wq[st] = cat8(trr(wb), trr(wb + 512));
        };
        WAIT_VM(R ? 5 : 4);
        BAR();
        pf_addr(0);
#pragma unroll
        for (int mt = 0; mt < 5; ++mt) pf_part(0, mt);
        sfor<0, 16>([&](auto stc) {
            constexpr int st = decltype(stc)::value;
            if constexpr ((st & 1) && st < 15) {
                constexpr int s1 = (st >> 1) + 1;
                if constexpr (R == 0) {
                    if constexpr (s1 == 1) WAIT_VM(7);
                    if constexpr (s1 == 2) WAIT_VM(1);
                    if constexpr (s1 == 5) WAIT_VM(0);
                } else {
                    constexpr int WN[8] = {0, 9, 9, 9, 9, 9, 10, 10};
                    WAIT_VM(WN[s1]);
                    if constexpr (s1 == 7) {
                        pin(s0v);
                        const int l2 = opq(tid) & 63, tr = hd * 20 + min(l2, 19), row = T0 - 16 + tr;
                        _Pragma("unroll 1") for (int spin = 0; spin < SPIN_CAP; ++spin) {
                            if (__builtin_expect(__all(row < 0 || (s0v[1] == tl && s0v[3] == th)), 1)) break;
                            __builtin_amdgcn_s_sleep(2);
                            ald4s(s0v, WS_ST0 + (u32)(max(row, 0) * 16));
                            WAIT_VM(0);
                            pin(s0v);
                        }
                        *(LAS u32*)(lds + L_TAB + tr * 4) = row < 0 ? 0u : s0v[0];
                        *(LAS u32*)(lds + L_TAB + 320 + tr * 4) = row < 0 ? 0x3F800000u : s0v[2];
                        asm volatile("s_waitcnt lgkmcnt(0)" ::: "memory");
                    }
                }
                RBAR();
                if constexpr (s1 == 5 && R == 0) { if (tid == 0) __builtin_amdgcn_raw_buffer_store_b64((u32x2){tl, th}, r_ws, WS_F1 + (mem * 32 + grp) * 128, 0, 16); CFENCE(); }
                if constexpr (s1 == 5 && R == 1) { const int row = T0 - 16 + hd * 20 + min(opq(tid) & 63, 19); ald4s(s0v, WS_ST0 + (u32)(max(row, 0) * 16)); }
            }
            if (st < 15) pf_addr(st + 1);
            if constexpr (st == 14) load_tab(false);
            SB();
            qkv_step(IC<0>{}, st, Ab[st & 1], [&](auto mc) {
                constexpr int mt = decltype(mc)::value;
                if (st < 15) pf_part(st + 1, mt);
                if constexpr (R == 1) {
                    if constexpr (st == 0) { issue_piece(IC<2>{}, IC<2 * mt>{}); if constexpr (mt < 4) issue_piece(IC<2>{}, IC<2 * mt + 1>{}); }
                    if constexpr (st >= 1 && st <= 10 && ((st - 1) & 1) * 5 + mt < 9) issue_piece(IC<((st - 1) >> 1) + 3>{}, IC<((st - 1) & 1) * 5 + mt>{});
                    if constexpr (st == 11 && mt == 0) { const u32 ho = (u32)(b * 8192 + opq(tid) * 16); ald4(hr[2], d_h, ho); ald4(hr[3], d_h, ho + 16); }
                    if constexpr (st == 13 && mt == 0) { if (wave == 7) { const int l2 = opq(tid) & 63; ald2s(flg, WS_F1 + (u32)((l2 < 32 ? (b >> 5) * 32 + l2 : (l2 & 7) * 32 + grp) * 128)); } }
                    if constexpr (st >= 11 && st <= 14 && mt < 4) issue_wo(IC<(st - 11) * 4 + mt>{});
                } else {
                    if constexpr (st == 0) {
                        if constexpr (mt == 0) { const u32 xo = (u32)(srow * 1024 + (opq(tid) & 63) * 16); ald4(xrow[0], d_x, xo); ald4(xrow[1], d_x, xo + 1024); }
                        issue_kv(mt);
                    }
                    if constexpr (st == 11 && mt < 2) { const u32 ho = (u32)(b * 8192 + mt * 4096 + opq(tid) * 16); ald4(hr[2 * mt], d_h, ho); ald4(hr[2 * mt + 1], d_h, ho + 16); }
                }
                SB();
            });
            if constexpr (R == 0 && st == 2) {
                WAIT_VM(5);
                pin(xrow[0]); pin(xrow[1]);
                float s[2] = {0.f, 0.f}, q[2] = {0.f, 0.f};
#pragma unroll
                for (int i = 0; i < 4; ++i)
#pragma unroll
                    for (int k = 0; k < 2; ++k) { s[k] = dot2(xrow[k][i], 0x3F803F80u, s[k]); q[k] = dot2(xrow[k][i], xrow[k][i], q[k]); }
                auto ss = __builtin_amdgcn_permlane32_swap(__float_as_uint(s[0]), __float_as_uint(s[1]), false, false);
                auto qq = __builtin_amdgcn_permlane32_swap(__float_as_uint(q[0]), __float_as_uint(q[1]), false, false);
                float sv1 = rowsum16(__uint_as_float(ss[0]) + __uint_as_float(ss[1])), qv1 = rowsum16(__uint_as_float(qq[0]) + __uint_as_float(qq[1]));
                auto s2 = __builtin_amdgcn_permlane16_swap(__float_as_uint(sv1), __float_as_uint(sv1), false, false);
                auto q2 = __builtin_amdgcn_permlane16_swap(__float_as_uint(qv1), __float_as_uint(qv1), false, false);
                const float m = (__uint_as_float(s2[0]) + __uint_as_float(s2[1])) * (1.f / 512.f);
                const float r = __builtin_amdgcn_rsqf(fmaxf((__uint_as_float(q2[0]) + __uint_as_float(q2[1])) * (1.f / 512.f) - m * m, 0.f) + 1e-5f);
                const u32x4 sv = {__float_as_uint(m), tl, __float_as_uint(r), th};
                const int l2 = opq(tid) & 63;
                __builtin_amdgcn_raw_buffer_store_b128(sv, r_ws, (l2 & 31) == 0 ? WS_ST0 + (u32)((srow + (l2 >> 5)) * 16) : 0xFFFFFF00u, 0, 16);
                CFENCE();
                SB();
            }
            if constexpr (R == 0 && (st == 4 || st == 6)) {
                const int l2 = opq(tid) & 63, tg = l2 & 7, cp = 8 * hd + (l2 >> 3);
                if constexpr (st == 4) {
                    skw = *(LAS u32x4*)(lds + L_SK + cp * 16);
                    u32 ka = L_K + tg * 1024 + ((((cp >> 2) ^ tg) & 7) << 4) + (cp & 3) * 4;
                    u32 kb = L_K + tg * 1024 + ((((cp >> 2) ^ (tg + 1)) & 7) << 4) + (cp & 3) * 4;
                    pin(ka); pin(kb);
#pragma unroll
                    for (int i = 0; i < 11; ++i) kw[i] = *(LAS u32*)(lds + ((i + 5 < 8) ? ka : kb) + (i + 5) * 128);
#pragma unroll
                    for (int i = 0; i < 8; ++i) vw[i] = *(LAS u32*)(lds + ka + (L_V - L_K) + i * 128);
                    SB();
                }
                constexpr bool HI = st == 6;
                auto ex = [&](u32 w) { return HI ? bfhi(w) : bflo(w); };
                const float a0 = bflo(skw[2 * HI]), a1 = bfhi(skw[2 * HI]), a2 = bflo(skw[2 * HI + 1]), a3 = bfhi(skw[2 * HI + 1]);
                float ua[8];
#pragma unroll
                for (int i = 0; i < 8; ++i) ua[i] = (ex(kw[i]) * a3 + ex(kw[i + 1]) * a2 + ex(kw[i + 2]) * a1 + ex(kw[i + 3]) * a0) * ex(vw[i]);
                const u32x4 oa = {pk2(ua[7], ua[6]), pk2(ua[5], ua[4]), pk2(ua[3], ua[2]), pk2(ua[1], ua[0])};
                __builtin_amdgcn_raw_buffer_store_b128(oa, r_ws, WS_UT + (u32)((mem * 64 + 2 * cp) * 4096 + (2040 - T0 - 8 * tg) * 2), HI * 4096, 16);
                __builtin_amdgcn_raw_buffer_store_b128((u32x4){SENT, SENT, SENT, SENT}, r_ws, (u32)((4 * grp + hd) * 16384 + mem * 2048 + HI * 1024 + l2 * 16), WS_O1, 16);
                CFENCE();
                SB();
            }
        });
    }

    u32x4 uu;
    float csV;
    auto attention = [&](auto lc) {
        constexpr int LAYER = decltype(lc)::value;
        const int lane = opq(tid) & 63, li = lane & 15, lq = lane >> 4;
        u32 ex = L_EX + hd * 2048 + lane * 8;
        pin(ex);
        u32 f1ok = 1;
        if constexpr (R == 0) {
            if constexpr (LAYER == 0) {
#pragma unroll
                for (int i = 0; i < 4; ++i) { csK[i] = bcast_row0(accM[0][i]); csQ[i] = bcast_row0(accQ[0][i]); }
                if (li == 0) { *(LAS f32x4*)(lds + L_CS + hd * 128 + lq * 16) = csK; *(LAS f32x4*)(lds + L_CS + hd * 128 + 64 + lq * 16) = csQ; }
            }
            const int par = li & 1;
            const bool cur0 = (4 * lq + par) <= li, cur1 = (4 * lq + par + 2) <= li;
            s16x4 Kb[5], Qb[5];
#pragma unroll
            for (int mt = 0; mt < 5; ++mt) {
                Kb[mt] = pack4((accM[mt] - m1[mt] * csK) * r1[mt]);
                if (mt) Qb[mt] = pack4(accQ[mt] - m1[mt] * csQ);
            }
            f32x4 Sc[5], Sp[5];
#pragma unroll
            for (int mt = 1; mt < 5; ++mt) { Sc[mt] = mfma16(Kb[mt], Qb[mt], zero4); Sp[mt] = mfma16(Kb[mt - 1], Qb[mt], zero4); }
            float s0[5], s1[5], mx[5];
#pragma unroll
            for (int mt = 1; mt < 5; ++mt) {
                const float sp0 = Sp[mt][0], sp1 = Sp[mt][1], sp2 = Sp[mt][2], sp3 = Sp[mt][3], c0 = Sc[mt][0], c1 = Sc[mt][1], c2 = Sc[mt][2], c3 = Sc[mt][3];
                float p0 = par ? sp1 : sp0, p1 = par ? sp3 : sp2, q0 = par ? c1 : c0, q1 = par ? c3 : c2;
                asm volatile("" : "+v"(p0), "+v"(p1), "+v"(q0), "+v"(q1));
                if (mt == 1 && grp == 0) { p0 = -3.0e38f; p1 = -3.0e38f; }
                s0[mt] = cur0 ? q0 : p0; s1[mt] = cur1 ? q1 : p1;
                mx[mt] = vmax(s0[mt], s1[mt]);
            }
#pragma unroll
            for (int mt = 1; mt < 5; ++mt) { auto r = __builtin_amdgcn_permlane32_swap(__float_as_uint(mx[mt]), __float_as_uint(mx[mt]), false, false); mx[mt] = vmax(__uint_as_float(r[0]), __uint_as_float(r[1])); }
#pragma unroll
            for (int mt = 1; mt < 5; ++mt) { auto r = __builtin_amdgcn_permlane16_swap(__float_as_uint(mx[mt]), __float_as_uint(mx[mt]), false, false); mx[mt] = vmax(__uint_as_float(r[0]), __uint_as_float(r[1])); }
#pragma unroll
            for (int mt = 1; mt < 5; ++mt) {
                const float sc = r1[mt] * (0.25f * 1.44269504088896341f);
                const float e0 = __builtin_amdgcn_exp2f((s0[mt] - mx[mt]) * sc), e1 = __builtin_amdgcn_exp2f((s1[mt] - mx[mt]) * sc);
                *(LAS u32x2*)(lds + ex + (mt - 1) * 512) = (u32x2){pk2(e0, e0), pk2(e1, e1)};
            }
            BAR();
            if constexpr (LAYER == 0) f1ok = __builtin_amdgcn_readfirstlane(*(LAS u32*)(lds + L_FLG));
        } else {
            if constexpr (LAYER == 0) {
                auto s = __builtin_amdgcn_permlane16_swap(__float_as_uint(accM[0][0]), __float_as_uint(accM[0][0]), false, false);
                auto t = __builtin_amdgcn_permlane32_swap(s[0], s[0], false, false);
                csV = __uint_as_float(t[0]);
                if (wave == 7) { WAIT_VM(8); pin(flg); const u32 ok = flag_ok(flg); *(LAS u32*)(lds + L_FLG) = ok; }
            }
            const int par = li & 1;
            const bool cur0 = (4 * lq + par) <= li, cur1 = (4 * lq + par + 2) <= li;
            const u32 pm = par ? 0xffff0000u : 0x0000ffffu;
            u32 mC0 = cur0 ? pm : 0u, mP0 = cur0 ? 0u : pm, mC1 = cur1 ? pm : 0u, mP1 = cur1 ? 0u : pm;
            u32 oo = (LAYER ? WS_O2 : WS_O1) + (u32)((4 * grp + (lq & 1)) * 16384 + (4 * mem + hd) * 512 + (lq >> 1) * 256 + li * 16);
            asm volatile("" : "+v"(mC0), "+v"(mP0), "+v"(mC1), "+v"(mP1), "+v"(oo));
            s16x4 Vb[5];
#pragma unroll
            for (int mt = 0; mt < 5; ++mt) { Vb[mt] = pack4((accM[mt] - m4[mt] * csV) * r4[mt]); pin(Vb[mt]); }
            BAR();
            u32x2 pw[4];
#pragma unroll
            for (int j = 0; j < 4; ++j) pw[j] = *(LAS u32x2*)(lds + ex + j * 512);
            if constexpr (LAYER == 0) {
                f1ok = __builtin_amdgcn_readfirstlane(*(LAS u32*)(lds + L_FLG));
                if (f1ok) ald4s(uu, WS_UT + (u32)(b * 8192 + opq(tid) * 16));
            }
            SB();
            f32x4 O[4], L[4];
#pragma unroll
            for (int j = 0; j < 4; ++j) {
                const s16x8 P = __builtin_bit_cast(s16x8, (u32x4){pw[j][0] & mC0, pw[j][1] & mC1, pw[j][0] & mP0, pw[j][1] & mP1});
                L[j] = mfma32(ones8, P, zero4);
                O[j] = mfma32(cat8(Vb[j + 1], Vb[j]), P, zero4);
            }
            u32 ow[4][2];
#pragma unroll
            for (int j = 0; j < 4; ++j) {
                const float inv = __builtin_amdgcn_rcpf(L[j][0]);
                ow[j][0] = nosent(pk2(O[j][0] * inv, O[j][1] * inv)); ow[j][1] = nosent(pk2(O[j][2] * inv, O[j][3] * inv));
            }
#pragma unroll
            for (int j = 0; j < 4; j += 2) {
                auto r0 = __builtin_amdgcn_permlane16_swap(ow[j][0], ow[j + 1][0], false, false);
                auto r1 = __builtin_amdgcn_permlane16_swap(ow[j][1], ow[j + 1][1], false, false);
                __builtin_amdgcn_raw_buffer_store_b128((u32x4){r0[0], r1[0], r0[1], r1[1]}, r_ws, oo, j * 16384, 16);
            }
            CFENCE();
        }
        if constexpr (LAYER == 0 && R == 0) { if (f1ok) ald4s(uu, WS_UT + (u32)(b * 8192 + opq(tid) * 16)); }
        SB();
        return f1ok;
    };
    const u32 f1ok = attention(IC<0>{});
    {
        const u32x4 sv = {SENT, SENT, SENT, SENT};
        const int t2 = opq(tid);
        const u32 tb = (u32)((4 * grp + hd) * 16384 + mem * 2048 + R * 1024 + (t2 & 63) * 16);
        __builtin_amdgcn_raw_buffer_store_b128(sv, r_ws, tb, WS_O2, 16);
        __builtin_amdgcn_raw_buffer_store_b128(sv, r_ws, tb, WS_X2, 16);
        __builtin_amdgcn_raw_buffer_store_b128(sv, r_ws, (u32)(b * 8192 + t2 * 16), WS_Y2, 16);
        CFENCE();
        SB();
    }

    const int TI = 4 * grp + hd;
    u32x4 of[8];
    auto load_o = [&](u32 ws_o) {
        const int lane = opq(tid) & 63;
#pragma unroll
        for (int i = 0; i < 8; ++i) of[i] = __builtin_amdgcn_raw_buffer_load_b128(r_ws, ws_o + (u32)(TI * 16384 + R * 8192 + lane * 16) + (i & 3) * 1024, (i >> 2) * 4096, 16);
    };
    u32 xw[8], yw[8];
    u32 woa[4];
    auto resid = [&](int n) { return R ? zero4 : (f32x4){bflo(xw[2 * n]), bfhi(xw[2 * n]), bflo(xw[2 * n + 1]), bfhi(xw[2 * n + 1])}; };
    s16x8 F[8][4];
    auto wo_addr = [&]() {
        const int l2 = opq(tid) & 63, lq = l2 >> 4, tq = (l2 & 15) >> 2, tp = l2 & 3;
#pragma unroll
        for (int n = 0; n < 4; ++n) woa[n] = L_WO + (u32)(R * 32768 + (8 * lq + tq) * 128 + ((n ^ (((lq & 1) << 1) | (tq >> 1))) << 5) + tp * 8);
    };
    auto load_F = [&](int ks) {
#pragma unroll
        for (int n = 0; n < 4; ++n) F[ks][n] = cat8(trr(woa[n] + ks * 4096), trr(woa[n] + ks * 4096 + 512));
    };

    {
        const int t2 = opq(tid), lane = t2 & 63, li = lane & 15, lq = lane >> 4;
        constexpr u32 OFFS = 0xEDA85410u;
        {
            const int j = t2 & 255;
            {
                if constexpr (R == 0) { if (f1ok) WAIT_VM(4); else WAIT_VM(3); }
                else WAIT_VM(5);
                u32 cb = L_HC + R * 34816 + (j + 2) * 16;
                pin(cb);
#pragma unroll
                for (int ch = R; ch < 2; ++ch) {
                    pin(hr[2 * ch]); pin(hr[2 * ch + 1]);
                    const u32 d[8] = {hr[2 * ch][0], hr[2 * ch][1], hr[2 * ch][2], hr[2 * ch][3], hr[2 * ch + 1][0], hr[2 * ch + 1][1], hr[2 * ch + 1][2], hr[2 * ch + 1][3]};
                    u32 e[7];
#pragma unroll
                    for (int i = 0; i < 7; ++i) e[i] = __builtin_amdgcn_alignbit(d[i + 1], d[i], 16);
                    if (ch > R) { cb += 34816; pin(cb); }
#pragma unroll
                    for (int p = (R ? 4 : 0); p < ((ch && !R) ? 4 : 8); ++p) {
                        const u32 po = (272 * p + ((OFFS >> (4 * p)) & 15)) * 16;
                        const int h = p >> 1;
                        const u32x4 w = (p & 1) ? (u32x4){e[h], e[h + 1], e[h + 2], e[h + 3]} : (u32x4){d[h], d[h + 1], d[h + 2], d[h + 3]};
                        *(LAS u32x4*)(lds + cb + po) = w;
                    }
                }
            }
            if constexpr (R == 0) {
                if (hd == 0) {
                    u32 hh[4];
#pragma unroll
                    for (int i = 0; i < 4; ++i) { const u32 a = __builtin_amdgcn_readfirstlane(hr[0][i]), c = __builtin_amdgcn_readfirstlane(hr[2][i]); hh[i] = (lane & 16) ? c : a; }
                    if (lane < 32) {
                        const int p = lane & 7;
                        const u32 po = (272 * p + ((OFFS >> (4 * p)) & 15)) * 16;
                        const unsigned __int128 hv = ((unsigned __int128)(((u64)hh[3] << 32) | hh[2]) << 64) | (((u64)hh[1] << 32) | hh[0]);
                        const unsigned __int128 sv = (p == 0 || !(lane & 8)) ? (unsigned __int128)0 : (hv << (16 * (8 - p)));
                        const u32x4 w = {(u32)sv, (u32)(sv >> 32), (u32)(sv >> 64), (u32)(sv >> 96)};
                        *(LAS u32x4*)(lds + L_HC + (lane >> 4) * 34816 + po + ((lane >> 3) & 1) * 16) = w;
                    }
                }
            }
            if (hd == 1 && lane < 30) *(LAS u32x4*)(lds + L_UR + R * 4576 + 4096 + lane * 16) = (u32x4){0, 0, 0, 0};
            if (!f1ok) {
                const u32 fo = WS_F1 + (u32)(((lane & 8) ? (lane & 7) * 32 + grp : (b >> 5) * 32 + 31 - hd * 8 - (lane & 7)) * 128);
                flg = (u32x2){0u, 0u};
                spin_flags(flg, fo);
                ald4s(uu, WS_UT + (u32)(b * 8192 + t2 * 16));
            }
            if (f1ok) WAIT_VM(R ? 5 : 3); else WAIT_VM(0);
            pin(uu);
            *(LAS u32x4*)(lds + L_UR + R * 4576 + j * 16) = uu;
        }
        BAR();
        const int ch = hd >> 1, e0 = (hd & 1) * 4;
        const int p = (li + 1) & 7;
        u32 aA = L_HC + ch * 34816 + (272 * p + ((OFFS >> (4 * p)) & 15) + ((li + 1) >> 3) + lq + 4 * e0) * 16;
        u32 aB = L_UR + ch * 4576 + (254 - 224 - 2 * li + lq + 4 * e0) * 16;
        asm volatile("" : "+v"(aA), "+v"(aB));
        f32x4 y[4];
        u32 ooff = WS_O1 + (u32)(TI * 16384 + R * 8192 + lane * 16);
        {
            constexpr ConvSched S(R);
            s16x8 A[4][8], B[4][8];
            auto loads_at = [&](auto pc) {
                constexpr int p = decltype(pc)::value;
                sfor<0, 64>([&](auto ic) {
                    constexpr int i = decltype(ic)::value, e = i >> 4, f = i & 7;
                    if constexpr (S.slot[i] == p) {
                        if constexpr (i & 8) B[e][f] = ldsA(aB + 64 * e + 512 * (7 - f)); else A[e][f] = ldsA(aA + 64 * (8 * f + e));
                    }
                });
            };
            loads_at(IC<-1>{});
            SB();
            sfor<0, 72>([&](auto pc) {
                constexpr int p = decltype(pc)::value, e = p / 18, f = S.f[p % 18], i = S.i[p % 18], g = S.n[i] - f;
                y[i] = mfma32(A[e][f], B[e][g], (e == 0 && f == 0) ? zero4 : y[i]);
                loads_at(pc);
                if constexpr (p == 54) WAIT_VM(0);
                if constexpr (p >= 55 && p < 71 && (p & 1)) {
                    constexpr int i = (p - 55) >> 1;
                    ald4o<(i & 3) * 1024>(of[i], ooff + (i >> 2) * 4096, d_ws);
                }
                SB();
            });
        }
        RBAR();
        if (R == 0 && tid == 0) __builtin_amdgcn_raw_buffer_store_b64((u32x2){tl, th}, r_ws, WS_F2 + (grp * 8 + mem) * 128, 0, 16);
        {
            constexpr int N0[4] = {0, 3, 4, 7};
            u32 ra = L_HC + hd * 8192 + lane * 16;
            asm volatile("" : "+v"(ra));
#pragma unroll
            for (int i = 0; i < 4; ++i) *(LAS f32x4*)(lds + ra + (N0[i] ^ R) * 1024) = y[i];
        }
        SB();
        if constexpr (R == 0) {
#pragma unroll
            for (int n = 0; n < 4; ++n) { const u32x2 w = __builtin_amdgcn_raw_buffer_load_b64(r_x, (u32)((TI * 16 + li) * 1024 + mem * 128 + lq * 8) + n * 32, 0, 0); xw[2 * n] = w[0]; xw[2 * n + 1] = w[1]; }
        }
        wo_addr();
        SB();
        BAR();
        {
            const float D0 = bflo(dw), D1 = bfhi(dw);
            u32 ra = L_HC + wave * 1024 + lane * 16;
            const int tb = 256 * wave + 16 * li + 4 * lq;
            u32 ru = L_UR + (2044 - tb) * 2;
            asm volatile("" : "+v"(ra), "+v"(ru));
            const f32x4 y0 = *(LAS f32x4*)(lds + ra), y1 = *(LAS f32x4*)(lds + ra + 8192), y2 = *(LAS f32x4*)(lds + ra + 16384), y3 = *(LAS f32x4*)(lds + ra + 24576);
            const u32x2 ua = *(LAS u32x2*)(lds + ru), ub = *(LAS u32x2*)(lds + ru + 4576);
            load_F(0); load_F(1);
            SB();
            const f32x4 ya = y0 + y1, yb = y2 + y3;
            const u32x4 w = {nosent(pk2(ya[0] + bfhi(ua[1]) * D0, yb[0] + bfhi(ub[1]) * D1)), nosent(pk2(ya[1] + bflo(ua[1]) * D0, yb[1] + bflo(ub[1]) * D1)),
                             nosent(pk2(ya[2] + bfhi(ua[0]) * D0, yb[2] + bfhi(ub[0]) * D1)), nosent(pk2(ya[3] + bflo(ua[0]) * D0, yb[3] + bflo(ub[0]) * D1))};
            __builtin_amdgcn_raw_buffer_store_b128(w, r_ws, WS_Y2 + (u32)(b * 8192 + tb * 4), 0, 16);
        }
        SB();
        WAIT_VM(1);

        u32 f2oth = 0;
        _Pragma("unroll 1") for (int attempt = 0; attempt < 64; ++attempt) {
            sfor<0, 8>([&](auto ic) { pin(of[decltype(ic)::value]); });
            u32 m0 = 0, m1 = 0;
#pragma unroll
            for (int ks = 0; ks < 8; ++ks) {
                load_F((ks + 2) & 7);
                if (R == 1 && ks == 4 && wave == 7) {
                    const int pb = 32 * mem + (lane & 31);
                    f2oth = WS_F2 + (u32)((lane < 32 ? ((pb & 7) * 4 + (pb >> 6)) * 8 + ((pb >> 3) & 7) : max(grp * 8 - 8, 0) + (lane & 15)) * 128);
                    ald2s(flg, f2oth);
                }
                SB();
#pragma unroll
                for (int n = 0; n < 4; ++n) accM[n] = mfma32(F[ks][n], __builtin_bit_cast(s16x8, of[ks]), ks ? accM[n] : resid(n));
                m0 = max(max(m0, of[ks][0]), of[ks][1]); m1 = max(max(m1, of[ks][2]), of[ks][3]);
                SB();
            }
            if (__builtin_expect(__all(max(m0, m1) != SENT), 1)) break;
            __builtin_amdgcn_s_sleep(4);
            WAIT_VM(0);
            sfor<0, 8>([&](auto ic) { constexpr int i = decltype(ic)::value; ald4o<(i & 3) * 1024>(of[i], ooff + (i >> 2) * 4096, d_ws); });
            WAIT_VM(0);
        }
        if constexpr (R == 1) { if (wave == 7) { WAIT_VM(0); pin(flg); spin_flags(flg, f2oth); } }
    }

    auto load_y = [&]() {
        const int l2 = opq(tid) & 63;
#pragma unroll
        for (int i = 0; i < 8; ++i)
            yw[i] = __builtin_amdgcn_raw_buffer_load_b32(r_ws, WS_Y2 + (u32)((32 * mem + 2 * (l2 >> 4)) * 8192 + (TI * 16 + (l2 & 15)) * 4), (8 * (i >> 1) + (i & 1)) * 8192, 16);
    };
    auto pub_off = [&](u32 ws_st) {
        const int lane = opq(tid) & 63;
        return (lane & 16) ? 0xFFFFFF00u : ws_st + (u32)((TI * 8 + mem) * 256 + (lane & 15) * 16 + (lane >> 5) * 8);
    };
    auto pub_stat = [&](u32 off, u32 tg, float s, float q) {
        auto r = __builtin_amdgcn_permlane32_swap(__float_as_uint(s), __float_as_uint(q), false, false);
        const float v = __uint_as_float(r[0]) + __uint_as_float(r[1]);
        auto t = __builtin_amdgcn_permlane16_swap(__float_as_uint(v), __float_as_uint(v), false, false);
        __builtin_amdgcn_raw_buffer_store_b64((u32x2){__float_as_uint(__uint_as_float(t[0]) + __uint_as_float(t[1])), tg}, r_ws, off, 0, 16);
    };
    auto stat_off = [&](u32 ws_st, int ti) { const int l2 = opq(tid) & 63; return ws_st + (u32)(ti * 2048 + (l2 >> 4) * 512 + (l2 & 15) * 16); };
    auto fin_stat = [&](u32x4& a, u32x4& c, u32 off, float& mean, float& rstd) {
        _Pragma("unroll 1") for (int spin = 0; spin < SPIN_CAP; ++spin) {
            if (__builtin_expect(__all(((a[1] ^ tl) | (a[3] ^ th) | (c[1] ^ tl) | (c[3] ^ th)) == 0), 1)) break;
            __builtin_amdgcn_s_sleep(1);
            asm volatile("buffer_load_dwordx4 %0, %2, %3, 0 offen sc1\n\tbuffer_load_dwordx4 %1, %2, %3, 0 offen offset:256 sc1\n\ts_waitcnt vmcnt(0)"
                         : "=&v"(a), "=&v"(c) : "v"(off), "s"(d_ws) : "memory");
        }
        auto r = __builtin_amdgcn_permlane32_swap(__float_as_uint(__uint_as_float(a[0]) + __uint_as_float(c[0])), __float_as_uint(__uint_as_float(a[2]) + __uint_as_float(c[2])), false, false);
        const float v = __uint_as_float(r[0]) + __uint_as_float(r[1]);
        auto t = __builtin_amdgcn_permlane16_swap(__float_as_uint(v), __float_as_uint(v), false, false);
        const u32 v2 = __float_as_uint(__uint_as_float(t[0]) + __uint_as_float(t[1]));
        auto u = __builtin_amdgcn_permlane32_swap(v2, v2, false, false);
        const float ts = __uint_as_float(u[0]), tq = __uint_as_float(u[1]);
        mean = ts * (1.f / 512.f);
        rstd = __builtin_amdgcn_rsqf(fmaxf(tq * (1.f / 512.f) - mean * mean, 0.f) + 1e-5f);
    };
    auto regroup = [&](u32 (&w)[8]) {
#pragma unroll
        for (int j = 0; j < 2; ++j) {
            auto r0 = __builtin_amdgcn_permlane16_swap(w[4 * j], w[4 * j + 2], false, false);
            auto r1 = __builtin_amdgcn_permlane16_swap(w[4 * j + 1], w[4 * j + 3], false, false);
            w[4 * j] = r0[0]; w[4 * j + 1] = r1[0]; w[4 * j + 2] = r0[1]; w[4 * j + 3] = r1[1];
        }
    };
    auto wo_finish = [&](auto lc) {
        constexpr int LAYER = decltype(lc)::value;
        const int lane = opq(tid) & 63, li = lane & 15, lq = lane >> 4;
        u32 wx = L_WX + hd * 4096 + lane * 16;
        pin(wx);
        u32 po = 0, so = 0, tg = 0, oa = 0;
        if constexpr (R == 1) {
#pragma unroll
            for (int n = 0; n < 4; ++n) *(LAS f32x4*)(lds + wx + n * 1024) = accM[n];
        } else {
            po = pub_off(LAYER ? WS_ST3 : WS_ST1); so = stat_off(LAYER ? WS_ST3 : WS_ST1, TI); tg = lane < 32 ? tl : th;
            const int c = 2 * (lq & 1) + (lq >> 1);
            oa = LAYER ? (u32)((TI * 16 + li) * 1024 + mem * 128 + c * 16) : WS_X2 + (u32)((TI * 8 + mem) * 2048 + li * 128 + ((c ^ (li >> 1)) << 4));
            pin(po); pin(so); pin(tg); pin(oa);
        }
        BAR();
        if constexpr (R == 0) {
            f32x4 t[4];
#pragma unroll
            for (int n = 0; n < 4; ++n) t[n] = *(LAS f32x4*)(lds + wx + n * 1024);
            SB();
            float s = 0.f, q = 0.f, s2 = 0.f, q2 = 0.f;
#pragma unroll
            for (int n = 0; n < 4; ++n) {
                const f32x4 v = accM[n] + t[n];
                const u32 w0 = pk2(v[0], v[1]), w1 = pk2(v[2], v[3]);
                xw[2 * n] = w0; xw[2 * n + 1] = w1;
                s = dot2(w0, 0x3F803F80u, s); q = dot2(w0, w0, q); s2 = dot2(w1, 0x3F803F80u, s2); q2 = dot2(w1, w1, q2);
            }
            pub_stat(po, tg, s + s2, q + q2);
            CFENCE();
            if constexpr (!LAYER) load_y();
            regroup(xw);
            SB();
            WAIT_VM(0);
            if constexpr (!LAYER) {
#pragma unroll
                for (int i = 0; i < 8; ++i) pin(yw[i]);
            }
            u32x4 a, c;
            ald4s(a, so); ald4s(c, so + 256);
            if constexpr (!LAYER) {
                _Pragma("unroll 1") for (int spin = 0; spin < SPIN_CAP; ++spin) {
                    if (__builtin_expect(__all(max(max(max(yw[0], yw[1]), max(yw[2], yw[3])), max(max(yw[4], yw[5]), max(yw[6], yw[7]))) != SENT), 1)) break;
                    __builtin_amdgcn_s_sleep(1);
                    CFENCE();
                    load_y();
                }
                regroup(yw);
            }
            f32x2 xa[8], ya[8], xy[8];
#pragma unroll
            for (int i = 0; i < 8; ++i) {
                xa[i] = (f32x2){bflo(xw[i]), bfhi(xw[i])}; ya[i] = (f32x2){bflo(yw[i]), bfhi(yw[i])}; xy[i] = xa[i] * ya[i];
                pin(xa[i]); pin(ya[i]); pin(xy[i]);
            }
            WAIT_VM(0);
            pin(a); pin(c);
            float mean, rstd;
            fin_stat(a, c, so, mean, rstd);
            const float nm = -mean * rstd;
#pragma unroll
            for (int i = 0; i < 8; ++i) {
                const f32x2 o = xy[i] * rstd + (ya[i] * nm + xa[i]);
                xw[i] = pk2(o[0], o[1]);
                if (!LAYER) xw[i] = nosent(xw[i]);
            }
#pragma unroll
            for (int j = 0; j < 2; ++j) {
                const u32x4 o = {xw[4 * j], xw[4 * j + 1], xw[4 * j + 2], xw[4 * j + 3]};
                if constexpr (LAYER) __builtin_amdgcn_raw_buffer_store_b128(o, r_out, oa + j * 64, 0, 0);
                else __builtin_amdgcn_raw_buffer_store_b128(o, r_ws, oa ^ (j * 64), 0, 16);
            }
            if constexpr (!LAYER) {
                float s = 0.f, q = 0.f, s2 = 0.f, q2 = 0.f;
#pragma unroll
                for (int i = 0; i < 8; i += 2) { s = dot2(xw[i], 0x3F803F80u, s); q = dot2(xw[i], xw[i], q); s2 = dot2(xw[i + 1], 0x3F803F80u, s2); q2 = dot2(xw[i + 1], xw[i + 1], q2); }
                pub_stat(po + (WS_ST2 - WS_ST1) * !(lane & 16), tg, s + s2, q + q2);
                CFENCE();
                regroup(xw);
#pragma unroll
                for (int i = 0; i < 8; ++i) { pin(xw[i]); pin(yw[i]); }
                WAIT_VM(0);
            }
        }
    };
    wo_finish(IC<0>{});
    if constexpr (R == 1) {
        if (b == 0 && wave == 7) {
            const u32 fo = WS_F1 + (u32)((opq(tid) & 63) * 128);
            u32x2 ef[4];
#pragma unroll
            for (int i = 0; i < 4; ++i) ald2s(ef[i], fo + i * 8192);
            WAIT_VM(0);
#pragma unroll
            for (int i = 0; i < 4; ++i) { pin(ef[i]); spin_flags(ef[i], fo + i * 8192); }
            if (fo == WS_F1) __builtin_amdgcn_raw_buffer_store_b64(__builtin_bit_cast(u32x2, epoch + 1), r_ws, WS_EPOCH, 0, 16);
            WAIT_VM(0);
        }
    }
    RBAR();

    {
        const int l3 = opq(tid) & 63;
        const u32 g0 = (u32)((4 * grp - 1 + (hd >> 1)) * 16384 + (hd & 1) * 1024 + l3 * 16);
        const u32 g2 = (u32)((4 * grp + 3) * 16384 + hd * 512 + l3 * 16);
        auto x2_piece = [&](auto ic) {
            constexpr int i = decltype(ic)::value, s = i / 3, p = i % 3;
            const u32 kp = (u32)((s + rot) & 7);
            if constexpr (p == 0) dma(IC<4>{}, IC<L_CX + s * 10240>{}, d_x2, hb1k, g0, kp * 2048);
            else if constexpr (p == 1) dma(IC<4>{}, IC<L_CX + s * 10240 + 4096>{}, d_x2, hb1k, g0 + 32768, kp * 2048);
            else dma(IC<5>{}, IC<L_CX + s * 10240 + 8192>{}, d_x2, hb512, g2, kp * 2048);
        };
        u32 va = L_CX + hd * 1024 + l3 * 16, vq = L_CX + 8192 + hd * 512 + (l3 & 31) * 16 + (l3 >> 5) * 10240;
        pin(va); pin(vq);
        u32x4 vr[5];
        auto ver_read = [&](auto cc) {
            constexpr int c = decltype(cc)::value;
            u32 a = va + c * 20480, q = vq + c * 20480;
            pin(a); pin(q); CFENCE();
            vr[0] = *(LAS u32x4*)(lds + a); vr[1] = *(LAS u32x4*)(lds + a + 4096); vr[2] = *(LAS u32x4*)(lds + a + 10240); vr[3] = *(LAS u32x4*)(lds + a + 14336);
            vr[4] = *(LAS u32x4*)(lds + q);
        };
        auto ver_check = [&](auto cc) {
            constexpr int c = decltype(cc)::value;
            _Pragma("unroll 1") for (int spin = 0; spin < SPIN_CAP; ++spin) {
                u32 m0 = 0, m1 = 0;
#pragma unroll
                for (int i = 0; i < 5; ++i) { m0 = max(max(m0, vr[i][0]), vr[i][1]); m1 = max(max(m1, vr[i][2]), vr[i][3]); }
                if (__builtin_expect(__all(max(m0, m1) != SENT), 1)) break;
                __builtin_amdgcn_s_sleep(1);
                sfor<6 * c, 6 * c + 6>([&](auto ic) { x2_piece(ic); });
                WAIT_VM(0);
                ver_read(cc);
            }
        };
        const u32 xb0 = (u32)((l3 & 15) * 128 + (((l3 >> 4) ^ ((l3 & 15) >> 1)) << 4));
        if constexpr (R == 1) {
            sfor<0, 12>([&](auto ic) { x2_piece(ic); });
            WAIT_VM(6);
            ver_read(IC<0>{});
            ver_check(IC<0>{});
        } else {
            u32x4 sa, sc, ha, hc;
            const u32 so = stat_off(WS_ST2, 4 * grp + hd);
            const bool halo = hd == 0 && grp > 0;
            ald4s(sa, so); ald4s(sc, so + 256);
            if (halo) { ald4s(ha, so - 2048); ald4s(hc, so - 2048 + 256); }
            WAIT_VM(0);
            pin(sa); pin(sc);
            float mean, rstd;
            fin_stat(sa, sc, so, mean, rstd);
            if (l3 < 16) { *(LAS float*)(lds + L_TAB + 64 + hd * 64 + l3 * 4) = mean; *(LAS float*)(lds + L_TAB + 320 + 64 + hd * 64 + l3 * 4) = rstd; }
            if (halo) {
                pin(ha); pin(hc);
                fin_stat(ha, hc, so - 2048, mean, rstd);
                if (l3 < 16) { *(LAS float*)(lds + L_TAB + l3 * 4) = mean; *(LAS float*)(lds + L_TAB + 320 + l3 * 4) = rstd; }
            }
        }
        BAR();
        u32 xa;
        auto pf_addr = [&](int st) { xa = L_CX + (st >> 1) * 10240 + (xb0 ^ ((st & 1) * 64)); pin(xa); };
        pf_addr(0);
#pragma unroll
        for (int mt = 0; mt < 5; ++mt) Ab[0][mt] = ldsA(xa + mt * 2048);
        sfor<0, 16>([&](auto stc) {
            constexpr int st = decltype(stc)::value;
            if constexpr (R == 1) {
                if constexpr (st == 2) { WAIT_VM(10); ver_read(IC<1>{}); }
                if constexpr (st == 3) ver_check(IC<1>{});
                if constexpr (st == 5) { WAIT_VM(6); ver_read(IC<2>{}); }
                if constexpr (st == 6) { ver_check(IC<2>{}); WAIT_VM(0); ver_read(IC<3>{}); }
                if constexpr (st == 7) ver_check(IC<3>{});
            }
            if constexpr (st == 3) { RBAR(); }
            if constexpr (st == 7) { RBAR(); }
            if (st < 15) pf_addr(st + 1);
            if constexpr (st == 14) load_tab(true);
            SB();
            qkv_step(IC<1>{}, st, Ab[st & 1], [&](auto mc) {
                constexpr int mt = decltype(mc)::value;
                if (st < 15) Ab[(st + 1) & 1][mt] = ldsA(xa + mt * 2048);
                if constexpr (R == 1 && 12 + 5 * st + mt < 24) x2_piece(IC<12 + 5 * st + mt>{});
                SB();
            });
        });
    }
    attention(IC<1>{});

    {
        wo_addr();
#pragma unroll
        for (int ks = 0; ks < 8; ++ks) load_F(ks);
        SB();
        WAIT_VM(0);
        RBAR();
        load_o(WS_O2);
        SB();
        _Pragma("unroll 1") for (int spin = 0; spin < 64; ++spin) {
            u32 m0 = 0, m1 = 0;
#pragma unroll
            for (int ks = 0; ks < 8; ++ks) {
#pragma unroll
                for (int n = 0; n < 4; ++n) accM[n] = mfma32(F[ks][n], __builtin_bit_cast(s16x8, of[ks]), ks ? accM[n] : resid(n));
                m0 = max(max(m0, of[ks][0]), of[ks][1]); m1 = max(max(m1, of[ks][2]), of[ks][3]);
                SB();
            }
            if (__builtin_expect(__all(max(m0, m1) != SENT), 1)) break;
            _Pragma("unroll 1") for (int sp2 = 0; sp2 < SPIN_CAP; ++sp2) {
                CFENCE();
                __builtin_amdgcn_s_sleep(1);
                const u32 pv = __builtin_amdgcn_raw_buffer_load_b32(r_ws, WS_O2 + (u32)(TI * 16384 + R * 8192 + (opq(tid) & 15) * 512 + 508), 0, 16);
                if (__all(pv != SENT)) break;
            }
            CFENCE();
            load_o(WS_O2);
        }
        SB();
    }
    wo_finish(IC<1>{});
}

__global__ void __launch_bounds__(512, 2)
fused_kernel(const u16* __restrict__ gx, const u16* __restrict__ gwqkv, const u16* __restrict__ gwo,
             const u16* __restrict__ gbk, const u16* __restrict__ gbv, const u16* __restrict__ gsk,
             const u16* __restrict__ ghk, const u16* __restrict__ gdv, u16* __restrict__ gout, char* __restrict__ ws) {
    __shared__ __attribute__((aligned(16))) char lds_[L_TOTAL];
    typedef u32 u32x16 __attribute__((ext_vector_type(16)));
    u32x16 ka; u32x4 kb;
    {
        auto kp = __builtin_amdgcn_kernarg_segment_ptr();
        asm volatile("s_load_dwordx16 %0, %2, 0x0\n\ts_load_dwordx4 %1, %2, 0x40\n\ts_waitcnt lgkmcnt(0)" : "=&s"(ka), "=&s"(kb) : "s"(kp) : "memory");
    }
    auto mkp = [](u32 lo, u32 hi) { return (char*)(((u64)hi << 32) | lo); };
    const Args a = {(const u16*)mkp(ka[0], ka[1]), (const u16*)mkp(ka[2], ka[3]), (const u16*)mkp(ka[4], ka[5]), (const u16*)mkp(ka[6], ka[7]), (const u16*)mkp(ka[8], ka[9]),
                    (const u16*)mkp(ka[10], ka[11]), (const u16*)mkp(ka[12], ka[13]), (const u16*)mkp(ka[14], ka[15]), (u16*)mkp(kb[0], kb[1]), mkp(kb[2], kb[3])};
    const int wave = __builtin_amdgcn_readfirstlane(threadIdx.x >> 6);
    if (wave < 4) body<0>(a, (LAS char*)lds_, wave); else body<1>(a, (LAS char*)lds_, wave);
}

extern "C" void kernel_launch(void* const* d_in, const int* in_sizes, int n_in,
                              void* d_out, int out_size, void* d_ws, size_t ws_size,
                              hipStream_t stream) {
    fused_kernel<<<256, 512, 0, stream>>>((const u16*)d_in[0], (const u16*)d_in[1], (const u16*)d_in[2],
                                          (const u16*)d_in[4], (const u16*)d_in[5], (const u16*)d_in[6],
                                          (const u16*)d_in[7], (const u16*)d_in[8], (u16*)d_out, (char*)d_ws);
}
```

```cpp
#include <hip/hip_runtime.h>
#include <stdint.h>

typedef unsigned short u16;
typedef unsigned u32;
typedef unsigned long long u64;
typedef short s16x4 __attribute__((ext_vector_type(4)));
typedef short s16x8 __attribute__((ext_vector_type(8)));
typedef __bf16 bf16x2 __attribute__((ext_vector_type(2)));
typedef float f32x2 __attribute__((ext_vector_type(2)));
typedef float f32x4 __attribute__((ext_vector_type(4)));
typedef u32 u32x2 __attribute__((ext_vector_type(2)));
typedef u32 u32x4 __attribute__((ext_vector_type(4)));
#define LAS __attribute__((address_space(3)))
#define DEV __device__ __forceinline__
typedef __amdgpu_buffer_rsrc_t rsrc_t;

constexpr u32 WS_EPOCH = 0;
constexpr u32 WS_F1 = 32768;
constexpr u32 WS_F2 = 65536;
constexpr u32 WS_ST0 = 98304;
constexpr u32 WS_ST1 = 131072;
constexpr u32 WS_ST2 = WS_ST1 + 262144;
constexpr u32 WS_ST3 = WS_ST2 + 262144;
constexpr u32 WS_UT = 1u << 20;
constexpr u32 WS_O1 = WS_UT + (2u << 20);
constexpr u32 WS_O2 = WS_O1 + (2u << 20);
constexpr u32 WS_Y2 = WS_O2 + (2u << 20);
constexpr u32 WS_X2 = WS_Y2 + (2u << 20);
constexpr u32 WS_END = WS_X2 + (2u << 20);

constexpr u32 L_SLOT = 34816;
constexpr u32 L_KV = 4 * L_SLOT;
constexpr u32 L_K = L_KV, L_V = L_KV + 9216;
constexpr u32 L_SK = L_KV + 17408;
constexpr u32 L_WO = 0;
constexpr u32 L_CX = 65536;
constexpr u32 L_HC = L_CX;
constexpr u32 L_UR = L_CX + 69632;
constexpr u32 L_WX = L_CX + 40960;
constexpr u32 L_EX = 147456;
constexpr u32 L_TAB = L_EX + 12288;
constexpr u32 L_CS = L_TAB + 1024;
constexpr u32 L_FLG = L_TAB + 2048;
constexpr u32 L_TOTAL = 160 * 1024;
static_assert(L_FLG + 64 <= L_TOTAL, "LDS map");

constexpr int SPIN_CAP = 1 << 14;
constexpr u32 SENT = 0xFFFFFFFFu;

#define WAIT_VM(n) asm volatile("s_waitcnt vmcnt(%0)" ::"i"(n) : "memory")
#define BAR() do { asm volatile("s_waitcnt lgkmcnt(0)" ::: "memory"); __builtin_amdgcn_s_barrier(); asm volatile("" ::: "memory"); } while (0)
#define RBAR() do { asm volatile("" ::: "memory"); __builtin_amdgcn_s_barrier(); asm volatile("" ::: "memory"); } while (0)
#define CFENCE() asm volatile("" ::: "memory")
#define SB() __builtin_amdgcn_sched_barrier(0)
template <int N> struct IC { static constexpr int value = N; };
template <int I, int N, class F> DEV void sfor(F&& f) { if constexpr (I < N) { f(IC<I>{}); sfor<I + 1, N>(f); } }

DEV f32x4 mfma32(s16x8 a, s16x8 b, f32x4 c) { return __builtin_amdgcn_mfma_f32_16x16x32_bf16(a, b, c, 0, 0, 0); }
DEV f32x4 mfma16(s16x4 a, s16x4 b, f32x4 c) { return __builtin_amdgcn_mfma_f32_16x16x16bf16_1k(a, b, c, 0, 0, 0); }
DEV float vmax(float a, float b) { float r; asm("v_max_f32 %0, %1, %2" : "=v"(r) : "v"(a), "v"(b)); return r; }
DEV u32 pk2(float lo, float hi) { f32x2 f = {lo, hi}; bf16x2 b = __builtin_convertvector(f, bf16x2); return __builtin_bit_cast(u32, b); }
DEV float bflo(u32 w) { return __uint_as_float(w << 16); }
DEV float bfhi(u32 w) { return __uint_as_float(w & 0xffff0000u); }
DEV float dot2(u32 a, u32 b, float c) { return __builtin_amdgcn_fdot2_f32_bf16(__builtin_bit_cast(bf16x2, a), __builtin_bit_cast(bf16x2, b), c, false); }
DEV u32 nosent(u32 w) { return min(w, 0xFFFFFFFEu); }
DEV float rowsum16(float v) {
    v += __builtin_bit_cast(float, __builtin_amdgcn_update_dpp(0, __builtin_bit_cast(int, v), 0xB1, 0xf, 0xf, true));
    v += __builtin_bit_cast(float, __builtin_amdgcn_update_dpp(0, __builtin_bit_cast(int, v), 0x4E, 0xf, 0xf, true));
    v += __builtin_bit_cast(float, __builtin_amdgcn_update_dpp(0, __builtin_bit_cast(int, v), 0x141, 0xf, 0xf, true));
    v += __builtin_bit_cast(float, __builtin_amdgcn_update_dpp(0, __builtin_bit_cast(int, v), 0x140, 0xf, 0xf, true));
    return v;
}
DEV float bcast_row0(float v) { return __builtin_bit_cast(float, __builtin_amdgcn_update_dpp(0, __builtin_bit_cast(int, v), 0x150, 0xf, 0xf, true)); }
template <int OFF> DEV void ald4o(u32x4& d, u32 voff, u32x4 rs) { asm volatile("buffer_load_dwordx4 %0, %1, %2, 0 offen offset:%3 sc1" : "=v"(d) : "v"(voff), "s"(rs), "i"(OFF) : "memory"); }
template <class T> DEV void pin(T& v) { asm volatile("" : "+v"(v)); }
DEV int opq(int v) { asm volatile("" : "+v"(v)); return v; }
DEV s16x8 cat8(s16x4 a, s16x4 b) { return __builtin_shufflevector(a, b, 0, 1, 2, 3, 4, 5, 6, 7); }
DEV s16x4 pack4(f32x4 v) { u32x2 w = {pk2(v[0], v[1]), pk2(v[2], v[3])}; return __builtin_bit_cast(s16x4, w); }
DEV u64 mix64(u64 z) { z += 0x9E3779B97F4A7C15ull; z = (z ^ (z >> 30)) * 0xBF58476D1CE4E5B9ull; z = (z ^ (z >> 27)) * 0x94D049BB133111EBull; return z ^ (z >> 31); }

struct ConvSched {
    int n[4], f[18], i[18], slot[64];
    constexpr ConvSched(int R) : n{}, f{}, i{}, slot{} {
        constexpr int LEAD = 9;
        const int N0[4] = {0, 3, 4, 7};
        for (int k = 0; k < 4; ++k) n[k] = N0[k] ^ R;
        int c = 0;
        for (int ff = 0; ff < 8; ++ff) for (int k = 3; k >= 0; --k) if (ff <= n[k]) { f[c] = ff; i[c] = k; ++c; }
        for (int k = 0; k < 64; ++k) slot[k] = 1000;
        for (int p = 71; p >= 0; --p) {
            const int e = p / 18, ff = f[p % 18], g = n[i[p % 18]] - ff, s = p - LEAD < -1 ? -1 : p - LEAD;
            slot[e * 16 + ff] = s; slot[e * 16 + 8 + g] = s;
        }
    }
};

struct Args { const u16 *gx, *gwqkv, *gwo, *gbk, *gbv, *gsk, *ghk, *gdv; u16* gout; char* ws; };

template <int R>
DEV void body(const Args& A_, LAS char* const lds, const int wave) {
    const u16* gx = A_.gx; char* ws = A_.ws;
    const int b = blockIdx.x, tid = threadIdx.x, lane = tid & 63;
    const int hd = wave & 3;
    const int grp = (b & 7) * 4 + (b >> 6), mem = (b >> 3) & 7;
    const int T0 = grp * 64;
    const int rot = (mem + 2 * (grp & 3)) & 7;

    u64 epoch;
    asm volatile("s_load_dwordx2 %0, %1, 0x0" : "=s"(epoch) : "s"(ws));
    u32 dw;
    asm volatile("s_load_dword %0, %1, 0x0" : "=s"(dw) : "s"(A_.gdv + 2 * b));
    const rsrc_t r_x = __builtin_amdgcn_make_buffer_rsrc((void*)gx, 0, 2048 * 512 * 2, 0x00020000);
    const rsrc_t r_ws = __builtin_amdgcn_make_buffer_rsrc((void*)ws, 0, WS_END, 0x00020000);
    const rsrc_t r_out = __builtin_amdgcn_make_buffer_rsrc((void*)A_.gout, 0, 2048 * 512 * 2, 0x00020000);
    const u32 ldsbase = (u32)(size_t)lds;
    auto mkdesc = [](const void* p, u32 bytes) { const u64 a = (u64)p; return (u32x4){(u32)a, (u32)(a >> 32) & 0xffffu, bytes, 0x00020000u}; };
    const u32x4 d_x = mkdesc(gx, 2048 * 512 * 2), d_w = mkdesc(A_.gwqkv, 512 * 1536 * 2), d_wo = mkdesc(A_.gwo, 512 * 512 * 2);
    const u32x4 d_k = mkdesc(A_.gbk, 2048 * 512 * 2), d_v = mkdesc(A_.gbv, 2048 * 512 * 2), d_sk = mkdesc(A_.gsk, 512 * 4 * 2);
    const u32x4 d_h = mkdesc(A_.ghk, 512 * 2048 * 2), d_x2 = mkdesc(ws + WS_X2, 2u << 20), d_ws = mkdesc(ws, WS_END);
    const u32 hb1k = ldsbase + hd * 1024, hb512 = ldsbase + hd * 512;
#define DMA_ASM(pre, mods, post) asm volatile("s_add_u32 m0, %0, %4\n\t" pre "buffer_load_dwordx4 %1, %2, %3 offen " mods "lds" post \
                     :: "s"(sbase), "v"(voff), "s"(rs), "s"(soff), "i"(IMM) : "memory", "scc")
    const u32 xmlo = hd == 0 ? ~0xFFu : ~0u;
    auto dma = [&](auto kind, auto imm, u32x4 rs, u32 sbase, u32 voff, u32 soff) {
        constexpr int K = decltype(kind)::value; constexpr int IMM = decltype(imm)::value;
        if constexpr (K == 0) DMA_ASM("s_nop 0\n\t", "", "");
        else if constexpr (K == 1) DMA_ASM("s_mov_b32 exec_hi, 0\n\t", "", "\n\ts_mov_b32 exec_hi, -1");
        else if constexpr (K == 2) {
            const u32 ml = xmlo;
            asm volatile("s_add_u32 m0, %0, %4\n\ts_mov_b32 exec_lo, %5\n\tbuffer_load_dwordx4 %1, %2, %3 offen lds\n\ts_mov_b32 exec_lo, -1"
                         :: "s"(sbase), "v"(voff), "s"(rs), "s"(soff), "i"(IMM), "s"(ml) : "memory", "scc");
        }
        else if constexpr (K == 4) DMA_ASM("s_nop 0\n\t", "sc1 ", "");
        else DMA_ASM("s_mov_b32 exec_hi, 0\n\t", "sc1 ", "\n\ts_mov_b32 exec_hi, -1");
    };
    auto ald4 = [&](u32x4& d, u32x4 rs, u32 voff) { asm volatile("buffer_load_dwordx4 %0, %1, %2, 0 offen" : "=v"(d) : "v"(voff), "s"(rs) : "memory"); };
    auto ald4s = [&](u32x4& d, u32 voff) { asm volatile("buffer_load_dwordx4 %0, %1, %2, 0 offen sc1" : "=v"(d) : "v"(voff), "s"(d_ws) : "memory"); };
    auto ald2s = [&](u32x2& d, u32 voff) { asm volatile("buffer_load_dwordx2 %0, %1, %2, 0 offen sc1" : "=v"(d) : "v"(voff), "s"(d_ws) : "memory"); };

    const int pr = lane >> 3, pc = lane & 7;
    const int xr0 = 8 * hd + pr, xr2 = 64 + 4 * hd + pr;
    const u32 xv0 = (u32)((T0 - 16 + xr0) * 1024 + ((pc ^ ((xr0 >> 1) & 7)) << 4));
    const u32 xv2 = (u32)((T0 - 16 + xr2) * 1024 + ((pc ^ ((xr2 >> 1) & 7)) << 4));
    const int wfw = ((xr0 >> 1) & 1) | (((xr0 >> 3) & 1) << 1);
    const u32 wv0 = (u32)(xr0 * 3072 + mem * 128 + ((((pc >> 1) ^ wfw) * 2 + (pc & 1)) << 4));
    auto issue_piece = [&](auto sc, auto ic) {
        constexpr int s = decltype(sc)::value, i = decltype(ic)::value;
        const u32 kp = (u32)((s + rot) & 7);
        constexpr int sb = (s & 3) * L_SLOT;
        if constexpr (i == 0) dma(IC<2>{}, IC<sb>{}, d_x, hb1k, xv0, kp * 128);
        else if constexpr (i == 1) dma(IC<0>{}, IC<sb + 4096>{}, d_x, hb1k, xv0 + 32768, kp * 128);
        else if constexpr (i == 2) dma(IC<1>{}, IC<sb + 8192>{}, d_x, hb512, xv2, kp * 128);
        else dma(IC<0>{}, IC<sb + 10240 + (i - 3) * 4096>{}, d_w, hb1k, wv0, kp * (64 * 3072) + ((i - 3) >> 1) * 1024 + ((i - 3) & 1) * (32 * 3072));
    };
    u32 wov = 0;
    auto issue_wo = [&](auto ic) {
        constexpr int i = decltype(ic)::value;
        if constexpr (i == 0) {
            const int l2 = opq(tid) & 63, pr = l2 >> 3, pc = l2 & 7;
            wov = (u32)((8 * hd + pr) * 1024 + mem * 128 + ((((pc >> 1) ^ (((pr >> 1) & 1) | ((hd & 1) << 1))) * 2 + (pc & 1)) << 4));
            pin(wov);
        }
        const u32 j = (u32)((i + 2 * grp) & 7);
        dma(IC<0>{}, IC<L_WO + (i & 8) * 4096>{}, d_wo, hb1k + j * 4096, wov, ((i & 8) + j) * 32768);
    };
    auto issue_kv = [&](int i) {
        const int l2 = opq(tid) & 63, pr = l2 >> 3, pc = l2 & 7, p = hd + 4 * (i & 1);
        const u32 cb = (u32)(mem * 128);
        if (i < 2) dma(IC<0>{}, IC<L_K>{}, d_k, hb1k + (i & 1) * 4096, (u32)((T0 - 8 + 8 * p + pr) * 1024) + cb + ((pc ^ p) << 4), 0);
        else if (i < 4) dma(IC<0>{}, IC<L_V>{}, d_v, hb1k + (i & 1) * 4096, (u32)((T0 + 8 * p + pr) * 1024) + cb + ((pc ^ p) << 4), 0);
        else if (hd == 0) dma(IC<0>{}, IC<L_K + 8192>{}, d_k, ldsbase, (u32)((T0 - 8 + 64 + pr) * 1024) + cb + (pc << 4), 0);
        else dma(IC<0>{}, IC<L_SK>{}, d_sk, ldsbase, (u32)(mem * 512 + l2 * 16), 0);
    };

    const int srow = T0 + 8 * mem + 2 * hd;
    u32x4 xrow[2];
    sfor<0, 9>([&](auto ic) { if constexpr ((decltype(ic)::value & 1) != R) issue_piece(IC<0>{}, ic); });
    sfor<0, 9>([&](auto ic) { if constexpr ((decltype(ic)::value & 1) != R) issue_piece(IC<1>{}, ic); });
    asm volatile("s_waitcnt lgkmcnt(0)" : "+s"(epoch), "+s"(dw));
    const u64 tag = mix64(epoch);
    const u32 tl = (u32)tag, th = (u32)(tag >> 32);
    if (R == 1 && wave == 7 && lane < 32) *(LAS u32x4*)(lds + (lane >> 3) * L_SLOT + (lane & 7) * 16) = (u32x4){0x3F803F80u, 0x3F803F80u, 0x3F803F80u, 0x3F803F80u};

    auto trr = [&](u32 off) { return __builtin_amdgcn_ds_read_tr16_b64_v4i16((LAS s16x4*)(lds + off)); };
    auto ldsA = [&](u32 off) { return *(LAS s16x8*)(lds + off); };
    auto flag_ok = [&](const u32x2& f) { return __all(f[0] == tl && f[1] == th); };
    auto spin_flags = [&](u32x2& f, u32 off) {
        _Pragma("unroll 1") for (int spin = 0; spin < SPIN_CAP; ++spin) {
            if (__builtin_expect(flag_ok(f), 1)) break;
            asm volatile("buffer_load_dwordx2 %0, %1, %2, 0 offen sc1\n\ts_waitcnt vmcnt(0)" : "=&v"(f) : "v"(off), "s"(d_ws) : "memory");
        }
    };

    s16x8 Wq[16], Wm[16];
    f32x4 accM[5], accQ[5];
    const f32x4 zero4 = {0.f, 0.f, 0.f, 0.f};
    const s16x8 ones8 = {0x3F80, 0x3F80, 0x3F80, 0x3F80, 0x3F80, 0x3F80, 0x3F80, 0x3F80};
    s16x8 Ab[3][5];
    auto qkv_step = [&](auto lc, int st, const s16x8 (&A)[5], auto fill) {
        sfor<0, 5>([&](auto mc) {
            constexpr int mt = decltype(mc)::value;
            const f32x4 cm = st ? accM[mt] : zero4;
            accM[mt] = R ? mfma32(A[mt], Wm[st], cm) : mfma32(Wm[st], A[mt], cm);
            if constexpr (R == 0 && (mt > 0 || decltype(lc)::value == 0)) accQ[mt] = mfma32(Wq[st], A[mt], st ? accQ[mt] : zero4);
            fill(mc);
        });
    };

    float m1[5], r1[5]; f32x4 m4[5], r4[5], csK, csQ;
    auto load_tab = [&](bool cs) {
        const int l2 = opq(tid) & 63;
        u32 ta = L_TAB + (R ? (l2 >> 4) * 16 : (l2 & 15) * 4);
        pin(ta);
        if (R == 0 && cs) { csK = *(LAS f32x4*)(lds + L_CS + hd * 128 + (l2 >> 4) * 16); csQ = *(LAS f32x4*)(lds + L_CS + hd * 128 + 64 + (l2 >> 4) * 16); }
#pragma unroll
        for (int mt = 0; mt < 5; ++mt) {
            if (R == 0) { m1[mt] = *(LAS float*)(lds + ta + mt * 64); r1[mt] = *(LAS float*)(lds + ta + 320 + mt * 64); }
            else { m4[mt] = *(LAS f32x4*)(lds + ta + mt * 64); r4[mt] = *(LAS f32x4*)(lds + ta + 320 + mt * 64); }
        }
    };

    u32x4 hr[4];
    u32x2 flg;
    {
        const int li = lane & 15, lq = lane >> 4, tq = li >> 2, tp = lane & 3;
        const u32 xa0 = (u32)(li * 128 + ((lq ^ (li >> 1)) << 4));
        const u32 wa = (u32)((8 * lq + tq) * 128 + ((hd ^ (((tq >> 1) & 1) | ((lq & 1) << 1))) << 5) + tp * 8);
        u32 xa, wb, kw[11], vw[8];
        u32x4 s0v, skw;
        auto pf_addr = [&](int st) {
            const u32 sb = (u32)((st >> 1) & 3) * L_SLOT;
            xa = sb + (xa0 ^ ((st & 1) * 64)); wb = sb + 10240 + (st & 1) * 4096 + wa;
            asm volatile("" : "+v"(xa), "+v"(wb));
        };
        auto pf_part = [&](int st, int mt) {
            if (mt == 0) Wm[st] = cat8(trr(wb + (1 + R) * 8192), trr(wb + (1 + R) * 8192 + 512));
            Ab[st & 1][mt] = ldsA(xa + mt * 2048);
            if (R == 0 && mt == 1) Wq[st] = cat8(trr(wb), trr(wb + 512));
        };
        WAIT_VM(R ? 5 : 4);
        BAR();
        pf_addr(0);
#pragma unroll
        for (int mt = 0; mt < 5; ++mt) pf_part(0, mt);
        sfor<0, 16>([&](auto stc) {
            constexpr int st = decltype(stc)::value;
            if constexpr ((st & 1) && st < 15) {
                constexpr int s1 = (st >> 1) + 1;
                if constexpr (R == 0) {
                    if constexpr (s1 == 1) WAIT_VM(7);
                    if constexpr (s1 == 2) WAIT_VM(1);
                    if constexpr (s1 == 5) WAIT_VM(0);
                } else {
                    constexpr int WN[8] = {0, 9, 9, 9, 9, 9, 10, 10};
                    WAIT_VM(WN[s1]);
                    if constexpr (s1 == 7) {
                        pin(s0v);
                        const int l2 = opq(tid) & 63, tr = hd * 20 + min(l2, 19), row = T0 - 16 + tr;
                        _Pragma("unroll 1") for (int spin = 0; spin < SPIN_CAP; ++spin) {
                            if (__builtin_expect(__all(row < 0 || (s0v[1] == tl && s0v[3] == th)), 1)) break;
                            ald4s(s0v, WS_ST0 + (u32)(max(row, 0) * 16));
                            WAIT_VM(0);
                            pin(s0v);
                        }
                        *(LAS u32*)(lds + L_TAB + tr * 4) = row < 0 ? 0u : s0v[0];
                        *(LAS u32*)(lds + L_TAB + 320 + tr * 4) = row < 0 ? 0x3F800000u : s0v[2];
                        asm volatile("s_waitcnt lgkmcnt(0)" ::: "memory");
                    }
                }
                RBAR();
                if constexpr (s1 == 5 && R == 0) { if (tid == 0) __builtin_amdgcn_raw_buffer_store_b64((u32x2){tl, th}, r_ws, WS_F1 + (mem * 32 + grp) * 128, 0, 16); CFENCE(); }
                if constexpr (s1 == 5 && R == 1) { const int row = T0 - 16 + hd * 20 + min(opq(tid) & 63, 19); ald4s(s0v, WS_ST0 + (u32)(max(row, 0) * 16)); }
            }
            if (st < 15) pf_addr(st + 1);
            if constexpr (st == 14) load_tab(false);
            SB();
            qkv_step(IC<0>{}, st, Ab[st & 1], [&](auto mc) {
                constexpr int mt = decltype(mc)::value;
                if (st < 15) pf_part(st + 1, mt);
                if constexpr (R == 1) {
                    if constexpr (st == 0) { issue_piece(IC<2>{}, IC<2 * mt>{}); if constexpr (mt < 4) issue_piece(IC<2>{}, IC<2 * mt + 1>{}); }
                    if constexpr (st >= 1 && st <= 10 && ((st - 1) & 1) * 5 + mt < 9) issue_piece(IC<((st - 1) >> 1) + 3>{}, IC<((st - 1) & 1) * 5 + mt>{});
                    if constexpr (st == 11 && mt == 0) { const u32 ho = (u32)(b * 8192 + opq(tid) * 16); ald4(hr[2], d_h, ho); ald4(hr[3], d_h, ho + 16); }
                    if constexpr (st == 13 && mt == 0) { if (wave == 7) { const int l2 = opq(tid) & 63; ald2s(flg, WS_F1 + (u32)((l2 < 32 ? (b >> 5) * 32 + l2 : (l2 & 7) * 32 + grp) * 128)); } }
                    if constexpr (st >= 11 && st <= 14 && mt < 4) issue_wo(IC<(st - 11) * 4 + mt>{});
                } else {
                    if constexpr (st == 0) {
                        if constexpr (mt == 0) { const u32 xo = (u32)(srow * 1024 + (opq(tid) & 63) * 16); ald4(xrow[0], d_x, xo); ald4(xrow[1], d_x, xo + 1024); }
                        issue_kv(mt);
                    }
                    if constexpr (st == 11 && mt < 2) { const u32 ho = (u32)(b * 8192 + mt * 4096 + opq(tid) * 16); ald4(hr[2 * mt], d_h, ho); ald4(hr[2 * mt + 1], d_h, ho + 16); }
                }
                SB();
            });
            if constexpr (R == 0 && st == 2) {
                WAIT_VM(5);
                pin(xrow[0]); pin(xrow[1]);
                float s[2] = {0.f, 0.f}, q[2] = {0.f, 0.f};
#pragma unroll
                for (int i = 0; i < 4; ++i)
#pragma unroll
                    for (int k = 0; k < 2; ++k) { s[k] = dot2(xrow[k][i], 0x3F803F80u, s[k]); q[k] = dot2(xrow[k][i], xrow[k][i], q[k]); }
                auto ss = __builtin_amdgcn_permlane32_swap(__float_as_uint(s[0]), __float_as_uint(s[1]), false, false);
                auto qq = __builtin_amdgcn_permlane32_swap(__float_as_uint(q[0]), __float_as_uint(q[1]), false, false);
                float sv1 = rowsum16(__uint_as_float(ss[0]) + __uint_as_float(ss[1])), qv1 = rowsum16(__uint_as_float(qq[0]) + __uint_as_float(qq[1]));
                auto s2 = __builtin_amdgcn_permlane16_swap(__float_as_uint(sv1), __float_as_uint(sv1), false, false);
                auto q2 = __builtin_amdgcn_permlane16_swap(__float_as_uint(qv1), __float_as_uint(qv1), false, false);
                const float m = (__uint_as_float(s2[0]) + __uint_as_float(s2[1])) * (1.f / 512.f);
                const float r = __builtin_amdgcn_rsqf(fmaxf((__uint_as_float(q2[0]) + __uint_as_float(q2[1])) * (1.f / 512.f) - m * m, 0.f) + 1e-5f);
                const u32x4 sv = {__float_as_uint(m), tl, __float_as_uint(r), th};
                const int l2 = opq(tid) & 63;
                __builtin_amdgcn_raw_buffer_store_b128(sv, r_ws, (l2 & 31) == 0 ? WS_ST0 + (u32)((srow + (l2 >> 5)) * 16) : 0xFFFFFF00u, 0, 16);
                CFENCE();
                SB();
            }
            if constexpr (R == 0 && (st == 4 || st == 6)) {
                const int l2 = opq(tid) & 63, tg = l2 & 7, cp = 8 * hd + (l2 >> 3);
                if constexpr (st == 4) {
                    skw = *(LAS u32x4*)(lds + L_SK + cp * 16);
                    u32 ka = L_K + tg * 1024 + ((((cp >> 2) ^ tg) & 7) << 4) + (cp & 3) * 4;
                    u32 kb = L_K + tg * 1024 + ((((cp >> 2) ^ (tg + 1)) & 7) << 4) + (cp & 3) * 4;
                    pin(ka); pin(kb);
#pragma unroll
                    for (int i = 0; i < 11; ++i) kw[i] = *(LAS u32*)(lds + ((i + 5 < 8) ? ka : kb) + (i + 5) * 128);
#pragma unroll
                    for (int i = 0; i < 8; ++i) vw[i] = *(LAS u32*)(lds + ka + (L_V - L_K) + i * 128);
                    SB();
                }
                constexpr bool HI = st == 6;
                auto ex = [&](u32 w) { return HI ? bfhi(w) : bflo(w); };
                const float a0 = bflo(skw[2 * HI]), a1 = bfhi(skw[2 * HI]), a2 = bflo(skw[2 * HI + 1]), a3 = bfhi(skw[2 * HI + 1]);
                float ua[8];
#pragma unroll
                for (int i = 0; i < 8; ++i) ua[i] = (ex(kw[i]) * a3 + ex(kw[i + 1]) * a2 + ex(kw[i + 2]) * a1 + ex(kw[i + 3]) * a0) * ex(vw[i]);
                const u32x4 oa = {pk2(ua[7], ua[6]), pk2(ua[5], ua[4]), pk2(ua[3], ua[2]), pk2(ua[1], ua[0])};
                __builtin_amdgcn_raw_buffer_store_b128(oa, r_ws, WS_UT + (u32)((mem * 64 + 2 * cp) * 4096 + (2040 - T0 - 8 * tg) * 2), HI * 4096, 16);
                __builtin_amdgcn_raw_buffer_store_b128((u32x4){SENT, SENT, SENT, SENT}, r_ws, (u32)((4 * grp + hd) * 16384 + mem * 2048 + HI * 1024 + l2 * 16), WS_O1, 16);
                CFENCE();
                SB();
            }
        });
    }

    u32x4 uu;
    float csV;
    auto attention = [&](auto lc) {
        constexpr int LAYER = decltype(lc)::value;
        const int lane = opq(tid) & 63, li = lane & 15, lq = lane >> 4;
        u32 ex = L_EX + hd * 2048 + lane * 8;
        pin(ex);
        u32 f1ok = 1;
        if constexpr (R == 0) {
            if constexpr (LAYER == 0) {
#pragma unroll
                for (int i = 0; i < 4; ++i) { csK[i] = bcast_row0(accM[0][i]); csQ[i] = bcast_row0(accQ[0][i]); }
                if (li == 0) { *(LAS f32x4*)(lds + L_CS + hd * 128 + lq * 16) = csK; *(LAS f32x4*)(lds + L_CS + hd * 128 + 64 + lq * 16) = csQ; }
            }
            const int par = li & 1;
            const bool cur0 = (4 * lq + par) <= li, cur1 = (4 * lq + par + 2) <= li;
            s16x4 Kb[5], Qb[5];
#pragma unroll
            for (int mt = 0; mt < 5; ++mt) {
                Kb[mt] = pack4((accM[mt] - m1[mt] * csK) * r1[mt]);
                if (mt) Qb[mt] = pack4(accQ[mt] - m1[mt] * csQ);
            }
            f32x4 Sc[5], Sp[5];
#pragma unroll
            for (int mt = 1; mt < 5; ++mt) { Sc[mt] = mfma16(Kb[mt], Qb[mt], zero4); Sp[mt] = mfma16(Kb[mt - 1], Qb[mt], zero4); }
            float s0[5], s1[5], mx[5];
#pragma unroll
            for (int mt = 1; mt < 5; ++mt) {
                const float sp0 = Sp[mt][0], sp1 = Sp[mt][1], sp2 = Sp[mt][2], sp3 = Sp[mt][3], c0 = Sc[mt][0], c1 = Sc[mt][1], c2 = Sc[mt][2], c3 = Sc[mt][3];
                float p0 = par ? sp1 : sp0, p1 = par ? sp3 : sp2, q0 = par ? c1 : c0, q1 = par ? c3 : c2;
                asm volatile("" : "+v"(p0), "+v"(p1), "+v"(q0), "+v"(q1));
                if (mt == 1 && grp == 0) { p0 = -3.0e38f; p1 = -3.0e38f; }
                s0[mt] = cur0 ? q0 : p0; s1[mt] = cur1 ? q1 : p1;
                mx[mt] = vmax(s0[mt], s1[mt]);
            }
#pragma unroll
            for (int mt = 1; mt < 5; ++mt) { auto r = __builtin_amdgcn_permlane32_swap(__float_as_uint(mx[mt]), __float_as_uint(mx[mt]), false, false); mx[mt] = vmax(__uint_as_float(r[0]), __uint_as_float(r[1])); }
#pragma unroll
            for (int mt = 1; mt < 5; ++mt) { auto r = __builtin_amdgcn_permlane16_swap(__float_as_uint(mx[mt]), __float_as_uint(mx[mt]), false, false); mx[mt] = vmax(__uint_as_float(r[0]), __uint_as_float(r[1])); }
#pragma unroll
            for (int mt = 1; mt < 5; ++mt) {
                const float sc = r1[mt] * (0.25f * 1.44269504088896341f);
                const float e0 = __builtin_amdgcn_exp2f((s0[mt] - mx[mt]) * sc), e1 = __builtin_amdgcn_exp2f((s1[mt] - mx[mt]) * sc);
                *(LAS u32x2*)(lds + ex + (mt - 1) * 512) = (u32x2){pk2(e0, e0), pk2(e1, e1)};
            }
            BAR();
            if constexpr (LAYER == 0) f1ok = __builtin_amdgcn_readfirstlane(*(LAS u32*)(lds + L_FLG));
        } else {
            if constexpr (LAYER == 0) {
                auto s = __builtin_amdgcn_permlane16_swap(__float_as_uint(accM[0][0]), __float_as_uint(accM[0][0]), false, false);
                auto t = __builtin_amdgcn_permlane32_swap(s[0], s[0], false, false);
                csV = __uint_as_float(t[0]);
                if (wave == 7) { WAIT_VM(8); pin(flg); const u32 ok = flag_ok(flg); *(LAS u32*)(lds + L_FLG) = ok; }
            }
            const int par = li & 1;
            const bool cur0 = (4 * lq + par) <= li, cur1 = (4 * lq + par + 2) <= li;
            const u32 pm = par ? 0xffff0000u : 0x0000ffffu;
            u32 mC0 = cur0 ? pm : 0u, mP0 = cur0 ? 0u : pm, mC1 = cur1 ? pm : 0u, mP1 = cur1 ? 0u : pm;
            u32 oo = (LAYER ? WS_O2 : WS_O1) + (u32)((4 * grp + (lq & 1)) * 16384 + (4 * mem + hd) * 512 + (lq >> 1) * 256 + li * 16);
            asm volatile("" : "+v"(mC0), "+v"(mP0), "+v"(mC1), "+v"(mP1), "+v"(oo));
            s16x4 Vb[5];
#pragma unroll
            for (int mt = 0; mt < 5; ++mt) { Vb[mt] = pack4((accM[mt] - m4[mt] * csV) * r4[mt]); pin(Vb[mt]); }
            BAR();
            u32x2 pw[4];
#pragma unroll
            for (int j = 0; j < 4; ++j) pw[j] = *(LAS u32x2*)(lds + ex + j * 512);
            if constexpr (LAYER == 0) {
                f1ok = __builtin_amdgcn_readfirstlane(*(LAS u32*)(lds + L_FLG));
                if (f1ok) ald4s(uu, WS_UT + (u32)(b * 8192 + opq(tid) * 16));
            }
            SB();
            f32x4 O[4], L[4];
#pragma unroll
            for (int j = 0; j < 4; ++j) {
                const s16x8 P = __builtin_bit_cast(s16x8, (u32x4){pw[j][0] & mC0, pw[j][1] & mC1, pw[j][0] & mP0, pw[j][1] & mP1});
                L[j] = mfma32(ones8, P, zero4);
                O[j] = mfma32(cat8(Vb[j + 1], Vb[j]), P, zero4);
            }
            u32 ow[4][2];
#pragma unroll
            for (int j = 0; j < 4; ++j) {
                const float inv = __builtin_amdgcn_rcpf(L[j][0]);
                ow[j][0] = nosent(pk2(O[j][0] * inv, O[j][1] * inv)); ow[j][1] = nosent(pk2(O[j][2] * inv, O[j][3] * inv));
            }
#pragma unroll
            for (int j = 0; j < 4; j += 2) {
                auto r0 = __builtin_amdgcn_permlane16_swap(ow[j][0], ow[j + 1][0], false, false);
                auto r1 = __builtin_amdgcn_permlane16_swap(ow[j][1], ow[j + 1][1], false, false);
                __builtin_amdgcn_raw_buffer_store_b128((u32x4){r0[0], r1[0], r0[1], r1[1]}, r_ws, oo, j * 16384, 16);
            }
            CFENCE();
        }
        if constexpr (LAYER == 0 && R == 0) { if (f1ok) ald4s(uu, WS_UT + (u32)(b * 8192 + opq(tid) * 16)); }
        SB();
        return f1ok;
    };
    const u32 f1ok = attention(IC<0>{});
    {
        const u32x4 sv = {SENT, SENT, SENT, SENT};
        const int t2 = opq(tid);
        const u32 tb = (u32)((4 * grp + hd) * 16384 + mem * 2048 + R * 1024 + (t2 & 63) * 16);
        __builtin_amdgcn_raw_buffer_store_b128(sv, r_ws, tb, WS_O2, 16);
        __builtin_amdgcn_raw_buffer_store_b128(sv, r_ws, tb, WS_X2, 16);
        __builtin_amdgcn_raw_buffer_store_b128(sv, r_ws, (u32)(b * 8192 + t2 * 16), WS_Y2, 16);
        CFENCE();
        SB();
    }

    const int TI = 4 * grp + hd;
    u32x4 of[8];
    auto load_o = [&](u32 ws_o) {
        const int lane = opq(tid) & 63;
#pragma unroll
        for (int i = 0; i < 8; ++i) of[i] = __builtin_amdgcn_raw_buffer_load_b128(r_ws, ws_o + (u32)(TI * 16384 + R * 8192 + lane * 16) + (i & 3) * 1024, (i >> 2) * 4096, 16);
    };
    u32 xw[8], yw[8];
    u32 woa[4];
    auto resid = [&](int n) { return R ? zero4 : (f32x4){bflo(xw[2 * n]), bfhi(xw[2 * n]), bflo(xw[2 * n + 1]), bfhi(xw[2 * n + 1])}; };
    s16x8 F[8][4];
    auto wo_addr = [&]() {
        const int l2 = opq(tid) & 63, lq = l2 >> 4, tq = (l2 & 15) >> 2, tp = l2 & 3;
#pragma unroll
        for (int n = 0; n < 4; ++n) woa[n] = L_WO + (u32)(R * 32768 + (8 * lq + tq) * 128 + ((n ^ (((lq & 1) << 1) | (tq >> 1))) << 5) + tp * 8);
    };
    auto load_F = [&](int ks) {
#pragma unroll
        for (int n = 0; n < 4; ++n) F[ks][n] = cat8(trr(woa[n] + ks * 4096), trr(woa[n] + ks * 4096 + 512));
    };

    {
        const int t2 = opq(tid), lane = t2 & 63, li = lane & 15, lq = lane >> 4;
        constexpr u32 OFFS = 0xEDA85410u;
        {
            const int j = t2 & 255;
            {
                if constexpr (R == 0) { if (f1ok) WAIT_VM(4); else WAIT_VM(3); }
                else WAIT_VM(5);
                u32 cb = L_HC + R * 34816 + (j + 2) * 16;
                pin(cb);
#pragma unroll
                for (int ch = R; ch < 2; ++ch) {
                    pin(hr[2 * ch]); pin(hr[2 * ch + 1]);
                    const u32 d[8] = {hr[2 * ch][0], hr[2 * ch][1], hr[2 * ch][2], hr[2 * ch][3], hr[2 * ch + 1][0], hr[2 * ch + 1][1], hr[2 * ch + 1][2], hr[2 * ch + 1][3]};
                    u32 e[7];
#pragma unroll
                    for (int i = 0; i < 7; ++i) e[i] = __builtin_amdgcn_alignbit(d[i + 1], d[i], 16);
                    if (ch > R) { cb += 34816; pin(cb); }
#pragma unroll
                    for (int p = (R ? 4 : 0); p < ((ch && !R) ? 4 : 8); ++p) {
                        const u32 po = (272 * p + ((OFFS >> (4 * p)) & 15)) * 16;
                        const int h = p >> 1;
                        const u32x4 w = (p & 1) ? (u32x4){e[h], e[h + 1], e[h + 2], e[h + 3]} : (u32x4){d[h], d[h + 1], d[h + 2], d[h + 3]};
                        *(LAS u32x4*)(lds + cb + po) = w;
                    }
                }
            }
            if constexpr (R == 0) {
                if (hd == 0) {
                    u32 hh[4];
#pragma unroll
                    for (int i = 0; i < 4; ++i) { const u32 a = __builtin_amdgcn_readfirstlane(hr[0][i]), c = __builtin_amdgcn_readfirstlane(hr[2][i]); hh[i] = (lane & 16) ? c : a; }
                    if (lane < 32) {
                        const int p = lane & 7;
                        const u32 po = (272 * p + ((OFFS >> (4 * p)) & 15)) * 16;
                        const unsigned __int128 hv = ((unsigned __int128)(((u64)hh[3] << 32) | hh[2]) << 64) | (((u64)hh[1] << 32) | hh[0]);
                        const unsigned __int128 sv = (p == 0 || !(lane & 8)) ? (unsigned __int128)0 : (hv << (16 * (8 - p)));
                        const u32x4 w = {(u32)sv, (u32)(sv >> 32), (u32)(sv >> 64), (u32)(sv >> 96)};
                        *(LAS u32x4*)(lds + L_HC + (lane >> 4) * 34816 + po + ((lane >> 3) & 1) * 16) = w;
                    }
                }
            }
            if (hd == 1 && lane < 30) *(LAS u32x4*)(lds + L_UR + R * 4576 + 4096 + lane * 16) = (u32x4){0, 0, 0, 0};
            if (!f1ok) {
                const u32 fo = WS_F1 + (u32)(((lane & 8) ? (lane & 7) * 32 + grp : (b >> 5) * 32 + 31 - hd * 8 - (lane & 7)) * 128);
                flg = (u32x2){0u, 0u};
                spin_flags(flg, fo);
                ald4s(uu, WS_UT + (u32)(b * 8192 + t2 * 16));
            }
            if (f1ok) WAIT_VM(R ? 5 : 3); else WAIT_VM(0);
            pin(uu);
            *(LAS u32x4*)(lds + L_UR + R * 4576 + j * 16) = uu;
        }
        BAR();
        const int ch = hd >> 1, e0 = (hd & 1) * 4;
        const int p = (li + 1) & 7;
        u32 aA = L_HC + ch * 34816 + (272 * p + ((OFFS >> (4 * p)) & 15) + ((li + 1) >> 3) + lq + 4 * e0) * 16;
        u32 aB = L_UR + ch * 4576 + (254 - 224 - 2 * li + lq + 4 * e0) * 16;
        asm volatile("" : "+v"(aA), "+v"(aB));
        f32x4 y[4];
        u32 ooff = WS_O1 + (u32)(TI * 16384 + R * 8192 + lane * 16);
        {
            constexpr ConvSched S(R);
            s16x8 A[4][8], B[4][8];
            auto loads_at = [&](auto pc) {
                constexpr int p = decltype(pc)::value;
                sfor<0, 64>([&](auto ic) {
                    constexpr int i = decltype(ic)::value, e = i >> 4, f = i & 7;
                    if constexpr (S.slot[i] == p) {
                        if constexpr (i & 8) B[e][f] = ldsA(aB + 64 * e + 512 * (7 - f)); else A[e][f] = ldsA(aA + 64 * (8 * f + e));
                    }
                });
            };
            loads_at(IC<-1>{});
            SB();
            sfor<0, 72>([&](auto pc) {
                constexpr int p = decltype(pc)::value, e = p / 18, f = S.f[p % 18], i = S.i[p % 18], g = S.n[i] - f;
                y[i] = mfma32(A[e][f], B[e][g], (e == 0 && f == 0) ? zero4 : y[i]);
                loads_at(pc);
                if constexpr (p == 54) WAIT_VM(0);
                if constexpr (p >= 55 && p < 71 && (p & 1)) {
                    constexpr int i = (p - 55) >> 1;
                    ald4o<(i & 3) * 1024>(of[i], ooff + (i >> 2) * 4096, d_ws);
                }
                SB();
            });
        }
        RBAR();
        if (R == 0 && tid == 0) __builtin_amdgcn_raw_buffer_store_b64((u32x2){tl, th}, r_ws, WS_F2 + (grp * 8 + mem) * 128, 0, 16);
        {
            constexpr int N0[4] = {0, 3, 4, 7};
            u32 ra = L_HC + hd * 8192 + lane * 16;
            asm volatile("" : "+v"(ra));
#pragma unroll
            for (int i = 0; i < 4; ++i) *(LAS f32x4*)(lds + ra + (N0[i] ^ R) * 1024) = y[i];
        }
        SB();
        if constexpr (R == 0) {
#pragma unroll
            for (int n = 0; n < 4; ++n) { const u32x2 w = __builtin_amdgcn_raw_buffer_load_b64(r_x, (u32)((TI * 16 + li) * 1024 + mem * 128 + lq * 8) + n * 32, 0, 0); xw[2 * n] = w[0]; xw[2 * n + 1] = w[1]; }
        }
        wo_addr();
        SB();
        BAR();
        {
            const float D0 = bflo(dw), D1 = bfhi(dw);
            u32 ra = L_HC + wave * 1024 + lane * 16;
            const int tb = 256 * wave + 16 * li + 4 * lq;
            u32 ru = L_UR + (2044 - tb) * 2;
            asm volatile("" : "+v"(ra), "+v"(ru));
            const f32x4 y0 = *(LAS f32x4*)(lds + ra), y1 = *(LAS f32x4*)(lds + ra + 8192), y2 = *(LAS f32x4*)(lds + ra + 16384), y3 = *(LAS f32x4*)(lds + ra + 24576);
            const u32x2 ua = *(LAS u32x2*)(lds + ru), ub = *(LAS u32x2*)(lds + ru + 4576);
            load_F(0); load_F(1);
            SB();
            const f32x4 ya = y0 + y1, yb = y2 + y3;
            const u32x4 w = {nosent(pk2(ya[0] + bfhi(ua[1]) * D0, yb[0] + bfhi(ub[1]) * D1)), nosent(pk2(ya[1] + bflo(ua[1]) * D0, yb[1] + bflo(ub[1]) * D1)),
                             nosent(pk2(ya[2] + bfhi(ua[0]) * D0, yb[2] + bfhi(ub[0]) * D1)), nosent(pk2(ya[3] + bflo(ua[0]) * D0, yb[3] + bflo(ub[0]) * D1))};
            __builtin_amdgcn_raw_buffer_store_b128(w, r_ws, WS_Y2 + (u32)(b * 8192 + tb * 4), 0, 16);
        }
        SB();
        WAIT_VM(1);

        u32 f2oth = 0;
        _Pragma("unroll 1") for (int attempt = 0; attempt < 64; ++attempt) {
            sfor<0, 8>([&](auto ic) { pin(of[decltype(ic)::value]); });
            u32 m0 = 0, m1 = 0;
#pragma unroll
            for (int ks = 0; ks < 8; ++ks) {
                load_F((ks + 2) & 7);
                if (R == 1 && ks == 4 && wave == 7) {
                    const int pb = 32 * mem + (lane & 31);
                    f2oth = WS_F2 + (u32)((lane < 32 ? ((pb & 7) * 4 + (pb >> 6)) * 8 + ((pb >> 3) & 7) : max(grp * 8 - 8, 0) + (lane & 15)) * 128);
                    ald2s(flg, f2oth);
                }
                SB();
#pragma unroll
                for (int n = 0; n < 4; ++n) accM[n] = mfma32(F[ks][n], __builtin_bit_cast(s16x8, of[ks]), ks ? accM[n] : resid(n));
                m0 = max(max(m0, of[ks][0]), of[ks][1]); m1 = max(max(m1, of[ks][2]), of[ks][3]);
                SB();
            }
            if (__builtin_expect(__all(max(m0, m1) != SENT), 1)) break;
            WAIT_VM(0);
            sfor<0, 8>([&](auto ic) { constexpr int i = decltype(ic)::value; ald4o<(i & 3) * 1024>(of[i], ooff + (i >> 2) * 4096, d_ws); });
            WAIT_VM(0);
        }
        if constexpr (R == 1) { if (wave == 7) { WAIT_VM(0); pin(flg); spin_flags(flg, f2oth); } }
    }

    auto load_y = [&]() {
        const int l2 = opq(tid) & 63;
#pragma unroll
        for (int i = 0; i < 8; ++i)
            yw[i] = __builtin_amdgcn_raw_buffer_load_b32(r_ws, WS_Y2 + (u32)((32 * mem + 2 * (l2 >> 4)) * 8192 + (TI * 16 + (l2 & 15)) * 4), (8 * (i >> 1) + (i & 1)) * 8192, 16);
    };
    auto pub_off = [&](u32 ws_st) {
        const int lane = opq(tid) & 63;
        return (lane & 16) ? 0xFFFFFF00u : ws_st + (u32)((TI * 8 + mem) * 256 + (lane & 15) * 16 + (lane >> 5) * 8);
    };
    auto pub_stat = [&](u32 off, u32 tg, float s, float q) {
        auto r = __builtin_amdgcn_permlane32_swap(__float_as_uint(s), __float_as_uint(q), false, false);
        const float v = __uint_as_float(r[0]) + __uint_as_float(r[1]);
        auto t = __builtin_amdgcn_permlane16_swap(__float_as_uint(v), __float_as_uint(v), false, false);
        __builtin_amdgcn_raw_buffer_store_b64((u32x2){__float_as_uint(__uint_as_float(t[0]) + __uint_as_float(t[1])), tg}, r_ws, off, 0, 16);
    };
    auto stat_off = [&](u32 ws_st, int ti) { const int l2 = opq(tid) & 63; return ws_st + (u32)(ti * 2048 + (l2 >> 4) * 512 + (l2 & 15) * 16); };
    auto fin_stat = [&](u32x4& a, u32x4& c, u32 off, float& mean, float& rstd) {
        _Pragma("unroll 1") for (int spin = 0; spin < SPIN_CAP; ++spin) {
            if (__builtin_expect(__all(((a[1] ^ tl) | (a[3] ^ th) | (c[1] ^ tl) | (c[3] ^ th)) == 0), 1)) break;
            asm volatile("buffer_load_dwordx4 %0, %2, %3, 0 offen sc1\n\tbuffer_load_dwordx4 %1, %2, %3, 0 offen offset:256 sc1\n\ts_waitcnt vmcnt(0)"
                         : "=&v"(a), "=&v"(c) : "v"(off), "s"(d_ws) : "memory");
        }
        auto r = __builtin_amdgcn_permlane32_swap(__float_as_uint(__uint_as_float(a[0]) + __uint_as_float(c[0])), __float_as_uint(__uint_as_float(a[2]) + __uint_as_float(c[2])), false, false);
        const float v = __uint_as_float(r[0]) + __uint_as_float(r[1]);
        auto t = __builtin_amdgcn_permlane16_swap(__float_as_uint(v), __float_as_uint(v), false, false);
        const u32 v2 = __float_as_uint(__uint_as_float(t[0]) + __uint_as_float(t[1]));
        auto u = __builtin_amdgcn_permlane32_swap(v2, v2, false, false);
        const float ts = __uint_as_float(u[0]), tq = __uint_as_float(u[1]);
        mean = ts * (1.f / 512.f);
        rstd = __builtin_amdgcn_rsqf(fmaxf(tq * (1.f / 512.f) - mean * mean, 0.f) + 1e-5f);
    };
    auto regroup = [&](u32 (&w)[8]) {
#pragma unroll
        for (int j = 0; j < 2; ++j) {
            auto r0 = __builtin_amdgcn_permlane16_swap(w[4 * j], w[4 * j + 2], false, false);
            auto r1 = __builtin_amdgcn_permlane16_swap(w[4 * j + 1], w[4 * j + 3], false, false);
            w[4 * j] = r0[0]; w[4 * j + 1] = r1[0]; w[4 * j + 2] = r0[1]; w[4 * j + 3] = r1[1];
        }
    };
    auto wo_finish = [&](auto lc) {
        constexpr int LAYER = decltype(lc)::value;
        const int lane = opq(tid) & 63, li = lane & 15, lq = lane >> 4;
        u32 wx = L_WX + hd * 4096 + lane * 16;
        pin(wx);
        u32 po = 0, so = 0, tg = 0, oa = 0;
        if constexpr (R == 1) {
#pragma unroll
            for (int n = 0; n < 4; ++n) *(LAS f32x4*)(lds + wx + n * 1024) = accM[n];
        } else {
            po = pub_off(LAYER ? WS_ST3 : WS_ST1); so = stat_off(LAYER ? WS_ST3 : WS_ST1, TI); tg = lane < 32 ? tl : th;
            const int c = 2 * (lq & 1) + (lq >> 1);
            oa = LAYER ? (u32)((TI * 16 + li) * 1024 + mem * 128 + c * 16) : WS_X2 + (u32)((TI * 8 + mem) * 2048 + li * 128 + ((c ^ (li >> 1)) << 4));
            pin(po); pin(so); pin(tg); pin(oa);
        }
        BAR();
        if constexpr (R == 0) {
            f32x4 t[4];
#pragma unroll
            for (int n = 0; n < 4; ++n) t[n] = *(LAS f32x4*)(lds + wx + n * 1024);
            SB();
            float s = 0.f, q = 0.f, s2 = 0.f, q2 = 0.f;
#pragma unroll
            for (int n = 0; n < 4; ++n) {
                const f32x4 v = accM[n] + t[n];
                const u32 w0 = pk2(v[0], v[1]), w1 = pk2(v[2], v[3]);
                xw[2 * n] = w0; xw[2 * n + 1] = w1;
                s = dot2(w0, 0x3F803F80u, s); q = dot2(w0, w0, q); s2 = dot2(w1, 0x3F803F80u, s2); q2 = dot2(w1, w1, q2);
            }
            pub_stat(po, tg, s + s2, q + q2);
            CFENCE();
            if constexpr (!LAYER) load_y();
            regroup(xw);
            SB();
            WAIT_VM(0);
            if constexpr (!LAYER) {
#pragma unroll
                for (int i = 0; i < 8; ++i) pin(yw[i]);
            }
            u32x4 a, c;
            ald4s(a, so); ald4s(c, so + 256);
            if constexpr (!LAYER) {
                _Pragma("unroll 1") for (int spin = 0; spin < SPIN_CAP; ++spin) {
                    if (__builtin_expect(__all(max(max(max(yw[0], yw[1]), max(yw[2], yw[3])), max(max(yw[4], yw[5]), max(yw[6], yw[7]))) != SENT), 1)) break;
                    CFENCE();
                    load_y();
                }
                regroup(yw);
            }
            f32x2 xa[8], ya[8], xy[8];
#pragma unroll
            for (int i = 0; i < 8; ++i) {
                xa[i] = (f32x2){bflo(xw[i]), bfhi(xw[i])}; ya[i] = (f32x2){bflo(yw[i]), bfhi(yw[i])}; xy[i] = xa[i] * ya[i];
                pin(xa[i]); pin(ya[i]); pin(xy[i]);
            }
            WAIT_VM(0);
            pin(a); pin(c);
            float mean, rstd;
            fin_stat(a, c, so, mean, rstd);
            const float nm = -mean * rstd;
#pragma unroll
            for (int i = 0; i < 8; ++i) {
                const f32x2 o = xy[i] * rstd + (ya[i] * nm + xa[i]);
                xw[i] = pk2(o[0], o[1]);
                if (!LAYER) xw[i] = nosent(xw[i]);
            }
#pragma unroll
            for (int j = 0; j < 2; ++j) {
                const u32x4 o = {xw[4 * j], xw[4 * j + 1], xw[4 * j + 2], xw[4 * j + 3]};
                if constexpr (LAYER) __builtin_amdgcn_raw_buffer_store_b128(o, r_out, oa + j * 64, 0, 0);
                else __builtin_amdgcn_raw_buffer_store_b128(o, r_ws, oa ^ (j * 64), 0, 16);
            }
            if constexpr (!LAYER) {
                float s = 0.f, q = 0.f, s2 = 0.f, q2 = 0.f;
#pragma unroll
                for (int i = 0; i < 8; i += 2) { s = dot2(xw[i], 0x3F803F80u, s); q = dot2(xw[i], xw[i], q); s2 = dot2(xw[i + 1], 0x3F803F80u, s2); q2 = dot2(xw[i + 1], xw[i + 1], q2); }
                pub_stat(po + (WS_ST2 - WS_ST1) * !(lane & 16), tg, s + s2, q + q2);
                CFENCE();
                regroup(xw);
#pragma unroll
                for (int i = 0; i < 8; ++i) { pin(xw[i]); pin(yw[i]); }
                WAIT_VM(0);
            }
        }
    };
    wo_finish(IC<0>{});
    if constexpr (R == 1) {
        if (b == 0 && wave == 7) {
            const u32 fo = WS_F1 + (u32)((opq(tid) & 63) * 128);
            u32x2 ef[4];
#pragma unroll
            for (int i = 0; i < 4; ++i) ald2s(ef[i], fo + i * 8192);
            WAIT_VM(0);
#pragma unroll
            for (int i = 0; i < 4; ++i) { pin(ef[i]); spin_flags(ef[i], fo + i * 8192); }
            if (fo == WS_F1) __builtin_amdgcn_raw_buffer_store_b64(__builtin_bit_cast(u32x2, epoch + 1), r_ws, WS_EPOCH, 0, 16);
            WAIT_VM(0);
        }
    }
    RBAR();

    {
        const int l3 = opq(tid) & 63;
        const u32 g0 = (u32)((4 * grp - 1 + (hd >> 1)) * 16384 + (hd & 1) * 1024 + l3 * 16);
        const u32 g2 = (u32)((4 * grp + 3) * 16384 + hd * 512 + l3 * 16);
        auto x2_piece = [&](auto ic) {
            constexpr int i = decltype(ic)::value, s = i / 3, p = i % 3;
            const u32 kp = (u32)((s + rot) & 7);
            if constexpr (p == 0) dma(IC<4>{}, IC<L_CX + s * 10240>{}, d_x2, hb1k, g0, kp * 2048);
            else if constexpr (p == 1) dma(IC<4>{}, IC<L_CX + s * 10240 + 4096>{}, d_x2, hb1k, g0 + 32768, kp * 2048);
            else dma(IC<5>{}, IC<L_CX + s * 10240 + 8192>{}, d_x2, hb512, g2, kp * 2048);
        };
        u32 va = L_CX + hd * 1024 + l3 * 16, vq = L_CX + 8192 + hd * 512 + (l3 & 31) * 16 + (l3 >> 5) * 10240;
        pin(va); pin(vq);
        u32x4 vr[5];
        auto ver_read = [&](auto cc) {
            constexpr int c = decltype(cc)::value;
            u32 a = va + c * 20480, q = vq + c * 20480;
            pin(a); pin(q); CFENCE();
            vr[0] = *(LAS u32x4*)(lds + a); vr[1] = *(LAS u32x4*)(lds + a + 4096); vr[2] = *(LAS u32x4*)(lds + a + 10240); vr[3] = *(LAS u32x4*)(lds + a + 14336);
            vr[4] = *(LAS u32x4*)(lds + q);
        };
        auto ver_check = [&](auto cc) {
            constexpr int c = decltype(cc)::value;
            _Pragma("unroll 1") for (int spin = 0; spin < SPIN_CAP; ++spin) {
                u32 m0 = 0, m1 = 0;
#pragma unroll
                for (int i = 0; i < 5; ++i) { m0 = max(max(m0, vr[i][0]), vr[i][1]); m1 = max(max(m1, vr[i][2]), vr[i][3]); }
                if (__builtin_expect(__all(max(m0, m1) != SENT), 1)) break;
                sfor<6 * c, 6 * c + 6>([&](auto ic) { x2_piece(ic); });
                WAIT_VM(0);
                ver_read(cc);
            }
        };
        const u32 xb0 = (u32)((l3 & 15) * 128 + (((l3 >> 4) ^ ((l3 & 15) >> 1)) << 4));
        if constexpr (R == 1) {
            sfor<0, 12>([&](auto ic) { x2_piece(ic); });
            WAIT_VM(6);
            ver_read(IC<0>{});
            ver_check(IC<0>{});
        } else {
            u32x4 sa, sc, ha, hc;
            const u32 so = stat_off(WS_ST2, 4 * grp + hd);
            const bool halo = hd == 0 && grp > 0;
            ald4s(sa, so); ald4s(sc, so + 256);
            if (halo) { ald4s(ha, so - 2048); ald4s(hc, so - 2048 + 256); }
            WAIT_VM(0);
            pin(sa); pin(sc);
            float mean, rstd;
            fin_stat(sa, sc, so, mean, rstd);
            if (l3 < 16) { *(LAS float*)(lds + L_TAB + 64 + hd * 64 + l3 * 4) = mean; *(LAS float*)(lds + L_TAB + 320 + 64 + hd * 64 + l3 * 4) = rstd; }
            if (halo) {
                pin(ha); pin(hc);
                fin_stat(ha, hc, so - 2048, mean, rstd);
                if (l3 < 16) { *(LAS float*)(lds + L_TAB + l3 * 4) = mean; *(LAS float*)(lds + L_TAB + 320 + l3 * 4) = rstd; }
            }
        }
        BAR();
        u32 xa;
        auto pf_addr = [&](int st) { xa = L_CX + (st >> 1) * 10240 + (xb0 ^ ((st & 1) * 64)); pin(xa); };
        pf_addr(0);
#pragma unroll
        for (int mt = 0; mt < 5; ++mt) Ab[0][mt] = ldsA(xa + mt * 2048);
        sfor<0, 16>([&](auto stc) {
            constexpr int st = decltype(stc)::value;
            if constexpr (R == 1) {
                if constexpr (st == 2) { WAIT_VM(10); ver_read(IC<1>{}); }
                if constexpr (st == 3) ver_check(IC<1>{});
                if constexpr (st == 5) { WAIT_VM(6); ver_read(IC<2>{}); }
                if constexpr (st == 6) { ver_check(IC<2>{}); WAIT_VM(0); ver_read(IC<3>{}); }
                if constexpr (st == 7) ver_check(IC<3>{});
            }
            if constexpr (st == 3) { RBAR(); }
            if constexpr (st == 7) { RBAR(); }
            if (st < 15) pf_addr(st + 1);
            if constexpr (st == 14) load_tab(true);
            SB();
            qkv_step(IC<1>{}, st, Ab[st & 1], [&](auto mc) {
                constexpr int mt = decltype(mc)::value;
                if (st < 15) Ab[(st + 1) & 1][mt] = ldsA(xa + mt * 2048);
                if constexpr (R == 1 && 12 + 5 * st + mt < 24) x2_piece(IC<12 + 5 * st + mt>{});
                SB();
            });
        });
    }
    attention(IC<1>{});

    {
        wo_addr();
#pragma unroll
        for (int ks = 0; ks < 8; ++ks) load_F(ks);
        SB();
        WAIT_VM(0);
        RBAR();
        load_o(WS_O2);
        SB();
        _Pragma("unroll 1") for (int spin = 0; spin < 64; ++spin) {
            u32 m0 = 0, m1 = 0;
#pragma unroll
            for (int ks = 0; ks < 8; ++ks) {
#pragma unroll
                for (int n = 0; n < 4; ++n) accM[n] = mfma32(F[ks][n], __builtin_bit_cast(s16x8, of[ks]), ks ? accM[n] : resid(n));
                m0 = max(max(m0, of[ks][0]), of[ks][1]); m1 = max(max(m1, of[ks][2]), of[ks][3]);
                SB();
            }
            if (__builtin_expect(__all(max(m0, m1) != SENT), 1)) break;
            _Pragma("unroll 1") for (int sp2 = 0; sp2 < SPIN_CAP; ++sp2) {
                CFENCE();
                const u32 pv = __builtin_amdgcn_raw_buffer_load_b32(r_ws, WS_O2 + (u32)(TI * 16384 + R * 8192 + (opq(tid) & 15) * 512 + 508), 0, 16);
                if (__all(pv != SENT)) break;
            }
            CFENCE();
            load_o(WS_O2);
        }
        SB();
    }
    wo_finish(IC<1>{});
}

__global__ void __launch_bounds__(512, 2)
fused_kernel(const u16* __restrict__ gx, const u16* __restrict__ gwqkv, const u16* __restrict__ gwo,
             const u16* __restrict__ gbk, const u16* __restrict__ gbv, const u16* __restrict__ gsk,
             const u16* __restrict__ ghk, const u16* __restrict__ gdv, u16* __restrict__ gout, char* __restrict__ ws) {
    __shared__ __attribute__((aligned(16))) char lds_[L_TOTAL];
    typedef u32 u32x16 __attribute__((ext_vector_type(16)));
    u32x16 ka; u32x4 kb;
    {
        auto kp = __builtin_amdgcn_kernarg_segment_ptr();
        asm volatile("s_load_dwordx16 %0, %2, 0x0\n\ts_load_dwordx4 %1, %2, 0x40\n\ts_waitcnt lgkmcnt(0)" : "=&s"(ka), "=&s"(kb) : "s"(kp) : "memory");
    }
    auto mkp = [](u32 lo, u32 hi) { return (char*)(((u64)hi << 32) | lo); };
    const Args a = {(const u16*)mkp(ka[0], ka[1]), (const u16*)mkp(ka[2], ka[3]), (const u16*)mkp(ka[4], ka[5]), (const u16*)mkp(ka[6], ka[7]), (const u16*)mkp(ka[8], ka[9]),
                    (const u16*)mkp(ka[10], ka[11]), (const u16*)mkp(ka[12], ka[13]), (const u16*)mkp(ka[14], ka[15]), (u16*)mkp(kb[0], kb[1]), mkp(kb[2], kb[3])};
    const int wave = __builtin_amdgcn_readfirstlane(threadIdx.x >> 6);
    if (wave < 4) body<0>(a, (LAS char*)lds_, wave); else body<1>(a, (LAS char*)lds_, wave);
}

extern "C" void kernel_launch(void* const* d_in, const int* in_sizes, int n_in,
                              void* d_out, int out_size, void* d_ws, size_t ws_size,
                              hipStream_t stream) {
    fused_kernel<<<256, 512, 0, stream>>>((const u16*)d_in[0], (const u16*)d_in[1], (const u16*)d_in[2],
                                          (const u16*)d_in[4], (const u16*)d_in[5], (const u16*)d_in[6],
                                          (const u16*)d_in[7], (const u16*)d_in[8], (u16*)d_out, (char*)d_ws);
}
```

```cpp
#include <hip/hip_runtime.h>
#include <stdint.h>

typedef unsigned short u16;
typedef unsigned u32;
typedef unsigned long long u64;
typedef short s16x4 __attribute__((ext_vector_type(4)));
typedef short s16x8 __attribute__((ext_vector_type(8)));
typedef __bf16 bf16x2 __attribute__((ext_vector_type(2)));
typedef float f32x2 __attribute__((ext_vector_type(2)));
typedef float f32x4 __attribute__((ext_vector_type(4)));
typedef u32 u32x2 __attribute__((ext_vector_type(2)));
typedef u32 u32x4 __attribute__((ext_vector_type(4)));
#define LAS __attribute__((address_space(3)))
#define DEV __device__ __forceinline__
typedef __amdgpu_buffer_rsrc_t rsrc_t;

constexpr u32 WS_EPOCH = 0;
constexpr u32 WS_F1 = 32768;
constexpr u32 WS_F2 = 65536;
constexpr u32 WS_ST0 = 98304;
constexpr u32 WS_ST1 = 131072;
constexpr u32 WS_ST2 = WS_ST1 + 262144;
constexpr u32 WS_ST3 = WS_ST2 + 262144;
constexpr u32 WS_UT = 1u << 20;
constexpr u32 WS_O1 = WS_UT + (2u << 20);
constexpr u32 WS_O2 = WS_O1 + (2u << 20);
constexpr u32 WS_Y2 = WS_O2 + (2u << 20);
constexpr u32 WS_X2 = WS_Y2 + (2u << 20);
constexpr u32 WS_END = WS_X2 + (2u << 20);

constexpr u32 L_SLOT = 34816;
constexpr u32 L_KV = 4 * L_SLOT;
constexpr u32 L_K = L_KV, L_V = L_KV + 9216;
constexpr u32 L_SK = L_KV + 17408;
constexpr u32 L_WO = 0;
constexpr u32 L_CX = 65536;
constexpr u32 L_HC = L_CX;
constexpr u32 L_UR = L_CX + 69632;
constexpr u32 L_WX = L_CX + 40960;
constexpr u32 L_EX = 147456;
constexpr u32 L_TAB = L_EX + 12288;
constexpr u32 L_CS = L_TAB + 1024;
constexpr u32 L_FLG = L_TAB + 2048;
constexpr u32 L_TOTAL = 160 * 1024;

constexpr int SPIN_CAP = 1 << 14;
constexpr u32 SENT = 0xFFFFFFFFu;

#define WAIT_VM(n) asm volatile("s_waitcnt vmcnt(%0)" ::"i"(n) : "memory")
#define BAR() do { asm volatile("s_waitcnt lgkmcnt(0)" ::: "memory"); __builtin_amdgcn_s_barrier(); asm volatile("" ::: "memory"); } while (0)
#define RBAR() do { asm volatile("" ::: "memory"); __builtin_amdgcn_s_barrier(); asm volatile("" ::: "memory"); } while (0)
#define CFENCE() asm volatile("" ::: "memory")
#define SB() __builtin_amdgcn_sched_barrier(0)
template <int N> struct IC { static constexpr int value = N; };
template <int I, int N, class F> DEV void sfor(F&& f) { if constexpr (I < N) { f(IC<I>{}); sfor<I + 1, N>(f); } }

DEV f32x4 mfma32(s16x8 a, s16x8 b, f32x4 c) { return __builtin_amdgcn_mfma_f32_16x16x32_bf16(a, b, c, 0, 0, 0); }
DEV f32x4 mfma16(s16x4 a, s16x4 b, f32x4 c) { return __builtin_amdgcn_mfma_f32_16x16x16bf16_1k(a, b, c, 0, 0, 0); }
DEV float vmax(float a, float b) { float r; asm("v_max_f32 %0, %1, %2" : "=v"(r) : "v"(a), "v"(b)); return r; }
DEV u32 pk2(float lo, float hi) { f32x2 f = {lo, hi}; bf16x2 b = __builtin_convertvector(f, bf16x2); return __builtin_bit_cast(u32, b); }
DEV float bflo(u32 w) { return __uint_as_float(w << 16); }
DEV float bfhi(u32 w) { return __uint_as_float(w & 0xffff0000u); }
DEV float dot2(u32 a, u32 b, float c) { return __builtin_amdgcn_fdot2_f32_bf16(__builtin_bit_cast(bf16x2, a), __builtin_bit_cast(bf16x2, b), c, false); }
DEV u32 nosent(u32 w) { return min(w, 0xFFFFFFFEu); }
DEV float rowsum16(float v) {
    v += __builtin_bit_cast(float, __builtin_amdgcn_update_dpp(0, __builtin_bit_cast(int, v), 0xB1, 0xf, 0xf, true));
    v += __builtin_bit_cast(float, __builtin_amdgcn_update_dpp(0, __builtin_bit_cast(int, v), 0x4E, 0xf, 0xf, true));
    v += __builtin_bit_cast(float, __builtin_amdgcn_update_dpp(0, __builtin_bit_cast(int, v), 0x141, 0xf, 0xf, true));
    v += __builtin_bit_cast(float, __builtin_amdgcn_update_dpp(0, __builtin_bit_cast(int, v), 0x140, 0xf, 0xf, true));
    return v;
}
DEV float bcast_row0(float v) { return __builtin_bit_cast(float, __builtin_amdgcn_update_dpp(0, __builtin_bit_cast(int, v), 0x150, 0xf, 0xf, true)); }
template <int OFF> DEV void ald4o(u32x4& d, u32 voff, u32x4 rs) { asm volatile("buffer_load_dwordx4 %0, %1, %2, 0 offen offset:%3 sc1" : "=v"(d) : "v"(voff), "s"(rs), "i"(OFF) : "memory"); }
template <class T> DEV void pin(T& v) { asm volatile("" : "+v"(v)); }
DEV int opq(int v) { asm volatile("" : "+v"(v)); return v; }
DEV s16x8 cat8(s16x4 a, s16x4 b) { return __builtin_shufflevector(a, b, 0, 1, 2, 3, 4, 5, 6, 7); }
DEV s16x4 pack4(f32x4 v) { u32x2 w = {pk2(v[0], v[1]), pk2(v[2], v[3])}; return __builtin_bit_cast(s16x4, w); }
DEV u64 mix64(u64 z) { z += 0x9E3779B97F4A7C15ull; z = (z ^ (z >> 30)) * 0xBF58476D1CE4E5B9ull; z = (z ^ (z >> 27)) * 0x94D049BB133111EBull; return z ^ (z >> 31); }

struct ConvSched {
    int n[4], f[18], i[18], slot[64];
    constexpr ConvSched(int R) : n{}, f{}, i{}, slot{} {
        constexpr int LEAD = 9;
        const int N0[4] = {0, 3, 4, 7};
        for (int k = 0; k < 4; ++k) n[k] = N0[k] ^ R;
        int c = 0;
        for (int ff = 0; ff < 8; ++ff) for (int k = 3; k >= 0; --k) if (ff <= n[k]) { f[c] = ff; i[c] = k; ++c; }
        for (int k = 0; k < 64; ++k) slot[k] = 1000;
        for (int p = 71; p >= 0; --p) {
            const int e = p / 18, ff = f[p % 18], g = n[i[p % 18]] - ff, s = p - LEAD < -1 ? -1 : p - LEAD;
            slot[e * 16 + ff] = s; slot[e * 16 + 8 + g] = s;
        }
    }
};

struct Args { const u16 *gx, *gwqkv, *gwo, *gbk, *gbv, *gsk, *ghk, *gdv; u16* gout; char* ws; };

template <int R>
DEV void body(const Args& A_, LAS char* const lds, const int wave) {
    const u16* gx = A_.gx; char* ws = A_.ws;
    const int b = blockIdx.x, tid = threadIdx.x, lane = tid & 63;
    const int hd = wave & 3;
    const int grp = (b & 7) * 4 + (b >> 6), mem = (b >> 3) & 7;
    const int T0 = grp * 64;
    const int rot = (mem + 2 * (grp & 3)) & 7;

    u64 epoch;
    asm volatile("s_load_dwordx2 %0, %1, 0x0" : "=s"(epoch) : "s"(ws));
    u32 dw;
    asm volatile("s_load_dword %0, %1, 0x0" : "=s"(dw) : "s"(A_.gdv + 2 * b));
    const rsrc_t r_x = __builtin_amdgcn_make_buffer_rsrc((void*)gx, 0, 2048 * 512 * 2, 0x00020000);
    const rsrc_t r_ws = __builtin_amdgcn_make_buffer_rsrc((void*)ws, 0, WS_END, 0x00020000);
    const rsrc_t r_out = __builtin_amdgcn_make_buffer_rsrc((void*)A_.gout, 0, 2048 * 512 * 2, 0x00020000);
    const u32 ldsbase = (u32)(size_t)lds;
    auto mkdesc = [](const void* p, u32 bytes) { const u64 a = (u64)p; return (u32x4){(u32)a, (u32)(a >> 32) & 0xffffu, bytes, 0x00020000u}; };
    const u32x4 d_x = mkdesc(gx, 2048 * 512 * 2), d_w = mkdesc(A_.gwqkv, 512 * 1536 * 2), d_wo = mkdesc(A_.gwo, 512 * 512 * 2);
    const u32x4 d_k = mkdesc(A_.gbk, 2048 * 512 * 2), d_v = mkdesc(A_.gbv, 2048 * 512 * 2), d_sk = mkdesc(A_.gsk, 512 * 4 * 2);
    const u32x4 d_h = mkdesc(A_.ghk, 512 * 2048 * 2), d_x2 = mkdesc(ws + WS_X2, 2u << 20), d_ws = mkdesc(ws, WS_END);
    const u32 hb1k = ldsbase + hd * 1024, hb512 = ldsbase + hd * 512;
#define DMA_ASM(pre, mods, post) asm volatile("s_add_u32 m0, %0, %4\n\t" pre "buffer_load_dwordx4 %1, %2, %3 offen " mods "lds" post \
                     :: "s"(sbase), "v"(voff), "s"(rs), "s"(soff), "i"(IMM) : "memory", "scc")
    const u32 xmlo = hd == 0 ? ~0xFFu : ~0u;
    auto dma = [&](auto kind, auto imm, u32x4 rs, u32 sbase, u32 voff, u32 soff) {
        constexpr int K = decltype(kind)::value; constexpr int IMM = decltype(imm)::value;
        if constexpr (K == 0) DMA_ASM("s_nop 0\n\t", "", "");
        else if constexpr (K == 1) DMA_ASM("s_mov_b32 exec_hi, 0\n\t", "", "\n\ts_mov_b32 exec_hi, -1");
        else if constexpr (K == 2) {
            const u32 ml = xmlo;
            asm volatile("s_add_u32 m0, %0, %4\n\ts_mov_b32 exec_lo, %5\n\tbuffer_load_dwordx4 %1, %2, %3 offen lds\n\ts_mov_b32 exec_lo, -1"
                         :: "s"(sbase), "v"(voff), "s"(rs), "s"(soff), "i"(IMM), "s"(ml) : "memory", "scc");
        }
        else if constexpr (K == 4) DMA_ASM("s_nop 0\n\t", "sc1 ", "");
        else DMA_ASM("s_mov_b32 exec_hi, 0\n\t", "sc1 ", "\n\ts_mov_b32 exec_hi, -1");
    };
    auto ald4 = [&](u32x4& d, u32x4 rs, u32 voff) { asm volatile("buffer_load_dwordx4 %0, %1, %2, 0 offen" : "=v"(d) : "v"(voff), "s"(rs) : "memory"); };
    auto ald4s = [&](u32x4& d, u32 voff) { asm volatile("buffer_load_dwordx4 %0, %1, %2, 0 offen sc1" : "=v"(d) : "v"(voff), "s"(d_ws) : "memory"); };
    auto ald2s = [&](u32x2& d, u32 voff) { asm volatile("buffer_load_dwordx2 %0, %1, %2, 0 offen sc1" : "=v"(d) : "v"(voff), "s"(d_ws) : "memory"); };

    const int pr = lane >> 3, pc = lane & 7;
    const int xr0 = 8 * hd + pr, xr2 = 64 + 4 * hd + pr;
    const u32 xv0 = (u32)((T0 - 16 + xr0) * 1024 + ((pc ^ ((xr0 >> 1) & 7)) << 4));
    const u32 xv2 = (u32)((T0 - 16 + xr2) * 1024 + ((pc ^ ((xr2 >> 1) & 7)) << 4));
    const int wfw = ((xr0 >> 1) & 1) | (((xr0 >> 3) & 1) << 1);
    const u32 wv0 = (u32)(xr0 * 3072 + mem * 128 + ((((pc >> 1) ^ wfw) * 2 + (pc & 1)) << 4));
    auto issue_piece = [&](auto sc, auto ic) {
        constexpr int s = decltype(sc)::value, i = decltype(ic)::value;
        const u32 kp = (u32)((s + rot) & 7);
        constexpr int sb = (s & 3) * L_SLOT;
        if constexpr (i == 0) dma(IC<2>{}, IC<sb>{}, d_x, hb1k, xv0, kp * 128);
        else if constexpr (i == 1) dma(IC<0>{}, IC<sb + 4096>{}, d_x, hb1k, xv0 + 32768, kp * 128);
        else if constexpr (i == 2) dma(IC<1>{}, IC<sb + 8192>{}, d_x, hb512, xv2, kp * 128);
        else dma(IC<0>{}, IC<sb + 10240 + (i - 3) * 4096>{}, d_w, hb1k, wv0, kp * (64 * 3072) + ((i - 3) >> 1) * 1024 + ((i - 3) & 1) * (32 * 3072));
    };
    u32 wov = 0;
    auto issue_wo = [&](auto ic) {
        constexpr int i = decltype(ic)::value;
        if constexpr (i == 0) {
            const int l2 = opq(tid) & 63, pr = l2 >> 3, pc = l2 & 7;
            wov = (u32)((8 * hd + pr) * 1024 + mem * 128 + ((((pc >> 1) ^ (((pr >> 1) & 1) | ((hd & 1) << 1))) * 2 + (pc & 1)) << 4));
            pin(wov);
        }
        const u32 j = (u32)((i + 2 * grp) & 7);
        dma(IC<0>{}, IC<L_WO + (i & 8) * 4096>{}, d_wo, hb1k + j * 4096, wov, ((i & 8) + j) * 32768);
    };
    auto issue_kv = [&](int i) {
        const int l2 = opq(tid) & 63, pr = l2 >> 3, pc = l2 & 7, p = hd + 4 * (i & 1);
        const u32 cb = (u32)(mem * 128);
        if (i < 2) dma(IC<0>{}, IC<L_K>{}, d_k, hb1k + (i & 1) * 4096, (u32)((T0 - 8 + 8 * p + pr) * 1024) + cb + ((pc ^ p) << 4), 0);
        else if (i < 4) dma(IC<0>{}, IC<L_V>{}, d_v, hb1k + (i & 1) * 4096, (u32)((T0 + 8 * p + pr) * 1024) + cb + ((pc ^ p) << 4), 0);
        else if (hd == 0) dma(IC<0>{}, IC<L_K + 8192>{}, d_k, ldsbase, (u32)((T0 - 8 + 64 + pr) * 1024) + cb + (pc << 4), 0);
        else dma(IC<0>{}, IC<L_SK>{}, d_sk, ldsbase, (u32)(mem * 512 + l2 * 16), 0);
    };

    const int srow = T0 + 8 * mem + 2 * hd;
    u32x4 xrow[2];
    sfor<0, 9>([&](auto ic) { if constexpr ((decltype(ic)::value & 1) != R) issue_piece(IC<0>{}, ic); });
    sfor<0, 9>([&](auto ic) { if constexpr ((decltype(ic)::value & 1) != R) issue_piece(IC<1>{}, ic); });
    asm volatile("s_waitcnt lgkmcnt(0)" : "+s"(epoch), "+s"(dw));
    const u64 tag = mix64(epoch);
    const u32 tl = (u32)tag, th = (u32)(tag >> 32);
    if (R == 1 && wave == 7 && lane < 32) *(LAS u32x4*)(lds + (lane >> 3) * L_SLOT + (lane & 7) * 16) = (u32x4){0x3F803F80u, 0x3F803F80u, 0x3F803F80u, 0x3F803F80u};

    auto trr = [&](u32 off) { return __builtin_amdgcn_ds_read_tr16_b64_v4i16((LAS s16x4*)(lds + off)); };
    auto ldsA = [&](u32 off) { return *(LAS s16x8*)(lds + off); };
    auto flag_ok = [&](const u32x2& f) { return __all(f[0] == tl && f[1] == th); };
    auto spin_flags = [&](u32x2& f, u32 off) {
        _Pragma("unroll 1") for (int spin = 0; spin < SPIN_CAP; ++spin) {
            if (__builtin_expect(flag_ok(f), 1)) break;
            __builtin_amdgcn_s_sleep(2);
            asm volatile("buffer_load_dwordx2 %0, %1, %2, 0 offen sc1\n\ts_waitcnt vmcnt(0)" : "=&v"(f) : "v"(off), "s"(d_ws) : "memory");
        }
    };

    s16x8 Wq[16], Wm[16];
    f32x4 accM[5], accQ[5];
    const f32x4 zero4 = {0.f, 0.f, 0.f, 0.f};
    const s16x8 ones8 = {0x3F80, 0x3F80, 0x3F80, 0x3F80, 0x3F80, 0x3F80, 0x3F80, 0x3F80};
    s16x8 Ab[3][5];
    auto qkv_step = [&](auto lc, int st, const s16x8 (&A)[5], auto fill) {
        sfor<0, 5>([&](auto mc) {
            constexpr int mt = decltype(mc)::value;
            const f32x4 cm = st ? accM[mt] : zero4;
            accM[mt] = R ? mfma32(A[mt], Wm[st], cm) : mfma32(Wm[st], A[mt], cm);
            if constexpr (R == 0 && (mt > 0 || decltype(lc)::value == 0)) accQ[mt] = mfma32(Wq[st], A[mt], st ? accQ[mt] : zero4);
            fill(mc);
        });
    };

    float m1[5], r1[5]; f32x4 m4[5], r4[5], csK, csQ;
    auto load_tab = [&](bool cs) {
        const int l2 = opq(tid) & 63;
        u32 ta = L_TAB + (R ? (l2 >> 4) * 16 : (l2 & 15) * 4);
        pin(ta);
        if (R == 0 && cs) { csK = *(LAS f32x4*)(lds + L_CS + hd * 128 + (l2 >> 4) * 16); csQ = *(LAS f32x4*)(lds + L_CS + hd * 128 + 64 + (l2 >> 4) * 16); }
#pragma unroll
        for (int mt = 0; mt < 5; ++mt) {
            if (R == 0) { m1[mt] = *(LAS float*)(lds + ta + mt * 64); r1[mt] = *(LAS float*)(lds + ta + 320 + mt * 64); }
            else { m4[mt] = *(LAS f32x4*)(lds + ta + mt * 64); r4[mt] = *(LAS f32x4*)(lds + ta + 320 + mt * 64); }
        }
    };

    u32x4 hr[4];
    u32x2 flg;
    {
        const int li = lane & 15, lq = lane >> 4, tq = li >> 2, tp = lane & 3;
        const u32 xa0 = (u32)(li * 128 + ((lq ^ (li >> 1)) << 4));
        const u32 wa = (u32)((8 * lq + tq) * 128 + ((hd ^ (((tq >> 1) & 1) | ((lq & 1) << 1))) << 5) + tp * 8);
        u32 xa, wb, kw[11], vw[8];
        u32x4 s0v, skw;
        auto pf_addr = [&](int st) {
            const u32 sb = (u32)((st >> 1) & 3) * L_SLOT;
            xa = sb + (xa0 ^ ((st & 1) * 64)); wb = sb + 10240 + (st & 1) * 4096 + wa;
            asm volatile("" : "+v"(xa), "+v"(wb));
        };
        auto pf_part = [&](int st, int mt) {
            if (mt == 0) Wm[st] = cat8(trr(wb + (1 + R) * 8192), trr(wb + (1 + R) * 8192 + 512));
            Ab[st & 1][mt] = ldsA(xa + mt * 2048);
            if (R == 0 && mt == 1) Wq[st] = cat8(trr(wb), trr(wb + 512));
        };
        WAIT_VM(R ? 5 : 4);
        BAR();
        pf_addr(0);
#pragma unroll
        for (int mt = 0; mt < 5; ++mt) pf_part(0, mt);
        sfor<0, 16>([&](auto stc) {
            constexpr int st = decltype(stc)::value;
            if constexpr ((st & 1) && st < 15) {
                constexpr int s1 = (st >> 1) + 1;
                if constexpr (R == 0) {
                    if constexpr (s1 == 1) WAIT_VM(7);
                    if constexpr (s1 == 2) WAIT_VM(1);
                    if constexpr (s1 == 5) WAIT_VM(0);
                } else {
                    constexpr int WN[8] = {0, 9, 9, 9, 9, 9, 10, 10};
                    WAIT_VM(WN[s1]);
                    if constexpr (s1 == 7) {
                        pin(s0v);
                        const int l2 = opq(tid) & 63, tr = hd * 20 + min(l2, 19), row = T0 - 16 + tr;
                        _Pragma("unroll 1") for (int spin = 0; spin < SPIN_CAP; ++spin) {
                            if (__builtin_expect(__all(row < 0 || (s0v[1] == tl && s0v[3] == th)), 1)) break;
                            __builtin_amdgcn_s_sleep(2);
                            ald4s(s0v, WS_ST0 + (u32)(max(row, 0) * 16));
                            WAIT_VM(0);
                            pin(s0v);
                        }
                        *(LAS u32*)(lds + L_TAB + tr * 4) = row < 0 ? 0u : s0v[0];
                        *(LAS u32*)(lds + L_TAB + 320 + tr * 4) = row < 0 ? 0x3F800000u : s0v[2];
                        asm volatile("s_waitcnt lgkmcnt(0)" ::: "memory");
                    }
                }
                RBAR();
                if constexpr (s1 == 5 && R == 0) { if (tid == 0) __builtin_amdgcn_raw_buffer_store_b64((u32x2){tl, th}, r_ws, WS_F1 + (mem * 32 + grp) * 128, 0, 16); CFENCE(); }
                if constexpr (s1 == 5 && R == 1) { const int row = T0 - 16 + hd * 20 + min(opq(tid) & 63, 19); ald4s(s0v, WS_ST0 + (u32)(max(row, 0) * 16)); }
            }
            if (st < 15) pf_addr(st + 1);
            if constexpr (st == 14) load_tab(false);
            SB();
            qkv_step(IC<0>{}, st, Ab[st & 1], [&](auto mc) {
                constexpr int mt = decltype(mc)::value;
                if (st < 15) pf_part(st + 1, mt);
                if constexpr (R == 1) {
                    if constexpr (st == 0) { issue_piece(IC<2>{}, IC<2 * mt>{}); if constexpr (mt < 4) issue_piece(IC<2>{}, IC<2 * mt + 1>{}); }
                    if constexpr (st >= 1 && st <= 10 && ((st - 1) & 1) * 5 + mt < 9) issue_piece(IC<((st - 1) >> 1) + 3>{}, IC<((st - 1) & 1) * 5 + mt>{});
                    if constexpr (st == 11 && mt == 0) { const u32 ho = (u32)(b * 8192 + opq(tid) * 16); ald4(hr[2], d_h, ho); ald4(hr[3], d_h, ho + 16); }
                    if constexpr (st == 13 && mt == 0) { if (wave == 7) { const int l2 = opq(tid) & 63; ald2s(flg, WS_F1 + (u32)((l2 < 32 ? (b >> 5) * 32 + l2 : (l2 & 7) * 32 + grp) * 128)); } }
                    if constexpr (st >= 11 && st <= 14 && mt < 4) issue_wo(IC<(st - 11) * 4 + mt>{});
                } else {
                    if constexpr (st == 0) {
                        if constexpr (mt == 0) { const u32 xo = (u32)(srow * 1024 + (opq(tid) & 63) * 16); ald4(xrow[0], d_x, xo); ald4(xrow[1], d_x, xo + 1024); }
                        issue_kv(mt);
                    }
                    if constexpr (st == 11 && mt < 2) { const u32 ho = (u32)(b * 8192 + mt * 4096 + opq(tid) * 16); ald4(hr[2 * mt], d_h, ho); ald4(hr[2 * mt + 1], d_h, ho + 16); }
                }
                SB();
            });
            if constexpr (R == 0 && st == 2) {
                WAIT_VM(5);
                pin(xrow[0]); pin(xrow[1]);
                float s[2] = {0.f, 0.f}, q[2] = {0.f, 0.f};
#pragma unroll
                for (int i = 0; i < 4; ++i)
#pragma unroll
                    for (int k = 0; k < 2; ++k) { s[k] = dot2(xrow[k][i], 0x3F803F80u, s[k]); q[k] = dot2(xrow[k][i], xrow[k][i], q[k]); }
                auto ss = __builtin_amdgcn_permlane32_swap(__float_as_uint(s[0]), __float_as_uint(s[1]), false, false);
                auto qq = __builtin_amdgcn_permlane32_swap(__float_as_uint(q[0]), __float_as_uint(q[1]), false, false);
                float sv1 = rowsum16(__uint_as_float(ss[0]) + __uint_as_float(ss[1])), qv1 = rowsum16(__uint_as_float(qq[0]) + __uint_as_float(qq[1]));
                auto s2 = __builtin_amdgcn_permlane16_swap(__float_as_uint(sv1), __float_as_uint(sv1), false, false);
                auto q2 = __builtin_amdgcn_permlane16_swap(__float_as_uint(qv1), __float_as_uint(qv1), false, false);
                const float m = (__uint_as_float(s2[0]) + __uint_as_float(s2[1])) * (1.f / 512.f);
                const float r = __builtin_amdgcn_rsqf(fmaxf((__uint_as_float(q2[0]) + __uint_as_float(q2[1])) * (1.f / 512.f) - m * m, 0.f) + 1e-5f);
                const u32x4 sv = {__float_as_uint(m), tl, __float_as_uint(r), th};
                const int l2 = opq(tid) & 63;
                __builtin_amdgcn_raw_buffer_store_b128(sv, r_ws, (l2 & 31) == 0 ? WS_ST0 + (u32)((srow + (l2 >> 5)) * 16) : 0xFFFFFF00u, 0, 16);
                CFENCE();
                SB();
            }
            if constexpr (R == 0 && (st == 4 || st == 6)) {
                const int l2 = opq(tid) & 63, tg = l2 & 7, cp = 8 * hd + (l2 >> 3);
                if constexpr (st == 4) {
                    skw = *(LAS u32x4*)(lds + L_SK + cp * 16);
                    u32 ka = L_K + tg * 1024 + ((((cp >> 2) ^ tg) & 7) << 4) + (cp & 3) * 4;
                    u32 kb = L_K + tg * 1024 + ((((cp >> 2) ^ (tg + 1)) & 7) << 4) + (cp & 3) * 4;
                    pin(ka); pin(kb);
#pragma unroll
                    for (int i = 0; i < 11; ++i) kw[i] = *(LAS u32*)(lds + ((i + 5 < 8) ? ka : kb) + (i + 5) * 128);
#pragma unroll
                    for (int i = 0; i < 8; ++i) vw[i] = *(LAS u32*)(lds + ka + (L_V - L_K) + i * 128);
                    SB();
                }
                constexpr bool HI = st == 6;
                auto ex = [&](u32 w) { return HI ? bfhi(w) : bflo(w); };
                const float a0 = bflo(skw[2 * HI]), a1 = bfhi(skw[2 * HI]), a2 = bflo(skw[2 * HI + 1]), a3 = bfhi(skw[2 * HI + 1]);
                float ua[8];
#pragma unroll
                for (int i = 0; i < 8; ++i) ua[i] = (ex(kw[i]) * a3 + ex(kw[i + 1]) * a2 + ex(kw[i + 2]) * a1 + ex(kw[i + 3]) * a0) * ex(vw[i]);
                const u32x4 oa = {pk2(ua[7], ua[6]), pk2(ua[5], ua[4]), pk2(ua[3], ua[2]), pk2(ua[1], ua[0])};
                __builtin_amdgcn_raw_buffer_store_b128(oa, r_ws, WS_UT + (u32)((mem * 64 + 2 * cp) * 4096 + (2040 - T0 - 8 * tg) * 2), HI * 4096, 16);
                __builtin_amdgcn_raw_buffer_store_b128((u32x4){SENT, SENT, SENT, SENT}, r_ws, (u32)((4 * grp + hd) * 16384 + mem * 2048 + HI * 1024 + l2 * 16), WS_O1, 16);
                CFENCE();
                SB();
            }
        });
    }

    u32x4 uu;
    float csV;
    auto attention = [&](auto lc) {
        constexpr int LAYER = decltype(lc)::value;
        const int lane = opq(tid) & 63, li = lane & 15, lq = lane >> 4;
        u32 ex = L_EX + hd * 2048 + lane * 8;
        pin(ex);
        u32 f1ok = 1;
        if constexpr (R == 0) {
            if constexpr (LAYER == 0) {
#pragma unroll
                for (int i = 0; i < 4; ++i) { csK[i] = bcast_row0(accM[0][i]); csQ[i] = bcast_row0(accQ[0][i]); }
                if (li == 0) { *(LAS f32x4*)(lds + L_CS + hd * 128 + lq * 16) = csK; *(LAS f32x4*)(lds + L_CS + hd * 128 + 64 + lq * 16) = csQ; }
            }
            const int par = li & 1;
            const bool cur0 = (4 * lq + par) <= li, cur1 = (4 * lq + par + 2) <= li;
            s16x4 Kb[5], Qb[5];
#pragma unroll
            for (int mt = 0; mt < 5; ++mt) {
                Kb[mt] = pack4((accM[mt] - m1[mt] * csK) * r1[mt]);
                if (mt) Qb[mt] = pack4(accQ[mt] - m1[mt] * csQ);
            }
            f32x4 Sc[5], Sp[5];
#pragma unroll
            for (int mt = 1; mt < 5; ++mt) { Sc[mt] = mfma16(Kb[mt], Qb[mt], zero4); Sp[mt] = mfma16(Kb[mt - 1], Qb[mt], zero4); }
            float s0[5], s1[5], mx[5];
#pragma unroll
            for (int mt = 1; mt < 5; ++mt) {
                const float sp0 = Sp[mt][0], sp1 = Sp[mt][1], sp2 = Sp[mt][2], sp3 = Sp[mt][3], c0 = Sc[mt][0], c1 = Sc[mt][1], c2 = Sc[mt][2], c3 = Sc[mt][3];
                float p0 = par ? sp1 : sp0, p1 = par ? sp3 : sp2, q0 = par ? c1 : c0, q1 = par ? c3 : c2;
                asm volatile("" : "+v"(p0), "+v"(p1), "+v"(q0), "+v"(q1));
                if (mt == 1 && grp == 0) { p0 = -3.0e38f; p1 = -3.0e38f; }
                s0[mt] = cur0 ? q0 : p0; s1[mt] = cur1 ? q1 : p1;
                mx[mt] = vmax(s0[mt], s1[mt]);
            }
#pragma unroll
            for (int mt = 1; mt < 5; ++mt) { auto r = __builtin_amdgcn_permlane32_swap(__float_as_uint(mx[mt]), __float_as_uint(mx[mt]), false, false); mx[mt] = vmax(__uint_as_float(r[0]), __uint_as_float(r[1])); }
#pragma unroll
            for (int mt = 1; mt < 5; ++mt) { auto r = __builtin_amdgcn_permlane16_swap(__float_as_uint(mx[mt]), __float_as_uint(mx[mt]), false, false); mx[mt] = vmax(__uint_as_float(r[0]), __uint_as_float(r[1])); }
#pragma unroll
            for (int mt = 1; mt < 5; ++mt) {
                const float sc = r1[mt] * (0.25f * 1.44269504088896341f);
                const float e0 = __builtin_amdgcn_exp2f((s0[mt] - mx[mt]) * sc), e1 = __builtin_amdgcn_exp2f((s1[mt] - mx[mt]) * sc);
                *(LAS u32x2*)(lds + ex + (mt - 1) * 512) = (u32x2){pk2(e0, e0), pk2(e1, e1)};
            }
            BAR();
            if constexpr (LAYER == 0) f1ok = __builtin_amdgcn_readfirstlane(*(LAS u32*)(lds + L_FLG));
        } else {
            if constexpr (LAYER == 0) {
                auto s = __builtin_amdgcn_permlane16_swap(__float_as_uint(accM[0][0]), __float_as_uint(accM[0][0]), false, false);
                auto t = __builtin_amdgcn_permlane32_swap(s[0], s[0], false, false);
                csV = __uint_as_float(t[0]);
                if (wave == 7) { WAIT_VM(8); pin(flg); const u32 ok = flag_ok(flg); *(LAS u32*)(lds + L_FLG) = ok; }
            }
            const int par = li & 1;
            const bool cur0 = (4 * lq + par) <= li, cur1 = (4 * lq + par + 2) <= li;
            const u32 pm = par ? 0xffff0000u : 0x0000ffffu;
            u32 mC0 = cur0 ? pm : 0u, mP0 = cur0 ? 0u : pm, mC1 = cur1 ? pm : 0u, mP1 = cur1 ? 0u : pm;
            u32 oo = (LAYER ? WS_O2 : WS_O1) + (u32)((4 * grp + (lq & 1)) * 16384 + (4 * mem + hd) * 512 + (lq >> 1) * 256 + li * 16);
            asm volatile("" : "+v"(mC0), "+v"(mP0), "+v"(mC1), "+v"(mP1), "+v"(oo));
            s16x4 Vb[5];
#pragma unroll
            for (int mt = 0; mt < 5; ++mt) { Vb[mt] = pack4((accM[mt] - m4[mt] * csV) * r4[mt]); pin(Vb[mt]); }
            BAR();
            u32x2 pw[4];
#pragma unroll
            for (int j = 0; j < 4; ++j) pw[j] = *(LAS u32x2*)(lds + ex + j * 512);
            if constexpr (LAYER == 0) {
                f1ok = __builtin_amdgcn_readfirstlane(*(LAS u32*)(lds + L_FLG));
                if (f1ok) ald4s(uu, WS_UT + (u32)(b * 8192 + opq(tid) * 16));
            }
            SB();
            f32x4 O[4], L[4];
#pragma unroll
            for (int j = 0; j < 4; ++j) {
                const s16x8 P = __builtin_bit_cast(s16x8, (u32x4){pw[j][0] & mC0, pw[j][1] & mC1, pw[j][0] & mP0, pw[j][1] & mP1});
                L[j] = mfma32(ones8, P, zero4);
                O[j] = mfma32(cat8(Vb[j + 1], Vb[j]), P, zero4);
            }
            u32 ow[4][2];
#pragma unroll
            for (int j = 0; j < 4; ++j) {
                const float inv = __builtin_amdgcn_rcpf(L[j][0]);
                ow[j][0] = nosent(pk2(O[j][0] * inv, O[j][1] * inv)); ow[j][1] = nosent(pk2(O[j][2] * inv, O[j][3] * inv));
            }
#pragma unroll
            for (int j = 0; j < 4; j += 2) {
                auto r0 = __builtin_amdgcn_permlane16_swap(ow[j][0], ow[j + 1][0], false, false);
                auto r1 = __builtin_amdgcn_permlane16_swap(ow[j][1], ow[j + 1][1], false, false);
                __builtin_amdgcn_raw_buffer_store_b128((u32x4){r0[0], r1[0], r0[1], r1[1]}, r_ws, oo, j * 16384, 16);
            }
            CFENCE();
        }
        if constexpr (LAYER == 0 && R == 0) { if (f1ok) ald4s(uu, WS_UT + (u32)(b * 8192 + opq(tid) * 16)); }
        SB();
        return f1ok;
    };
    const u32 f1ok = attention(IC<0>{});
    {
        const u32x4 sv = {SENT, SENT, SENT, SENT};
        const int t2 = opq(tid);
        const u32 tb = (u32)((4 * grp + hd) * 16384 + mem * 2048 + R * 1024 + (t2 & 63) * 16);
        __builtin_amdgcn_raw_buffer_store_b128(sv, r_ws, tb, WS_O2, 16);
        __builtin_amdgcn_raw_buffer_store_b128(sv, r_ws, tb, WS_X2, 16);
        __builtin_amdgcn_raw_buffer_store_b128(sv, r_ws, (u32)(b * 8192 + t2 * 16), WS_Y2, 16);
        CFENCE();
        SB();
    }

    const int TI = 4 * grp + hd;
    u32x4 of[8];
    auto load_o = [&](u32 ws_o) {
        const int lane = opq(tid) & 63;
#pragma unroll
        for (int i = 0; i < 8; ++i) of[i] = __builtin_amdgcn_raw_buffer_load_b128(r_ws, ws_o + (u32)(TI * 16384 + R * 8192 + lane * 16) + (i & 3) * 1024, (i >> 2) * 4096, 16);
    };
    u32 xw[8], yw[8];
    u32 woa[4];
    auto resid = [&](int n) { return R ? zero4 : (f32x4){bflo(xw[2 * n]), bfhi(xw[2 * n]), bflo(xw[2 * n + 1]), bfhi(xw[2 * n + 1])}; };
    s16x8 F[8][4];
    auto wo_addr = [&]() {
        const int l2 = opq(tid) & 63, lq = l2 >> 4, tq = (l2 & 15) >> 2, tp = l2 & 3;
#pragma unroll
        for (int n = 0; n < 4; ++n) woa[n] = L_WO + (u32)(R * 32768 + (8 * lq + tq) * 128 + ((n ^ (((lq & 1) << 1) | (tq >> 1))) << 5) + tp * 8);
    };
    auto load_F = [&](int ks) {
#pragma unroll
        for (int n = 0; n < 4; ++n) F[ks][n] = cat8(trr(woa[n] + ks * 4096), trr(woa[n] + ks * 4096 + 512));
    };

    {
        const int t2 = opq(tid), lane = t2 & 63, li = lane & 15, lq = lane >> 4;
        constexpr u32 OFFS = 0xEDA85410u;
        {
            const int j = t2 & 255;
            {
                if constexpr (R == 0) { if (f1ok) WAIT_VM(4); else WAIT_VM(3); }
                else WAIT_VM(5);
                u32 cb = L_HC + R * 34816 + (j + 2) * 16;
                pin(cb);
#pragma unroll
                for (int ch = R; ch < 2; ++ch) {
                    pin(hr[2 * ch]); pin(hr[2 * ch + 1]);
                    const u32 d[8] = {hr[2 * ch][0], hr[2 * ch][1], hr[2 * ch][2], hr[2 * ch][3], hr[2 * ch + 1][0], hr[2 * ch + 1][1], hr[2 * ch + 1][2], hr[2 * ch + 1][3]};
                    u32 e[7];
#pragma unroll
                    for (int i = 0; i < 7; ++i) e[i] = __builtin_amdgcn_alignbit(d[i + 1], d[i], 16);
                    if (ch > R) { cb += 34816; pin(cb); }
#pragma unroll
                    for (int p = (R ? 4 : 0); p < ((ch && !R) ? 4 : 8); ++p) {
                        const u32 po = (272 * p + ((OFFS >> (4 * p)) & 15)) * 16;
                        const int h = p >> 1;
                        const u32x4 w = (p & 1) ? (u32x4){e[h], e[h + 1], e[h + 2], e[h + 3]} : (u32x4){d[h], d[h + 1], d[h + 2], d[h + 3]};
                        *(LAS u32x4*)(lds + cb + po) = w;
                    }
                }
            }
            if constexpr (R == 0) {
                if (hd == 0) {
                    u32 hh[4];
#pragma unroll
                    for (int i = 0; i < 4; ++i) { const u32 a = __builtin_amdgcn_readfirstlane(hr[0][i]), c = __builtin_amdgcn_readfirstlane(hr[2][i]); hh[i] = (lane & 16) ? c : a; }
                    if (lane < 32) {
                        const int p = lane & 7;
                        const u32 po = (272 * p + ((OFFS >> (4 * p)) & 15)) * 16;
                        const unsigned __int128 hv = ((unsigned __int128)(((u64)hh[3] << 32) | hh[2]) << 64) | (((u64)hh[1] << 32) | hh[0]);
                        const unsigned __int128 sv = (p == 0 || !(lane & 8)) ? (unsigned __int128)0 : (hv << (16 * (8 - p)));
                        const u32x4 w = {(u32)sv, (u32)(sv >> 32), (u32)(sv >> 64), (u32)(sv >> 96)};
                        *(LAS u32x4*)(lds + L_HC + (lane >> 4) * 34816 + po + ((lane >> 3) & 1) * 16) = w;
                    }
                }
            }
            if (hd == 1 && lane < 30) *(LAS u32x4*)(lds + L_UR + R * 4576 + 4096 + lane * 16) = (u32x4){0, 0, 0, 0};
            if (!f1ok) {
                const u32 fo = WS_F1 + (u32)(((lane & 8) ? (lane & 7) * 32 + grp : (b >> 5) * 32 + 31 - hd * 8 - (lane & 7)) * 128);
                flg = (u32x2){0u, 0u};
                spin_flags(flg, fo);
                ald4s(uu, WS_UT + (u32)(b * 8192 + t2 * 16));
            }
            if (f1ok) WAIT_VM(R ? 5 : 3); else WAIT_VM(0);
            pin(uu);
            *(LAS u32x4*)(lds + L_UR + R * 4576 + j * 16) = uu;
        }
        BAR();
        const int ch = hd >> 1, e0 = (hd & 1) * 4;
        const int p = (li + 1) & 7;
        u32 aA = L_HC + ch * 34816 + (272 * p + ((OFFS >> (4 * p)) & 15) + ((li + 1) >> 3) + lq + 4 * e0) * 16;
        u32 aB = L_UR + ch * 4576 + (254 - 224 - 2 * li + lq + 4 * e0) * 16;
        asm volatile("" : "+v"(aA), "+v"(aB));
        f32x4 y[4];
        u32 ooff = WS_O1 + (u32)(TI * 16384 + R * 8192 + lane * 16);
        {
            constexpr ConvSched S(R);
            s16x8 A[4][8], B[4][8];
            auto loads_at = [&](auto pc) {
                constexpr int p = decltype(pc)::value;
                sfor<0, 64>([&](auto ic) {
                    constexpr int i = decltype(ic)::value, e = i >> 4, f = i & 7;
                    if constexpr (S.slot[i] == p) {
                        if constexpr (i & 8) B[e][f] = ldsA(aB + 64 * e + 512 * (7 - f)); else A[e][f] = ldsA(aA + 64 * (8 * f + e));
                    }
                });
            };
            loads_at(IC<-1>{});
            SB();
            sfor<0, 72>([&](auto pc) {
                constexpr int p = decltype(pc)::value, e = p / 18, f = S.f[p % 18], i = S.i[p % 18], g = S.n[i] - f;
                y[i] = mfma32(A[e][f], B[e][g], (e == 0 && f == 0) ? zero4 : y[i]);
                loads_at(pc);
                if constexpr (p == 54) WAIT_VM(0);
                if constexpr (p >= 55 && p < 71 && (p & 1)) {
                    constexpr int i = (p - 55) >> 1;
                    ald4o<(i & 3) * 1024>(of[i], ooff + (i >> 2) * 4096, d_ws);
                }
                SB();
            });
        }
        RBAR();
        if (R == 0 && tid == 0) __builtin_amdgcn_raw_buffer_store_b64((u32x2){tl, th}, r_ws, WS_F2 + (grp * 8 + mem) * 128, 0, 16);
        {
            constexpr int N0[4] = {0, 3, 4, 7};
            u32 ra = L_HC + hd * 8192 + lane * 16;
            asm volatile("" : "+v"(ra));
#pragma unroll
            for (int i = 0; i < 4; ++i) *(LAS f32x4*)(lds + ra + (N0[i] ^ R) * 1024) = y[i];
        }
        SB();
        if constexpr (R == 0) {
#pragma unroll
            for (int n = 0; n < 4; ++n) { const u32x2 w = __builtin_amdgcn_raw_buffer_load_b64(r_x, (u32)((TI * 16 + li) * 1024 + mem * 128 + lq * 8) + n * 32, 0, 0); xw[2 * n] = w[0]; xw[2 * n + 1] = w[1]; }
        }
        wo_addr();
        SB();
        BAR();
        {
            const float D0 = bflo(dw), D1 = bfhi(dw);
            u32 ra = L_HC + wave * 1024 + lane * 16;
            const int tb = 256 * wave + 16 * li + 4 * lq;
            u32 ru = L_UR + (2044 - tb) * 2;
            asm volatile("" : "+v"(ra), "+v"(ru));
            const f32x4 y0 = *(LAS f32x4*)(lds + ra), y1 = *(LAS f32x4*)(lds + ra + 8192), y2 = *(LAS f32x4*)(lds + ra + 16384), y3 = *(LAS f32x4*)(lds + ra + 24576);
            const u32x2 ua = *(LAS u32x2*)(lds + ru), ub = *(LAS u32x2*)(lds + ru + 4576);
            load_F(0); load_F(1);
            SB();
            const f32x4 ya = y0 + y1, yb = y2 + y3;
            const u32x4 w = {nosent(pk2(ya[0] + bfhi(ua[1]) * D0, yb[0] + bfhi(ub[1]) * D1)), nosent(pk2(ya[1] + bflo(ua[1]) * D0, yb[1] + bflo(ub[1]) * D1)),
                             nosent(pk2(ya[2] + bfhi(ua[0]) * D0, yb[2] + bfhi(ub[0]) * D1)), nosent(pk2(ya[3] + bflo(ua[0]) * D0, yb[3] + bflo(ub[0]) * D1))};
            __builtin_amdgcn_raw_buffer_store_b128(w, r_ws, WS_Y2 + (u32)(b * 8192 + tb * 4), 0, 16);
        }
        SB();
        WAIT_VM(1);

        u32 f2oth = 0;
        _Pragma("unroll 1") for (int attempt = 0; attempt < 4096; ++attempt) {
            sfor<0, 8>([&](auto ic) { pin(of[decltype(ic)::value]); });
            u32 m0 = 0, m1 = 0;
#pragma unroll
            for (int ks = 0; ks < 8; ++ks) {
                load_F((ks + 2) & 7);
                if (R == 1 && ks == 4 && wave == 7) {
                    const int pb = 32 * mem + (lane & 31);
                    f2oth = WS_F2 + (u32)((lane < 32 ? ((pb & 7) * 4 + (pb >> 6)) * 8 + ((pb >> 3) & 7) : max(grp * 8 - 8, 0) + (lane & 15)) * 128);
                    ald2s(flg, f2oth);
                }
                SB();
#pragma unroll
                for (int n = 0; n < 4; ++n) accM[n] = mfma32(F[ks][n], __builtin_bit_cast(s16x8, of[ks]), ks ? accM[n] : resid(n));
                m0 = max(max(m0, of[ks][0]), of[ks][1]); m1 = max(max(m1, of[ks][2]), of[ks][3]);
                SB();
            }
            if (__builtin_expect(__all(max(m0, m1) != SENT), 1)) break;
            __builtin_amdgcn_s_sleep(4);
            WAIT_VM(0);
            sfor<0, 8>([&](auto ic) { constexpr int i = decltype(ic)::value; ald4o<(i & 3) * 1024>(of[i], ooff + (i >> 2) * 4096, d_ws); });
            WAIT_VM(0);
        }
        if constexpr (R == 1) { if (wave == 7) { WAIT_VM(0); pin(flg); spin_flags(flg, f2oth); } }
    }

    auto load_y = [&]() {
        const int l2 = opq(tid) & 63;
#pragma unroll
        for (int i = 0; i < 8; ++i)
            yw[i] = __builtin_amdgcn_raw_buffer_load_b32(r_ws, WS_Y2 + (u32)((32 * mem + 2 * (l2 >> 4)) * 8192 + (TI * 16 + (l2 & 15)) * 4), (8 * (i >> 1) + (i & 1)) * 8192, 16);
    };
    auto pub_off = [&](u32 ws_st) {
        const int lane = opq(tid) & 63;
        return (lane & 16) ? 0xFFFFFF00u : ws_st + (u32)((TI * 8 + mem) * 256 + (lane & 15) * 16 + (lane >> 5) * 8);
    };
    auto pub_stat = [&](u32 off, u32 tg, float s, float q) {
        auto r = __builtin_amdgcn_permlane32_swap(__float_as_uint(s), __float_as_uint(q), false, false);
        const float v = __uint_as_float(r[0]) + __uint_as_float(r[1]);
        auto t = __builtin_amdgcn_permlane16_swap(__float_as_uint(v), __float_as_uint(v), false, false);
        __builtin_amdgcn_raw_buffer_store_b64((u32x2){__float_as_uint(__uint_as_float(t[0]) + __uint_as_float(t[1])), tg}, r_ws, off, 0, 16);
    };
    auto stat_off = [&](u32 ws_st, int ti) { const int l2 = opq(tid) & 63; return ws_st + (u32)(ti * 2048 + (l2 >> 4) * 512 + (l2 & 15) * 16); };
    auto fin_stat = [&](u32x4& a, u32x4& c, u32 off, float& mean, float& rstd) {
        _Pragma("unroll 1") for (int spin = 0; spin < SPIN_CAP; ++spin) {
            if (__builtin_expect(__all(((a[1] ^ tl) | (a[3] ^ th) | (c[1] ^ tl) | (c[3] ^ th)) == 0), 1)) break;
            __builtin_amdgcn_s_sleep(1);
            asm volatile("buffer_load_dwordx4 %0, %2, %3, 0 offen sc1\n\tbuffer_load_dwordx4 %1, %2, %3, 0 offen offset:256 sc1\n\ts_waitcnt vmcnt(0)"
                         : "=&v"(a), "=&v"(c) : "v"(off), "s"(d_ws) : "memory");
        }
        auto r = __builtin_amdgcn_permlane32_swap(__float_as_uint(__uint_as_float(a[0]) + __uint_as_float(c[0])), __float_as_uint(__uint_as_float(a[2]) + __uint_as_float(c[2])), false, false);
        const float v = __uint_as_float(r[0]) + __uint_as_float(r[1]);
        auto t = __builtin_amdgcn_permlane16_swap(__float_as_uint(v), __float_as_uint(v), false, false);
        const u32 v2 = __float_as_uint(__uint_as_float(t[0]) + __uint_as_float(t[1]));
        auto u = __builtin_amdgcn_permlane32_swap(v2, v2, false, false);
        const float ts = __uint_as_float(u[0]), tq = __uint_as_float(u[1]);
        mean = ts * (1.f / 512.f);
        rstd = __builtin_amdgcn_rsqf(fmaxf(tq * (1.f / 512.f) - mean * mean, 0.f) + 1e-5f);
    };
    auto regroup = [&](u32 (&w)[8]) {
#pragma unroll
        for (int j = 0; j < 2; ++j) {
            auto r0 = __builtin_amdgcn_permlane16_swap(w[4 * j], w[4 * j + 2], false, false);
            auto r1 = __builtin_amdgcn_permlane16_swap(w[4 * j + 1], w[4 * j + 3], false, false);
            w[4 * j] = r0[0]; w[4 * j + 1] = r1[0]; w[4 * j + 2] = r0[1]; w[4 * j + 3] = r1[1];
        }
    };
    auto wo_finish = [&](auto lc) {
        constexpr int LAYER = decltype(lc)::value;
        const int lane = opq(tid) & 63, li = lane & 15, lq = lane >> 4;
        u32 wx = L_WX + hd * 4096 + lane * 16;
        pin(wx);
        u32 po = 0, so = 0, tg = 0, oa = 0;
        if constexpr (R == 1) {
#pragma unroll
            for (int n = 0; n < 4; ++n) *(LAS f32x4*)(lds + wx + n * 1024) = accM[n];
        } else {
            po = pub_off(LAYER ? WS_ST3 : WS_ST1); so = stat_off(LAYER ? WS_ST3 : WS_ST1, TI); tg = lane < 32 ? tl : th;
            const int c = 2 * (lq & 1) + (lq >> 1);
            oa = LAYER ? (u32)((TI * 16 + li) * 1024 + mem * 128 + c * 16) : WS_X2 + (u32)((TI * 8 + mem) * 2048 + li * 128 + ((c ^ (li >> 1)) << 4));
            pin(po); pin(so); pin(tg); pin(oa);
        }
        BAR();
        if constexpr (R == 0) {
            f32x4 t[4];
#pragma unroll
            for (int n = 0; n < 4; ++n) t[n] = *(LAS f32x4*)(lds + wx + n * 1024);
            SB();
            float s = 0.f, q = 0.f, s2 = 0.f, q2 = 0.f;
#pragma unroll
            for (int n = 0; n < 4; ++n) {
                const f32x4 v = accM[n] + t[n];
                const u32 w0 = pk2(v[0], v[1]), w1 = pk2(v[2], v[3]);
                xw[2 * n] = w0; xw[2 * n + 1] = w1;
                s = dot2(w0, 0x3F803F80u, s); q = dot2(w0, w0, q); s2 = dot2(w1, 0x3F803F80u, s2); q2 = dot2(w1, w1, q2);
            }
            pub_stat(po, tg, s + s2, q + q2);
            CFENCE();
            if constexpr (!LAYER) load_y();
            regroup(xw);
            SB();
            WAIT_VM(0);
            if constexpr (!LAYER) {
#pragma unroll
                for (int i = 0; i < 8; ++i) pin(yw[i]);
            }
            u32x4 a, c;
            ald4s(a, so); ald4s(c, so + 256);
            if constexpr (!LAYER) {
                _Pragma("unroll 1") for (int spin = 0; spin < SPIN_CAP; ++spin) {
                    if (__builtin_expect(__all(max(max(max(yw[0], yw[1]), max(yw[2], yw[3])), max(max(yw[4], yw[5]), max(yw[6], yw[7]))) != SENT), 1)) break;
                    __builtin_amdgcn_s_sleep(1);
                    CFENCE();
                    load_y();
                }
                regroup(yw);
            }
            f32x2 xa[8], ya[8], xy[8];
#pragma unroll
            for (int i = 0; i < 8; ++i) {
                xa[i] = (f32x2){bflo(xw[i]), bfhi(xw[i])}; ya[i] = (f32x2){bflo(yw[i]), bfhi(yw[i])}; xy[i] = xa[i] * ya[i];
                pin(xa[i]); pin(ya[i]); pin(xy[i]);
            }
            WAIT_VM(0);
            pin(a); pin(c);
            float mean, rstd;
            fin_stat(a, c, so, mean, rstd);
            const float nm = -mean * rstd;
#pragma unroll
            for (int i = 0; i < 8; ++i) {
                const f32x2 o = xy[i] * rstd + (ya[i] * nm + xa[i]);
                xw[i] = pk2(o[0], o[1]);
                if (!LAYER) xw[i] = nosent(xw[i]);
            }
#pragma unroll
            for (int j = 0; j < 2; ++j) {
                const u32x4 o = {xw[4 * j], xw[4 * j + 1], xw[4 * j + 2], xw[4 * j + 3]};
                if constexpr (LAYER) __builtin_amdgcn_raw_buffer_store_b128(o, r_out, oa + j * 64, 0, 0);
                else __builtin_amdgcn_raw_buffer_store_b128(o, r_ws, oa ^ (j * 64), 0, 16);
            }
            if constexpr (!LAYER) {
                float s = 0.f, q = 0.f, s2 = 0.f, q2 = 0.f;
#pragma unroll
                for (int i = 0; i < 8; i += 2) { s = dot2(xw[i], 0x3F803F80u, s); q = dot2(xw[i], xw[i], q); s2 = dot2(xw[i + 1], 0x3F803F80u, s2); q2 = dot2(xw[i + 1], xw[i + 1], q2); }
                pub_stat(po + (WS_ST2 - WS_ST1) * !(lane & 16), tg, s + s2, q + q2);
                CFENCE();
                regroup(xw);
#pragma unroll
                for (int i = 0; i < 8; ++i) { pin(xw[i]); pin(yw[i]); }
                WAIT_VM(0);
            }
        }
    };
    wo_finish(IC<0>{});
    if constexpr (R == 1) {
        if (b == 0 && wave == 7) {
            const u32 fo = WS_F1 + (u32)((opq(tid) & 63) * 128);
            u32x2 ef[4];
#pragma unroll
            for (int i = 0; i < 4; ++i) ald2s(ef[i], fo + i * 8192);
            WAIT_VM(0);
#pragma unroll
            for (int i = 0; i < 4; ++i) { pin(ef[i]); spin_flags(ef[i], fo + i * 8192); }
            if (fo == WS_F1) __builtin_amdgcn_raw_buffer_store_b64(__builtin_bit_cast(u32x2, epoch + 1), r_ws, WS_EPOCH, 0, 16);
            WAIT_VM(0);
        }
    }
    RBAR();

    {
        const int l3 = opq(tid) & 63;
        const u32 g0 = (u32)((4 * grp - 1 + (hd >> 1)) * 16384 + (hd & 1) * 1024 + l3 * 16);
        const u32 g2 = (u32)((4 * grp + 3) * 16384 + hd * 512 + l3 * 16);
        auto x2_piece = [&](auto ic) {
            constexpr int i = decltype(ic)::value, s = i / 3, p = i % 3;
            const u32 kp = (u32)((s + rot) & 7);
            if constexpr (p == 0) dma(IC<4>{}, IC<L_CX + s * 10240>{}, d_x2, hb1k, g0, kp * 2048);
            else if constexpr (p == 1) dma(IC<4>{}, IC<L_CX + s * 10240 + 4096>{}, d_x2, hb1k, g0 + 32768, kp * 2048);
            else dma(IC<5>{}, IC<L_CX + s * 10240 + 8192>{}, d_x2, hb512, g2, kp * 2048);
        };
        u32 va = L_CX + hd * 1024 + l3 * 16, vq = L_CX + 8192 + hd * 512 + (l3 & 31) * 16 + (l3 >> 5) * 10240;
        pin(va); pin(vq);
        u32x4 vr[5];
        auto ver_read = [&](auto cc) {
            constexpr int c = decltype(cc)::value;
            u32 a = va + c * 20480, q = vq + c * 20480;
            pin(a); pin(q); CFENCE();
            vr[0] = *(LAS u32x4*)(lds + a); vr[1] = *(LAS u32x4*)(lds + a + 4096); vr[2] = *(LAS u32x4*)(lds + a + 10240); vr[3] = *(LAS u32x4*)(lds + a + 14336);
            vr[4] = *(LAS u32x4*)(lds + q);
        };
        auto ver_check = [&](auto cc) {
            constexpr int c = decltype(cc)::value;
            _Pragma("unroll 1") for (int spin = 0; spin < SPIN_CAP; ++spin) {
                u32 m0 = 0, m1 = 0;
#pragma unroll
                for (int i = 0; i < 5; ++i) { m0 = max(max(m0, vr[i][0]), vr[i][1]); m1 = max(max(m1, vr[i][2]), vr[i][3]); }
                if (__builtin_expect(__all(max(m0, m1) != SENT), 1)) break;
                __builtin_amdgcn_s_sleep(1);
                sfor<6 * c, 6 * c + 6>([&](auto ic) { x2_piece(ic); });
                WAIT_VM(0);
                ver_read(cc);
            }
        };
        const u32 xb0 = (u32)((l3 & 15) * 128 + (((l3 >> 4) ^ ((l3 & 15) >> 1)) << 4));
        if constexpr (R == 1) {
            sfor<0, 12>([&](auto ic) { x2_piece(ic); });
            WAIT_VM(6);
            ver_read(IC<0>{});
            ver_check(IC<0>{});
        } else {
            u32x4 sa, sc, ha, hc;
            const u32 so = stat_off(WS_ST2, 4 * grp + hd);
            const bool halo = hd == 0 && grp > 0;
            ald4s(sa, so); ald4s(sc, so + 256);
            if (halo) { ald4s(ha, so - 2048); ald4s(hc, so - 2048 + 256); }
            WAIT_VM(0);
            pin(sa); pin(sc);
            float mean, rstd;
            fin_stat(sa, sc, so, mean, rstd);
            if (l3 < 16) { *(LAS float*)(lds + L_TAB + 64 + hd * 64 + l3 * 4) = mean; *(LAS float*)(lds + L_TAB + 320 + 64 + hd * 64 + l3 * 4) = rstd; }
            if (halo) {
                pin(ha); pin(hc);
                fin_stat(ha, hc, so - 2048, mean, rstd);
                if (l3 < 16) { *(LAS float*)(lds + L_TAB + l3 * 4) = mean; *(LAS float*)(lds + L_TAB + 320 + l3 * 4) = rstd; }
            }
        }
        BAR();
        u32 xa;
        auto pf_addr = [&](int st) { xa = L_CX + (st >> 1) * 10240 + (xb0 ^ ((st & 1) * 64)); pin(xa); };
        pf_addr(0);
#pragma unroll
        for (int mt = 0; mt < 5; ++mt) Ab[0][mt] = ldsA(xa + mt * 2048);
        sfor<0, 16>([&](auto stc) {
            constexpr int st = decltype(stc)::value;
            if constexpr (R == 1) {
                if constexpr (st == 2) { WAIT_VM(10); ver_read(IC<1>{}); }
                if constexpr (st == 3) ver_check(IC<1>{});
                if constexpr (st == 5) { WAIT_VM(6); ver_read(IC<2>{}); }
                if constexpr (st == 6) { ver_check(IC<2>{}); WAIT_VM(0); ver_read(IC<3>{}); }
                if constexpr (st == 7) ver_check(IC<3>{});
            }
            if constexpr (st == 3) { RBAR(); }
            if constexpr (st == 7) { RBAR(); }
            if (st < 15) pf_addr(st + 1);
            if constexpr (st == 14) load_tab(true);
            SB();
            qkv_step(IC<1>{}, st, Ab[st & 1], [&](auto mc) {
                constexpr int mt = decltype(mc)::value;
                if (st < 15) Ab[(st + 1) & 1][mt] = ldsA(xa + mt * 2048);
                if constexpr (R == 1 && 12 + 5 * st + mt < 24) x2_piece(IC<12 + 5 * st + mt>{});
                SB();
            });
        });
    }
    attention(IC<1>{});

    {
        wo_addr();
#pragma unroll
        for (int ks = 0; ks < 8; ++ks) load_F(ks);
        SB();
        WAIT_VM(0);
        RBAR();
        load_o(WS_O2);
        SB();
        _Pragma("unroll 1") for (int spin = 0; spin < 64; ++spin) {
            u32 m0 = 0, m1 = 0;
#pragma unroll
            for (int ks = 0; ks < 8; ++ks) {
#pragma unroll
                for (int n = 0; n < 4; ++n) accM[n] = mfma32(F[ks][n], __builtin_bit_cast(s16x8, of[ks]), ks ? accM[n] : resid(n));
                m0 = max(max(m0, of[ks][0]), of[ks][1]); m1 = max(max(m1, of[ks][2]), of[ks][3]);
                SB();
            }
            if (__builtin_expect(__all(max(m0, m1) != SENT), 1)) break;
            _Pragma("unroll 1") for (int sp2 = 0; sp2 < SPIN_CAP; ++sp2) {
                CFENCE();
                __builtin_amdgcn_s_sleep(1);
                const u32 pv = __builtin_amdgcn_raw_buffer_load_b32(r_ws, WS_O2 + (u32)(TI * 16384 + R * 8192 + (opq(tid) & 15) * 512 + 508), 0, 16);
                if (__all(pv != SENT)) break;
            }
            CFENCE();
            load_o(WS_O2);
        }
        SB();
    }
    wo_finish(IC<1>{});
}

__global__ void __launch_bounds__(512, 2)
fused_kernel(const u16* __restrict__ gx, const u16* __restrict__ gwqkv, const u16* __restrict__ gwo,
             const u16* __restrict__ gbk, const u16* __restrict__ gbv, const u16* __restrict__ gsk,
             const u16* __restrict__ ghk, const u16* __restrict__ gdv, u16* __restrict__ gout, char* __restrict__ ws) {
    __shared__ __attribute__((aligned(16))) char lds_[L_TOTAL];
    typedef u32 u32x16 __attribute__((ext_vector_type(16)));
    u32x16 ka; u32x4 kb;
    {
        auto kp = __builtin_amdgcn_kernarg_segment_ptr();
        asm volatile("s_load_dwordx16 %0, %2, 0x0\n\ts_load_dwordx4 %1, %2, 0x40\n\ts_waitcnt lgkmcnt(0)" : "=&s"(ka), "=&s"(kb) : "s"(kp) : "memory");
    }
    auto mkp = [](u32 lo, u32 hi) { return (char*)(((u64)hi << 32) | lo); };
    const Args a = {(const u16*)mkp(ka[0], ka[1]), (const u16*)mkp(ka[2], ka[3]), (const u16*)mkp(ka[4], ka[5]), (const u16*)mkp(ka[6], ka[7]), (const u16*)mkp(ka[8], ka[9]),
                    (const u16*)mkp(ka[10], ka[11]), (const u16*)mkp(ka[12], ka[13]), (const u16*)mkp(ka[14], ka[15]), (u16*)mkp(kb[0], kb[1]), mkp(kb[2], kb[3])};
    const int wave = __builtin_amdgcn_readfirstlane(threadIdx.x >> 6);
    if (wave < 4) body<0>(a, (LAS char*)lds_, wave); else body<1>(a, (LAS char*)lds_, wave);
}

extern "C" void kernel_launch(void* const* d_in, const int* in_sizes, int n_in,
                              void* d_out, int out_size, void* d_ws, size_t ws_size,
                              hipStream_t stream) {
    fused_kernel<<<256, 512, 0, stream>>>((const u16*)d_in[0], (const u16*)d_in[1], (const u16*)d_in[2],
                                          (const u16*)d_in[4], (const u16*)d_in[5], (const u16*)d_in[6],
                                          (const u16*)d_in[7], (const u16*)d_in[8], (u16*)d_out, (char*)d_ws);
}
```

```cpp
#define DIAG 16
#include <hip/hip_runtime.h>
#include <stdint.h>

typedef unsigned short u16;
typedef unsigned u32;
typedef unsigned long long u64;
typedef short s16x4 __attribute__((ext_vector_type(4)));
typedef short s16x8 __attribute__((ext_vector_type(8)));
typedef __bf16 bf16x2 __attribute__((ext_vector_type(2)));
typedef float f32x2 __attribute__((ext_vector_type(2)));
typedef float f32x4 __attribute__((ext_vector_type(4)));
typedef u32 u32x2 __attribute__((ext_vector_type(2)));
typedef u32 u32x4 __attribute__((ext_vector_type(4)));
#define LAS __attribute__((address_space(3)))
#define DEV __device__ __forceinline__
typedef __amdgpu_buffer_rsrc_t rsrc_t;

constexpr u32 WS_EPOCH = 0;
constexpr u32 WS_F1 = 32768;
constexpr u32 WS_F2 = 65536;
constexpr u32 WS_ST0 = 98304;
constexpr u32 WS_ST1 = 131072;
constexpr u32 WS_ST2 = WS_ST1 + 262144;
constexpr u32 WS_ST3 = WS_ST2 + 262144;
constexpr u32 WS_UT = 1u << 20;
constexpr u32 WS_O1 = WS_UT + (2u << 20);
constexpr u32 WS_O2 = WS_O1 + (2u << 20);
constexpr u32 WS_Y2 = WS_O2 + (2u << 20);
constexpr u32 WS_X2 = WS_Y2 + (2u << 20);
constexpr u32 WS_END = WS_X2 + (2u << 20);

constexpr u32 L_SLOT = 34816;
constexpr u32 L_KV = 4 * L_SLOT;
constexpr u32 L_K = L_KV, L_V = L_KV + 9216;
constexpr u32 L_SK = L_KV + 17408;
constexpr u32 L_WO = 0;
constexpr u32 L_CX = 65536;
constexpr u32 L_HC = L_CX;
constexpr u32 L_UR = L_CX + 69632;
constexpr u32 L_WX = L_CX + 40960;
constexpr u32 L_EX = 147456;
constexpr u32 L_TAB = L_EX + 12288;
constexpr u32 L_CS = L_TAB + 1024;
constexpr u32 L_FLG = L_TAB + 2048;
constexpr u32 L_DUMMY = L_FLG + 1024;
constexpr u32 L_TOTAL = 160 * 1024;
static_assert(L_DUMMY + 1024 <= L_TOTAL, "LDS map");

constexpr int SPIN_CAP = 1 << 14;
constexpr u32 SENT = 0xFFFFFFFFu;

#define WAIT_VM(n) asm volatile("s_waitcnt vmcnt(%0)" ::"i"(n) : "memory")
#define BAR() do { asm volatile("s_waitcnt lgkmcnt(0)" ::: "memory"); __builtin_amdgcn_s_barrier(); asm volatile("" ::: "memory"); } while (0)
#define RBAR() do { asm volatile("" ::: "memory"); __builtin_amdgcn_s_barrier(); asm volatile("" ::: "memory"); } while (0)
#define CFENCE() asm volatile("" ::: "memory")
#define SB() __builtin_amdgcn_sched_barrier(0)
template <int N> struct IC { static constexpr int value = N; };
template <int I, int N, class F> DEV void sfor(F&& f) { if constexpr (I < N) { f(IC<I>{}); sfor<I + 1, N>(f); } }

DEV f32x4 mfma32(s16x8 a, s16x8 b, f32x4 c) { return __builtin_amdgcn_mfma_f32_16x16x32_bf16(a, b, c, 0, 0, 0); }
DEV f32x4 mfma16(s16x4 a, s16x4 b, f32x4 c) { return __builtin_amdgcn_mfma_f32_16x16x16bf16_1k(a, b, c, 0, 0, 0); }
DEV float vmax(float a, float b) { float r; asm("v_max_f32 %0, %1, %2" : "=v"(r) : "v"(a), "v"(b)); return r; }
DEV u32 pk2(float lo, float hi) { f32x2 f = {lo, hi}; bf16x2 b = __builtin_convertvector(f, bf16x2); return __builtin_bit_cast(u32, b); }
DEV float bflo(u32 w) { return __uint_as_float(w << 16); }
DEV float bfhi(u32 w) { return __uint_as_float(w & 0xffff0000u); }
DEV float dot2(u32 a, u32 b, float c) { return __builtin_amdgcn_fdot2_f32_bf16(__builtin_bit_cast(bf16x2, a), __builtin_bit_cast(bf16x2, b), c, false); }
DEV u32 nosent(u32 w) { return min(w, 0xFFFFFFFEu); }
DEV float xsum(float v) {
    auto r = __builtin_amdgcn_permlane32_swap(__float_as_uint(v), __float_as_uint(v), false, false);
    v = __uint_as_float(r[0]) + __uint_as_float(r[1]);
    auto s = __builtin_amdgcn_permlane16_swap(__float_as_uint(v), __float_as_uint(v), false, false);
    return __uint_as_float(s[0]) + __uint_as_float(s[1]);
}
DEV float xmax(float v) {
    auto r = __builtin_amdgcn_permlane32_swap(__float_as_uint(v), __float_as_uint(v), false, false);
    v = vmax(__uint_as_float(r[0]), __uint_as_float(r[1]));
    auto s = __builtin_amdgcn_permlane16_swap(__float_as_uint(v), __float_as_uint(v), false, false);
    return vmax(__uint_as_float(s[0]), __uint_as_float(s[1]));
}
DEV float rowsum16(float v) {
    v += __builtin_bit_cast(float, __builtin_amdgcn_update_dpp(0, __builtin_bit_cast(int, v), 0xB1, 0xf, 0xf, true));
    v += __builtin_bit_cast(float, __builtin_amdgcn_update_dpp(0, __builtin_bit_cast(int, v), 0x4E, 0xf, 0xf, true));
    v += __builtin_bit_cast(float, __builtin_amdgcn_update_dpp(0, __builtin_bit_cast(int, v), 0x141, 0xf, 0xf, true));
    v += __builtin_bit_cast(float, __builtin_amdgcn_update_dpp(0, __builtin_bit_cast(int, v), 0x140, 0xf, 0xf, true));
    return v;
}
DEV float bcast_row0(float v) { return __builtin_bit_cast(float, __builtin_amdgcn_update_dpp(0, __builtin_bit_cast(int, v), 0x150, 0xf, 0xf, true)); }
template <int OFF> DEV void ald4o(u32x4& d, u32 voff, u32x4 rs) { asm volatile("buffer_load_dwordx4 %0, %1, %2, 0 offen offset:%3 sc1" : "=v"(d) : "v"(voff), "s"(rs), "i"(OFF) : "memory"); }
template <class T> DEV void pin(T& v) { asm volatile("" : "+v"(v)); }
DEV int opq(int v) { asm volatile("" : "+v"(v)); return v; }
DEV s16x8 cat8(s16x4 a, s16x4 b) { return __builtin_shufflevector(a, b, 0, 1, 2, 3, 4, 5, 6, 7); }
DEV s16x4 pack4(f32x4 v) { u32x2 w = {pk2(v[0], v[1]), pk2(v[2], v[3])}; return __builtin_bit_cast(s16x4, w); }
DEV u64 mix64(u64 z) { z += 0x9E3779B97F4A7C15ull; z = (z ^ (z >> 30)) * 0xBF58476D1CE4E5B9ull; z = (z ^ (z >> 27)) * 0x94D049BB133111EBull; return z ^ (z >> 31); }

struct ConvSched {
    int n[4], f[18], i[18], slot[64];
    constexpr ConvSched(int R) : n{}, f{}, i{}, slot{} {
        constexpr int LEAD = 9;
        const int N0[4] = {0, 3, 4, 7};
        for (int k = 0; k < 4; ++k) n[k] = N0[k] ^ R;
        int c = 0;
        for (int ff = 0; ff < 8; ++ff) for (int k = 3; k >= 0; --k) if (ff <= n[k]) { f[c] = ff; i[c] = k; ++c; }
        for (int k = 0; k < 64; ++k) slot[k] = 1000;
        for (int p = 71; p >= 0; --p) {
            const int e = p / 18, ff = f[p % 18], g = n[i[p % 18]] - ff, s = p - LEAD < -1 ? -1 : p - LEAD;
            slot[e * 16 + ff] = s; slot[e * 16 + 8 + g] = s;
        }
    }
};

#define STAMP(i) do { if constexpr ((i) == 0 || (i) == DIAG) { u64 t_; asm volatile("s_memrealtime %0\n\ts_waitcnt lgkmcnt(0)" : "=s"(t_)::"memory"); if ((i) == 0) dg0 = (u32)t_; else dg1 = (u32)t_; } } while (0)
#define CSTAMP(i) do { } while (0)

struct Args { const u16 *gx, *gwqkv, *gwo, *gbk, *gbv, *gsk, *ghk, *gdv; u16* gout; char* ws; };

template <int R>
DEV void body(const Args& A_, LAS char* const lds, const int wave) {
    const u16* gx = A_.gx; char* ws = A_.ws;
    u32 dg0 = 0, dg1 = 0;
    STAMP(0);
    const int b = blockIdx.x, tid = threadIdx.x, lane = tid & 63;
    const int hd = wave & 3;
    const int grp = (b & 7) * 4 + (b >> 6), mem = (b >> 3) & 7;
    const int T0 = grp * 64;
    const int rot = (mem + 2 * (grp & 3)) & 7;

    u64 epoch;
    asm volatile("s_load_dwordx2 %0, %1, 0x0" : "=s"(epoch) : "s"(ws));
    u32 dw;
    asm volatile("s_load_dword %0, %1, 0x0" : "=s"(dw) : "s"(A_.gdv + 2 * b));
    const rsrc_t r_x = __builtin_amdgcn_make_buffer_rsrc((void*)gx, 0, 2048 * 512 * 2, 0x00020000);
    const rsrc_t r_ws = __builtin_amdgcn_make_buffer_rsrc((void*)ws, 0, WS_END, 0x00020000);
    const rsrc_t r_out = __builtin_amdgcn_make_buffer_rsrc((void*)A_.gout, 0, 2048 * 512 * 2, 0x00020000);
    const u32 ldsbase = (u32)(size_t)lds;
    auto mkdesc = [](const void* p, u32 bytes) { const u64 a = (u64)p; return (u32x4){(u32)a, (u32)(a >> 32) & 0xffffu, bytes, 0x00020000u}; };
    const u32x4 d_x = mkdesc(gx, 2048 * 512 * 2), d_w = mkdesc(A_.gwqkv, 512 * 1536 * 2), d_wo = mkdesc(A_.gwo, 512 * 512 * 2);
    const u32x4 d_k = mkdesc(A_.gbk, 2048 * 512 * 2), d_v = mkdesc(A_.gbv, 2048 * 512 * 2), d_sk = mkdesc(A_.gsk, 512 * 4 * 2);
    const u32x4 d_h = mkdesc(A_.ghk, 512 * 2048 * 2), d_x2 = mkdesc(ws + WS_X2, 2u << 20), d_ws = mkdesc(ws, WS_END);
    const u32 hb1k = ldsbase + hd * 1024, hb512 = ldsbase + hd * 512;
#define DMA_ASM(pre, mods, post) asm volatile("s_add_u32 m0, %0, %4\n\t" pre "buffer_load_dwordx4 %1, %2, %3 offen " mods "lds" post \
                     :: "s"(sbase), "v"(voff), "s"(rs), "s"(soff), "i"(IMM) : "memory", "scc")
    const u32 xmlo = hd == 0 ? ~0xFFu : ~0u;
    auto dma = [&](auto kind, auto imm, u32x4 rs, u32 sbase, u32 voff, u32 soff) {
        constexpr int K = decltype(kind)::value; constexpr int IMM = decltype(imm)::value;
        if constexpr (K == 0) DMA_ASM("s_nop 0\n\t", "", "");
        else if constexpr (K == 1) DMA_ASM("s_mov_b32 exec_hi, 0\n\t", "", "\n\ts_mov_b32 exec_hi, -1");
        else if constexpr (K == 2) {
            const u32 ml = xmlo;
            asm volatile("s_add_u32 m0, %0, %4\n\ts_mov_b32 exec_lo, %5\n\tbuffer_load_dwordx4 %1, %2, %3 offen lds\n\ts_mov_b32 exec_lo, -1"
                         :: "s"(sbase), "v"(voff), "s"(rs), "s"(soff), "i"(IMM), "s"(ml) : "memory", "scc");
        }
        else if constexpr (K == 4) DMA_ASM("s_nop 0\n\t", "sc1 ", "");
        else DMA_ASM("s_mov_b32 exec_hi, 0\n\t", "sc1 ", "\n\ts_mov_b32 exec_hi, -1");
    };
    auto ald4 = [&](u32x4& d, u32x4 rs, u32 voff) { asm volatile("buffer_load_dwordx4 %0, %1, %2, 0 offen" : "=v"(d) : "v"(voff), "s"(rs) : "memory"); };
    auto ald4s = [&](u32x4& d, u32 voff) { asm volatile("buffer_load_dwordx4 %0, %1, %2, 0 offen sc1" : "=v"(d) : "v"(voff), "s"(d_ws) : "memory"); };
    auto ald2s = [&](u32x2& d, u32 voff) { asm volatile("buffer_load_dwordx2 %0, %1, %2, 0 offen sc1" : "=v"(d) : "v"(voff), "s"(d_ws) : "memory"); };

    const int pr = lane >> 3, pc = lane & 7;
    const int xr0 = 8 * hd + pr, xr2 = 64 + 4 * hd + pr;
    const u32 xv0 = (u32)((T0 - 16 + xr0) * 1024 + ((pc ^ ((xr0 >> 1) & 7)) << 4));
    const u32 xv2 = (u32)((T0 - 16 + xr2) * 1024 + ((pc ^ ((xr2 >> 1) & 7)) << 4));
    const int wfw = ((xr0 >> 1) & 1) | (((xr0 >> 3) & 1) << 1);
    const u32 wv0 = (u32)(xr0 * 3072 + mem * 128 + ((((pc >> 1) ^ wfw) * 2 + (pc & 1)) << 4));
    auto issue_piece = [&](auto sc, auto ic) {
        constexpr int s = decltype(sc)::value, i = decltype(ic)::value;
        const u32 kp = (u32)((s + rot) & 7);
        constexpr int sb = (s & 3) * L_SLOT;
        if constexpr (i == 0) dma(IC<2>{}, IC<sb>{}, d_x, hb1k, xv0, kp * 128);
        else if constexpr (i == 1) dma(IC<0>{}, IC<sb + 4096>{}, d_x, hb1k, xv0 + 32768, kp * 128);
        else if constexpr (i == 2) dma(IC<1>{}, IC<sb + 8192>{}, d_x, hb512, xv2, kp * 128);
        else dma(IC<0>{}, IC<sb + 10240 + (i - 3) * 4096>{}, d_w, hb1k, wv0, kp * (64 * 3072) + ((i - 3) >> 1) * 1024 + ((i - 3) & 1) * (32 * 3072));
    };
    u32 wov = 0;
    auto issue_wo = [&](auto ic) {
        constexpr int i = decltype(ic)::value;
        if constexpr (i == 0) {
            const int l2 = opq(tid) & 63, pr = l2 >> 3, pc = l2 & 7;
            wov = (u32)((8 * hd + pr) * 1024 + mem * 128 + ((((pc >> 1) ^ (((pr >> 1) & 1) | ((hd & 1) << 1))) * 2 + (pc & 1)) << 4));
            pin(wov);
        }
        const u32 j = (u32)((i + 2 * grp) & 7);
        dma(IC<0>{}, IC<L_WO + (i & 8) * 4096>{}, d_wo, hb1k + j * 4096, wov, ((i & 8) + j) * 32768);
    };
    auto issue_kv = [&](int i) {
        const int l2 = opq(tid) & 63, pr = l2 >> 3, pc = l2 & 7, p = hd + 4 * (i & 1);
        const u32 cb = (u32)(mem * 128);
        if (i < 2) dma(IC<0>{}, IC<L_K>{}, d_k, hb1k + (i & 1) * 4096, (u32)((T0 - 8 + 8 * p + pr) * 1024) + cb + ((pc ^ p) << 4), 0);
        else if (i < 4) dma(IC<0>{}, IC<L_V>{}, d_v, hb1k + (i & 1) * 4096, (u32)((T0 + 8 * p + pr) * 1024) + cb + ((pc ^ p) << 4), 0);
        else if (hd == 0) dma(IC<0>{}, IC<L_K + 8192>{}, d_k, ldsbase, (u32)((T0 - 8 + 64 + pr) * 1024) + cb + (pc << 4), 0);
        else dma(IC<0>{}, IC<L_SK>{}, d_sk, ldsbase, (u32)(mem * 512 + l2 * 16), 0);
    };

    const int srow = T0 + 8 * mem + 2 * hd;
    u32x4 xrow[2];
    sfor<0, 9>([&](auto ic) { if constexpr ((decltype(ic)::value & 1) != R) issue_piece(IC<0>{}, ic); });
    sfor<0, 9>([&](auto ic) { if constexpr ((decltype(ic)::value & 1) != R) issue_piece(IC<1>{}, ic); });
    asm volatile("s_waitcnt lgkmcnt(0)" : "+s"(epoch), "+s"(dw));
    const u64 tag = mix64(epoch);
    const u32 tl = (u32)tag, th = (u32)(tag >> 32);
    if (R == 1 && wave == 7 && lane < 32) *(LAS u32x4*)(lds + (lane >> 3) * L_SLOT + (lane & 7) * 16) = (u32x4){0x3F803F80u, 0x3F803F80u, 0x3F803F80u, 0x3F803F80u};

    auto trr = [&](u32 off) { return __builtin_amdgcn_ds_read_tr16_b64_v4i16((LAS s16x4*)(lds + off)); };
    auto ldsA = [&](u32 off) { return *(LAS s16x8*)(lds + off); };
    auto flag_ok = [&](const u32x2& f) { return __all(f[0] == tl && f[1] == th); };
    auto spin_flags = [&](u32x2& f, u32 off) {
        _Pragma("unroll 1") for (int spin = 0; spin < SPIN_CAP; ++spin) {
            if (__builtin_expect(flag_ok(f), 1)) break;
            __builtin_amdgcn_s_sleep(2);
            asm volatile("buffer_load_dwordx2 %0, %1, %2, 0 offen sc1\n\ts_waitcnt vmcnt(0)" : "=&v"(f) : "v"(off), "s"(d_ws) : "memory");
        }
    };

    s16x8 Wq[16], Wm[16];
    f32x4 accM[5], accQ[5];
    const f32x4 zero4 = {0.f, 0.f, 0.f, 0.f};
    const s16x8 ones8 = {0x3F80, 0x3F80, 0x3F80, 0x3F80, 0x3F80, 0x3F80, 0x3F80, 0x3F80};
    s16x8 Ab[3][5];
    auto qkv_step = [&](auto lc, int st, const s16x8 (&A)[5], auto fill) {
        sfor<0, 5>([&](auto mc) {
            constexpr int mt = decltype(mc)::value;
            const f32x4 cm = st ? accM[mt] : zero4;
            accM[mt] = R ? mfma32(A[mt], Wm[st], cm) : mfma32(Wm[st], A[mt], cm);
            if constexpr (R == 0 && (mt > 0 || decltype(lc)::value == 0)) accQ[mt] = mfma32(Wq[st], A[mt], st ? accQ[mt] : zero4);
            fill(mc);
        });
    };

    float m1[5], r1[5]; f32x4 m4[5], r4[5], csK, csQ;
    auto load_tab = [&](bool cs) {
        const int l2 = opq(tid) & 63;
        u32 ta = L_TAB + (R ? (l2 >> 4) * 16 : (l2 & 15) * 4);
        pin(ta);
        if (R == 0 && cs) { csK = *(LAS f32x4*)(lds + L_CS + hd * 128 + (l2 >> 4) * 16); csQ = *(LAS f32x4*)(lds + L_CS + hd * 128 + 64 + (l2 >> 4) * 16); }
#pragma unroll
        for (int mt = 0; mt < 5; ++mt) {
            if (R == 0) { m1[mt] = *(LAS float*)(lds + ta + mt * 64); r1[mt] = *(LAS float*)(lds + ta + 320 + mt * 64); }
            else { m4[mt] = *(LAS f32x4*)(lds + ta + mt * 64); r4[mt] = *(LAS f32x4*)(lds + ta + 320 + mt * 64); }
        }
    };

    u32x4 hr[4];
    u32x2 flg;
    {
        const int li = lane & 15, lq = lane >> 4, tq = li >> 2, tp = lane & 3;
        const u32 xa0 = (u32)(li * 128 + ((lq ^ (li >> 1)) << 4));
        const u32 wa = (u32)((8 * lq + tq) * 128 + ((hd ^ (((tq >> 1) & 1) | ((lq & 1) << 1))) << 5) + tp * 8);
        u32 xa, wb, kw[11], vw[8];
        u32x4 s0v, skw;
        auto pf_addr = [&](int st) {
            const u32 sb = (u32)((st >> 1) & 3) * L_SLOT;
            xa = sb + (xa0 ^ ((st & 1) * 64)); wb = sb + 10240 + (st & 1) * 4096 + wa;
            asm volatile("" : "+v"(xa), "+v"(wb));
        };
        auto pf_part = [&](int st, int mt) {
            if (mt == 0) Wm[st] = cat8(trr(wb + (1 + R) * 8192), trr(wb + (1 + R) * 8192 + 512));
            Ab[st & 1][mt] = ldsA(xa + mt * 2048);
            if (R == 0 && mt == 1) Wq[st] = cat8(trr(wb), trr(wb + 512));
        };
        STAMP(15);
        WAIT_VM(R ? 5 : 4);
        BAR();
        STAMP(16);
        pf_addr(0);
#pragma unroll
        for (int mt = 0; mt < 5; ++mt) pf_part(0, mt);
        sfor<0, 16>([&](auto stc) {
            constexpr int st = decltype(stc)::value;
            if constexpr ((st & 1) && st < 15) {
                constexpr int s1 = (st >> 1) + 1;
                if constexpr (R == 0) {
                    if constexpr (s1 == 1) WAIT_VM(7);
                    if constexpr (s1 == 2) WAIT_VM(1);
                    if constexpr (s1 == 5) WAIT_VM(0);
                } else {
                    constexpr int WN[8] = {0, 9, 9, 9, 9, 9, 10, 10};
                    WAIT_VM(WN[s1]);
                    if constexpr (s1 == 7) {
                        pin(s0v);
                        const int l2 = opq(tid) & 63, tr = hd * 20 + min(l2, 19), row = T0 - 16 + tr;
                        _Pragma("unroll 1") for (int spin = 0; spin < SPIN_CAP; ++spin) {
                            if (__builtin_expect(__all(row < 0 || (s0v[1] == tl && s0v[3] == th)), 1)) break;
                            __builtin_amdgcn_s_sleep(2);
                            ald4s(s0v, WS_ST0 + (u32)(max(row, 0) * 16));
                            WAIT_VM(0);
                            pin(s0v);
                        }
                        *(LAS u32*)(lds + L_TAB + tr * 4) = row < 0 ? 0u : s0v[0];
                        *(LAS u32*)(lds + L_TAB + 320 + tr * 4) = row < 0 ? 0x3F800000u : s0v[2];
                        asm volatile("s_waitcnt lgkmcnt(0)" ::: "memory");
                    }
                }
                RBAR();
                STAMP(16 + s1);
                if constexpr (s1 == 5 && R == 0) { if (tid == 0) __builtin_amdgcn_raw_buffer_store_b64((u32x2){tl, th}, r_ws, WS_F1 + (mem * 32 + grp) * 128, 0, 16); CFENCE(); }
                if constexpr (s1 == 5 && R == 1) { const int row = T0 - 16 + hd * 20 + min(opq(tid) & 63, 19); ald4s(s0v, WS_ST0 + (u32)(max(row, 0) * 16)); }
            }
            if (st < 15) pf_addr(st + 1);
            if constexpr (st == 14) load_tab(false);
            SB();
            qkv_step(IC<0>{}, st, Ab[st & 1], [&](auto mc) {
                constexpr int mt = decltype(mc)::value;
                if (st < 15) pf_part(st + 1, mt);
                if constexpr (R == 1) {
                    if constexpr (st == 0) { issue_piece(IC<2>{}, IC<2 * mt>{}); if constexpr (mt < 4) issue_piece(IC<2>{}, IC<2 * mt + 1>{}); }
                    if constexpr (st >= 1 && st <= 10 && ((st - 1) & 1) * 5 + mt < 9) issue_piece(IC<((st - 1) >> 1) + 3>{}, IC<((st - 1) & 1) * 5 + mt>{});
                    if constexpr (st == 11 && mt == 0) { const u32 ho = (u32)(b * 8192 + opq(tid) * 16); ald4(hr[2], d_h, ho); ald4(hr[3], d_h, ho + 16); }
                    if constexpr (st == 13 && mt == 0) { if (wave == 7) { const int l2 = opq(tid) & 63; ald2s(flg, WS_F1 + (u32)((l2 < 32 ? (b >> 5) * 32 + l2 : (l2 & 7) * 32 + grp) * 128)); } }
                    if constexpr (st >= 11 && st <= 14 && mt < 4) issue_wo(IC<(st - 11) * 4 + mt>{});
                } else {
                    if constexpr (st == 0) {
                        if constexpr (mt == 0) { const u32 xo = (u32)(srow * 1024 + (opq(tid) & 63) * 16); ald4(xrow[0], d_x, xo); ald4(xrow[1], d_x, xo + 1024); }
                        issue_kv(mt);
                    }
                    if constexpr (st == 11 && mt < 2) { const u32 ho = (u32)(b * 8192 + mt * 4096 + opq(tid) * 16); ald4(hr[2 * mt], d_h, ho); ald4(hr[2 * mt + 1], d_h, ho + 16); }
                }
                SB();
            });
            if constexpr (R == 0 && st == 2) {
                WAIT_VM(5);
                pin(xrow[0]); pin(xrow[1]);
                float s[2] = {0.f, 0.f}, q[2] = {0.f, 0.f};
#pragma unroll
                for (int i = 0; i < 4; ++i)
#pragma unroll
                    for (int k = 0; k < 2; ++k) { s[k] = dot2(xrow[k][i], 0x3F803F80u, s[k]); q[k] = dot2(xrow[k][i], xrow[k][i], q[k]); }
                auto ss = __builtin_amdgcn_permlane32_swap(__float_as_uint(s[0]), __float_as_uint(s[1]), false, false);
                auto qq = __builtin_amdgcn_permlane32_swap(__float_as_uint(q[0]), __float_as_uint(q[1]), false, false);
                float sv1 = rowsum16(__uint_as_float(ss[0]) + __uint_as_float(ss[1])), qv1 = rowsum16(__uint_as_float(qq[0]) + __uint_as_float(qq[1]));
                auto s2 = __builtin_amdgcn_permlane16_swap(__float_as_uint(sv1), __float_as_uint(sv1), false, false);
                auto q2 = __builtin_amdgcn_permlane16_swap(__float_as_uint(qv1), __float_as_uint(qv1), false, false);
                const float m = (__uint_as_float(s2[0]) + __uint_as_float(s2[1])) * (1.f / 512.f);
                const float r = __builtin_amdgcn_rsqf(fmaxf((__uint_as_float(q2[0]) + __uint_as_float(q2[1])) * (1.f / 512.f) - m * m, 0.f) + 1e-5f);
                const u32x4 sv = {__float_as_uint(m), tl, __float_as_uint(r), th};
                const int l2 = opq(tid) & 63;
                __builtin_amdgcn_raw_buffer_store_b128(sv, r_ws, (l2 & 31) == 0 ? WS_ST0 + (u32)((srow + (l2 >> 5)) * 16) : 0xFFFFFF00u, 0, 16);
                CFENCE();
                SB();
            }
            if constexpr (R == 0 && (st == 4 || st == 6)) {
                const int l2 = opq(tid) & 63, tg = l2 & 7, cp = 8 * hd + (l2 >> 3);
                if constexpr (st == 4) {
                    skw = *(LAS u32x4*)(lds + L_SK + cp * 16);
                    u32 ka = L_K + tg * 1024 + ((((cp >> 2) ^ tg) & 7) << 4) + (cp & 3) * 4;
                    u32 kb = L_K + tg * 1024 + ((((cp >> 2) ^ (tg + 1)) & 7) << 4) + (cp & 3) * 4;
                    pin(ka); pin(kb);
#pragma unroll
                    for (int i = 0; i < 11; ++i) kw[i] = *(LAS u32*)(lds + ((i + 5 < 8) ? ka : kb) + (i + 5) * 128);
#pragma unroll
                    for (int i = 0; i < 8; ++i) vw[i] = *(LAS u32*)(lds + ka + (L_V - L_K) + i * 128);
                    SB();
                }
                constexpr bool HI = st == 6;
                auto ex = [&](u32 w) { return HI ? bfhi(w) : bflo(w); };
                const float a0 = bflo(skw[2 * HI]), a1 = bfhi(skw[2 * HI]), a2 = bflo(skw[2 * HI + 1]), a3 = bfhi(skw[2 * HI + 1]);
                float ua[8];
#pragma unroll
                for (int i = 0; i < 8; ++i) ua[i] = (ex(kw[i]) * a3 + ex(kw[i + 1]) * a2 + ex(kw[i + 2]) * a1 + ex(kw[i + 3]) * a0) * ex(vw[i]);
                const u32x4 oa = {pk2(ua[7], ua[6]), pk2(ua[5], ua[4]), pk2(ua[3], ua[2]), pk2(ua[1], ua[0])};
                __builtin_amdgcn_raw_buffer_store_b128(oa, r_ws, WS_UT + (u32)((mem * 64 + 2 * cp) * 4096 + (2040 - T0 - 8 * tg) * 2), HI * 4096, 16);
                __builtin_amdgcn_raw_buffer_store_b128((u32x4){SENT, SENT, SENT, SENT}, r_ws, (u32)((4 * grp + hd) * 16384 + mem * 2048 + HI * 1024 + l2 * 16), WS_O1, 16);
                CFENCE();
                SB();
            }
        });
    }
    STAMP(R ? 1 : 53);

    u32x4 uu;
    float csV;
    auto attention = [&](auto lc) {
        constexpr int LAYER = decltype(lc)::value;
        constexpr int SBASE = LAYER ? 32 : 28;
        const int lane = opq(tid) & 63, li = lane & 15, lq = lane >> 4;
        u32 ex = L_EX + hd * 2048 + lane * 8;
        pin(ex);
        u32 f1ok = 1;
        if constexpr (R == 0) {
            if constexpr (LAYER == 0) {
#pragma unroll
                for (int i = 0; i < 4; ++i) { csK[i] = bcast_row0(accM[0][i]); csQ[i] = bcast_row0(accQ[0][i]); }
                if (li == 0) { *(LAS f32x4*)(lds + L_CS + hd * 128 + lq * 16) = csK; *(LAS f32x4*)(lds + L_CS + hd * 128 + 64 + lq * 16) = csQ; }
            }
            const int par = li & 1;
            const bool cur0 = (4 * lq + par) <= li, cur1 = (4 * lq + par + 2) <= li;
            s16x4 Kb[5], Qb[5];
#pragma unroll
            for (int mt = 0; mt < 5; ++mt) {
                Kb[mt] = pack4((accM[mt] - m1[mt] * csK) * r1[mt]);
                if (mt) Qb[mt] = pack4(accQ[mt] - m1[mt] * csQ);
            }
            f32x4 Sc[5], Sp[5];
#pragma unroll
            for (int mt = 1; mt < 5; ++mt) { Sc[mt] = mfma16(Kb[mt], Qb[mt], zero4); Sp[mt] = mfma16(Kb[mt - 1], Qb[mt], zero4); }
            float s0[5], s1[5], mx[5];
#pragma unroll
            for (int mt = 1; mt < 5; ++mt) {
                const float sp0 = Sp[mt][0], sp1 = Sp[mt][1], sp2 = Sp[mt][2], sp3 = Sp[mt][3], c0 = Sc[mt][0], c1 = Sc[mt][1], c2 = Sc[mt][2], c3 = Sc[mt][3];
                float p0 = par ? sp1 : sp0, p1 = par ? sp3 : sp2, q0 = par ? c1 : c0, q1 = par ? c3 : c2;
                asm volatile("" : "+v"(p0), "+v"(p1), "+v"(q0), "+v"(q1));
                if (mt == 1 && grp == 0) { p0 = -3.0e38f; p1 = -3.0e38f; }
                s0[mt] = cur0 ? q0 : p0; s1[mt] = cur1 ? q1 : p1;
                mx[mt] = vmax(s0[mt], s1[mt]);
            }
#pragma unroll
            for (int mt = 1; mt < 5; ++mt) { auto r = __builtin_amdgcn_permlane32_swap(__float_as_uint(mx[mt]), __float_as_uint(mx[mt]), false, false); mx[mt] = vmax(__uint_as_float(r[0]), __uint_as_float(r[1])); }
#pragma unroll
            for (int mt = 1; mt < 5; ++mt) { auto r = __builtin_amdgcn_permlane16_swap(__float_as_uint(mx[mt]), __float_as_uint(mx[mt]), false, false); mx[mt] = vmax(__uint_as_float(r[0]), __uint_as_float(r[1])); }
#pragma unroll
            for (int mt = 1; mt < 5; ++mt) {
                const float sc = r1[mt] * (0.25f * 1.44269504088896341f);
                const float e0 = __builtin_amdgcn_exp2f((s0[mt] - mx[mt]) * sc), e1 = __builtin_amdgcn_exp2f((s1[mt] - mx[mt]) * sc);
                *(LAS u32x2*)(lds + ex + (mt - 1) * 512) = (u32x2){pk2(e0, e0), pk2(e1, e1)};
            }
            STAMP(SBASE + 2);
            BAR();
            STAMP(SBASE + 1);
            if constexpr (LAYER == 0) f1ok = __builtin_amdgcn_readfirstlane(*(LAS u32*)(lds + L_FLG));
        } else {
            if constexpr (LAYER == 0) {
                auto s = __builtin_amdgcn_permlane16_swap(__float_as_uint(accM[0][0]), __float_as_uint(accM[0][0]), false, false);
                auto t = __builtin_amdgcn_permlane32_swap(s[0], s[0], false, false);
                csV = __uint_as_float(t[0]);
                if (wave == 7) { WAIT_VM(8); pin(flg); const u32 ok = flag_ok(flg); *(LAS u32*)(lds + L_FLG) = ok; }
            }
            const int par = li & 1;
            const bool cur0 = (4 * lq + par) <= li, cur1 = (4 * lq + par + 2) <= li;
            const u32 pm = par ? 0xffff0000u : 0x0000ffffu;
            u32 mC0 = cur0 ? pm : 0u, mP0 = cur0 ? 0u : pm, mC1 = cur1 ? pm : 0u, mP1 = cur1 ? 0u : pm;
            u32 oo = (LAYER ? WS_O2 : WS_O1) + (u32)((4 * grp + (lq & 1)) * 16384 + (4 * mem + hd) * 512 + (lq >> 1) * 256 + li * 16);
            asm volatile("" : "+v"(mC0), "+v"(mP0), "+v"(mC1), "+v"(mP1), "+v"(oo));
            s16x4 Vb[5];
#pragma unroll
            for (int mt = 0; mt < 5; ++mt) { Vb[mt] = pack4((accM[mt] - m4[mt] * csV) * r4[mt]); pin(Vb[mt]); }
            STAMP(SBASE);
            BAR();
            STAMP(SBASE + 1);
            u32x2 pw[4];
#pragma unroll
            for (int j = 0; j < 4; ++j) pw[j] = *(LAS u32x2*)(lds + ex + j * 512);
            if constexpr (LAYER == 0) {
                f1ok = __builtin_amdgcn_readfirstlane(*(LAS u32*)(lds + L_FLG));
                if (f1ok) ald4s(uu, WS_UT + (u32)(b * 8192 + opq(tid) * 16));
            }
            SB();
            f32x4 O[4], L[4];
#pragma unroll
            for (int j = 0; j < 4; ++j) {
                const s16x8 P = __builtin_bit_cast(s16x8, (u32x4){pw[j][0] & mC0, pw[j][1] & mC1, pw[j][0] & mP0, pw[j][1] & mP1});
                L[j] = mfma32(ones8, P, zero4);
                O[j] = mfma32(cat8(Vb[j + 1], Vb[j]), P, zero4);
            }
            u32 ow[4][2];
#pragma unroll
            for (int j = 0; j < 4; ++j) {
                const float inv = __builtin_amdgcn_rcpf(L[j][0]);
                ow[j][0] = nosent(pk2(O[j][0] * inv, O[j][1] * inv)); ow[j][1] = nosent(pk2(O[j][2] * inv, O[j][3] * inv));
            }
#pragma unroll
            for (int j = 0; j < 4; j += 2) {
                auto r0 = __builtin_amdgcn_permlane16_swap(ow[j][0], ow[j + 1][0], false, false);
                auto r1 = __builtin_amdgcn_permlane16_swap(ow[j][1], ow[j + 1][1], false, false);
                __builtin_amdgcn_raw_buffer_store_b128((u32x4){r0[0], r1[0], r0[1], r1[1]}, r_ws, oo, j * 16384, 16);
            }
            CFENCE();
        }
        if constexpr (LAYER == 0 && R == 0) { if (f1ok) ald4s(uu, WS_UT + (u32)(b * 8192 + opq(tid) * 16)); }
        SB();
        return f1ok;
    };
    const u32 f1ok = attention(IC<0>{});
    {
        const u32x4 sv = {SENT, SENT, SENT, SENT};
        const int t2 = opq(tid);
        const u32 tb = (u32)((4 * grp + hd) * 16384 + mem * 2048 + R * 1024 + (t2 & 63) * 16);
        __builtin_amdgcn_raw_buffer_store_b128(sv, r_ws, tb, WS_O2, 16);
        __builtin_amdgcn_raw_buffer_store_b128(sv, r_ws, tb, WS_X2, 16);
        __builtin_amdgcn_raw_buffer_store_b128(sv, r_ws, (u32)(b * 8192 + t2 * 16), WS_Y2, 16);
        CFENCE();
        SB();
    }
    STAMP(2);

    const int TI = 4 * grp + hd;
    u32x4 of[8];
    auto load_o = [&](u32 ws_o) {
        const int lane = opq(tid) & 63;
#pragma unroll
        for (int i = 0; i < 8; ++i) of[i] = __builtin_amdgcn_raw_buffer_load_b128(r_ws, ws_o + (u32)(TI * 16384 + R * 8192 + lane * 16) + (i & 3) * 1024, (i >> 2) * 4096, 16);
    };
    u32 xw[8], yw[8];
    u32 woa[4];
    auto resid = [&](int n) { return R ? zero4 : (f32x4){bflo(xw[2 * n]), bfhi(xw[2 * n]), bflo(xw[2 * n + 1]), bfhi(xw[2 * n + 1])}; };
    s16x8 F[8][4];
    auto wo_addr = [&]() {
        const int l2 = opq(tid) & 63, lq = l2 >> 4, tq = (l2 & 15) >> 2, tp = l2 & 3;
#pragma unroll
        for (int n = 0; n < 4; ++n) woa[n] = L_WO + (u32)(R * 32768 + (8 * lq + tq) * 128 + ((n ^ (((lq & 1) << 1) | (tq >> 1))) << 5) + tp * 8);
    };
    auto load_F = [&](int ks) {
#pragma unroll
        for (int n = 0; n < 4; ++n) F[ks][n] = cat8(trr(woa[n] + ks * 4096), trr(woa[n] + ks * 4096 + 512));
    };

    {
        const int t2 = opq(tid), lane = t2 & 63, li = lane & 15, lq = lane >> 4;
        constexpr u32 OFFS = 0xEDA85410u;
        {
            const int j = t2 & 255;
            {
                if constexpr (R == 0) { if (f1ok) WAIT_VM(4); else WAIT_VM(3); }
                else WAIT_VM(5);
                u32 cb = L_HC + R * 34816 + (j + 2) * 16;
                pin(cb);
#pragma unroll
                for (int ch = R; ch < 2; ++ch) {
                    pin(hr[2 * ch]); pin(hr[2 * ch + 1]);
                    const u32 d[8] = {hr[2 * ch][0], hr[2 * ch][1], hr[2 * ch][2], hr[2 * ch][3], hr[2 * ch + 1][0], hr[2 * ch + 1][1], hr[2 * ch + 1][2], hr[2 * ch + 1][3]};
                    u32 e[7];
#pragma unroll
                    for (int i = 0; i < 7; ++i) e[i] = __builtin_amdgcn_alignbit(d[i + 1], d[i], 16);
                    if (ch > R) { cb += 34816; pin(cb); }
#pragma unroll
                    for (int p = (R ? 4 : 0); p < ((ch && !R) ? 4 : 8); ++p) {
                        const u32 po = (272 * p + ((OFFS >> (4 * p)) & 15)) * 16;
                        const int h = p >> 1;
                        const u32x4 w = (p & 1) ? (u32x4){e[h], e[h + 1], e[h + 2], e[h + 3]} : (u32x4){d[h], d[h + 1], d[h + 2], d[h + 3]};
                        *(LAS u32x4*)(lds + cb + po) = w;
                    }
                }
            }
            if constexpr (R == 0) {
                if (hd == 0) {
                    u32 hh[4];
#pragma unroll
                    for (int i = 0; i < 4; ++i) { const u32 a = __builtin_amdgcn_readfirstlane(hr[0][i]), c = __builtin_amdgcn_readfirstlane(hr[2][i]); hh[i] = (lane & 16) ? c : a; }
                    if (lane < 32) {
                        const int p = lane & 7;
                        const u32 po = (272 * p + ((OFFS >> (4 * p)) & 15)) * 16;
                        const unsigned __int128 hv = ((unsigned __int128)(((u64)hh[3] << 32) | hh[2]) << 64) | (((u64)hh[1] << 32) | hh[0]);
                        const unsigned __int128 sv = (p == 0 || !(lane & 8)) ? (unsigned __int128)0 : (hv << (16 * (8 - p)));
                        const u32x4 w = {(u32)sv, (u32)(sv >> 32), (u32)(sv >> 64), (u32)(sv >> 96)};
                        *(LAS u32x4*)(lds + L_HC + (lane >> 4) * 34816 + po + ((lane >> 3) & 1) * 16) = w;
                    }
                }
            }
            if (hd == 1 && lane < 30) *(LAS u32x4*)(lds + L_UR + R * 4576 + 4096 + lane * 16) = (u32x4){0, 0, 0, 0};
            STAMP(3);
            if (!f1ok) {
                const u32 fo = WS_F1 + (u32)(((lane & 8) ? (lane & 7) * 32 + grp : (b >> 5) * 32 + 31 - hd * 8 - (lane & 7)) * 128);
                flg = (u32x2){0u, 0u};
                spin_flags(flg, fo);
                ald4s(uu, WS_UT + (u32)(b * 8192 + t2 * 16));
            }
            if (f1ok) WAIT_VM(R ? 5 : 3); else WAIT_VM(0);
            pin(uu);
            *(LAS u32x4*)(lds + L_UR + R * 4576 + j * 16) = uu;
        }
        BAR();
        STAMP(4);
        const int ch = hd >> 1, e0 = (hd & 1) * 4;
        const int p = (li + 1) & 7;
        u32 aA = L_HC + ch * 34816 + (272 * p + ((OFFS >> (4 * p)) & 15) + ((li + 1) >> 3) + lq + 4 * e0) * 16;
        u32 aB = L_UR + ch * 4576 + (254 - 224 - 2 * li + lq + 4 * e0) * 16;
        asm volatile("" : "+v"(aA), "+v"(aB));
        f32x4 y[4];
        u32 ooff = WS_O1 + (u32)(TI * 16384 + R * 8192 + lane * 16);
        {
            constexpr ConvSched S(R);
            s16x8 A[4][8], B[4][8];
            auto loads_at = [&](auto pc) {
                constexpr int p = decltype(pc)::value;
                sfor<0, 64>([&](auto ic) {
                    constexpr int i = decltype(ic)::value, e = i >> 4, f = i & 7;
                    if constexpr (S.slot[i] == p) {
                        if constexpr (i & 8) B[e][f] = ldsA(aB + 64 * e + 512 * (7 - f)); else A[e][f] = ldsA(aA + 64 * (8 * f + e));
                    }
                });
            };
            loads_at(IC<-1>{});
            SB();
            sfor<0, 72>([&](auto pc) {
                constexpr int p = decltype(pc)::value, e = p / 18, f = S.f[p % 18], i = S.i[p % 18], g = S.n[i] - f;
                y[i] = mfma32(A[e][f], B[e][g], (e == 0 && f == 0) ? zero4 : y[i]);
                loads_at(pc);
                if constexpr (p == 54) WAIT_VM(0);
                if constexpr (p >= 55 && p < 71 && (p & 1)) {
                    constexpr int i = (p - 55) >> 1;
                    ald4o<(i & 3) * 1024>(of[i], ooff + (i >> 2) * 4096, d_ws);
                }
                SB();
            });
        }
        STAMP(14);
        RBAR();
        if (R == 0 && tid == 0) __builtin_amdgcn_raw_buffer_store_b64((u32x2){tl, th}, r_ws, WS_F2 + (grp * 8 + mem) * 128, 0, 16);
        {
            constexpr int N0[4] = {0, 3, 4, 7};
            u32 ra = L_HC + hd * 8192 + lane * 16;
            asm volatile("" : "+v"(ra));
#pragma unroll
            for (int i = 0; i < 4; ++i) *(LAS f32x4*)(lds + ra + (N0[i] ^ R) * 1024) = y[i];
        }
        SB();
        if constexpr (R == 0) {
#pragma unroll
            for (int n = 0; n < 4; ++n) { const u32x2 w = __builtin_amdgcn_raw_buffer_load_b64(r_x, (u32)((TI * 16 + li) * 1024 + mem * 128 + lq * 8) + n * 32, 0, 0); xw[2 * n] = w[0]; xw[2 * n + 1] = w[1]; }
        }
        wo_addr();
        SB();
        STAMP(37);
        BAR();
        STAMP(38);
        {
            const float D0 = bflo(dw), D1 = bfhi(dw);
            u32 ra = L_HC + wave * 1024 + lane * 16;
            const int tb = 256 * wave + 16 * li + 4 * lq;
            u32 ru = L_UR + (2044 - tb) * 2;
            asm volatile("" : "+v"(ra), "+v"(ru));
            const f32x4 y0 = *(LAS f32x4*)(lds + ra), y1 = *(LAS f32x4*)(lds + ra + 8192), y2 = *(LAS f32x4*)(lds + ra + 16384), y3 = *(LAS f32x4*)(lds + ra + 24576);
            const u32x2 ua = *(LAS u32x2*)(lds + ru), ub = *(LAS u32x2*)(lds + ru + 4576);
            load_F(0); load_F(1);
            SB();
            const f32x4 ya = y0 + y1, yb = y2 + y3;
            const u32x4 w = {nosent(pk2(ya[0] + bfhi(ua[1]) * D0, yb[0] + bfhi(ub[1]) * D1)), nosent(pk2(ya[1] + bflo(ua[1]) * D0, yb[1] + bflo(ub[1]) * D1)),
                             nosent(pk2(ya[2] + bfhi(ua[0]) * D0, yb[2] + bfhi(ub[0]) * D1)), nosent(pk2(ya[3] + bflo(ua[0]) * D0, yb[3] + bflo(ub[0]) * D1))};
            __builtin_amdgcn_raw_buffer_store_b128(w, r_ws, WS_Y2 + (u32)(b * 8192 + tb * 4), 0, 16);
        }
        SB();
        STAMP(5);
        WAIT_VM(1);
        STAMP(40);

        u32 f2oth = 0;
        _Pragma("unroll 1") for (int attempt = 0; attempt < 4096; ++attempt) {
            sfor<0, 8>([&](auto ic) { pin(of[decltype(ic)::value]); });
            u32 m0 = 0, m1 = 0;
#pragma unroll
            for (int ks = 0; ks < 8; ++ks) {
                load_F((ks + 2) & 7);
                if (R == 1 && ks == 4 && wave == 7) {
                    const int pb = 32 * mem + (lane & 31);
                    f2oth = WS_F2 + (u32)((lane < 32 ? ((pb & 7) * 4 + (pb >> 6)) * 8 + ((pb >> 3) & 7) : max(grp * 8 - 8, 0) + (lane & 15)) * 128);
                    ald2s(flg, f2oth);
                }
                SB();
#pragma unroll
                for (int n = 0; n < 4; ++n) accM[n] = mfma32(F[ks][n], __builtin_bit_cast(s16x8, of[ks]), ks ? accM[n] : resid(n));
                m0 = max(max(m0, of[ks][0]), of[ks][1]); m1 = max(max(m1, of[ks][2]), of[ks][3]);
                SB();
            }
            if (__builtin_expect(__all(max(m0, m1) != SENT), 1)) break;
            __builtin_amdgcn_s_sleep(4);
            WAIT_VM(0);
            sfor<0, 8>([&](auto ic) { constexpr int i = decltype(ic)::value; ald4o<(i & 3) * 1024>(of[i], ooff + (i >> 2) * 4096, d_ws); });
            WAIT_VM(0);
        }
        STAMP(41);
        if constexpr (R == 1) { if (wave == 7) { WAIT_VM(0); pin(flg); spin_flags(flg, f2oth); } }
    }

    auto load_y = [&]() {
        const int l2 = opq(tid) & 63;
#pragma unroll
        for (int i = 0; i < 8; ++i)
            yw[i] = __builtin_amdgcn_raw_buffer_load_b32(r_ws, WS_Y2 + (u32)((32 * mem + 2 * (l2 >> 4)) * 8192 + (TI * 16 + (l2 & 15)) * 4), (8 * (i >> 1) + (i & 1)) * 8192, 16);
    };
    auto pub_off = [&](u32 ws_st) {
        const int lane = opq(tid) & 63;
        return (lane & 16) ? 0xFFFFFF00u : ws_st + (u32)((TI * 8 + mem) * 256 + (lane & 15) * 16 + (lane >> 5) * 8);
    };
    auto pub_stat = [&](u32 off, u32 tg, float s, float q) {
        auto r = __builtin_amdgcn_permlane32_swap(__float_as_uint(s), __float_as_uint(q), false, false);
        const float v = __uint_as_float(r[0]) + __uint_as_float(r[1]);
        auto t = __builtin_amdgcn_permlane16_swap(__float_as_uint(v), __float_as_uint(v), false, false);
        __builtin_amdgcn_raw_buffer_store_b64((u32x2){__float_as_uint(__uint_as_float(t[0]) + __uint_as_float(t[1])), tg}, r_ws, off, 0, 16);
    };
    auto stat_off = [&](u32 ws_st, int ti) { const int l2 = opq(tid) & 63; return ws_st + (u32)(ti * 2048 + (l2 >> 4) * 512 + (l2 & 15) * 16); };
    auto fin_stat = [&](u32x4& a, u32x4& c, u32 off, float& mean, float& rstd) {
        _Pragma("unroll 1") for (int spin = 0; spin < SPIN_CAP; ++spin) {
            if (__builtin_expect(__all(((a[1] ^ tl) | (a[3] ^ th) | (c[1] ^ tl) | (c[3] ^ th)) == 0), 1)) break;
            __builtin_amdgcn_s_sleep(1);
            asm volatile("buffer_load_dwordx4 %0, %2, %3, 0 offen sc1\n\tbuffer_load_dwordx4 %1, %2, %3, 0 offen offset:256 sc1\n\ts_waitcnt vmcnt(0)"
                         : "=&v"(a), "=&v"(c) : "v"(off), "s"(d_ws) : "memory");
        }
        auto r = __builtin_amdgcn_permlane32_swap(__float_as_uint(__uint_as_float(a[0]) + __uint_as_float(c[0])), __float_as_uint(__uint_as_float(a[2]) + __uint_as_float(c[2])), false, false);
        const float v = __uint_as_float(r[0]) + __uint_as_float(r[1]);
        auto t = __builtin_amdgcn_permlane16_swap(__float_as_uint(v), __float_as_uint(v), false, false);
        const u32 v2 = __float_as_uint(__uint_as_float(t[0]) + __uint_as_float(t[1]));
        auto u = __builtin_amdgcn_permlane32_swap(v2, v2, false, false);
        const float ts = __uint_as_float(u[0]), tq = __uint_as_float(u[1]);
        mean = ts * (1.f / 512.f);
        rstd = __builtin_amdgcn_rsqf(fmaxf(tq * (1.f / 512.f) - mean * mean, 0.f) + 1e-5f);
    };
    auto regroup = [&](u32 (&w)[8]) {
#pragma unroll
        for (int j = 0; j < 2; ++j) {
            auto r0 = __builtin_amdgcn_permlane16_swap(w[4 * j], w[4 * j + 2], false, false);
            auto r1 = __builtin_amdgcn_permlane16_swap(w[4 * j + 1], w[4 * j + 3], false, false);
            w[4 * j] = r0[0]; w[4 * j + 1] = r1[0]; w[4 * j + 2] = r0[1]; w[4 * j + 3] = r1[1];
        }
    };
    auto wo_finish = [&](auto lc) {
        constexpr int LAYER = decltype(lc)::value;
        const int lane = opq(tid) & 63, li = lane & 15, lq = lane >> 4;
        u32 wx = L_WX + hd * 4096 + lane * 16;
        pin(wx);
        u32 po = 0, so = 0, tg = 0, oa = 0;
        if constexpr (R == 1) {
#pragma unroll
            for (int n = 0; n < 4; ++n) *(LAS f32x4*)(lds + wx + n * 1024) = accM[n];
        } else {
            po = pub_off(LAYER ? WS_ST3 : WS_ST1); so = stat_off(LAYER ? WS_ST3 : WS_ST1, TI); tg = lane < 32 ? tl : th;
            const int c = 2 * (lq & 1) + (lq >> 1);
            oa = LAYER ? (u32)((TI * 16 + li) * 1024 + mem * 128 + c * 16) : WS_X2 + (u32)((TI * 8 + mem) * 2048 + li * 128 + ((c ^ (li >> 1)) << 4));
            pin(po); pin(so); pin(tg); pin(oa);
        }
        BAR();
        STAMP(LAYER ? 25 : 42);
        if constexpr (R == 0) {
            f32x4 t[4];
#pragma unroll
            for (int n = 0; n < 4; ++n) t[n] = *(LAS f32x4*)(lds + wx + n * 1024);
            SB();
            float s = 0.f, q = 0.f, s2 = 0.f, q2 = 0.f;
#pragma unroll
            for (int n = 0; n < 4; ++n) {
                const f32x4 v = accM[n] + t[n];
                const u32 w0 = pk2(v[0], v[1]), w1 = pk2(v[2], v[3]);
                xw[2 * n] = w0; xw[2 * n + 1] = w1;
                s = dot2(w0, 0x3F803F80u, s); q = dot2(w0, w0, q); s2 = dot2(w1, 0x3F803F80u, s2); q2 = dot2(w1, w1, q2);
            }
            pub_stat(po, tg, s + s2, q + q2);
            CFENCE();
            if constexpr (!LAYER) load_y();
            regroup(xw);
            SB();
            STAMP(LAYER ? 46 : 43);
            WAIT_VM(0);
            STAMP(LAYER ? 26 : 44);
            if constexpr (!LAYER) {
#pragma unroll
                for (int i = 0; i < 8; ++i) pin(yw[i]);
            }
            u32x4 a, c;
            ald4s(a, so); ald4s(c, so + 256);
            if constexpr (!LAYER) {
                _Pragma("unroll 1") for (int spin = 0; spin < SPIN_CAP; ++spin) {
                    if (__builtin_expect(__all(max(max(max(yw[0], yw[1]), max(yw[2], yw[3])), max(max(yw[4], yw[5]), max(yw[6], yw[7]))) != SENT), 1)) break;
                    __builtin_amdgcn_s_sleep(1);
                    CFENCE();
                    load_y();
                }
                regroup(yw);
            }
            f32x2 xa[8], ya[8], xy[8];
#pragma unroll
            for (int i = 0; i < 8; ++i) {
                xa[i] = (f32x2){bflo(xw[i]), bfhi(xw[i])}; ya[i] = (f32x2){bflo(yw[i]), bfhi(yw[i])}; xy[i] = xa[i] * ya[i];
                pin(xa[i]); pin(ya[i]); pin(xy[i]);
            }
            WAIT_VM(0);
            pin(a); pin(c);
            float mean, rstd;
            fin_stat(a, c, so, mean, rstd);
            STAMP(LAYER ? 11 : 6);
            const float nm = -mean * rstd;
#pragma unroll
            for (int i = 0; i < 8; ++i) {
                const f32x2 o = xy[i] * rstd + (ya[i] * nm + xa[i]);
                xw[i] = pk2(o[0], o[1]);
                if (!LAYER) xw[i] = nosent(xw[i]);
            }
#pragma unroll
            for (int j = 0; j < 2; ++j) {
                const u32x4 o = {xw[4 * j], xw[4 * j + 1], xw[4 * j + 2], xw[4 * j + 3]};
                if constexpr (LAYER) __builtin_amdgcn_raw_buffer_store_b128(o, r_out, oa + j * 64, 0, 0);
                else __builtin_amdgcn_raw_buffer_store_b128(o, r_ws, oa ^ (j * 64), 0, 16);
            }
            if constexpr (!LAYER) {
                float s = 0.f, q = 0.f, s2 = 0.f, q2 = 0.f;
#pragma unroll
                for (int i = 0; i < 8; i += 2) { s = dot2(xw[i], 0x3F803F80u, s); q = dot2(xw[i], xw[i], q); s2 = dot2(xw[i + 1], 0x3F803F80u, s2); q2 = dot2(xw[i + 1], xw[i + 1], q2); }
                pub_stat(po + (WS_ST2 - WS_ST1) * !(lane & 16), tg, s + s2, q + q2);
                CFENCE();
                regroup(xw);
#pragma unroll
                for (int i = 0; i < 8; ++i) { pin(xw[i]); pin(yw[i]); }
                STAMP(48);
                WAIT_VM(0);
            }
        }
    };
    wo_finish(IC<0>{});
    if constexpr (R == 1) {
        if (b == 0 && wave == 7) {
            const u32 fo = WS_F1 + (u32)((opq(tid) & 63) * 128);
            u32x2 ef[4];
#pragma unroll
            for (int i = 0; i < 4; ++i) ald2s(ef[i], fo + i * 8192);
            WAIT_VM(0);
#pragma unroll
            for (int i = 0; i < 4; ++i) { pin(ef[i]); spin_flags(ef[i], fo + i * 8192); }
            if (fo == WS_F1) __builtin_amdgcn_raw_buffer_store_b64(__builtin_bit_cast(u32x2, epoch + 1), r_ws, WS_EPOCH, 0, 16);
            WAIT_VM(0);
        }
    }
    RBAR();
    STAMP(7);

    {
        const int l3 = opq(tid) & 63;
        const u32 g0 = (u32)((4 * grp - 1 + (hd >> 1)) * 16384 + (hd & 1) * 1024 + l3 * 16);
        const u32 g2 = (u32)((4 * grp + 3) * 16384 + hd * 512 + l3 * 16);
        auto x2_piece = [&](auto ic) {
            constexpr int i = decltype(ic)::value, s = i / 3, p = i % 3;
            const u32 kp = (u32)((s + rot) & 7);
            if constexpr (p == 0) dma(IC<4>{}, IC<L_CX + s * 10240>{}, d_x2, hb1k, g0, kp * 2048);
            else if constexpr (p == 1) dma(IC<4>{}, IC<L_CX + s * 10240 + 4096>{}, d_x2, hb1k, g0 + 32768, kp * 2048);
            else dma(IC<5>{}, IC<L_CX + s * 10240 + 8192>{}, d_x2, hb512, g2, kp * 2048);
        };
        u32 va = L_CX + hd * 1024 + l3 * 16, vq = L_CX + 8192 + hd * 512 + (l3 & 31) * 16 + (l3 >> 5) * 10240;
        pin(va); pin(vq);
        u32x4 vr[5];
        auto ver_read = [&](auto cc) {
            constexpr int c = decltype(cc)::value;
            u32 a = va + c * 20480, q = vq + c * 20480;
            pin(a); pin(q); CFENCE();
            vr[0] = *(LAS u32x4*)(lds + a); vr[1] = *(LAS u32x4*)(lds + a + 4096); vr[2] = *(LAS u32x4*)(lds + a + 10240); vr[3] = *(LAS u32x4*)(lds + a + 14336);
            vr[4] = *(LAS u32x4*)(lds + q);
        };
        auto ver_check = [&](auto cc) {
            constexpr int c = decltype(cc)::value;
            _Pragma("unroll 1") for (int spin = 0; spin < SPIN_CAP; ++spin) {
                u32 m0 = 0, m1 = 0;
#pragma unroll
                for (int i = 0; i < 5; ++i) { m0 = max(max(m0, vr[i][0]), vr[i][1]); m1 = max(max(m1, vr[i][2]), vr[i][3]); }
                if (__builtin_expect(__all(max(m0, m1) != SENT), 1)) break;
                __builtin_amdgcn_s_sleep(1);
                sfor<6 * c, 6 * c + 6>([&](auto ic) { x2_piece(ic); });
                WAIT_VM(0);
                ver_read(cc);
            }
        };
        const u32 xb0 = (u32)((l3 & 15) * 128 + (((l3 >> 4) ^ ((l3 & 15) >> 1)) << 4));
        if constexpr (R == 1) {
            STAMP(39);
            sfor<0, 12>([&](auto ic) { x2_piece(ic); });
            WAIT_VM(6);
            STAMP(8);
            ver_read(IC<0>{});
            ver_check(IC<0>{});
        } else {
            u32x4 sa, sc, ha, hc;
            const u32 so = stat_off(WS_ST2, 4 * grp + hd);
            const bool halo = hd == 0 && grp > 0;
            ald4s(sa, so); ald4s(sc, so + 256);
            if (halo) { ald4s(ha, so - 2048); ald4s(hc, so - 2048 + 256); }
            WAIT_VM(0);
            pin(sa); pin(sc);
            float mean, rstd;
            fin_stat(sa, sc, so, mean, rstd);
            if (l3 < 16) { *(LAS float*)(lds + L_TAB + 64 + hd * 64 + l3 * 4) = mean; *(LAS float*)(lds + L_TAB + 320 + 64 + hd * 64 + l3 * 4) = rstd; }
            if (halo) {
                pin(ha); pin(hc);
                fin_stat(ha, hc, so - 2048, mean, rstd);
                if (l3 < 16) { *(LAS float*)(lds + L_TAB + l3 * 4) = mean; *(LAS float*)(lds + L_TAB + 320 + l3 * 4) = rstd; }
            }
        }
        BAR();
        STAMP(13);
        u32 xa;
        auto pf_addr = [&](int st) { xa = L_CX + (st >> 1) * 10240 + (xb0 ^ ((st & 1) * 64)); pin(xa); };
        pf_addr(0);
#pragma unroll
        for (int mt = 0; mt < 5; ++mt) Ab[0][mt] = ldsA(xa + mt * 2048);
        sfor<0, 16>([&](auto stc) {
            constexpr int st = decltype(stc)::value;
            if constexpr (R == 1) {
                if constexpr (st == 2) { WAIT_VM(10); ver_read(IC<1>{}); }
                if constexpr (st == 3) ver_check(IC<1>{});
                if constexpr (st == 5) { WAIT_VM(6); ver_read(IC<2>{}); }
                if constexpr (st == 6) { ver_check(IC<2>{}); WAIT_VM(0); ver_read(IC<3>{}); }
                if constexpr (st == 7) ver_check(IC<3>{});
            }
            if constexpr (st == 3) { RBAR(); STAMP(49); }
            if constexpr (st == 7) { RBAR(); STAMP(50); CSTAMP(60); }
            if (st < 15) pf_addr(st + 1);
            if constexpr (st == 14) load_tab(true);
            SB();
            qkv_step(IC<1>{}, st, Ab[st & 1], [&](auto mc) {
                constexpr int mt = decltype(mc)::value;
                if (st < 15) Ab[(st + 1) & 1][mt] = ldsA(xa + mt * 2048);
                if constexpr (R == 1 && 12 + 5 * st + mt < 24) x2_piece(IC<12 + 5 * st + mt>{});
                SB();
            });
        });
    }
    STAMP(R ? 9 : 52);
    CSTAMP(61);
    attention(IC<1>{});
    STAMP(27);

    {
        wo_addr();
#pragma unroll
        for (int ks = 0; ks < 8; ++ks) load_F(ks);
        SB();
        WAIT_VM(0);
        RBAR();
        STAMP(10);
        load_o(WS_O2);
        SB();
        _Pragma("unroll 1") for (int spin = 0; spin < 64; ++spin) {
            u32 m0 = 0, m1 = 0;
#pragma unroll
            for (int ks = 0; ks < 8; ++ks) {
#pragma unroll
                for (int n = 0; n < 4; ++n) accM[n] = mfma32(F[ks][n], __builtin_bit_cast(s16x8, of[ks]), ks ? accM[n] : resid(n));
                m0 = max(max(m0, of[ks][0]), of[ks][1]); m1 = max(max(m1, of[ks][2]), of[ks][3]);
                SB();
            }
            if (__builtin_expect(__all(max(m0, m1) != SENT), 1)) break;
            _Pragma("unroll 1") for (int sp2 = 0; sp2 < SPIN_CAP; ++sp2) {
                CFENCE();
                __builtin_amdgcn_s_sleep(1);
                const u32 pv = __builtin_amdgcn_raw_buffer_load_b32(r_ws, WS_O2 + (u32)(TI * 16384 + R * 8192 + (opq(tid) & 15) * 512 + 508), 0, 16);
                if (__all(pv != SENT)) break;
            }
            CFENCE();
            load_o(WS_O2);
        }
        SB();
        STAMP(45);
    }
    wo_finish(IC<1>{});
    if constexpr (R == 0) {
        WAIT_VM(0);
        STAMP(12);
        const u32 d = dg1 - dg0;
        u64 t0_; asm volatile("s_memrealtime %0\n\ts_waitcnt lgkmcnt(0)" : "=s"(t0_)::"memory");
        _Pragma("unroll 1") for (int i = 0; i < 100000; ++i) { u64 t_; asm volatile("s_memrealtime %0\n\ts_waitcnt lgkmcnt(0)" : "=s"(t_)::"memory"); if ((u32)t_ - (u32)t0_ >= d) break; }
    }
}

__global__ void __launch_bounds__(512, 2)
fused_kernel(const u16* __restrict__ gx, const u16* __restrict__ gwqkv, const u16* __restrict__ gwo,
             const u16* __restrict__ gbk, const u16* __restrict__ gbv, const u16* __restrict__ gsk,
             const u16* __restrict__ ghk, const u16* __restrict__ gdv, u16* __restrict__ gout, char* __restrict__ ws) {
    __shared__ __attribute__((aligned(16))) char lds_[L_TOTAL];
    typedef u32 u32x16 __attribute__((ext_vector_type(16)));
    u32x16 ka; u32x4 kb;
    {
        auto kp = __builtin_amdgcn_kernarg_segment_ptr();
        asm volatile("s_load_dwordx16 %0, %2, 0x0\n\ts_load_dwordx4 %1, %2, 0x40\n\ts_waitcnt lgkmcnt(0)" : "=&s"(ka), "=&s"(kb) : "s"(kp) : "memory");
    }
    auto mkp = [](u32 lo, u32 hi) { return (char*)(((u64)hi << 32) | lo); };
    const Args a = {(const u16*)mkp(ka[0], ka[1]), (const u16*)mkp(ka[2], ka[3]), (const u16*)mkp(ka[4], ka[5]), (const u16*)mkp(ka[6], ka[7]), (const u16*)mkp(ka[8], ka[9]),
                    (const u16*)mkp(ka[10], ka[11]), (const u16*)mkp(ka[12], ka[13]), (const u16*)mkp(ka[14], ka[15]), (u16*)mkp(kb[0], kb[1]), mkp(kb[2], kb[3])};
    const int wave = __builtin_amdgcn_readfirstlane(threadIdx.x >> 6);
    if (wave < 4) body<0>(a, (LAS char*)lds_, wave); else body<1>(a, (LAS char*)lds_, wave);
}

extern "C" void kernel_launch(void* const* d_in, const int* in_sizes, int n_in,
                              void* d_out, int out_size, void* d_ws, size_t ws_size,
                              hipStream_t stream) {
    fused_kernel<<<256, 512, 0, stream>>>((const u16*)d_in[0], (const u16*)d_in[1], (const u16*)d_in[2],
                                          (const u16*)d_in[4], (const u16*)d_in[5], (const u16*)d_in[6],
                                          (const u16*)d_in[7], (const u16*)d_in[8], (u16*)d_out, (char*)d_ws);
}
```

```cpp
#include <hip/hip_runtime.h>
#include <stdint.h>

typedef unsigned short u16;
typedef unsigned u32;
typedef unsigned long long u64;
typedef short s16x4 __attribute__((ext_vector_type(4)));
typedef short s16x8 __attribute__((ext_vector_type(8)));
typedef __bf16 bf16x2 __attribute__((ext_vector_type(2)));
typedef float f32x2 __attribute__((ext_vector_type(2)));
typedef float f32x4 __attribute__((ext_vector_type(4)));
typedef u32 u32x2 __attribute__((ext_vector_type(2)));
typedef u32 u32x4 __attribute__((ext_vector_type(4)));
#define LAS __attribute__((address_space(3)))
#define DEV __device__ __forceinline__
typedef __amdgpu_buffer_rsrc_t rsrc_t;

constexpr u32 WS_EPOCH = 0;
constexpr u32 WS_F1 = 32768;
constexpr u32 WS_F2 = 65536;
constexpr u32 WS_ST0 = 98304;
constexpr u32 WS_ST1 = 131072;
constexpr u32 WS_ST2 = WS_ST1 + 262144;
constexpr u32 WS_ST3 = WS_ST2 + 262144;
constexpr u32 WS_UT = 1u << 20;
constexpr u32 WS_O1 = WS_UT + (2u << 20);
constexpr u32 WS_O2 = WS_O1 + (2u << 20);
constexpr u32 WS_Y2 = WS_O2 + (2u << 20);
constexpr u32 WS_X2 = WS_Y2 + (2u << 20);
constexpr u32 WS_END = WS_X2 + (2u << 20);

constexpr u32 L_SLOT = 34816;
constexpr u32 L_KV = 4 * L_SLOT;
constexpr u32 L_K = L_KV, L_V = L_KV + 9216;
constexpr u32 L_SK = L_KV + 17408;
constexpr u32 L_WO = 0;
constexpr u32 L_CX = 65536;
constexpr u32 L_HC = L_CX;
constexpr u32 L_UR = L_CX + 69632;
constexpr u32 L_WX = L_CX + 40960;
constexpr u32 L_EX = 147456;
constexpr u32 L_TAB = L_EX + 12288;
constexpr u32 L_CS = L_TAB + 1024;
constexpr u32 L_FLG = L_TAB + 2048;
constexpr u32 L_TOTAL = 160 * 1024;

constexpr u32 CODE_LINES = 304;
constexpr int SPIN_CAP = 1 << 14;
constexpr u32 SENT = 0xFFFFFFFFu;

#define WAIT_VM(n) asm volatile("s_waitcnt vmcnt(%0)" ::"i"(n) : "memory")
#define BAR() do { asm volatile("s_waitcnt lgkmcnt(0)" ::: "memory"); __builtin_amdgcn_s_barrier(); asm volatile("" ::: "memory"); } while (0)
#define RBAR() do { asm volatile("" ::: "memory"); __builtin_amdgcn_s_barrier(); asm volatile("" ::: "memory"); } while (0)
#define CFENCE() asm volatile("" ::: "memory")
#define SB() __builtin_amdgcn_sched_barrier(0)
template <int N> struct IC { static constexpr int value = N; };
template <int I, int N, class F> DEV void sfor(F&& f) { if constexpr (I < N) { f(IC<I>{}); sfor<I + 1, N>(f); } }

DEV f32x4 mfma32(s16x8 a, s16x8 b, f32x4 c) { return __builtin_amdgcn_mfma_f32_16x16x32_bf16(a, b, c, 0, 0, 0); }
DEV f32x4 mfma16(s16x4 a, s16x4 b, f32x4 c) { return __builtin_amdgcn_mfma_f32_16x16x16bf16_1k(a, b, c, 0, 0, 0); }
DEV float vmax(float a, float b) { float r; asm("v_max_f32 %0, %1, %2" : "=v"(r) : "v"(a), "v"(b)); return r; }
DEV u32 pk2(float lo, float hi) { f32x2 f = {lo, hi}; bf16x2 b = __builtin_convertvector(f, bf16x2); return __builtin_bit_cast(u32, b); }
DEV float bflo(u32 w) { return __uint_as_float(w << 16); }
DEV float bfhi(u32 w) { return __uint_as_float(w & 0xffff0000u); }
DEV float dot2(u32 a, u32 b, float c) { return __builtin_amdgcn_fdot2_f32_bf16(__builtin_bit_cast(bf16x2, a), __builtin_bit_cast(bf16x2, b), c, false); }
DEV u32 nosent(u32 w) { return min(w, 0xFFFFFFFEu); }
DEV float rowsum16(float v) {
    v += __builtin_bit_cast(float, __builtin_amdgcn_update_dpp(0, __builtin_bit_cast(int, v), 0xB1, 0xf, 0xf, true));
    v += __builtin_bit_cast(float, __builtin_amdgcn_update_dpp(0, __builtin_bit_cast(int, v), 0x4E, 0xf, 0xf, true));
    v += __builtin_bit_cast(float, __builtin_amdgcn_update_dpp(0, __builtin_bit_cast(int, v), 0x141, 0xf, 0xf, true));
    v += __builtin_bit_cast(float, __builtin_amdgcn_update_dpp(0, __builtin_bit_cast(int, v), 0x140, 0xf, 0xf, true));
    return v;
}
DEV float bcast_row0(float v) { return __builtin_bit_cast(float, __builtin_amdgcn_update_dpp(0, __builtin_bit_cast(int, v), 0x150, 0xf, 0xf, true)); }
template <int OFF> DEV void ald4o(u32x4& d, u32 voff, u32x4 rs) { asm volatile("buffer_load_dwordx4 %0, %1, %2, 0 offen offset:%3 sc1" : "=v"(d) : "v"(voff), "s"(rs), "i"(OFF) : "memory"); }
DEV void sdrain(u32& a, u32& b) { asm volatile("s_waitcnt lgkmcnt(0)" : "+s"(a), "+s"(b)); }
template <class T> DEV void pin(T& v) { asm volatile("" : "+v"(v)); }
DEV int opq(int v) { asm volatile("" : "+v"(v)); return v; }
DEV s16x8 cat8(s16x4 a, s16x4 b) { return __builtin_shufflevector(a, b, 0, 1, 2, 3, 4, 5, 6, 7); }
DEV s16x4 pack4(f32x4 v) { u32x2 w = {pk2(v[0], v[1]), pk2(v[2], v[3])}; return __builtin_bit_cast(s16x4, w); }
DEV u64 mix64(u64 z) { z += 0x9E3779B97F4A7C15ull; z = (z ^ (z >> 30)) * 0xBF58476D1CE4E5B9ull; z = (z ^ (z >> 27)) * 0x94D049BB133111EBull; return z ^ (z >> 31); }

struct ConvSched {
    int n[4], f[18], i[18], slot[64];
    constexpr ConvSched(int R) : n{}, f{}, i{}, slot{} {
        constexpr int LEAD = 9;
        const int N0[4] = {0, 3, 4, 7};
        for (int k = 0; k < 4; ++k) n[k] = N0[k] ^ R;
        int c = 0;
        for (int ff = 0; ff < 8; ++ff) for (int k = 3; k >= 0; --k) if (ff <= n[k]) { f[c] = ff; i[c] = k; ++c; }
        for (int k = 0; k < 64; ++k) slot[k] = 1000;
        for (int p = 71; p >= 0; --p) {
            const int e = p / 18, ff = f[p % 18], g = n[i[p % 18]] - ff, s = p - LEAD < -1 ? -1 : p - LEAD;
            slot[e * 16 + ff] = s; slot[e * 16 + 8 + g] = s;
        }
    }
};

struct Args { const u16 *gx, *gwqkv, *gwo, *gbk, *gbv, *gsk, *ghk, *gdv; u16* gout; char* ws; };

template <int R>
DEV void body(const Args& A_, LAS char* const lds, const int wave, const u64 pc0) {
    const u16* gx = A_.gx; char* ws = A_.ws;
    const int b = blockIdx.x, tid = threadIdx.x, lane = tid & 63;
    const int hd = wave & 3;
    const int grp = (b & 7) * 4 + (b >> 6), mem = (b >> 3) & 7;
    const int T0 = grp * 64;
    const int rot = (mem + 2 * (grp & 3)) & 7;

    u64 epoch;
    asm volatile("s_load_dwordx2 %0, %1, 0x0" : "=s"(epoch) : "s"(ws));
    u32 dw;
    asm volatile("s_load_dword %0, %1, 0x0" : "=s"(dw) : "s"(A_.gdv + 2 * b));
    const rsrc_t r_x = __builtin_amdgcn_make_buffer_rsrc((void*)gx, 0, 2048 * 512 * 2, 0x00020000);
    const rsrc_t r_ws = __builtin_amdgcn_make_buffer_rsrc((void*)ws, 0, WS_END, 0x00020000);
    const rsrc_t r_out = __builtin_amdgcn_make_buffer_rsrc((void*)A_.gout, 0, 2048 * 512 * 2, 0x00020000);
    const u32 ldsbase = (u32)(size_t)lds;
    auto mkdesc = [](const void* p, u32 bytes) { const u64 a = (u64)p; return (u32x4){(u32)a, (u32)(a >> 32) & 0xffffu, bytes, 0x00020000u}; };
    const u32x4 d_x = mkdesc(gx, 2048 * 512 * 2), d_w = mkdesc(A_.gwqkv, 512 * 1536 * 2), d_wo = mkdesc(A_.gwo, 512 * 512 * 2);
    const u32x4 d_k = mkdesc(A_.gbk, 2048 * 512 * 2), d_v = mkdesc(A_.gbv, 2048 * 512 * 2), d_sk = mkdesc(A_.gsk, 512 * 4 * 2);
    const u32x4 d_h = mkdesc(A_.ghk, 512 * 2048 * 2), d_x2 = mkdesc(ws + WS_X2, 2u << 20), d_ws = mkdesc(ws, WS_END);
    const u32 hb1k = ldsbase + hd * 1024, hb512 = ldsbase + hd * 512;
#define DMA_ASM(pre, mods, post) asm volatile("s_add_u32 m0, %0, %4\n\t" pre "buffer_load_dwordx4 %1, %2, %3 offen " mods "lds" post \
                     :: "s"(sbase), "v"(voff), "s"(rs), "s"(soff), "i"(IMM) : "memory", "scc")
    const u32 xmlo = hd == 0 ? ~0xFFu : ~0u;
    auto dma = [&](auto kind, auto imm, u32x4 rs, u32 sbase, u32 voff, u32 soff) {
        constexpr int K = decltype(kind)::value; constexpr int IMM = decltype(imm)::value;
        if constexpr (K == 0) DMA_ASM("s_nop 0\n\t", "", "");
        else if constexpr (K == 1) DMA_ASM("s_mov_b32 exec_hi, 0\n\t", "", "\n\ts_mov_b32 exec_hi, -1");
        else if constexpr (K == 2) {
            const u32 ml = xmlo;
            asm volatile("s_add_u32 m0, %0, %4\n\ts_mov_b32 exec_lo, %5\n\tbuffer_load_dwordx4 %1, %2, %3 offen lds\n\ts_mov_b32 exec_lo, -1"
                         :: "s"(sbase), "v"(voff), "s"(rs), "s"(soff), "i"(IMM), "s"(ml) : "memory", "scc");
        }
        else if constexpr (K == 4) DMA_ASM("s_nop 0\n\t", "sc1 ", "");
        else DMA_ASM("s_mov_b32 exec_hi, 0\n\t", "sc1 ", "\n\ts_mov_b32 exec_hi, -1");
    };
    auto ald4 = [&](u32x4& d, u32x4 rs, u32 voff) { asm volatile("buffer_load_dwordx4 %0, %1, %2, 0 offen" : "=v"(d) : "v"(voff), "s"(rs) : "memory"); };
    auto ald4s = [&](u32x4& d, u32 voff) { asm volatile("buffer_load_dwordx4 %0, %1, %2, 0 offen sc1" : "=v"(d) : "v"(voff), "s"(d_ws) : "memory"); };
    auto ald2s = [&](u32x2& d, u32 voff) { asm volatile("buffer_load_dwordx2 %0, %1, %2, 0 offen sc1" : "=v"(d) : "v"(voff), "s"(d_ws) : "memory"); };

    const int pr = lane >> 3, pc = lane & 7;
    const int xr0 = 8 * hd + pr, xr2 = 64 + 4 * hd + pr;
    const u32 xv0 = (u32)((T0 - 16 + xr0) * 1024 + ((pc ^ ((xr0 >> 1) & 7)) << 4));
    const u32 xv2 = (u32)((T0 - 16 + xr2) * 1024 + ((pc ^ ((xr2 >> 1) & 7)) << 4));
    const int wfw = ((xr0 >> 1) & 1) | (((xr0 >> 3) & 1) << 1);
    const u32 wv0 = (u32)(xr0 * 3072 + mem * 128 + ((((pc >> 1) ^ wfw) * 2 + (pc & 1)) << 4));
    auto issue_piece = [&](auto sc, auto ic) {
        constexpr int s = decltype(sc)::value, i = decltype(ic)::value;
        const u32 kp = (u32)((s + rot) & 7);
        constexpr int sb = (s & 3) * L_SLOT;
        if constexpr (i == 0) dma(IC<2>{}, IC<sb>{}, d_x, hb1k, xv0, kp * 128);
        else if constexpr (i == 1) dma(IC<0>{}, IC<sb + 4096>{}, d_x, hb1k, xv0 + 32768, kp * 128);
        else if constexpr (i == 2) dma(IC<1>{}, IC<sb + 8192>{}, d_x, hb512, xv2, kp * 128);
        else dma(IC<0>{}, IC<sb + 10240 + (i - 3) * 4096>{}, d_w, hb1k, wv0, kp * (64 * 3072) + ((i - 3) >> 1) * 1024 + ((i - 3) & 1) * (32 * 3072));
    };
    u32 wov = 0;
    auto issue_wo = [&](auto ic) {
        constexpr int i = decltype(ic)::value;
        if constexpr (i == 0) {
            const int l2 = opq(tid) & 63, pr = l2 >> 3, pc = l2 & 7;
            wov = (u32)((8 * hd + pr) * 1024 + mem * 128 + ((((pc >> 1) ^ (((pr >> 1) & 1) | ((hd & 1) << 1))) * 2 + (pc & 1)) << 4));
            pin(wov);
        }
        const u32 j = (u32)((i + 2 * grp) & 7);
        dma(IC<0>{}, IC<L_WO + (i & 8) * 4096>{}, d_wo, hb1k + j * 4096, wov, ((i & 8) + j) * 32768);
    };
    auto issue_kv = [&](int i) {
        const int l2 = opq(tid) & 63, pr = l2 >> 3, pc = l2 & 7, p = hd + 4 * (i & 1);
        const u32 cb = (u32)(mem * 128);
        if (i < 2) dma(IC<0>{}, IC<L_K>{}, d_k, hb1k + (i & 1) * 4096, (u32)((T0 - 8 + 8 * p + pr) * 1024) + cb + ((pc ^ p) << 4), 0);
        else if (i < 4) dma(IC<0>{}, IC<L_V>{}, d_v, hb1k + (i & 1) * 4096, (u32)((T0 + 8 * p + pr) * 1024) + cb + ((pc ^ p) << 4), 0);
        else if (hd == 0) dma(IC<0>{}, IC<L_K + 8192>{}, d_k, ldsbase, (u32)((T0 - 8 + 64 + pr) * 1024) + cb + (pc << 4), 0);
        else dma(IC<0>{}, IC<L_SK>{}, d_sk, ldsbase, (u32)(mem * 512 + l2 * 16), 0);
    };

    const int srow = T0 + 8 * mem + 2 * hd;
    u32x4 xrow[2];
    sfor<0, 9>([&](auto ic) { if constexpr ((decltype(ic)::value & 1) != R) issue_piece(IC<0>{}, ic); });
    sfor<0, 9>([&](auto ic) { if constexpr ((decltype(ic)::value & 1) != R) issue_piece(IC<1>{}, ic); });
    asm volatile("s_waitcnt lgkmcnt(0)" : "+s"(epoch), "+s"(dw));
    const u64 tag = mix64(epoch);
    const u32 tl = (u32)tag, th = (u32)(tag >> 32);
    if (R == 1 && wave == 7 && lane < 32) *(LAS u32x4*)(lds + (lane >> 3) * L_SLOT + (lane & 7) * 16) = (u32x4){0x3F803F80u, 0x3F803F80u, 0x3F803F80u, 0x3F803F80u};

    auto trr = [&](u32 off) { return __builtin_amdgcn_ds_read_tr16_b64_v4i16((LAS s16x4*)(lds + off)); };
    auto ldsA = [&](u32 off) { return *(LAS s16x8*)(lds + off); };
    auto flag_ok = [&](const u32x2& f) { return __all(f[0] == tl && f[1] == th); };
    auto spin_flags = [&](u32x2& f, u32 off) {
        _Pragma("unroll 1") for (int spin = 0; spin < SPIN_CAP; ++spin) {
            if (__builtin_expect(flag_ok(f), 1)) break;
            __builtin_amdgcn_s_sleep(2);
            asm volatile("buffer_load_dwordx2 %0, %1, %2, 0 offen sc1\n\ts_waitcnt vmcnt(0)" : "=&v"(f) : "v"(off), "s"(d_ws) : "memory");
        }
    };

    s16x8 Wq[16], Wm[16];
    f32x4 accM[5], accQ[5];
    const f32x4 zero4 = {0.f, 0.f, 0.f, 0.f};
    const s16x8 ones8 = {0x3F80, 0x3F80, 0x3F80, 0x3F80, 0x3F80, 0x3F80, 0x3F80, 0x3F80};
    s16x8 Ab[3][5];
    auto qkv_step = [&](auto lc, int st, const s16x8 (&A)[5], auto fill) {
        sfor<0, 5>([&](auto mc) {
            constexpr int mt = decltype(mc)::value;
            const f32x4 cm = st ? accM[mt] : zero4;
            accM[mt] = R ? mfma32(A[mt], Wm[st], cm) : mfma32(Wm[st], A[mt], cm);
            if constexpr (R == 0 && (mt > 0 || decltype(lc)::value == 0)) accQ[mt] = mfma32(Wq[st], A[mt], st ? accQ[mt] : zero4);
            fill(mc);
        });
    };

    float m1[5], r1[5]; f32x4 m4[5], r4[5], csK, csQ;
    auto load_tab = [&](bool cs) {
        const int l2 = opq(tid) & 63;
        u32 ta = L_TAB + (R ? (l2 >> 4) * 16 : (l2 & 15) * 4);
        pin(ta);
        if (R == 0 && cs) { csK = *(LAS f32x4*)(lds + L_CS + hd * 128 + (l2 >> 4) * 16); csQ = *(LAS f32x4*)(lds + L_CS + hd * 128 + 64 + (l2 >> 4) * 16); }
#pragma unroll
        for (int mt = 0; mt < 5; ++mt) {
            if (R == 0) { m1[mt] = *(LAS float*)(lds + ta + mt * 64); r1[mt] = *(LAS float*)(lds + ta + 320 + mt * 64); }
            else { m4[mt] = *(LAS f32x4*)(lds + ta + mt * 64); r4[mt] = *(LAS f32x4*)(lds + ta + 320 + mt * 64); }
        }
    };

    u32x4 hr[4];
    u32x2 flg;
    {
        const int li = lane & 15, lq = lane >> 4, tq = li >> 2, tp = lane & 3;
        const u32 xa0 = (u32)(li * 128 + ((lq ^ (li >> 1)) << 4));
        const u32 wa = (u32)((8 * lq + tq) * 128 + ((hd ^ (((tq >> 1) & 1) | ((lq & 1) << 1))) << 5) + tp * 8);
        u32 xa, wb, kw[11], vw[8];
        u32x4 s0v, skw;
        auto pf_addr = [&](int st) {
            const u32 sb = (u32)((st >> 1) & 3) * L_SLOT;
            xa = sb + (xa0 ^ ((st & 1) * 64)); wb = sb + 10240 + (st & 1) * 4096 + wa;
            asm volatile("" : "+v"(xa), "+v"(wb));
        };
        auto pf_part = [&](int st, int mt) {
            if (mt == 0) Wm[st] = cat8(trr(wb + (1 + R) * 8192), trr(wb + (1 + R) * 8192 + 512));
            Ab[st & 1][mt] = ldsA(xa + mt * 2048);
            if (R == 0 && mt == 1) Wq[st] = cat8(trr(wb), trr(wb + 512));
        };
        WAIT_VM(R ? 5 : 4);
        BAR();
        pf_addr(0);
#pragma unroll
        for (int mt = 0; mt < 5; ++mt) pf_part(0, mt);
        u32 j0, j1;
        {
            const u32 ln = (u32)((b >> 3) * 8 + wave);
            const u64 pa = (pc0 & ~127ull) + ln * 128, pb = pa + (ln + 256 < CODE_LINES ? 32768 : 0);
            asm volatile("s_load_dword %0, %2, 0x0\n\ts_load_dword %1, %3, 0x0" : "=&s"(j0), "=&s"(j1) : "s"(pa), "s"(pb));
        }
        sfor<0, 16>([&](auto stc) {
            constexpr int st = decltype(stc)::value;
            if constexpr ((st & 1) && st < 15) {
                constexpr int s1 = (st >> 1) + 1;
                if constexpr (R == 0) {
                    if constexpr (s1 == 1) WAIT_VM(7);
                    if constexpr (s1 == 2) WAIT_VM(1);
                    if constexpr (s1 == 5) WAIT_VM(0);
                } else {
                    constexpr int WN[8] = {0, 9, 9, 9, 9, 9, 10, 10};
                    WAIT_VM(WN[s1]);
                    if constexpr (s1 == 7) {
                        pin(s0v);
                        const int l2 = opq(tid) & 63, tr = hd * 20 + min(l2, 19), row = T0 - 16 + tr;
                        _Pragma("unroll 1") for (int spin = 0; spin < SPIN_CAP; ++spin) {
                            if (__builtin_expect(__all(row < 0 || (s0v[1] == tl && s0v[3] == th)), 1)) break;
                            __builtin_amdgcn_s_sleep(2);
                            ald4s(s0v, WS_ST0 + (u32)(max(row, 0) * 16));
                            WAIT_VM(0);
                            pin(s0v);
                        }
                        *(LAS u32*)(lds + L_TAB + tr * 4) = row < 0 ? 0u : s0v[0];
                        *(LAS u32*)(lds + L_TAB + 320 + tr * 4) = row < 0 ? 0x3F800000u : s0v[2];
                        asm volatile("s_waitcnt lgkmcnt(0)" ::: "memory");
                    }
                }
                if constexpr (s1 == 4) sdrain(j0, j1);
                RBAR();
                if constexpr (s1 == 5 && R == 0) { if (tid == 0) __builtin_amdgcn_raw_buffer_store_b64((u32x2){tl, th}, r_ws, WS_F1 + (mem * 32 + grp) * 128, 0, 16); CFENCE(); }
                if constexpr (s1 == 5 && R == 1) { const int row = T0 - 16 + hd * 20 + min(opq(tid) & 63, 19); ald4s(s0v, WS_ST0 + (u32)(max(row, 0) * 16)); }
            }
            if (st < 15) pf_addr(st + 1);
            if constexpr (st == 14) load_tab(false);
            SB();
            qkv_step(IC<0>{}, st, Ab[st & 1], [&](auto mc) {
                constexpr int mt = decltype(mc)::value;
                if (st < 15) pf_part(st + 1, mt);
                if constexpr (R == 1) {
                    if constexpr (st == 0) { issue_piece(IC<2>{}, IC<2 * mt>{}); if constexpr (mt < 4) issue_piece(IC<2>{}, IC<2 * mt + 1>{}); }
                    if constexpr (st >= 1 && st <= 10 && ((st - 1) & 1) * 5 + mt < 9) issue_piece(IC<((st - 1) >> 1) + 3>{}, IC<((st - 1) & 1) * 5 + mt>{});
                    if constexpr (st == 11 && mt == 0) { const u32 ho = (u32)(b * 8192 + opq(tid) * 16); ald4(hr[2], d_h, ho); ald4(hr[3], d_h, ho + 16); }
                    if constexpr (st == 13 && mt == 0) { if (wave == 7) { const int l2 = opq(tid) & 63; ald2s(flg, WS_F1 + (u32)((l2 < 32 ? (b >> 5) * 32 + l2 : (l2 & 7) * 32 + grp) * 128)); } }
                    if constexpr (st >= 11 && st <= 14 && mt < 4) issue_wo(IC<(st - 11) * 4 + mt>{});
                } else {
                    if constexpr (st == 0) {
                        if constexpr (mt == 0) { const u32 xo = (u32)(srow * 1024 + (opq(tid) & 63) * 16); ald4(xrow[0], d_x, xo); ald4(xrow[1], d_x, xo + 1024); }
                        issue_kv(mt);
                    }
                    if constexpr (st == 11 && mt < 2) { const u32 ho = (u32)(b * 8192 + mt * 4096 + opq(tid) * 16); ald4(hr[2 * mt], d_h, ho); ald4(hr[2 * mt + 1], d_h, ho + 16); }
                }
                SB();
            });
            if constexpr (R == 0 && st == 2) {
                WAIT_VM(5);
                pin(xrow[0]); pin(xrow[1]);
                float s[2] = {0.f, 0.f}, q[2] = {0.f, 0.f};
#pragma unroll
                for (int i = 0; i < 4; ++i)
#pragma unroll
                    for (int k = 0; k < 2; ++k) { s[k] = dot2(xrow[k][i], 0x3F803F80u, s[k]); q[k] = dot2(xrow[k][i], xrow[k][i], q[k]); }
                auto ss = __builtin_amdgcn_permlane32_swap(__float_as_uint(s[0]), __float_as_uint(s[1]), false, false);
                auto qq = __builtin_amdgcn_permlane32_swap(__float_as_uint(q[0]), __float_as_uint(q[1]), false, false);
                float sv1 = rowsum16(__uint_as_float(ss[0]) + __uint_as_float(ss[1])), qv1 = rowsum16(__uint_as_float(qq[0]) + __uint_as_float(qq[1]));
                auto s2 = __builtin_amdgcn_permlane16_swap(__float_as_uint(sv1), __float_as_uint(sv1), false, false);
                auto q2 = __builtin_amdgcn_permlane16_swap(__float_as_uint(qv1), __float_as_uint(qv1), false, false);
                const float m = (__uint_as_float(s2[0]) + __uint_as_float(s2[1])) * (1.f / 512.f);
                const float r = __builtin_amdgcn_rsqf(fmaxf((__uint_as_float(q2[0]) + __uint_as_float(q2[1])) * (1.f / 512.f) - m * m, 0.f) + 1e-5f);
                const u32x4 sv = {__float_as_uint(m), tl, __float_as_uint(r), th};
                const int l2 = opq(tid) & 63;
                __builtin_amdgcn_raw_buffer_store_b128(sv, r_ws, (l2 & 31) == 0 ? WS_ST0 + (u32)((srow + (l2 >> 5)) * 16) : 0xFFFFFF00u, 0, 16);
                CFENCE();
                SB();
            }
            if constexpr (R == 0 && (st == 4 || st == 6)) {
                const int l2 = opq(tid) & 63, tg = l2 & 7, cp = 8 * hd + (l2 >> 3);
                if constexpr (st == 4) {
                    skw = *(LAS u32x4*)(lds + L_SK + cp * 16);
                    u32 ka = L_K + tg * 1024 + ((((cp >> 2) ^ tg) & 7) << 4) + (cp & 3) * 4;
                    u32 kb = L_K + tg * 1024 + ((((cp >> 2) ^ (tg + 1)) & 7) << 4) + (cp & 3) * 4;
                    pin(ka); pin(kb);
#pragma unroll
                    for (int i = 0; i < 11; ++i) kw[i] = *(LAS u32*)(lds + ((i + 5 < 8) ? ka : kb) + (i + 5) * 128);
#pragma unroll
                    for (int i = 0; i < 8; ++i) vw[i] = *(LAS u32*)(lds + ka + (L_V - L_K) + i * 128);
                    SB();
                }
                constexpr bool HI = st == 6;
                auto ex = [&](u32 w) { return HI ? bfhi(w) : bflo(w); };
                const float a0 = bflo(skw[2 * HI]), a1 = bfhi(skw[2 * HI]), a2 = bflo(skw[2 * HI + 1]), a3 = bfhi(skw[2 * HI + 1]);
                float ua[8];
#pragma unroll
                for (int i = 0; i < 8; ++i) ua[i] = (ex(kw[i]) * a3 + ex(kw[i + 1]) * a2 + ex(kw[i + 2]) * a1 + ex(kw[i + 3]) * a0) * ex(vw[i]);
                const u32x4 oa = {pk2(ua[7], ua[6]), pk2(ua[5], ua[4]), pk2(ua[3], ua[2]), pk2(ua[1], ua[0])};
                __builtin_amdgcn_raw_buffer_store_b128(oa, r_ws, WS_UT + (u32)((mem * 64 + 2 * cp) * 4096 + (2040 - T0 - 8 * tg) * 2), HI * 4096, 16);
                __builtin_amdgcn_raw_buffer_store_b128((u32x4){SENT, SENT, SENT, SENT}, r_ws, (u32)((4 * grp + hd) * 16384 + mem * 2048 + HI * 1024 + l2 * 16), WS_O1, 16);
                CFENCE();
                SB();
            }
        });
    }

    u32x4 uu;
    float csV;
    auto attention = [&](auto lc) {
        constexpr int LAYER = decltype(lc)::value;
        const int lane = opq(tid) & 63, li = lane & 15, lq = lane >> 4;
        u32 ex = L_EX + hd * 2048 + lane * 8;
        pin(ex);
        u32 f1ok = 1;
        if constexpr (R == 0) {
            if constexpr (LAYER == 0) {
#pragma unroll
                for (int i = 0; i < 4; ++i) { csK[i] = bcast_row0(accM[0][i]); csQ[i] = bcast_row0(accQ[0][i]); }
                if (li == 0) { *(LAS f32x4*)(lds + L_CS + hd * 128 + lq * 16) = csK; *(LAS f32x4*)(lds + L_CS + hd * 128 + 64 + lq * 16) = csQ; }
            }
            const int par = li & 1;
            const bool cur0 = (4 * lq + par) <= li, cur1 = (4 * lq + par + 2) <= li;
            s16x4 Kb[5], Qb[5];
#pragma unroll
            for (int mt = 0; mt < 5; ++mt) {
                Kb[mt] = pack4((accM[mt] - m1[mt] * csK) * r1[mt]);
                if (mt) Qb[mt] = pack4(accQ[mt] - m1[mt] * csQ);
            }
            f32x4 Sc[5], Sp[5];
#pragma unroll
            for (int mt = 1; mt < 5; ++mt) { Sc[mt] = mfma16(Kb[mt], Qb[mt], zero4); Sp[mt] = mfma16(Kb[mt - 1], Qb[mt], zero4); }
            float s0[5], s1[5], mx[5];
#pragma unroll
            for (int mt = 1; mt < 5; ++mt) {
                const float sp0 = Sp[mt][0], sp1 = Sp[mt][1], sp2 = Sp[mt][2], sp3 = Sp[mt][3], c0 = Sc[mt][0], c1 = Sc[mt][1], c2 = Sc[mt][2], c3 = Sc[mt][3];
                float p0 = par ? sp1 : sp0, p1 = par ? sp3 : sp2, q0 = par ? c1 : c0, q1 = par ? c3 : c2;
                asm volatile("" : "+v"(p0), "+v"(p1), "+v"(q0), "+v"(q1));
                if (mt == 1 && grp == 0) { p0 = -3.0e38f; p1 = -3.0e38f; }
                s0[mt] = cur0 ? q0 : p0; s1[mt] = cur1 ? q1 : p1;
                mx[mt] = vmax(s0[mt], s1[mt]);
            }
#pragma unroll
            for (int mt = 1; mt < 5; ++mt) { auto r = __builtin_amdgcn_permlane32_swap(__float_as_uint(mx[mt]), __float_as_uint(mx[mt]), false, false); mx[mt] = vmax(__uint_as_float(r[0]), __uint_as_float(r[1])); }
#pragma unroll
            for (int mt = 1; mt < 5; ++mt) { auto r = __builtin_amdgcn_permlane16_swap(__float_as_uint(mx[mt]), __float_as_uint(mx[mt]), false, false); mx[mt] = vmax(__uint_as_float(r[0]), __uint_as_float(r[1])); }
#pragma unroll
            for (int mt = 1; mt < 5; ++mt) {
                const float sc = r1[mt] * (0.25f * 1.44269504088896341f);
                const float e0 = __builtin_amdgcn_exp2f((s0[mt] - mx[mt]) * sc), e1 = __builtin_amdgcn_exp2f((s1[mt] - mx[mt]) * sc);
                *(LAS u32x2*)(lds + ex + (mt - 1) * 512) = (u32x2){pk2(e0, e0), pk2(e1, e1)};
            }
            BAR();
            if constexpr (LAYER == 0) f1ok = __builtin_amdgcn_readfirstlane(*(LAS u32*)(lds + L_FLG));
        } else {
            if constexpr (LAYER == 0) {
                auto s = __builtin_amdgcn_permlane16_swap(__float_as_uint(accM[0][0]), __float_as_uint(accM[0][0]), false, false);
                auto t = __builtin_amdgcn_permlane32_swap(s[0], s[0], false, false);
                csV = __uint_as_float(t[0]);
                if (wave == 7) { WAIT_VM(8); pin(flg); const u32 ok = flag_ok(flg); *(LAS u32*)(lds + L_FLG) = ok; }
            }
            const int par = li & 1;
            const bool cur0 = (4 * lq + par) <= li, cur1 = (4 * lq + par + 2) <= li;
            const u32 pm = par ? 0xffff0000u : 0x0000ffffu;
            u32 mC0 = cur0 ? pm : 0u, mP0 = cur0 ? 0u : pm, mC1 = cur1 ? pm : 0u, mP1 = cur1 ? 0u : pm;
            u32 oo = (LAYER ? WS_O2 : WS_O1) + (u32)((4 * grp + (lq & 1)) * 16384 + (4 * mem + hd) * 512 + (lq >> 1) * 256 + li * 16);
            asm volatile("" : "+v"(mC0), "+v"(mP0), "+v"(mC1), "+v"(mP1), "+v"(oo));
            s16x4 Vb[5];
#pragma unroll
            for (int mt = 0; mt < 5; ++mt) { Vb[mt] = pack4((accM[mt] - m4[mt] * csV) * r4[mt]); pin(Vb[mt]); }
            BAR();
            u32x2 pw[4];
#pragma unroll
            for (int j = 0; j < 4; ++j) pw[j] = *(LAS u32x2*)(lds + ex + j * 512);
            if constexpr (LAYER == 0) {
                f1ok = __builtin_amdgcn_readfirstlane(*(LAS u32*)(lds + L_FLG));
                if (f1ok) ald4s(uu, WS_UT + (u32)(b * 8192 + opq(tid) * 16));
            }
            SB();
            f32x4 O[4], L[4];
#pragma unroll
            for (int j = 0; j < 4; ++j) {
                const s16x8 P = __builtin_bit_cast(s16x8, (u32x4){pw[j][0] & mC0, pw[j][1] & mC1, pw[j][0] & mP0, pw[j][1] & mP1});
                L[j] = mfma32(ones8, P, zero4);
                O[j] = mfma32(cat8(Vb[j + 1], Vb[j]), P, zero4);
            }
            u32 ow[4][2];
#pragma unroll
            for (int j = 0; j < 4; ++j) {
                const float inv = __builtin_amdgcn_rcpf(L[j][0]);
                ow[j][0] = nosent(pk2(O[j][0] * inv, O[j][1] * inv)); ow[j][1] = nosent(pk2(O[j][2] * inv, O[j][3] * inv));
            }
#pragma unroll
            for (int j = 0; j < 4; j += 2) {
                auto r0 = __builtin_amdgcn_permlane16_swap(ow[j][0], ow[j + 1][0], false, false);
                auto r1 = __builtin_amdgcn_permlane16_swap(ow[j][1], ow[j + 1][1], false, false);
                __builtin_amdgcn_raw_buffer_store_b128((u32x4){r0[0], r1[0], r0[1], r1[1]}, r_ws, oo, j * 16384, 16);
            }
            CFENCE();
        }
        if constexpr (LAYER == 0 && R == 0) { if (f1ok) ald4s(uu, WS_UT + (u32)(b * 8192 + opq(tid) * 16)); }
        SB();
        return f1ok;
    };
    const u32 f1ok = attention(IC<0>{});
    {
        const u32x4 sv = {SENT, SENT, SENT, SENT};
        const int t2 = opq(tid);
        const u32 tb = (u32)((4 * grp + hd) * 16384 + mem * 2048 + R * 1024 + (t2 & 63) * 16);
        __builtin_amdgcn_raw_buffer_store_b128(sv, r_ws, tb, WS_O2, 16);
        __builtin_amdgcn_raw_buffer_store_b128(sv, r_ws, tb, WS_X2, 16);
        __builtin_amdgcn_raw_buffer_store_b128(sv, r_ws, (u32)(b * 8192 + t2 * 16), WS_Y2, 16);
        CFENCE();
        SB();
    }

    const int TI = 4 * grp + hd;
    u32x4 of[8];
    auto load_o = [&](u32 ws_o) {
        const int lane = opq(tid) & 63;
#pragma unroll
        for (int i = 0; i < 8; ++i) of[i] = __builtin_amdgcn_raw_buffer_load_b128(r_ws, ws_o + (u32)(TI * 16384 + R * 8192 + lane * 16) + (i & 3) * 1024, (i >> 2) * 4096, 16);
    };
    u32 xw[8], yw[8];
    u32 woa[4];
    auto resid = [&](int n) { return R ? zero4 : (f32x4){bflo(xw[2 * n]), bfhi(xw[2 * n]), bflo(xw[2 * n + 1]), bfhi(xw[2 * n + 1])}; };
    s16x8 F[8][4];
    auto wo_addr = [&]() {
        const int l2 = opq(tid) & 63, lq = l2 >> 4, tq = (l2 & 15) >> 2, tp = l2 & 3;
#pragma unroll
        for (int n = 0; n < 4; ++n) woa[n] = L_WO + (u32)(R * 32768 + (8 * lq + tq) * 128 + ((n ^ (((lq & 1) << 1) | (tq >> 1))) << 5) + tp * 8);
    };
    auto load_F = [&](int ks) {
#pragma unroll
        for (int n = 0; n < 4; ++n) F[ks][n] = cat8(trr(woa[n] + ks * 4096), trr(woa[n] + ks * 4096 + 512));
    };

    {
        const int t2 = opq(tid), lane = t2 & 63, li = lane & 15, lq = lane >> 4;
        constexpr u32 OFFS = 0xEDA85410u;
        {
            const int j = t2 & 255;
            {
                if constexpr (R == 0) { if (f1ok) WAIT_VM(4); else WAIT_VM(3); }
                else WAIT_VM(5);
                u32 cb = L_HC + R * 34816 + (j + 2) * 16;
                pin(cb);
#pragma unroll
                for (int ch = R; ch < 2; ++ch) {
                    pin(hr[2 * ch]); pin(hr[2 * ch + 1]);
                    const u32 d[8] = {hr[2 * ch][0], hr[2 * ch][1], hr[2 * ch][2], hr[2 * ch][3], hr[2 * ch + 1][0], hr[2 * ch + 1][1], hr[2 * ch + 1][2], hr[2 * ch + 1][3]};
                    u32 e[7];
#pragma unroll
                    for (int i = 0; i < 7; ++i) e[i] = __builtin_amdgcn_alignbit(d[i + 1], d[i], 16);
                    if (ch > R) { cb += 34816; pin(cb); }
#pragma unroll
                    for (int p = (R ? 4 : 0); p < ((ch && !R) ? 4 : 8); ++p) {
                        const u32 po = (272 * p + ((OFFS >> (4 * p)) & 15)) * 16;
                        const int h = p >> 1;
                        const u32x4 w = (p & 1) ? (u32x4){e[h], e[h + 1], e[h + 2], e[h + 3]} : (u32x4){d[h], d[h + 1], d[h + 2], d[h + 3]};
                        *(LAS u32x4*)(lds + cb + po) = w;
                    }
                }
            }
            if constexpr (R == 0) {
                if (hd == 0) {
                    u32 hh[4];
#pragma unroll
                    for (int i = 0; i < 4; ++i) { const u32 a = __builtin_amdgcn_readfirstlane(hr[0][i]), c = __builtin_amdgcn_readfirstlane(hr[2][i]); hh[i] = (lane & 16) ? c : a; }
                    if (lane < 32) {
                        const int p = lane & 7;
                        const u32 po = (272 * p + ((OFFS >> (4 * p)) & 15)) * 16;
                        const unsigned __int128 hv = ((unsigned __int128)(((u64)hh[3] << 32) | hh[2]) << 64) | (((u64)hh[1] << 32) | hh[0]);
                        const unsigned __int128 sv = (p == 0 || !(lane & 8)) ? (unsigned __int128)0 : (hv << (16 * (8 - p)));
                        const u32x4 w = {(u32)sv, (u32)(sv >> 32), (u32)(sv >> 64), (u32)(sv >> 96)};
                        *(LAS u32x4*)(lds + L_HC + (lane >> 4) * 34816 + po + ((lane >> 3) & 1) * 16) = w;
                    }
                }
            }
            if (hd == 1 && lane < 30) *(LAS u32x4*)(lds + L_UR + R * 4576 + 4096 + lane * 16) = (u32x4){0, 0, 0, 0};
            if (!f1ok) {
                const u32 fo = WS_F1 + (u32)(((lane & 8) ? (lane & 7) * 32 + grp : (b >> 5) * 32 + 31 - hd * 8 - (lane & 7)) * 128);
                flg = (u32x2){0u, 0u};
                spin_flags(flg, fo);
                ald4s(uu, WS_UT + (u32)(b * 8192 + t2 * 16));
            }
            if (f1ok) WAIT_VM(R ? 5 : 3); else WAIT_VM(0);
            pin(uu);
            *(LAS u32x4*)(lds + L_UR + R * 4576 + j * 16) = uu;
        }
        BAR();
        const int ch = hd >> 1, e0 = (hd & 1) * 4;
        const int p = (li + 1) & 7;
        u32 aA = L_HC + ch * 34816 + (272 * p + ((OFFS >> (4 * p)) & 15) + ((li + 1) >> 3) + lq + 4 * e0) * 16;
        u32 aB = L_UR + ch * 4576 + (254 - 224 - 2 * li + lq + 4 * e0) * 16;
        asm volatile("" : "+v"(aA), "+v"(aB));
        f32x4 y[4];
        u32 ooff = WS_O1 + (u32)(TI * 16384 + R * 8192 + lane * 16);
        {
            constexpr ConvSched S(R);
            s16x8 A[4][8], B[4][8];
            auto loads_at = [&](auto pc) {
                constexpr int p = decltype(pc)::value;
                sfor<0, 64>([&](auto ic) {
                    constexpr int i = decltype(ic)::value, e = i >> 4, f = i & 7;
                    if constexpr (S.slot[i] == p) {
                        if constexpr (i & 8) B[e][f] = ldsA(aB + 64 * e + 512 * (7 - f)); else A[e][f] = ldsA(aA + 64 * (8 * f + e));
                    }
                });
            };
            loads_at(IC<-1>{});
            SB();
            sfor<0, 72>([&](auto pc) {
                constexpr int p = decltype(pc)::value, e = p / 18, f = S.f[p % 18], i = S.i[p % 18], g = S.n[i] - f;
                y[i] = mfma32(A[e][f], B[e][g], (e == 0 && f == 0) ? zero4 : y[i]);
                loads_at(pc);
                if constexpr (p == 54) WAIT_VM(0);
                if constexpr (p >= 55 && p < 71 && (p & 1)) {
                    constexpr int i = (p - 55) >> 1;
                    ald4o<(i & 3) * 1024>(of[i], ooff + (i >> 2) * 4096, d_ws);
                }
                SB();
            });
        }
        RBAR();
        if (R == 0 && tid == 0) __builtin_amdgcn_raw_buffer_store_b64((u32x2){tl, th}, r_ws, WS_F2 + (grp * 8 + mem) * 128, 0, 16);
        {
            constexpr int N0[4] = {0, 3, 4, 7};
            u32 ra = L_HC + hd * 8192 + lane * 16;
            asm volatile("" : "+v"(ra));
#pragma unroll
            for (int i = 0; i < 4; ++i) *(LAS f32x4*)(lds + ra + (N0[i] ^ R) * 1024) = y[i];
        }
        SB();
        if constexpr (R == 0) {
#pragma unroll
            for (int n = 0; n < 4; ++n) { const u32x2 w = __builtin_amdgcn_raw_buffer_load_b64(r_x, (u32)((TI * 16 + li) * 1024 + mem * 128 + lq * 8) + n * 32, 0, 0); xw[2 * n] = w[0]; xw[2 * n + 1] = w[1]; }
        }
        wo_addr();
        SB();
        BAR();
        {
            const float D0 = bflo(dw), D1 = bfhi(dw);
            u32 ra = L_HC + wave * 1024 + lane * 16;
            const int tb = 256 * wave + 16 * li + 4 * lq;
            u32 ru = L_UR + (2044 - tb) * 2;
            asm volatile("" : "+v"(ra), "+v"(ru));
            const f32x4 y0 = *(LAS f32x4*)(lds + ra), y1 = *(LAS f32x4*)(lds + ra + 8192), y2 = *(LAS f32x4*)(lds + ra + 16384), y3 = *(LAS f32x4*)(lds + ra + 24576);
            const u32x2 ua = *(LAS u32x2*)(lds + ru), ub = *(LAS u32x2*)(lds + ru + 4576);
            load_F(0); load_F(1);
            SB();
            const f32x4 ya = y0 + y1, yb = y2 + y3;
            const u32x4 w = {nosent(pk2(ya[0] + bfhi(ua[1]) * D0, yb[0] + bfhi(ub[1]) * D1)), nosent(pk2(ya[1] + bflo(ua[1]) * D0, yb[1] + bflo(ub[1]) * D1)),
                             nosent(pk2(ya[2] + bfhi(ua[0]) * D0, yb[2] + bfhi(ub[0]) * D1)), nosent(pk2(ya[3] + bflo(ua[0]) * D0, yb[3] + bflo(ub[0]) * D1))};
            __builtin_amdgcn_raw_buffer_store_b128(w, r_ws, WS_Y2 + (u32)(b * 8192 + tb * 4), 0, 16);
        }
        SB();
        WAIT_VM(1);

        u32 f2oth = 0;
        _Pragma("unroll 1") for (int attempt = 0; attempt < 4096; ++attempt) {
            sfor<0, 8>([&](auto ic) { pin(of[decltype(ic)::value]); });
            u32 m0 = 0, m1 = 0;
#pragma unroll
            for (int ks = 0; ks < 8; ++ks) {
                load_F((ks + 2) & 7);
                if (R == 1 && ks == 4 && wave == 7) {
                    const int pb = 32 * mem + (lane & 31);
                    f2oth = WS_F2 + (u32)((lane < 32 ? ((pb & 7) * 4 + (pb >> 6)) * 8 + ((pb >> 3) & 7) : max(grp * 8 - 8, 0) + (lane & 15)) * 128);
                    ald2s(flg, f2oth);
                }
                SB();
#pragma unroll
                for (int n = 0; n < 4; ++n) accM[n] = mfma32(F[ks][n], __builtin_bit_cast(s16x8, of[ks]), ks ? accM[n] : resid(n));
                m0 = max(max(m0, of[ks][0]), of[ks][1]); m1 = max(max(m1, of[ks][2]), of[ks][3]);
                SB();
            }
            if (__builtin_expect(__all(max(m0, m1) != SENT), 1)) break;
            __builtin_amdgcn_s_sleep(4);
            WAIT_VM(0);
            sfor<0, 8>([&](auto ic) { constexpr int i = decltype(ic)::value; ald4o<(i & 3) * 1024>(of[i], ooff + (i >> 2) * 4096, d_ws); });
            WAIT_VM(0);
        }
        if constexpr (R == 1) { if (wave == 7) { WAIT_VM(0); pin(flg); spin_flags(flg, f2oth); } }
    }

    auto load_y = [&]() {
        const int l2 = opq(tid) & 63;
#pragma unroll
        for (int i = 0; i < 8; ++i)
            yw[i] = __builtin_amdgcn_raw_buffer_load_b32(r_ws, WS_Y2 + (u32)((32 * mem + 2 * (l2 >> 4)) * 8192 + (TI * 16 + (l2 & 15)) * 4), (8 * (i >> 1) + (i & 1)) * 8192, 16);
    };
    auto pub_off = [&](u32 ws_st) {
        const int lane = opq(tid) & 63;
        return (lane & 16) ? 0xFFFFFF00u : ws_st + (u32)((TI * 8 + mem) * 256 + (lane & 15) * 16 + (lane >> 5) * 8);
    };
    auto pub_stat = [&](u32 off, u32 tg, float s, float q) {
        auto r = __builtin_amdgcn_permlane32_swap(__float_as_uint(s), __float_as_uint(q), false, false);
        const float v = __uint_as_float(r[0]) + __uint_as_float(r[1]);
        auto t = __builtin_amdgcn_permlane16_swap(__float_as_uint(v), __float_as_uint(v), false, false);
        __builtin_amdgcn_raw_buffer_store_b64((u32x2){__float_as_uint(__uint_as_float(t[0]) + __uint_as_float(t[1])), tg}, r_ws, off, 0, 16);
    };
    auto stat_off = [&](u32 ws_st, int ti) { const int l2 = opq(tid) & 63; return ws_st + (u32)(ti * 2048 + (l2 >> 4) * 512 + (l2 & 15) * 16); };
    auto fin_stat = [&](u32x4& a, u32x4& c, u32 off, float& mean, float& rstd) {
        _Pragma("unroll 1") for (int spin = 0; spin < SPIN_CAP; ++spin) {
            if (__builtin_expect(__all(((a[1] ^ tl) | (a[3] ^ th) | (c[1] ^ tl) | (c[3] ^ th)) == 0), 1)) break;
            __builtin_amdgcn_s_sleep(1);
            asm volatile("buffer_load_dwordx4 %0, %2, %3, 0 offen sc1\n\tbuffer_load_dwordx4 %1, %2, %3, 0 offen offset:256 sc1\n\ts_waitcnt vmcnt(0)"
                         : "=&v"(a), "=&v"(c) : "v"(off), "s"(d_ws) : "memory");
        }
        auto r = __builtin_amdgcn_permlane32_swap(__float_as_uint(__uint_as_float(a[0]) + __uint_as_float(c[0])), __float_as_uint(__uint_as_float(a[2]) + __uint_as_float(c[2])), false, false);
        const float v = __uint_as_float(r[0]) + __uint_as_float(r[1]);
        auto t = __builtin_amdgcn_permlane16_swap(__float_as_uint(v), __float_as_uint(v), false, false);
        const u32 v2 = __float_as_uint(__uint_as_float(t[0]) + __uint_as_float(t[1]));
        auto u = __builtin_amdgcn_permlane32_swap(v2, v2, false, false);
        const float ts = __uint_as_float(u[0]), tq = __uint_as_float(u[1]);
        mean = ts * (1.f / 512.f);
        rstd = __builtin_amdgcn_rsqf(fmaxf(tq * (1.f / 512.f) - mean * mean, 0.f) + 1e-5f);
    };
    auto regroup = [&](u32 (&w)[8]) {
#pragma unroll
        for (int j = 0; j < 2; ++j) {
            auto r0 = __builtin_amdgcn_permlane16_swap(w[4 * j], w[4 * j + 2], false, false);
            auto r1 = __builtin_amdgcn_permlane16_swap(w[4 * j + 1], w[4 * j + 3], false, false);
            w[4 * j] = r0[0]; w[4 * j + 1] = r1[0]; w[4 * j + 2] = r0[1]; w[4 * j + 3] = r1[1];
        }
    };
    auto wo_finish = [&](auto lc) {
        constexpr int LAYER = decltype(lc)::value;
        const int lane = opq(tid) & 63, li = lane & 15, lq = lane >> 4;
        u32 wx = L_WX + hd * 4096 + lane * 16;
        pin(wx);
        u32 po = 0, so = 0, tg = 0, oa = 0;
        if constexpr (R == 1) {
#pragma unroll
            for (int n = 0; n < 4; ++n) *(LAS f32x4*)(lds + wx + n * 1024) = accM[n];
        } else {
            po = pub_off(LAYER ? WS_ST3 : WS_ST1); so = stat_off(LAYER ? WS_ST3 : WS_ST1, TI); tg = lane < 32 ? tl : th;
            const int c = 2 * (lq & 1) + (lq >> 1);
            oa = LAYER ? (u32)((TI * 16 + li) * 1024 + mem * 128 + c * 16) : WS_X2 + (u32)((TI * 8 + mem) * 2048 + li * 128 + ((c ^ (li >> 1)) << 4));
            pin(po); pin(so); pin(tg); pin(oa);
        }
        BAR();
        if constexpr (R == 0) {
            f32x4 t[4];
#pragma unroll
            for (int n = 0; n < 4; ++n) t[n] = *(LAS f32x4*)(lds + wx + n * 1024);
            SB();
            float s = 0.f, q = 0.f, s2 = 0.f, q2 = 0.f;
#pragma unroll
            for (int n = 0; n < 4; ++n) {
                const f32x4 v = accM[n] + t[n];
                const u32 w0 = pk2(v[0], v[1]), w1 = pk2(v[2], v[3]);
                xw[2 * n] = w0; xw[2 * n + 1] = w1;
                s = dot2(w0, 0x3F803F80u, s); q = dot2(w0, w0, q); s2 = dot2(w1, 0x3F803F80u, s2); q2 = dot2(w1, w1, q2);
            }
            pub_stat(po, tg, s + s2, q + q2);
            CFENCE();
            if constexpr (!LAYER) load_y();
            regroup(xw);
            SB();
            WAIT_VM(0);
            if constexpr (!LAYER) {
#pragma unroll
                for (int i = 0; i < 8; ++i) pin(yw[i]);
            }
            u32x4 a, c;
            ald4s(a, so); ald4s(c, so + 256);
            if constexpr (!LAYER) {
                _Pragma("unroll 1") for (int spin = 0; spin < SPIN_CAP; ++spin) {
                    if (__builtin_expect(__all(max(max(max(yw[0], yw[1]), max(yw[2], yw[3])), max(max(yw[4], yw[5]), max(yw[6], yw[7]))) != SENT), 1)) break;
                    __builtin_amdgcn_s_sleep(1);
                    CFENCE();
                    load_y();
                }
                regroup(yw);
            }
            f32x2 xa[8], ya[8], xy[8];
#pragma unroll
            for (int i = 0; i < 8; ++i) {
                xa[i] = (f32x2){bflo(xw[i]), bfhi(xw[i])}; ya[i] = (f32x2){bflo(yw[i]), bfhi(yw[i])}; xy[i] = xa[i] * ya[i];
                pin(xa[i]); pin(ya[i]); pin(xy[i]);
            }
            WAIT_VM(0);
            pin(a); pin(c);
            float mean, rstd;
            fin_stat(a, c, so, mean, rstd);
            const float nm = -mean * rstd;
#pragma unroll
            for (int i = 0; i < 8; ++i) {
                const f32x2 o = xy[i] * rstd + (ya[i] * nm + xa[i]);
                xw[i] = pk2(o[0], o[1]);
                if (!LAYER) xw[i] = nosent(xw[i]);
            }
#pragma unroll
            for (int j = 0; j < 2; ++j) {
                const u32x4 o = {xw[4 * j], xw[4 * j + 1], xw[4 * j + 2], xw[4 * j + 3]};
                if constexpr (LAYER) __builtin_amdgcn_raw_buffer_store_b128(o, r_out, oa + j * 64, 0, 0);
                else __builtin_amdgcn_raw_buffer_store_b128(o, r_ws, oa ^ (j * 64), 0, 16);
            }
            if constexpr (!LAYER) {
                float s = 0.f, q = 0.f, s2 = 0.f, q2 = 0.f;
#pragma unroll
                for (int i = 0; i < 8; i += 2) { s = dot2(xw[i], 0x3F803F80u, s); q = dot2(xw[i], xw[i], q); s2 = dot2(xw[i + 1], 0x3F803F80u, s2); q2 = dot2(xw[i + 1], xw[i + 1], q2); }
                pub_stat(po + (WS_ST2 - WS_ST1) * !(lane & 16), tg, s + s2, q + q2);
                CFENCE();
                regroup(xw);
#pragma unroll
                for (int i = 0; i < 8; ++i) { pin(xw[i]); pin(yw[i]); }
                WAIT_VM(0);
            }
        }
    };
    wo_finish(IC<0>{});
    if constexpr (R == 1) {
        if (b == 0 && wave == 7) {
            const u32 fo = WS_F1 + (u32)((opq(tid) & 63) * 128);
            u32x2 ef[4];
#pragma unroll
            for (int i = 0; i < 4; ++i) ald2s(ef[i], fo + i * 8192);
            WAIT_VM(0);
#pragma unroll
            for (int i = 0; i < 4; ++i) { pin(ef[i]); spin_flags(ef[i], fo + i * 8192); }
            if (fo == WS_F1) __builtin_amdgcn_raw_buffer_store_b64(__builtin_bit_cast(u32x2, epoch + 1), r_ws, WS_EPOCH, 0, 16);
            WAIT_VM(0);
        }
    }
    RBAR();

    {
        const int l3 = opq(tid) & 63;
        const u32 g0 = (u32)((4 * grp - 1 + (hd >> 1)) * 16384 + (hd & 1) * 1024 + l3 * 16);
        const u32 g2 = (u32)((4 * grp + 3) * 16384 + hd * 512 + l3 * 16);
        auto x2_piece = [&](auto ic) {
            constexpr int i = decltype(ic)::value, s = i / 3, p = i % 3;
            const u32 kp = (u32)((s + rot) & 7);
            if constexpr (p == 0) dma(IC<4>{}, IC<L_CX + s * 10240>{}, d_x2, hb1k, g0, kp * 2048);
            else if constexpr (p == 1) dma(IC<4>{}, IC<L_CX + s * 10240 + 4096>{}, d_x2, hb1k, g0 + 32768, kp * 2048);
            else dma(IC<5>{}, IC<L_CX + s * 10240 + 8192>{}, d_x2, hb512, g2, kp * 2048);
        };
        u32 va = L_CX + hd * 1024 + l3 * 16, vq = L_CX + 8192 + hd * 512 + (l3 & 31) * 16 + (l3 >> 5) * 10240;
        pin(va); pin(vq);
        u32x4 vr[5];
        auto ver_read = [&](auto cc) {
            constexpr int c = decltype(cc)::value;
            u32 a = va + c * 20480, q = vq + c * 20480;
            pin(a); pin(q); CFENCE();
            vr[0] = *(LAS u32x4*)(lds + a); vr[1] = *(LAS u32x4*)(lds + a + 4096); vr[2] = *(LAS u32x4*)(lds + a + 10240); vr[3] = *(LAS u32x4*)(lds + a + 14336);
            vr[4] = *(LAS u32x4*)(lds + q);
        };
        auto ver_check = [&](auto cc) {
            constexpr int c = decltype(cc)::value;
            _Pragma("unroll 1") for (int spin = 0; spin < SPIN_CAP; ++spin) {
                u32 m0 = 0, m1 = 0;
#pragma unroll
                for (int i = 0; i < 5; ++i) { m0 = max(max(m0, vr[i][0]), vr[i][1]); m1 = max(max(m1, vr[i][2]), vr[i][3]); }
                if (__builtin_expect(__all(max(m0, m1) != SENT), 1)) break;
                __builtin_amdgcn_s_sleep(1);
                sfor<6 * c, 6 * c + 6>([&](auto ic) { x2_piece(ic); });
                WAIT_VM(0);
                ver_read(cc);
            }
        };
        const u32 xb0 = (u32)((l3 & 15) * 128 + (((l3 >> 4) ^ ((l3 & 15) >> 1)) << 4));
        if constexpr (R == 1) {
            sfor<0, 12>([&](auto ic) { x2_piece(ic); });
            WAIT_VM(6);
            ver_read(IC<0>{});
            ver_check(IC<0>{});
        } else {
            u32x4 sa, sc, ha, hc;
            const u32 so = stat_off(WS_ST2, 4 * grp + hd);
            const bool halo = hd == 0 && grp > 0;
            ald4s(sa, so); ald4s(sc, so + 256);
            if (halo) { ald4s(ha, so - 2048); ald4s(hc, so - 2048 + 256); }
            WAIT_VM(0);
            pin(sa); pin(sc);
            float mean, rstd;
            fin_stat(sa, sc, so, mean, rstd);
            if (l3 < 16) { *(LAS float*)(lds + L_TAB + 64 + hd * 64 + l3 * 4) = mean; *(LAS float*)(lds + L_TAB + 320 + 64 + hd * 64 + l3 * 4) = rstd; }
            if (halo) {
                pin(ha); pin(hc);
                fin_stat(ha, hc, so - 2048, mean, rstd);
                if (l3 < 16) { *(LAS float*)(lds + L_TAB + l3 * 4) = mean; *(LAS float*)(lds + L_TAB + 320 + l3 * 4) = rstd; }
            }
        }
        BAR();
        u32 xa;
        auto pf_addr = [&](int st) { xa = L_CX + (st >> 1) * 10240 + (xb0 ^ ((st & 1) * 64)); pin(xa); };
        pf_addr(0);
#pragma unroll
        for (int mt = 0; mt < 5; ++mt) Ab[0][mt] = ldsA(xa + mt * 2048);
        sfor<0, 16>([&](auto stc) {
            constexpr int st = decltype(stc)::value;
            if constexpr (R == 1) {
                if constexpr (st == 2) { WAIT_VM(10); ver_read(IC<1>{}); }
                if constexpr (st == 3) ver_check(IC<1>{});
                if constexpr (st == 5) { WAIT_VM(6); ver_read(IC<2>{}); }
                if constexpr (st == 6) { ver_check(IC<2>{}); WAIT_VM(0); ver_read(IC<3>{}); }
                if constexpr (st == 7) ver_check(IC<3>{});
            }
            if constexpr (st == 3) { RBAR(); }
            if constexpr (st == 7) { RBAR(); }
            if (st < 15) pf_addr(st + 1);
            if constexpr (st == 14) load_tab(true);
            SB();
            qkv_step(IC<1>{}, st, Ab[st & 1], [&](auto mc) {
                constexpr int mt = decltype(mc)::value;
                if (st < 15) Ab[(st + 1) & 1][mt] = ldsA(xa + mt * 2048);
                if constexpr (R == 1 && 12 + 5 * st + mt < 24) x2_piece(IC<12 + 5 * st + mt>{});
                SB();
            });
        });
    }
    attention(IC<1>{});

    {
        wo_addr();
#pragma unroll
        for (int ks = 0; ks < 8; ++ks) load_F(ks);
        SB();
        WAIT_VM(0);
        RBAR();
        load_o(WS_O2);
        SB();
        _Pragma("unroll 1") for (int spin = 0; spin < 64; ++spin) {
            u32 m0 = 0, m1 = 0;
#pragma unroll
            for (int ks = 0; ks < 8; ++ks) {
#pragma unroll
                for (int n = 0; n < 4; ++n) accM[n] = mfma32(F[ks][n], __builtin_bit_cast(s16x8, of[ks]), ks ? accM[n] : resid(n));
                m0 = max(max(m0, of[ks][0]), of[ks][1]); m1 = max(max(m1, of[ks][2]), of[ks][3]);
                SB();
            }
            if (__builtin_expect(__all(max(m0, m1) != SENT), 1)) break;
            _Pragma("unroll 1") for (int sp2 = 0; sp2 < SPIN_CAP; ++sp2) {
                CFENCE();
                __builtin_amdgcn_s_sleep(1);
                const u32 pv = __builtin_amdgcn_raw_buffer_load_b32(r_ws, WS_O2 + (u32)(TI * 16384 + R * 8192 + (opq(tid) & 15) * 512 + 508), 0, 16);
                if (__all(pv != SENT)) break;
            }
            CFENCE();
            load_o(WS_O2);
        }
        SB();
    }
    wo_finish(IC<1>{});
}

__global__ void __launch_bounds__(512, 2)
fused_kernel(const u16* __restrict__ gx, const u16* __restrict__ gwqkv, const u16* __restrict__ gwo,
             const u16* __restrict__ gbk, const u16* __restrict__ gbv, const u16* __restrict__ gsk,
             const u16* __restrict__ ghk, const u16* __restrict__ gdv, u16* __restrict__ gout, char* __restrict__ ws) {
    __shared__ __attribute__((aligned(16))) char lds_[L_TOTAL];
    typedef u32 u32x16 __attribute__((ext_vector_type(16)));
    u32x16 ka; u32x4 kb;
    {
        auto kp = __builtin_amdgcn_kernarg_segment_ptr();
        asm volatile("s_load_dwordx16 %0, %2, 0x0\n\ts_load_dwordx4 %1, %2, 0x40\n\ts_waitcnt lgkmcnt(0)" : "=&s"(ka), "=&s"(kb) : "s"(kp) : "memory");
    }
    auto mkp = [](u32 lo, u32 hi) { return (char*)(((u64)hi << 32) | lo); };
    const Args a = {(const u16*)mkp(ka[0], ka[1]), (const u16*)mkp(ka[2], ka[3]), (const u16*)mkp(ka[4], ka[5]), (const u16*)mkp(ka[6], ka[7]), (const u16*)mkp(ka[8], ka[9]),
                    (const u16*)mkp(ka[10], ka[11]), (const u16*)mkp(ka[12], ka[13]), (const u16*)mkp(ka[14], ka[15]), (u16*)mkp(kb[0], kb[1]), mkp(kb[2], kb[3])};
    const int wave = __builtin_amdgcn_readfirstlane(threadIdx.x >> 6);
    u64 pc0;
    asm volatile("s_getpc_b64 %0" : "=s"(pc0));
    if (wave < 4) body<0>(a, (LAS char*)lds_, wave, pc0); else body<1>(a, (LAS char*)lds_, wave, pc0);
}

extern "C" void kernel_launch(void* const* d_in, const int* in_sizes, int n_in,
                              void* d_out, int out_size, void* d_ws, size_t ws_size,
                              hipStream_t stream) {
    fused_kernel<<<256, 512, 0, stream>>>((const u16*)d_in[0], (const u16*)d_in[1], (const u16*)d_in[2],
                                          (const u16*)d_in[4], (const u16*)d_in[5], (const u16*)d_in[6],
                                          (const u16*)d_in[7], (const u16*)d_in[8], (u16*)d_out, (char*)d_ws);
}
```

```cpp
#include <hip/hip_runtime.h>
#include <stdint.h>

typedef unsigned short u16;
typedef unsigned u32;
typedef unsigned long long u64;
typedef short s16x4 __attribute__((ext_vector_type(4)));
typedef short s16x8 __attribute__((ext_vector_type(8)));
typedef __bf16 bf16x2 __attribute__((ext_vector_type(2)));
typedef float f32x2 __attribute__((ext_vector_type(2)));
typedef float f32x4 __attribute__((ext_vector_type(4)));
typedef u32 u32x2 __attribute__((ext_vector_type(2)));
typedef u32 u32x4 __attribute__((ext_vector_type(4)));
#define LAS __attribute__((address_space(3)))
#define DEV __device__ __forceinline__
typedef __amdgpu_buffer_rsrc_t rsrc_t;

constexpr u32 WS_EPOCH = 0;
constexpr u32 WS_F1 = 32768;
constexpr u32 WS_F2 = 65536;
constexpr u32 WS_ST0 = 98304;
constexpr u32 WS_ST1 = 131072;
constexpr u32 WS_ST2 = WS_ST1 + 262144;
constexpr u32 WS_ST3 = WS_ST2 + 262144;
constexpr u32 WS_UT = 1u << 20;
constexpr u32 WS_O1 = WS_UT + (2u << 20);
constexpr u32 WS_O2 = WS_O1 + (2u << 20);
constexpr u32 WS_Y2 = WS_O2 + (2u << 20);
constexpr u32 WS_X2 = WS_Y2 + (2u << 20);
constexpr u32 WS_END = WS_X2 + (2u << 20);

constexpr u32 L_SLOT = 34816;
constexpr u32 L_KV = 4 * L_SLOT;
constexpr u32 L_K = L_KV, L_V = L_KV + 9216;
constexpr u32 L_SK = L_KV + 17408;
constexpr u32 L_WO = 0;
constexpr u32 L_CX = 65536;
constexpr u32 L_HC = L_CX;
constexpr u32 L_UR = L_CX + 69632;
constexpr u32 L_WX = L_CX + 40960;
constexpr u32 L_EX = 147456;
constexpr u32 L_TAB = L_EX + 12288;
constexpr u32 L_CS = L_TAB + 1024;
constexpr u32 L_FLG = L_TAB + 2048;
constexpr u32 L_DUMMY = L_FLG + 1024;
constexpr u32 L_TOTAL = 160 * 1024;

constexpr int SPIN_CAP = 1 << 14;
constexpr u32 SENT = 0xFFFFFFFFu;

#define WAIT_VM(n) asm volatile("s_waitcnt vmcnt(%0)" ::"i"(n) : "memory")
#define BAR() do { asm volatile("s_waitcnt lgkmcnt(0)" ::: "memory"); __builtin_amdgcn_s_barrier(); asm volatile("" ::: "memory"); } while (0)
#define RBAR() do { asm volatile("" ::: "memory"); __builtin_amdgcn_s_barrier(); asm volatile("" ::: "memory"); } while (0)
#define CFENCE() asm volatile("" ::: "memory")
#define SB() __builtin_amdgcn_sched_barrier(0)
template <int N> struct IC { static constexpr int value = N; };
template <int I, int N, class F> DEV void sfor(F&& f) { if constexpr (I < N) { f(IC<I>{}); sfor<I + 1, N>(f); } }

DEV f32x4 mfma32(s16x8 a, s16x8 b, f32x4 c) { return __builtin_amdgcn_mfma_f32_16x16x32_bf16(a, b, c, 0, 0, 0); }
DEV f32x4 mfma16(s16x4 a, s16x4 b, f32x4 c) { return __builtin_amdgcn_mfma_f32_16x16x16bf16_1k(a, b, c, 0, 0, 0); }
DEV float vmax(float a, float b) { float r; asm("v_max_f32 %0, %1, %2" : "=v"(r) : "v"(a), "v"(b)); return r; }
DEV u32 pk2(float lo, float hi) { f32x2 f = {lo, hi}; bf16x2 b = __builtin_convertvector(f, bf16x2); return __builtin_bit_cast(u32, b); }
DEV float bflo(u32 w) { return __uint_as_float(w << 16); }
DEV float bfhi(u32 w) { return __uint_as_float(w & 0xffff0000u); }
DEV float dot2(u32 a, u32 b, float c) { return __builtin_amdgcn_fdot2_f32_bf16(__builtin_bit_cast(bf16x2, a), __builtin_bit_cast(bf16x2, b), c, false); }
DEV u32 nosent(u32 w) { return min(w, 0xFFFFFFFEu); }
DEV float rowsum16(float v) {
    v += __builtin_bit_cast(float, __builtin_amdgcn_update_dpp(0, __builtin_bit_cast(int, v), 0xB1, 0xf, 0xf, true));
    v += __builtin_bit_cast(float, __builtin_amdgcn_update_dpp(0, __builtin_bit_cast(int, v), 0x4E, 0xf, 0xf, true));
    v += __builtin_bit_cast(float, __builtin_amdgcn_update_dpp(0, __builtin_bit_cast(int, v), 0x141, 0xf, 0xf, true));
    v += __builtin_bit_cast(float, __builtin_amdgcn_update_dpp(0, __builtin_bit_cast(int, v), 0x140, 0xf, 0xf, true));
    return v;
}
DEV float bcast_row0(float v) { return __builtin_bit_cast(float, __builtin_amdgcn_update_dpp(0, __builtin_bit_cast(int, v), 0x150, 0xf, 0xf, true)); }
template <int OFF> DEV void ald4o(u32x4& d, u32 voff, u32x4 rs) { asm volatile("buffer_load_dwordx4 %0, %1, %2, 0 offen offset:%3 sc1" : "=v"(d) : "v"(voff), "s"(rs), "i"(OFF) : "memory"); }
template <class T> DEV void pin(T& v) { asm volatile("" : "+v"(v)); }
DEV int opq(int v) { asm volatile("" : "+v"(v)); return v; }
DEV s16x8 cat8(s16x4 a, s16x4 b) { return __builtin_shufflevector(a, b, 0, 1, 2, 3, 4, 5, 6, 7); }
DEV s16x4 pack4(f32x4 v) { u32x2 w = {pk2(v[0], v[1]), pk2(v[2], v[3])}; return __builtin_bit_cast(s16x4, w); }
DEV u64 mix64(u64 z) { z += 0x9E3779B97F4A7C15ull; z = (z ^ (z >> 30)) * 0xBF58476D1CE4E5B9ull; z = (z ^ (z >> 27)) * 0x94D049BB133111EBull; return z ^ (z >> 31); }

struct ConvSched {
    int n[4], f[18], i[18], slot[64];
    constexpr ConvSched(int R) : n{}, f{}, i{}, slot{} {
        constexpr int LEAD = 9;
        const int N0[4] = {0, 3, 4, 7};
        for (int k = 0; k < 4; ++k) n[k] = N0[k] ^ R;
        int c = 0;
        for (int ff = 0; ff < 8; ++ff) for (int k = 3; k >= 0; --k) if (ff <= n[k]) { f[c] = ff; i[c] = k; ++c; }
        for (int k = 0; k < 64; ++k) slot[k] = 1000;
        for (int p = 71; p >= 0; --p) {
            const int e = p / 18, ff = f[p % 18], g = n[i[p % 18]] - ff, s = p - LEAD < -1 ? -1 : p - LEAD;
            slot[e * 16 + ff] = s; slot[e * 16 + 8 + g] = s;
        }
    }
};

struct Args { const u16 *gx, *gwqkv, *gwo, *gbk, *gbv, *gsk, *ghk, *gdv; u16* gout; char* ws; };

template <int R>
DEV void body(const Args& A_, LAS char* const lds, const int wave) {
    const u16* gx = A_.gx; char* ws = A_.ws;
    const int b = blockIdx.x, tid = threadIdx.x, lane = tid & 63;
    const int hd = wave & 3;
    const int grp = (b & 7) * 4 + (b >> 6), mem = (b >> 3) & 7;
    const int T0 = grp * 64;
    const int rot = (mem + 2 * (grp & 3)) & 7;

    u64 epoch;
    asm volatile("s_load_dwordx2 %0, %1, 0x0" : "=s"(epoch) : "s"(ws));
    u32 dw;
    asm volatile("s_load_dword %0, %1, 0x0" : "=s"(dw) : "s"(A_.gdv + 2 * b));
    const rsrc_t r_x = __builtin_amdgcn_make_buffer_rsrc((void*)gx, 0, 2048 * 512 * 2, 0x00020000);
    const rsrc_t r_ws = __builtin_amdgcn_make_buffer_rsrc((void*)ws, 0, WS_END, 0x00020000);
    const rsrc_t r_out = __builtin_amdgcn_make_buffer_rsrc((void*)A_.gout, 0, 2048 * 512 * 2, 0x00020000);
    const u32 ldsbase = (u32)(size_t)lds;
    auto mkdesc = [](const void* p, u32 bytes) { const u64 a = (u64)p; return (u32x4){(u32)a, (u32)(a >> 32) & 0xffffu, bytes, 0x00020000u}; };
    const u32x4 d_x = mkdesc(gx, 2048 * 512 * 2), d_w = mkdesc(A_.gwqkv, 512 * 1536 * 2), d_wo = mkdesc(A_.gwo, 512 * 512 * 2);
    const u32x4 d_k = mkdesc(A_.gbk, 2048 * 512 * 2), d_v = mkdesc(A_.gbv, 2048 * 512 * 2), d_sk = mkdesc(A_.gsk, 512 * 4 * 2);
    const u32x4 d_h = mkdesc(A_.ghk, 512 * 2048 * 2), d_x2 = mkdesc(ws + WS_X2, 2u << 20), d_ws = mkdesc(ws, WS_END);
    const u32 hb1k = ldsbase + hd * 1024, hb512 = ldsbase + hd * 512;
#define DMA_ASM(pre, mods, post) asm volatile("s_add_u32 m0, %0, %4\n\t" pre "buffer_load_dwordx4 %1, %2, %3 offen " mods "lds" post \
                     :: "s"(sbase), "v"(voff), "s"(rs), "s"(soff), "i"(IMM) : "memory", "scc")
    const u32 xmlo = hd == 0 ? ~0xFFu : ~0u;
    auto dma = [&](auto kind, auto imm, u32x4 rs, u32 sbase, u32 voff, u32 soff) {
        constexpr int K = decltype(kind)::value; constexpr int IMM = decltype(imm)::value;
        if constexpr (K == 0) DMA_ASM("s_nop 0\n\t", "", "");
        else if constexpr (K == 1) DMA_ASM("s_mov_b32 exec_hi, 0\n\t", "", "\n\ts_mov_b32 exec_hi, -1");
        else if constexpr (K == 2) {
            const u32 ml = xmlo;
            asm volatile("s_add_u32 m0, %0, %4\n\ts_mov_b32 exec_lo, %5\n\tbuffer_load_dwordx4 %1, %2, %3 offen lds\n\ts_mov_b32 exec_lo, -1"
                         :: "s"(sbase), "v"(voff), "s"(rs), "s"(soff), "i"(IMM), "s"(ml) : "memory", "scc");
        }
        else if constexpr (K == 4) DMA_ASM("s_nop 0\n\t", "sc1 ", "");
        else DMA_ASM("s_mov_b32 exec_hi, 0\n\t", "sc1 ", "\n\ts_mov_b32 exec_hi, -1");
    };
    auto ald4 = [&](u32x4& d, u32x4 rs, u32 voff) { asm volatile("buffer_load_dwordx4 %0, %1, %2, 0 offen" : "=v"(d) : "v"(voff), "s"(rs) : "memory"); };
    auto ald4s = [&](u32x4& d, u32 voff) { asm volatile("buffer_load_dwordx4 %0, %1, %2, 0 offen sc1" : "=v"(d) : "v"(voff), "s"(d_ws) : "memory"); };
    auto ald2s = [&](u32x2& d, u32 voff) { asm volatile("buffer_load_dwordx2 %0, %1, %2, 0 offen sc1" : "=v"(d) : "v"(voff), "s"(d_ws) : "memory"); };

    const int pr = lane >> 3, pc = lane & 7;
    const int xr0 = 8 * hd + pr, xr2 = 64 + 4 * hd + pr;
    const u32 xv0 = (u32)((T0 - 16 + xr0) * 1024 + ((pc ^ ((xr0 >> 1) & 7)) << 4));
    const u32 xv2 = (u32)((T0 - 16 + xr2) * 1024 + ((pc ^ ((xr2 >> 1) & 7)) << 4));
    const int wfw = ((xr0 >> 1) & 1) | (((xr0 >> 3) & 1) << 1);
    const u32 wv0 = (u32)(xr0 * 3072 + mem * 128 + ((((pc >> 1) ^ wfw) * 2 + (pc & 1)) << 4));
    auto issue_piece = [&](auto sc, auto ic) {
        constexpr int s = decltype(sc)::value, i = decltype(ic)::value;
        const u32 kp = (u32)((s + rot) & 7);
        constexpr int sb = (s & 3) * L_SLOT;
        if constexpr (i == 0) dma(IC<2>{}, IC<sb>{}, d_x, hb1k, xv0, kp * 128);
        else if constexpr (i == 1) dma(IC<0>{}, IC<sb + 4096>{}, d_x, hb1k, xv0 + 32768, kp * 128);
        else if constexpr (i == 2) dma(IC<1>{}, IC<sb + 8192>{}, d_x, hb512, xv2, kp * 128);
        else dma(IC<0>{}, IC<sb + 10240 + (i - 3) * 4096>{}, d_w, hb1k, wv0, kp * (64 * 3072) + ((i - 3) >> 1) * 1024 + ((i - 3) & 1) * (32 * 3072));
    };
    u32 wov = 0;
    auto issue_wo = [&](auto ic) {
        constexpr int i = decltype(ic)::value;
        if constexpr ((i & 7) == 0) {
            const int l2 = opq(tid) & 63, pr = l2 >> 3, pc = l2 & 7;
            wov = (u32)((8 * hd + pr) * 1024 + mem * 128 + ((((pc >> 1) ^ (((pr >> 1) & 1) | ((hd & 1) << 1))) * 2 + (pc & 1)) << 4));
            pin(wov);
        }
        const u32 j = (u32)((i + 2 * grp) & 7);
        dma(IC<0>{}, IC<L_WO + (i & 8) * 4096>{}, d_wo, hb1k + j * 4096, wov, ((i & 8) + j) * 32768);
    };
    auto issue_kv = [&](int i) {
        const int l2 = opq(tid) & 63, pr = l2 >> 3, pc = l2 & 7, p = hd + 4 * (i & 1);
        const u32 cb = (u32)(mem * 128);
        if (i < 2) dma(IC<0>{}, IC<L_K>{}, d_k, hb1k + (i & 1) * 4096, (u32)((T0 - 8 + 8 * p + pr) * 1024) + cb + ((pc ^ p) << 4), 0);
        else if (i < 4) dma(IC<0>{}, IC<L_V>{}, d_v, hb1k + (i & 1) * 4096, (u32)((T0 + 8 * p + pr) * 1024) + cb + ((pc ^ p) << 4), 0);
        else if (hd == 0) dma(IC<0>{}, IC<L_K + 8192>{}, d_k, ldsbase, (u32)((T0 - 8 + 64 + pr) * 1024) + cb + (pc << 4), 0);
        else dma(IC<0>{}, IC<L_SK>{}, d_sk, ldsbase, (u32)(mem * 512 + l2 * 16), 0);
    };

    const int srow = T0 + 8 * mem + 2 * hd;
    u32x4 xrow[2];
    sfor<0, 9>([&](auto ic) { if constexpr ((decltype(ic)::value & 1) != R) issue_piece(IC<0>{}, ic); });
    if constexpr (R == 0) {
#pragma unroll
        for (int i = 0; i < 5; ++i) issue_kv(i);
    }
    sfor<0, 9>([&](auto ic) { if constexpr ((decltype(ic)::value & 1) != R) issue_piece(IC<1>{}, ic); });
    asm volatile("s_waitcnt lgkmcnt(0)" : "+s"(epoch), "+s"(dw));
    const u64 tag = mix64(epoch);
    const u32 tl = (u32)tag, th = (u32)(tag >> 32);
    if (R == 1 && wave == 7 && lane < 32) *(LAS u32x4*)(lds + (lane >> 3) * L_SLOT + (lane & 7) * 16) = (u32x4){0x3F803F80u, 0x3F803F80u, 0x3F803F80u, 0x3F803F80u};

    auto trr = [&](u32 off) { return __builtin_amdgcn_ds_read_tr16_b64_v4i16((LAS s16x4*)(lds + off)); };
    auto ldsA = [&](u32 off) { return *(LAS s16x8*)(lds + off); };
    auto flag_ok = [&](const u32x2& f) { return __all(f[0] == tl && f[1] == th); };
    auto spin_flags = [&](u32x2& f, u32 off) {
        _Pragma("unroll 1") for (int spin = 0; spin < SPIN_CAP; ++spin) {
            if (__builtin_expect(flag_ok(f), 1)) break;
            __builtin_amdgcn_s_sleep(2);
            asm volatile("buffer_load_dwordx2 %0, %1, %2, 0 offen sc1\n\ts_waitcnt vmcnt(0)" : "=&v"(f) : "v"(off), "s"(d_ws) : "memory");
        }
    };

    s16x8 Wq[16], Wm[16];
    f32x4 accM[5], accQ[5];
    const f32x4 zero4 = {0.f, 0.f, 0.f, 0.f};
    const s16x8 ones8 = {0x3F80, 0x3F80, 0x3F80, 0x3F80, 0x3F80, 0x3F80, 0x3F80, 0x3F80};
    s16x8 Ab[3][5];
    auto qkv_step = [&](auto lc, int st, const s16x8 (&A)[5], auto fill) {
        sfor<0, 5>([&](auto mc) {
            constexpr int mt = decltype(mc)::value;
            const f32x4 cm = st ? accM[mt] : zero4;
            accM[mt] = R ? mfma32(A[mt], Wm[st], cm) : mfma32(Wm[st], A[mt], cm);
            if constexpr (R == 0 && (mt > 0 || decltype(lc)::value == 0)) accQ[mt] = mfma32(Wq[st], A[mt], st ? accQ[mt] : zero4);
            fill(mc);
        });
    };

    float m1[5], r1[5]; f32x4 m4[5], r4[5], csK, csQ;
    auto load_tab = [&](bool cs) {
        const int l2 = opq(tid) & 63;
        u32 ta = L_TAB + (R ? (l2 >> 4) * 16 : (l2 & 15) * 4);
        pin(ta);
        if (R == 0 && cs) { csK = *(LAS f32x4*)(lds + L_CS + hd * 128 + (l2 >> 4) * 16); csQ = *(LAS f32x4*)(lds + L_CS + hd * 128 + 64 + (l2 >> 4) * 16); }
#pragma unroll
        for (int mt = 0; mt < 5; ++mt) {
            if (R == 0) { m1[mt] = *(LAS float*)(lds + ta + mt * 64); r1[mt] = *(LAS float*)(lds + ta + 320 + mt * 64); }
            else { m4[mt] = *(LAS f32x4*)(lds + ta + mt * 64); r4[mt] = *(LAS f32x4*)(lds + ta + 320 + mt * 64); }
        }
    };

    u32x4 hr[4];
    u32x2 flg;
    {
        const int li = lane & 15, lq = lane >> 4, tq = li >> 2, tp = lane & 3;
        const u32 xa0 = (u32)(li * 128 + ((lq ^ (li >> 1)) << 4));
        const u32 wa = (u32)((8 * lq + tq) * 128 + ((hd ^ (((tq >> 1) & 1) | ((lq & 1) << 1))) << 5) + tp * 8);
        u32 xa, wb, kw[11], vw[8];
        u32x4 s0v, skw;
        auto pf_addr = [&](int st) {
            const u32 sb = (u32)((st >> 1) & 3) * L_SLOT;
            xa = sb + (xa0 ^ ((st & 1) * 64)); wb = sb + 10240 + (st & 1) * 4096 + wa;
            asm volatile("" : "+v"(xa), "+v"(wb));
        };
        auto pf_part = [&](int st, int mt) {
            if (mt == 0) Wm[st] = cat8(trr(wb + (1 + R) * 8192), trr(wb + (1 + R) * 8192 + 512));
            Ab[st & 1][mt] = ldsA(xa + mt * 2048);
            if (R == 0 && mt == 1) Wq[st] = cat8(trr(wb), trr(wb + 512));
        };
        WAIT_VM(R ? 5 : 4);
        BAR();
        pf_addr(0);
#pragma unroll
        for (int mt = 0; mt < 5; ++mt) pf_part(0, mt);
        sfor<0, 16>([&](auto stc) {
            constexpr int st = decltype(stc)::value;
            if constexpr ((st & 1) && st < 15) {
                constexpr int s1 = (st >> 1) + 1;
                if constexpr (R == 0) {
                    if constexpr (s1 == 1) WAIT_VM(4);
                    if constexpr (s1 == 3) WAIT_VM(0);
                } else {
                    constexpr int WN[8] = {0, 9, 9, 9, 9, 9, 10, 2};
                    WAIT_VM(WN[s1]);
                    if constexpr (s1 == 7) {
                        pin(s0v);
                        const int l2 = opq(tid) & 63, tr = hd * 20 + min(l2, 19), row = T0 - 16 + tr;
                        _Pragma("unroll 1") for (int spin = 0; spin < SPIN_CAP; ++spin) {
                            if (__builtin_expect(__all(row < 0 || (s0v[1] == tl && s0v[3] == th)), 1)) break;
                            __builtin_amdgcn_s_sleep(2);
                            ald4s(s0v, WS_ST0 + (u32)(max(row, 0) * 16));
                            WAIT_VM(0);
                            pin(s0v);
                        }
                        *(LAS u32*)(lds + L_TAB + tr * 4) = row < 0 ? 0u : s0v[0];
                        *(LAS u32*)(lds + L_TAB + 320 + tr * 4) = row < 0 ? 0x3F800000u : s0v[2];
                        asm volatile("s_waitcnt lgkmcnt(0)" ::: "memory");
                    }
                }
                RBAR();
                if constexpr (s1 == 3 && R == 0) { if (tid == 0) __builtin_amdgcn_raw_buffer_store_b64((u32x2){tl, th}, r_ws, WS_F1 + (mem * 32 + grp) * 128, 0, 16); CFENCE(); }
                if constexpr (s1 == 5 && R == 1) { const int row = T0 - 16 + hd * 20 + min(opq(tid) & 63, 19); ald4s(s0v, WS_ST0 + (u32)(max(row, 0) * 16)); }
            }
            if (st < 15) pf_addr(st + 1);
            if constexpr (st == 14) load_tab(false);
            SB();
            qkv_step(IC<0>{}, st, Ab[st & 1], [&](auto mc) {
                constexpr int mt = decltype(mc)::value;
                if (st < 15) pf_part(st + 1, mt);
                if constexpr (R == 1) {
                    if constexpr (st == 0) { issue_piece(IC<2>{}, IC<2 * mt>{}); if constexpr (mt < 4) issue_piece(IC<2>{}, IC<2 * mt + 1>{}); }
                    if constexpr (st >= 1 && st <= 10 && ((st - 1) & 1) * 5 + mt < 9) issue_piece(IC<((st - 1) >> 1) + 3>{}, IC<((st - 1) & 1) * 5 + mt>{});
                    if constexpr (st == 11 && mt == 0) { const u32 ho = (u32)(b * 8192 + opq(tid) * 16); ald4(hr[2], d_h, ho); ald4(hr[3], d_h, ho + 16); }
                    if constexpr (st == 13 && mt == 0) { if (wave == 7) { const int l2 = opq(tid) & 63; ald2s(flg, WS_F1 + (u32)((l2 < 32 ? (b >> 5) * 32 + l2 : (l2 & 7) * 32 + grp) * 128)); } }
                    if constexpr (st == 13 && mt >= 1) {
                        if constexpr (mt == 1) { const int l2 = opq(tid) & 63; wov = (u32)((l2 >> 3) * 1024 + (l2 & 7) * 16); pin(wov); }
                        dma(IC<0>{}, IC<L_DUMMY>{}, d_wo, ldsbase, wov, (u32)(((grp & 3) * 128 + hd * 32 + (mt - 1) * 8) * 1024 + mem * 128));
                    }
                } else {
                    if constexpr (st == 0) {
                        if constexpr (mt == 0) { const u32 xo = (u32)(srow * 1024 + (opq(tid) & 63) * 16); ald4(xrow[0], d_x, xo); ald4(xrow[1], d_x, xo + 1024); }
                    }
                    if constexpr (st >= 6 && st <= 8 && mt == 4) {
                        const int l2 = opq(tid) & 63;
                        const u32 co = st == 8 ? WS_Y2 + (u32)(b * 8192 + hd * 2048 + l2 * 16) : (st == 6 ? WS_O2 : WS_X2) + (u32)((4 * grp + hd) * 16384 + mem * 2048 + l2 * 16);
                        __builtin_amdgcn_raw_buffer_store_b128((u32x4){SENT, SENT, SENT, SENT}, r_ws, co, 0, 16);
                        __builtin_amdgcn_raw_buffer_store_b128((u32x4){SENT, SENT, SENT, SENT}, r_ws, co + 1024, 0, 16);
                        CFENCE();
                    }
                    if constexpr (st == 11 && mt < 2) { const u32 ho = (u32)(b * 8192 + mt * 4096 + opq(tid) * 16); ald4(hr[2 * mt], d_h, ho); ald4(hr[2 * mt + 1], d_h, ho + 16); }
                }
                SB();
            });
            if constexpr (R == 0 && st == 2) {
                WAIT_VM(4);
                pin(xrow[0]); pin(xrow[1]);
                float s[2] = {0.f, 0.f}, q[2] = {0.f, 0.f};
#pragma unroll
                for (int i = 0; i < 4; ++i)
#pragma unroll
                    for (int k = 0; k < 2; ++k) { s[k] = dot2(xrow[k][i], 0x3F803F80u, s[k]); q[k] = dot2(xrow[k][i], xrow[k][i], q[k]); }
                auto ss = __builtin_amdgcn_permlane32_swap(__float_as_uint(s[0]), __float_as_uint(s[1]), false, false);
                auto qq = __builtin_amdgcn_permlane32_swap(__float_as_uint(q[0]), __float_as_uint(q[1]), false, false);
                float sv1 = rowsum16(__uint_as_float(ss[0]) + __uint_as_float(ss[1])), qv1 = rowsum16(__uint_as_float(qq[0]) + __uint_as_float(qq[1]));
                auto s2 = __builtin_amdgcn_permlane16_swap(__float_as_uint(sv1), __float_as_uint(sv1), false, false);
                auto q2 = __builtin_amdgcn_permlane16_swap(__float_as_uint(qv1), __float_as_uint(qv1), false, false);
                const float m = (__uint_as_float(s2[0]) + __uint_as_float(s2[1])) * (1.f / 512.f);
                const float r = __builtin_amdgcn_rsqf(fmaxf((__uint_as_float(q2[0]) + __uint_as_float(q2[1])) * (1.f / 512.f) - m * m, 0.f) + 1e-5f);
                const u32x4 sv = {__float_as_uint(m), tl, __float_as_uint(r), th};
                const int l2 = opq(tid) & 63;
                __builtin_amdgcn_raw_buffer_store_b128(sv, r_ws, (l2 & 31) == 0 ? WS_ST0 + (u32)((srow + (l2 >> 5)) * 16) : 0xFFFFFF00u, 0, 16);
                CFENCE();
                SB();
            }
            if constexpr (R == 0 && (st == 0 || st == 1)) {
                const int l2 = opq(tid) & 63, tg = l2 & 7, cp = 8 * hd + (l2 >> 3);
                if constexpr (st == 0) {
                    skw = *(LAS u32x4*)(lds + L_SK + cp * 16);
                    u32 ka = L_K + tg * 1024 + ((((cp >> 2) ^ tg) & 7) << 4) + (cp & 3) * 4;
                    u32 kb = L_K + tg * 1024 + ((((cp >> 2) ^ (tg + 1)) & 7) << 4) + (cp & 3) * 4;
                    pin(ka); pin(kb);
#pragma unroll
                    for (int i = 0; i < 11; ++i) kw[i] = *(LAS u32*)(lds + ((i + 5 < 8) ? ka : kb) + (i + 5) * 128);
#pragma unroll
                    for (int i = 0; i < 8; ++i) vw[i] = *(LAS u32*)(lds + ka + (L_V - L_K) + i * 128);
                    SB();
                }
                constexpr bool HI = st == 1;
                auto ex = [&](u32 w) { return HI ? bfhi(w) : bflo(w); };
                const float a0 = bflo(skw[2 * HI]), a1 = bfhi(skw[2 * HI]), a2 = bflo(skw[2 * HI + 1]), a3 = bfhi(skw[2 * HI + 1]);
                float ua[8];
#pragma unroll
                for (int i = 0; i < 8; ++i) ua[i] = (ex(kw[i]) * a3 + ex(kw[i + 1]) * a2 + ex(kw[i + 2]) * a1 + ex(kw[i + 3]) * a0) * ex(vw[i]);
                const u32x4 oa = {pk2(ua[7], ua[6]), pk2(ua[5], ua[4]), pk2(ua[3], ua[2]), pk2(ua[1], ua[0])};
                __builtin_amdgcn_raw_buffer_store_b128(oa, r_ws, WS_UT + (u32)((mem * 64 + 2 * cp) * 4096 + (2040 - T0 - 8 * tg) * 2), HI * 4096, 16);
                __builtin_amdgcn_raw_buffer_store_b128((u32x4){SENT, SENT, SENT, SENT}, r_ws, (u32)((4 * grp + hd) * 16384 + mem * 2048 + HI * 1024 + l2 * 16), WS_O1, 16);
                CFENCE();
                SB();
            }
        });
    }

    u32x4 uu;
    float csV;
    auto attention = [&](auto lc) {
        constexpr int LAYER = decltype(lc)::value;
        const int lane = opq(tid) & 63, li = lane & 15, lq = lane >> 4;
        u32 ex = L_EX + hd * 2048 + lane * 8;
        pin(ex);
        u32 f1ok = 1;
        if constexpr (R == 0) {
            if constexpr (LAYER == 0) {
#pragma unroll
                for (int i = 0; i < 4; ++i) { csK[i] = bcast_row0(accM[0][i]); csQ[i] = bcast_row0(accQ[0][i]); }
                if (li == 0) { *(LAS f32x4*)(lds + L_CS + hd * 128 + lq * 16) = csK; *(LAS f32x4*)(lds + L_CS + hd * 128 + 64 + lq * 16) = csQ; }
            }
            const int par = li & 1;
            const bool cur0 = (4 * lq + par) <= li, cur1 = (4 * lq + par + 2) <= li;
            s16x4 Kb[5], Qb[5];
#pragma unroll
            for (int mt = 0; mt < 5; ++mt) {
                Kb[mt] = pack4((accM[mt] - m1[mt] * csK) * r1[mt]);
                if (mt) Qb[mt] = pack4(accQ[mt] - m1[mt] * csQ);
            }
            f32x4 Sc[5], Sp[5];
#pragma unroll
            for (int mt = 1; mt < 5; ++mt) { Sc[mt] = mfma16(Kb[mt], Qb[mt], zero4); Sp[mt] = mfma16(Kb[mt - 1], Qb[mt], zero4); }
            float s0[5], s1[5], mx[5];
#pragma unroll
            for (int mt = 1; mt < 5; ++mt) {
                const float sp0 = Sp[mt][0], sp1 = Sp[mt][1], sp2 = Sp[mt][2], sp3 = Sp[mt][3], c0 = Sc[mt][0], c1 = Sc[mt][1], c2 = Sc[mt][2], c3 = Sc[mt][3];
                float p0 = par ? sp1 : sp0, p1 = par ? sp3 : sp2, q0 = par ? c1 : c0, q1 = par ? c3 : c2;
                asm volatile("" : "+v"(p0), "+v"(p1), "+v"(q0), "+v"(q1));
                if (mt == 1 && grp == 0) { p0 = -3.0e38f; p1 = -3.0e38f; }
                s0[mt] = cur0 ? q0 : p0; s1[mt] = cur1 ? q1 : p1;
                mx[mt] = vmax(s0[mt], s1[mt]);
            }
#pragma unroll
            for (int mt = 1; mt < 5; ++mt) { auto r = __builtin_amdgcn_permlane32_swap(__float_as_uint(mx[mt]), __float_as_uint(mx[mt]), false, false); mx[mt] = vmax(__uint_as_float(r[0]), __uint_as_float(r[1])); }
#pragma unroll
            for (int mt = 1; mt < 5; ++mt) { auto r = __builtin_amdgcn_permlane16_swap(__float_as_uint(mx[mt]), __float_as_uint(mx[mt]), false, false); mx[mt] = vmax(__uint_as_float(r[0]), __uint_as_float(r[1])); }
#pragma unroll
            for (int mt = 1; mt < 5; ++mt) {
                const float sc = r1[mt] * (0.25f * 1.44269504088896341f);
                const float e0 = __builtin_amdgcn_exp2f((s0[mt] - mx[mt]) * sc), e1 = __builtin_amdgcn_exp2f((s1[mt] - mx[mt]) * sc);
                *(LAS u32x2*)(lds + ex + (mt - 1) * 512) = (u32x2){pk2(e0, e0), pk2(e1, e1)};
            }
            BAR();
            if constexpr (LAYER == 0) f1ok = __builtin_amdgcn_readfirstlane(*(LAS u32*)(lds + L_FLG));
        } else {
            if constexpr (LAYER == 0) {
                auto s = __builtin_amdgcn_permlane16_swap(__float_as_uint(accM[0][0]), __float_as_uint(accM[0][0]), false, false);
                auto t = __builtin_amdgcn_permlane32_swap(s[0], s[0], false, false);
                csV = __uint_as_float(t[0]);
                if (wave == 7) { WAIT_VM(4); pin(flg); const u32 ok = flag_ok(flg); *(LAS u32*)(lds + L_FLG) = ok; }
            }
            const int par = li & 1;
            const bool cur0 = (4 * lq + par) <= li, cur1 = (4 * lq + par + 2) <= li;
            const u32 pm = par ? 0xffff0000u : 0x0000ffffu;
            u32 mC0 = cur0 ? pm : 0u, mP0 = cur0 ? 0u : pm, mC1 = cur1 ? pm : 0u, mP1 = cur1 ? 0u : pm;
            u32 oo = (LAYER ? WS_O2 : WS_O1) + (u32)((4 * grp + (lq & 1)) * 16384 + (4 * mem + hd) * 512 + (lq >> 1) * 256 + li * 16);
            asm volatile("" : "+v"(mC0), "+v"(mP0), "+v"(mC1), "+v"(mP1), "+v"(oo));
            s16x4 Vb[5];
#pragma unroll
            for (int mt = 0; mt < 5; ++mt) { Vb[mt] = pack4((accM[mt] - m4[mt] * csV) * r4[mt]); pin(Vb[mt]); }
            BAR();
            u32x2 pw[4];
#pragma unroll
            for (int j = 0; j < 4; ++j) pw[j] = *(LAS u32x2*)(lds + ex + j * 512);
            if constexpr (LAYER == 0) {
                f1ok = __builtin_amdgcn_readfirstlane(*(LAS u32*)(lds + L_FLG));
                if (f1ok) ald4s(uu, WS_UT + (u32)(b * 8192 + opq(tid) * 16));
            }
            SB();
            f32x4 O[4], L[4];
#pragma unroll
            for (int j = 0; j < 4; ++j) {
                const s16x8 P = __builtin_bit_cast(s16x8, (u32x4){pw[j][0] & mC0, pw[j][1] & mC1, pw[j][0] & mP0, pw[j][1] & mP1});
                L[j] = mfma32(ones8, P, zero4);
                O[j] = mfma32(cat8(Vb[j + 1], Vb[j]), P, zero4);
            }
            u32 ow[4][2];
#pragma unroll
            for (int j = 0; j < 4; ++j) {
                const float inv = __builtin_amdgcn_rcpf(L[j][0]);
                ow[j][0] = nosent(pk2(O[j][0] * inv, O[j][1] * inv)); ow[j][1] = nosent(pk2(O[j][2] * inv, O[j][3] * inv));
            }
#pragma unroll
            for (int j = 0; j < 4; j += 2) {
                auto r0 = __builtin_amdgcn_permlane16_swap(ow[j][0], ow[j + 1][0], false, false);
                auto r1 = __builtin_amdgcn_permlane16_swap(ow[j][1], ow[j + 1][1], false, false);
                __builtin_amdgcn_raw_buffer_store_b128((u32x4){r0[0], r1[0], r0[1], r1[1]}, r_ws, oo, j * 16384, 16);
            }
            CFENCE();
        }
        if constexpr (LAYER == 0 && R == 0) { if (f1ok) ald4s(uu, WS_UT + (u32)(b * 8192 + opq(tid) * 16)); }
        SB();
        return f1ok;
    };
    const u32 f1ok = attention(IC<0>{});

    const int TI = 4 * grp + hd;
    u32x4 of[8];
    auto load_o = [&](u32 ws_o) {
        const int lane = opq(tid) & 63;
#pragma unroll
        for (int i = 0; i < 8; ++i) of[i] = __builtin_amdgcn_raw_buffer_load_b128(r_ws, ws_o + (u32)(TI * 16384 + R * 8192 + lane * 16) + (i & 3) * 1024, (i >> 2) * 4096, 16);
    };
    u32 xw[8], yw[8];
    u32 woa[4];
    auto resid = [&](int n) { return R ? zero4 : (f32x4){bflo(xw[2 * n]), bfhi(xw[2 * n]), bflo(xw[2 * n + 1]), bfhi(xw[2 * n + 1])}; };
    s16x8 F[8][4];
    auto wo_addr = [&]() {
        const int l2 = opq(tid) & 63, lq = l2 >> 4, tq = (l2 & 15) >> 2, tp = l2 & 3;
#pragma unroll
        for (int n = 0; n < 4; ++n) woa[n] = L_WO + (u32)(R * 32768 + (8 * lq + tq) * 128 + ((n ^ (((lq & 1) << 1) | (tq >> 1))) << 5) + tp * 8);
    };
    auto load_F = [&](int ks) {
#pragma unroll
        for (int n = 0; n < 4; ++n) F[ks][n] = cat8(trr(woa[n] + ks * 4096), trr(woa[n] + ks * 4096 + 512));
    };

    {
        const int t2 = opq(tid), lane = t2 & 63, li = lane & 15, lq = lane >> 4;
        constexpr u32 OFFS = 0xEDA85410u;
        {
            const int j = t2 & 255;
            {
                if constexpr (R == 0) { if (f1ok) WAIT_VM(1); else WAIT_VM(0); }
                else WAIT_VM(2);
                u32 cb = L_HC + R * 34816 + (j + 2) * 16;
                pin(cb);
#pragma unroll
                for (int ch = R; ch < 2; ++ch) {
                    pin(hr[2 * ch]); pin(hr[2 * ch + 1]);
                    const u32 d[8] = {hr[2 * ch][0], hr[2 * ch][1], hr[2 * ch][2], hr[2 * ch][3], hr[2 * ch + 1][0], hr[2 * ch + 1][1], hr[2 * ch + 1][2], hr[2 * ch + 1][3]};
                    u32 e[7];
#pragma unroll
                    for (int i = 0; i < 7; ++i) e[i] = __builtin_amdgcn_alignbit(d[i + 1], d[i], 16);
                    if (ch > R) { cb += 34816; pin(cb); }
#pragma unroll
                    for (int p = (R ? 4 : 0); p < ((ch && !R) ? 4 : 8); ++p) {
                        const u32 po = (272 * p + ((OFFS >> (4 * p)) & 15)) * 16;
                        const int h = p >> 1;
                        const u32x4 w = (p & 1) ? (u32x4){e[h], e[h + 1], e[h + 2], e[h + 3]} : (u32x4){d[h], d[h + 1], d[h + 2], d[h + 3]};
                        *(LAS u32x4*)(lds + cb + po) = w;
                    }
                }
            }
            if constexpr (R == 0) {
                if (hd == 0) {
                    u32 hh[4];
#pragma unroll
                    for (int i = 0; i < 4; ++i) { const u32 a = __builtin_amdgcn_readfirstlane(hr[0][i]), c = __builtin_amdgcn_readfirstlane(hr[2][i]); hh[i] = (lane & 16) ? c : a; }
                    if (lane < 32) {
                        const int p = lane & 7;
                        const u32 po = (272 * p + ((OFFS >> (4 * p)) & 15)) * 16;
                        const unsigned __int128 hv = ((unsigned __int128)(((u64)hh[3] << 32) | hh[2]) << 64) | (((u64)hh[1] << 32) | hh[0]);
                        const unsigned __int128 sv = (p == 0 || !(lane & 8)) ? (unsigned __int128)0 : (hv << (16 * (8 - p)));
                        const u32x4 w = {(u32)sv, (u32)(sv >> 32), (u32)(sv >> 64), (u32)(sv >> 96)};
                        *(LAS u32x4*)(lds + L_HC + (lane >> 4) * 34816 + po + ((lane >> 3) & 1) * 16) = w;
                    }
                }
            }
            if (hd == 1 && lane < 30) *(LAS u32x4*)(lds + L_UR + R * 4576 + 4096 + lane * 16) = (u32x4){0, 0, 0, 0};
            if (!f1ok) {
                const u32 fo = WS_F1 + (u32)(((lane & 8) ? (lane & 7) * 32 + grp : (b >> 5) * 32 + 31 - hd * 8 - (lane & 7)) * 128);
                flg = (u32x2){0u, 0u};
                spin_flags(flg, fo);
                ald4s(uu, WS_UT + (u32)(b * 8192 + t2 * 16));
            }
            if (f1ok) WAIT_VM(R ? 2 : 0); else WAIT_VM(0);
            pin(uu);
            *(LAS u32x4*)(lds + L_UR + R * 4576 + j * 16) = uu;
        }
        BAR();
        if (R == 0 && tid == 0) __builtin_amdgcn_raw_buffer_store_b64((u32x2){tl, th}, r_ws, WS_F2 + (grp * 8 + mem) * 128, 0, 16);
        const int ch = hd >> 1, e0 = (hd & 1) * 4;
        const int p = (li + 1) & 7;
        u32 aA = L_HC + ch * 34816 + (272 * p + ((OFFS >> (4 * p)) & 15) + ((li + 1) >> 3) + lq + 4 * e0) * 16;
        u32 aB = L_UR + ch * 4576 + (254 - 224 - 2 * li + lq + 4 * e0) * 16;
        asm volatile("" : "+v"(aA), "+v"(aB));
        f32x4 y[4];
        u32 ooff = WS_O1 + (u32)(TI * 16384 + R * 8192 + lane * 16);
        {
            constexpr ConvSched S(R);
            s16x8 A[4][8], B[4][8];
            auto loads_at = [&](auto pc) {
                constexpr int p = decltype(pc)::value;
                sfor<0, 64>([&](auto ic) {
                    constexpr int i = decltype(ic)::value, e = i >> 4, f = i & 7;
                    if constexpr (S.slot[i] == p) {
                        if constexpr (i & 8) B[e][f] = ldsA(aB + 64 * e + 512 * (7 - f)); else A[e][f] = ldsA(aA + 64 * (8 * f + e));
                    }
                });
            };
            loads_at(IC<-1>{});
            SB();
            sfor<0, 72>([&](auto pc) {
                constexpr int p = decltype(pc)::value, e = p / 18, f = S.f[p % 18], i = S.i[p % 18], g = S.n[i] - f;
                y[i] = mfma32(A[e][f], B[e][g], (e == 0 && f == 0) ? zero4 : y[i]);
                loads_at(pc);
                if constexpr (p < 24 && (p % 3) == 1) issue_wo(IC<R * 8 + p / 3>{});
                if constexpr (p == 54) WAIT_VM(0);
                if constexpr (p >= 55 && p < 71 && (p & 1)) {
                    constexpr int i = (p - 55) >> 1;
                    ald4o<(i & 3) * 1024>(of[i], ooff + (i >> 2) * 4096, d_ws);
                }
                SB();
            });
        }
        RBAR();
        {
            constexpr int N0[4] = {0, 3, 4, 7};
            u32 ra = L_HC + hd * 8192 + lane * 16;
            asm volatile("" : "+v"(ra));
#pragma unroll
            for (int i = 0; i < 4; ++i) *(LAS f32x4*)(lds + ra + (N0[i] ^ R) * 1024) = y[i];
        }
        SB();
        if constexpr (R == 0) {
#pragma unroll
            for (int n = 0; n < 4; ++n) { const u32x2 w = __builtin_amdgcn_raw_buffer_load_b64(r_x, (u32)((TI * 16 + li) * 1024 + mem * 128 + lq * 8) + n * 32, 0, 0); xw[2 * n] = w[0]; xw[2 * n + 1] = w[1]; }
        }
        wo_addr();
        SB();
        BAR();
        {
            const float D0 = bflo(dw), D1 = bfhi(dw);
            u32 ra = L_HC + wave * 1024 + lane * 16;
            const int tb = 256 * wave + 16 * li + 4 * lq;
            u32 ru = L_UR + (2044 - tb) * 2;
            asm volatile("" : "+v"(ra), "+v"(ru));
            const f32x4 y0 = *(LAS f32x4*)(lds + ra), y1 = *(LAS f32x4*)(lds + ra + 8192), y2 = *(LAS f32x4*)(lds + ra + 16384), y3 = *(LAS f32x4*)(lds + ra + 24576);
            const u32x2 ua = *(LAS u32x2*)(lds + ru), ub = *(LAS u32x2*)(lds + ru + 4576);
            load_F(0); load_F(1);
            SB();
            const f32x4 ya = y0 + y1, yb = y2 + y3;
            const u32x4 w = {nosent(pk2(ya[0] + bfhi(ua[1]) * D0, yb[0] + bfhi(ub[1]) * D1)), nosent(pk2(ya[1] + bflo(ua[1]) * D0, yb[1] + bflo(ub[1]) * D1)),
                             nosent(pk2(ya[2] + bfhi(ua[0]) * D0, yb[2] + bfhi(ub[0]) * D1)), nosent(pk2(ya[3] + bflo(ua[0]) * D0, yb[3] + bflo(ub[0]) * D1))};
            __builtin_amdgcn_raw_buffer_store_b128(w, r_ws, WS_Y2 + (u32)(b * 8192 + tb * 4), 0, 16);
        }
        SB();
        WAIT_VM(1);

        u32 f2oth = 0;
        _Pragma("unroll 1") for (int attempt = 0; attempt < 4096; ++attempt) {
            sfor<0, 8>([&](auto ic) { pin(of[decltype(ic)::value]); });
            u32 m0 = 0, m1 = 0;
#pragma unroll
            for (int ks = 0; ks < 8; ++ks) {
                load_F((ks + 2) & 7);
                if (R == 1 && ks == 4 && wave == 7) {
                    const int pb = 32 * mem + (lane & 31);
                    f2oth = WS_F2 + (u32)((lane < 32 ? ((pb & 7) * 4 + (pb >> 6)) * 8 + ((pb >> 3) & 7) : max(grp * 8 - 8, 0) + (lane & 15)) * 128);
                    ald2s(flg, f2oth);
                }
                SB();
#pragma unroll
                for (int n = 0; n < 4; ++n) accM[n] = mfma32(F[ks][n], __builtin_bit_cast(s16x8, of[ks]), ks ? accM[n] : resid(n));
                m0 = max(max(m0, of[ks][0]), of[ks][1]); m1 = max(max(m1, of[ks][2]), of[ks][3]);
                SB();
            }
            if (__builtin_expect(__all(max(m0, m1) != SENT), 1)) break;
            __builtin_amdgcn_s_sleep(4);
            WAIT_VM(0);
            sfor<0, 8>([&](auto ic) { constexpr int i = decltype(ic)::value; ald4o<(i & 3) * 1024>(of[i], ooff + (i >> 2) * 4096, d_ws); });
            WAIT_VM(0);
        }
        if constexpr (R == 1) { if (wave == 7) { WAIT_VM(0); pin(flg); spin_flags(flg, f2oth); } }
    }

    auto load_y = [&]() {
        const int l2 = opq(tid) & 63;
#pragma unroll
        for (int i = 0; i < 8; ++i)
            yw[i] = __builtin_amdgcn_raw_buffer_load_b32(r_ws, WS_Y2 + (u32)((32 * mem + 2 * (l2 >> 4)) * 8192 + (TI * 16 + (l2 & 15)) * 4), (8 * (i >> 1) + (i & 1)) * 8192, 16);
    };
    auto pub_off = [&](u32 ws_st) {
        const int lane = opq(tid) & 63;
        return (lane & 16) ? 0xFFFFFF00u : ws_st + (u32)((TI * 8 + mem) * 256 + (lane & 15) * 16 + (lane >> 5) * 8);
    };
    auto pub_stat = [&](u32 off, u32 tg, float s, float q) {
        auto r = __builtin_amdgcn_permlane32_swap(__float_as_uint(s), __float_as_uint(q), false, false);
        const float v = __uint_as_float(r[0]) + __uint_as_float(r[1]);
        auto t = __builtin_amdgcn_permlane16_swap(__float_as_uint(v), __float_as_uint(v), false, false);
        __builtin_amdgcn_raw_buffer_store_b64((u32x2){__float_as_uint(__uint_as_float(t[0]) + __uint_as_float(t[1])), tg}, r_ws, off, 0, 16);
    };
    auto stat_off = [&](u32 ws_st, int ti) { const int l2 = opq(tid) & 63; return ws_st + (u32)(ti * 2048 + (l2 >> 4) * 512 + (l2 & 15) * 16); };
    auto fin_stat = [&](u32x4& a, u32x4& c, u32 off, float& mean, float& rstd) {
        _Pragma("unroll 1") for (int spin = 0; spin < SPIN_CAP; ++spin) {
            if (__builtin_expect(__all(((a[1] ^ tl) | (a[3] ^ th) | (c[1] ^ tl) | (c[3] ^ th)) == 0), 1)) break;
            __builtin_amdgcn_s_sleep(1);
            asm volatile("buffer_load_dwordx4 %0, %2, %3, 0 offen sc1\n\tbuffer_load_dwordx4 %1, %2, %3, 0 offen offset:256 sc1\n\ts_waitcnt vmcnt(0)"
                         : "=&v"(a), "=&v"(c) : "v"(off), "s"(d_ws) : "memory");
        }
        auto r = __builtin_amdgcn_permlane32_swap(__float_as_uint(__uint_as_float(a[0]) + __uint_as_float(c[0])), __float_as_uint(__uint_as_float(a[2]) + __uint_as_float(c[2])), false, false);
        const float v = __uint_as_float(r[0]) + __uint_as_float(r[1]);
        auto t = __builtin_amdgcn_permlane16_swap(__float_as_uint(v), __float_as_uint(v), false, false);
        const u32 v2 = __float_as_uint(__uint_as_float(t[0]) + __uint_as_float(t[1]));
        auto u = __builtin_amdgcn_permlane32_swap(v2, v2, false, false);
        const float ts = __uint_as_float(u[0]), tq = __uint_as_float(u[1]);
        mean = ts * (1.f / 512.f);
        rstd = __builtin_amdgcn_rsqf(fmaxf(tq * (1.f / 512.f) - mean * mean, 0.f) + 1e-5f);
    };
    auto regroup = [&](u32 (&w)[8]) {
#pragma unroll
        for (int j = 0; j < 2; ++j) {
            auto r0 = __builtin_amdgcn_permlane16_swap(w[4 * j], w[4 * j + 2], false, false);
            auto r1 = __builtin_amdgcn_permlane16_swap(w[4 * j + 1], w[4 * j + 3], false, false);
            w[4 * j] = r0[0]; w[4 * j + 1] = r1[0]; w[4 * j + 2] = r0[1]; w[4 * j + 3] = r1[1];
        }
    };
    auto wo_finish = [&](auto lc) {
        constexpr int LAYER = decltype(lc)::value;
        const int lane = opq(tid) & 63, li = lane & 15, lq = lane >> 4;
        u32 wx = L_WX + hd * 4096 + lane * 16;
        pin(wx);
        u32 po = 0, so = 0, tg = 0, oa = 0;
        if constexpr (R == 1) {
#pragma unroll
            for (int n = 0; n < 4; ++n) *(LAS f32x4*)(lds + wx + n * 1024) = accM[n];
        } else {
            po = pub_off(LAYER ? WS_ST3 : WS_ST1); so = stat_off(LAYER ? WS_ST3 : WS_ST1, TI); tg = lane < 32 ? tl : th;
            const int c = 2 * (lq & 1) + (lq >> 1);
            oa = LAYER ? (u32)((TI * 16 + li) * 1024 + mem * 128 + c * 16) : WS_X2 + (u32)((TI * 8 + mem) * 2048 + li * 128 + ((c ^ (li >> 1)) << 4));
            pin(po); pin(so); pin(tg); pin(oa);
        }
        BAR();
        if constexpr (R == 0) {
            f32x4 t[4];
#pragma unroll
            for (int n = 0; n < 4; ++n) t[n] = *(LAS f32x4*)(lds + wx + n * 1024);
            SB();
            float s = 0.f, q = 0.f, s2 = 0.f, q2 = 0.f;
#pragma unroll
            for (int n = 0; n < 4; ++n) {
                const f32x4 v = accM[n] + t[n];
                const u32 w0 = pk2(v[0], v[1]), w1 = pk2(v[2], v[3]);
                xw[2 * n] = w0; xw[2 * n + 1] = w1;
                s = dot2(w0, 0x3F803F80u, s); q = dot2(w0, w0, q); s2 = dot2(w1, 0x3F803F80u, s2); q2 = dot2(w1, w1, q2);
            }
            pub_stat(po, tg, s + s2, q + q2);
            CFENCE();
            if constexpr (!LAYER) load_y();
            regroup(xw);
            SB();
            WAIT_VM(0);
            if constexpr (!LAYER) {
#pragma unroll
                for (int i = 0; i < 8; ++i) pin(yw[i]);
            }
            u32x4 a, c;
            ald4s(a, so); ald4s(c, so + 256);
            if constexpr (!LAYER) {
                _Pragma("unroll 1") for (int spin = 0; spin < SPIN_CAP; ++spin) {
                    if (__builtin_expect(__all(max(max(max(yw[0], yw[1]), max(yw[2], yw[3])), max(max(yw[4], yw[5]), max(yw[6], yw[7]))) != SENT), 1)) break;
                    __builtin_amdgcn_s_sleep(1);
                    CFENCE();
                    load_y();
                }
                regroup(yw);
            }
            f32x2 xa[8], ya[8], xy[8];
#pragma unroll
            for (int i = 0; i < 8; ++i) {
                xa[i] = (f32x2){bflo(xw[i]), bfhi(xw[i])}; ya[i] = (f32x2){bflo(yw[i]), bfhi(yw[i])}; xy[i] = xa[i] * ya[i];
                pin(xa[i]); pin(ya[i]); pin(xy[i]);
            }
            WAIT_VM(0);
            pin(a); pin(c);
            float mean, rstd;
            fin_stat(a, c, so, mean, rstd);
            const float nm = -mean * rstd;
#pragma unroll
            for (int i = 0; i < 8; ++i) {
                const f32x2 o = xy[i] * rstd + (ya[i] * nm + xa[i]);
                xw[i] = pk2(o[0], o[1]);
                if (!LAYER) xw[i] = nosent(xw[i]);
            }
#pragma unroll
            for (int j = 0; j < 2; ++j) {
                const u32x4 o = {xw[4 * j], xw[4 * j + 1], xw[4 * j + 2], xw[4 * j + 3]};
                if constexpr (LAYER) __builtin_amdgcn_raw_buffer_store_b128(o, r_out, oa + j * 64, 0, 0);
                else __builtin_amdgcn_raw_buffer_store_b128(o, r_ws, oa ^ (j * 64), 0, 16);
            }
            if constexpr (!LAYER) {
                float s = 0.f, q = 0.f, s2 = 0.f, q2 = 0.f;
#pragma unroll
                for (int i = 0; i < 8; i += 2) { s = dot2(xw[i], 0x3F803F80u, s); q = dot2(xw[i], xw[i], q); s2 = dot2(xw[i + 1], 0x3F803F80u, s2); q2 = dot2(xw[i + 1], xw[i + 1], q2); }
                pub_stat(po + (WS_ST2 - WS_ST1) * !(lane & 16), tg, s + s2, q + q2);
                CFENCE();
                regroup(xw);
#pragma unroll
                for (int i = 0; i < 8; ++i) { pin(xw[i]); pin(yw[i]); }
                WAIT_VM(0);
            }
        }
    };
    wo_finish(IC<0>{});
    if constexpr (R == 1) {
        if (b == 0 && wave == 7) {
            const u32 fo = WS_F1 + (u32)((opq(tid) & 63) * 128);
            u32x2 ef[4];
#pragma unroll
            for (int i = 0; i < 4; ++i) ald2s(ef[i], fo + i * 8192);
            WAIT_VM(0);
#pragma unroll
            for (int i = 0; i < 4; ++i) { pin(ef[i]); spin_flags(ef[i], fo + i * 8192); }
            if (fo == WS_F1) __builtin_amdgcn_raw_buffer_store_b64(__builtin_bit_cast(u32x2, epoch + 1), r_ws, WS_EPOCH, 0, 16);
            WAIT_VM(0);
        }
    }
    RBAR();

    {
        const int l3 = opq(tid) & 63;
        const u32 g0 = (u32)((4 * grp - 1 + (hd >> 1)) * 16384 + (hd & 1) * 1024 + l3 * 16);
        const u32 g2 = (u32)((4 * grp + 3) * 16384 + hd * 512 + l3 * 16);
        auto x2_piece = [&](auto ic) {
            constexpr int i = decltype(ic)::value, s = i / 3, p = i % 3;
            const u32 kp = (u32)((s + rot) & 7);
            if constexpr (p == 0) dma(IC<4>{}, IC<L_CX + s * 10240>{}, d_x2, hb1k, g0, kp * 2048);
            else if constexpr (p == 1) dma(IC<4>{}, IC<L_CX + s * 10240 + 4096>{}, d_x2, hb1k, g0 + 32768, kp * 2048);
            else dma(IC<5>{}, IC<L_CX + s * 10240 + 8192>{}, d_x2, hb512, g2, kp * 2048);
        };
        u32 va = L_CX + hd * 1024 + l3 * 16, vq = L_CX + 8192 + hd * 512 + (l3 & 31) * 16 + (l3 >> 5) * 10240;
        pin(va); pin(vq);
        u32x4 vr[5];
        auto ver_read = [&](auto cc) {
            constexpr int c = decltype(cc)::value;
            u32 a = va + c * 20480, q = vq + c * 20480;
            pin(a); pin(q); CFENCE();
            vr[0] = *(LAS u32x4*)(lds + a); vr[1] = *(LAS u32x4*)(lds + a + 4096); vr[2] = *(LAS u32x4*)(lds + a + 10240); vr[3] = *(LAS u32x4*)(lds + a + 14336);
            vr[4] = *(LAS u32x4*)(lds + q);
        };
        auto ver_check = [&](auto cc) {
            constexpr int c = decltype(cc)::value;
            _Pragma("unroll 1") for (int spin = 0; spin < SPIN_CAP; ++spin) {
                u32 m0 = 0, m1 = 0;
#pragma unroll
                for (int i = 0; i < 5; ++i) { m0 = max(max(m0, vr[i][0]), vr[i][1]); m1 = max(max(m1, vr[i][2]), vr[i][3]); }
                if (__builtin_expect(__all(max(m0, m1) != SENT), 1)) break;
                __builtin_amdgcn_s_sleep(1);
                sfor<6 * c, 6 * c + 6>([&](auto ic) { x2_piece(ic); });
                WAIT_VM(0);
                ver_read(cc);
            }
        };
        const u32 xb0 = (u32)((l3 & 15) * 128 + (((l3 >> 4) ^ ((l3 & 15) >> 1)) << 4));
        if constexpr (R == 1) {
            sfor<0, 12>([&](auto ic) { x2_piece(ic); });
            WAIT_VM(6);
            ver_read(IC<0>{});
            ver_check(IC<0>{});
        } else {
            u32x4 sa, sc, ha, hc;
            const u32 so = stat_off(WS_ST2, 4 * grp + hd);
            const bool halo = hd == 0 && grp > 0;
            ald4s(sa, so); ald4s(sc, so + 256);
            if (halo) { ald4s(ha, so - 2048); ald4s(hc, so - 2048 + 256); }
            WAIT_VM(0);
            pin(sa); pin(sc);
            float mean, rstd;
            fin_stat(sa, sc, so, mean, rstd);
            if (l3 < 16) { *(LAS float*)(lds + L_TAB + 64 + hd * 64 + l3 * 4) = mean; *(LAS float*)(lds + L_TAB + 320 + 64 + hd * 64 + l3 * 4) = rstd; }
            if (halo) {
                pin(ha); pin(hc);
                fin_stat(ha, hc, so - 2048, mean, rstd);
                if (l3 < 16) { *(LAS float*)(lds + L_TAB + l3 * 4) = mean; *(LAS float*)(lds + L_TAB + 320 + l3 * 4) = rstd; }
            }
        }
        BAR();
        u32 xa;
        auto pf_addr = [&](int st) { xa = L_CX + (st >> 1) * 10240 + (xb0 ^ ((st & 1) * 64)); pin(xa); };
        pf_addr(0);
#pragma unroll
        for (int mt = 0; mt < 5; ++mt) Ab[0][mt] = ldsA(xa + mt * 2048);
        sfor<0, 16>([&](auto stc) {
            constexpr int st = decltype(stc)::value;
            if constexpr (R == 1) {
                if constexpr (st == 2) { WAIT_VM(10); ver_read(IC<1>{}); }
                if constexpr (st == 3) ver_check(IC<1>{});
                if constexpr (st == 5) { WAIT_VM(6); ver_read(IC<2>{}); }
                if constexpr (st == 6) { ver_check(IC<2>{}); WAIT_VM(0); ver_read(IC<3>{}); }
                if constexpr (st == 7) ver_check(IC<3>{});
            }
            if constexpr (st == 3) { RBAR(); }
            if constexpr (st == 7) { RBAR(); }
            if (st < 15) pf_addr(st + 1);
            if constexpr (st == 14) load_tab(true);
            SB();
            qkv_step(IC<1>{}, st, Ab[st & 1], [&](auto mc) {
                constexpr int mt = decltype(mc)::value;
                if (st < 15) Ab[(st + 1) & 1][mt] = ldsA(xa + mt * 2048);
                if constexpr (R == 1 && 12 + 5 * st + mt < 24) x2_piece(IC<12 + 5 * st + mt>{});
                SB();
            });
        });
    }
    attention(IC<1>{});

    {
        wo_addr();
#pragma unroll
        for (int ks = 0; ks < 8; ++ks) load_F(ks);
        SB();
        WAIT_VM(0);
        RBAR();
        load_o(WS_O2);
        SB();
        _Pragma("unroll 1") for (int spin = 0; spin < 64; ++spin) {
            u32 m0 = 0, m1 = 0;
#pragma unroll
            for (int ks = 0; ks < 8; ++ks) {
#pragma unroll
                for (int n = 0; n < 4; ++n) accM[n] = mfma32(F[ks][n], __builtin_bit_cast(s16x8, of[ks]), ks ? accM[n] : resid(n));
                m0 = max(max(m0, of[ks][0]), of[ks][1]); m1 = max(max(m1, of[ks][2]), of[ks][3]);
                SB();
            }
            if (__builtin_expect(__all(max(m0, m1) != SENT), 1)) break;
            _Pragma("unroll 1") for (int sp2 = 0; sp2 < SPIN_CAP; ++sp2) {
                CFENCE();
                __builtin_amdgcn_s_sleep(1);
                const u32 pv = __builtin_amdgcn_raw_buffer_load_b32(r_ws, WS_O2 + (u32)(TI * 16384 + R * 8192 + (opq(tid) & 15) * 512 + 508), 0, 16);
                if (__all(pv != SENT)) break;
            }
            CFENCE();
            load_o(WS_O2);
        }
        SB();
    }
    wo_finish(IC<1>{});
}

__global__ void __launch_bounds__(512, 2)
fused_kernel(const u16* __restrict__ gx, const u16* __restrict__ gwqkv, const u16* __restrict__ gwo,
             const u16* __restrict__ gbk, const u16* __restrict__ gbv, const u16* __restrict__ gsk,
             const u16* __restrict__ ghk, const u16* __restrict__ gdv, u16* __restrict__ gout, char* __restrict__ ws) {
    __shared__ __attribute__((aligned(16))) char lds_[L_TOTAL];
    typedef u32 u32x16 __attribute__((ext_vector_type(16)));
    u32x16 ka; u32x4 kb;
    {
        auto kp = __builtin_amdgcn_kernarg_segment_ptr();
        asm volatile("s_load_dwordx16 %0, %2, 0x0\n\ts_load_dwordx4 %1, %2, 0x40\n\ts_waitcnt lgkmcnt(0)" : "=&s"(ka), "=&s"(kb) : "s"(kp) : "memory");
    }
    auto mkp = [](u32 lo, u32 hi) { return (char*)(((u64)hi << 32) | lo); };
    const Args a = {(const u16*)mkp(ka[0], ka[1]), (const u16*)mkp(ka[2], ka[3]), (const u16*)mkp(ka[4], ka[5]), (const u16*)mkp(ka[6], ka[7]), (const u16*)mkp(ka[8], ka[9]),
                    (const u16*)mkp(ka[10], ka[11]), (const u16*)mkp(ka[12], ka[13]), (const u16*)mkp(ka[14], ka[15]), (u16*)mkp(kb[0], kb[1]), mkp(kb[2], kb[3])};
    const int wave = __builtin_amdgcn_readfirstlane(threadIdx.x >> 6);
    if (wave < 4) body<0>(a, (LAS char*)lds_, wave); else body<1>(a, (LAS char*)lds_, wave);
}

extern "C" void kernel_launch(void* const* d_in, const int* in_sizes, int n_in,
                              void* d_out, int out_size, void* d_ws, size_t ws_size,
                              hipStream_t stream) {
    fused_kernel<<<256, 512, 0, stream>>>((const u16*)d_in[0], (const u16*)d_in[1], (const u16*)d_in[2],
                                          (const u16*)d_in[4], (const u16*)d_in[5], (const u16*)d_in[6],
                                          (const u16*)d_in[7], (const u16*)d_in[8], (u16*)d_out, (char*)d_ws);
}
```

```cpp
#include <hip/hip_runtime.h>
#include <stdint.h>

typedef unsigned short u16;
typedef unsigned u32;
typedef unsigned long long u64;
typedef short s16x4 __attribute__((ext_vector_type(4)));
typedef short s16x8 __attribute__((ext_vector_type(8)));
typedef __bf16 bf16x2 __attribute__((ext_vector_type(2)));
typedef float f32x2 __attribute__((ext_vector_type(2)));
typedef float f32x4 __attribute__((ext_vector_type(4)));
typedef u32 u32x2 __attribute__((ext_vector_type(2)));
typedef u32 u32x4 __attribute__((ext_vector_type(4)));
#define LAS __attribute__((address_space(3)))
#define DEV __device__ __forceinline__
typedef __amdgpu_buffer_rsrc_t rsrc_t;

constexpr u32 WS_EPOCH = 0;
constexpr u32 WS_F1 = 32768;
constexpr u32 WS_F2 = 65536;
constexpr u32 WS_ST0 = 98304;
constexpr u32 WS_ST1 = 131072;
constexpr u32 WS_ST2 = WS_ST1 + 262144;
constexpr u32 WS_ST3 = WS_ST2 + 262144;
constexpr u32 WS_UT = 1u << 20;
constexpr u32 WS_O1 = WS_UT + (2u << 20);
constexpr u32 WS_O2 = WS_O1 + (2u << 20);
constexpr u32 WS_Y2 = WS_O2 + (2u << 20);
constexpr u32 WS_X2 = WS_Y2 + (2u << 20);
constexpr u32 WS_END = WS_X2 + (2u << 20);

constexpr u32 L_SLOT = 34816;
constexpr u32 L_KV = 4 * L_SLOT;
constexpr u32 L_K = L_KV, L_V = L_KV + 9216;
constexpr u32 L_SK = L_KV + 17408;
constexpr u32 L_WO = 0;
constexpr u32 L_CX = 65536;
constexpr u32 L_HC = L_CX;
constexpr u32 L_UR = L_CX + 69632;
constexpr u32 L_WX = L_CX + 40960;
constexpr u32 L_EX = 147456;
constexpr u32 L_TAB = L_EX + 12288;
constexpr u32 L_CS = L_TAB + 1024;
constexpr u32 L_FLG = L_TAB + 2048;
constexpr u32 L_DUMMY = L_FLG + 1024;
constexpr u32 L_TOTAL = 160 * 1024;

constexpr int SPIN_CAP = 1 << 14;
constexpr u32 SENT = 0xFFFFFFFFu;

#define WAIT_VM(n) asm volatile("s_waitcnt vmcnt(%0)" ::"i"(n) : "memory")
#define BAR() do { asm volatile("s_waitcnt lgkmcnt(0)" ::: "memory"); __builtin_amdgcn_s_barrier(); asm volatile("" ::: "memory"); } while (0)
#define RBAR() do { asm volatile("" ::: "memory"); __builtin_amdgcn_s_barrier(); asm volatile("" ::: "memory"); } while (0)
#define CFENCE() asm volatile("" ::: "memory")
#define SB() __builtin_amdgcn_sched_barrier(0)
template <int N> struct IC { static constexpr int value = N; };
template <int I, int N, class F> DEV void sfor(F&& f) { if constexpr (I < N) { f(IC<I>{}); sfor<I + 1, N>(f); } }

DEV f32x4 mfma32(s16x8 a, s16x8 b, f32x4 c) { return __builtin_amdgcn_mfma_f32_16x16x32_bf16(a, b, c, 0, 0, 0); }
DEV f32x4 mfma16(s16x4 a, s16x4 b, f32x4 c) { return __builtin_amdgcn_mfma_f32_16x16x16bf16_1k(a, b, c, 0, 0, 0); }
DEV float vmax(float a, float b) { float r; asm("v_max_f32 %0, %1, %2" : "=v"(r) : "v"(a), "v"(b)); return r; }
DEV u32 pk2(float lo, float hi) { f32x2 f = {lo, hi}; bf16x2 b = __builtin_convertvector(f, bf16x2); return __builtin_bit_cast(u32, b); }
DEV float bflo(u32 w) { return __uint_as_float(w << 16); }
DEV float bfhi(u32 w) { return __uint_as_float(w & 0xffff0000u); }
DEV float dot2(u32 a, u32 b, float c) { return __builtin_amdgcn_fdot2_f32_bf16(__builtin_bit_cast(bf16x2, a), __builtin_bit_cast(bf16x2, b), c, false); }
DEV u32 nosent(u32 w) { return min(w, 0xFFFFFFFEu); }
DEV float rowsum16(float v) {
    v += __builtin_bit_cast(float, __builtin_amdgcn_update_dpp(0, __builtin_bit_cast(int, v), 0xB1, 0xf, 0xf, true));
    v += __builtin_bit_cast(float, __builtin_amdgcn_update_dpp(0, __builtin_bit_cast(int, v), 0x4E, 0xf, 0xf, true));
    v += __builtin_bit_cast(float, __builtin_amdgcn_update_dpp(0, __builtin_bit_cast(int, v), 0x141, 0xf, 0xf, true));
    v += __builtin_bit_cast(float, __builtin_amdgcn_update_dpp(0, __builtin_bit_cast(int, v), 0x140, 0xf, 0xf, true));
    return v;
}
DEV float bcast_row0(float v) { return __builtin_bit_cast(float, __builtin_amdgcn_update_dpp(0, __builtin_bit_cast(int, v), 0x150, 0xf, 0xf, true)); }
template <int OFF> DEV void ald4o(u32x4& d, u32 voff, u32x4 rs) { asm volatile("buffer_load_dwordx4 %0, %1, %2, 0 offen offset:%3 sc1" : "=v"(d) : "v"(voff), "s"(rs), "i"(OFF) : "memory"); }
template <class T> DEV void pin(T& v) { asm volatile("" : "+v"(v)); }
DEV int opq(int v) { asm volatile("" : "+v"(v)); return v; }
DEV s16x8 cat8(s16x4 a, s16x4 b) { return __builtin_shufflevector(a, b, 0, 1, 2, 3, 4, 5, 6, 7); }
DEV s16x4 pack4(f32x4 v) { u32x2 w = {pk2(v[0], v[1]), pk2(v[2], v[3])}; return __builtin_bit_cast(s16x4, w); }
DEV u64 mix64(u64 z) { z += 0x9E3779B97F4A7C15ull; z = (z ^ (z >> 30)) * 0xBF58476D1CE4E5B9ull; z = (z ^ (z >> 27)) * 0x94D049BB133111EBull; return z ^ (z >> 31); }

struct ConvSched {
    int n[4], f[18], i[18], slot[64];
    constexpr ConvSched(int R) : n{}, f{}, i{}, slot{} {
        constexpr int LEAD = 9;
        const int N0[4] = {0, 3, 4, 7};
        for (int k = 0; k < 4; ++k) n[k] = N0[k] ^ R;
        int c = 0;
        for (int ff = 0; ff < 8; ++ff) for (int k = 3; k >= 0; --k) if (ff <= n[k]) { f[c] = ff; i[c] = k; ++c; }
        for (int k = 0; k < 64; ++k) slot[k] = 1000;
        for (int p = 71; p >= 0; --p) {
            const int e = p / 18, ff = f[p % 18], g = n[i[p % 18]] - ff, s = p - LEAD < -1 ? -1 : p - LEAD;
            slot[e * 16 + ff] = s; slot[e * 16 + 8 + g] = s;
        }
    }
};

struct Args { const u16 *gx, *gwqkv, *gwo, *gbk, *gbv, *gsk, *ghk, *gdv; u16* gout; char* ws; };

template <int R>
DEV void body(const Args& A_, LAS char* const lds, const int wave) {
    const u16* gx = A_.gx; char* ws = A_.ws;
    const int b = blockIdx.x, tid = threadIdx.x, lane = tid & 63;
    const int hd = wave & 3;
    const int grp = (b & 7) * 4 + (b >> 6), mem = (b >> 3) & 7;
    const int T0 = grp * 64;
    const int rot = (mem + 2 * (grp & 3)) & 7;

    u64 epoch;
    asm volatile("s_load_dwordx2 %0, %1, 0x0" : "=s"(epoch) : "s"(ws));
    u32 dw;
    asm volatile("s_load_dword %0, %1, 0x0" : "=s"(dw) : "s"(A_.gdv + 2 * b));
    const rsrc_t r_x = __builtin_amdgcn_make_buffer_rsrc((void*)gx, 0, 2048 * 512 * 2, 0x00020000);
    const rsrc_t r_ws = __builtin_amdgcn_make_buffer_rsrc((void*)ws, 0, WS_END, 0x00020000);
    const rsrc_t r_out = __builtin_amdgcn_make_buffer_rsrc((void*)A_.gout, 0, 2048 * 512 * 2, 0x00020000);
    const u32 ldsbase = (u32)(size_t)lds;
    auto mkdesc = [](const void* p, u32 bytes) { const u64 a = (u64)p; return (u32x4){(u32)a, (u32)(a >> 32) & 0xffffu, bytes, 0x00020000u}; };
    const u32x4 d_x = mkdesc(gx, 2048 * 512 * 2), d_w = mkdesc(A_.gwqkv, 512 * 1536 * 2), d_wo = mkdesc(A_.gwo, 512 * 512 * 2);
    const u32x4 d_k = mkdesc(A_.gbk, 2048 * 512 * 2), d_v = mkdesc(A_.gbv, 2048 * 512 * 2), d_sk = mkdesc(A_.gsk, 512 * 4 * 2);
    const u32x4 d_h = mkdesc(A_.ghk, 512 * 2048 * 2), d_x2 = mkdesc(ws + WS_X2, 2u << 20), d_ws = mkdesc(ws, WS_END);
    const u32 hb1k = ldsbase + hd * 1024, hb512 = ldsbase + hd * 512;
#define DMA_ASM(pre, mods, post) asm volatile("s_add_u32 m0, %0, %4\n\t" pre "buffer_load_dwordx4 %1, %2, %3 offen " mods "lds" post \
                     :: "s"(sbase), "v"(voff), "s"(rs), "s"(soff), "i"(IMM) : "memory", "scc")
    const u32 xmlo = hd == 0 ? ~0xFFu : ~0u;
    auto dma = [&](auto kind, auto imm, u32x4 rs, u32 sbase, u32 voff, u32 soff) {
        constexpr int K = decltype(kind)::value; constexpr int IMM = decltype(imm)::value;
        if constexpr (K == 0) DMA_ASM("s_nop 0\n\t", "", "");
        else if constexpr (K == 1) DMA_ASM("s_mov_b32 exec_hi, 0\n\t", "", "\n\ts_mov_b32 exec_hi, -1");
        else if constexpr (K == 2) {
            const u32 ml = xmlo;
            asm volatile("s_add_u32 m0, %0, %4\n\ts_mov_b32 exec_lo, %5\n\tbuffer_load_dwordx4 %1, %2, %3 offen lds\n\ts_mov_b32 exec_lo, -1"
                         :: "s"(sbase), "v"(voff), "s"(rs), "s"(soff), "i"(IMM), "s"(ml) : "memory", "scc");
        }
        else if constexpr (K == 4) DMA_ASM("s_nop 0\n\t", "sc1 ", "");
        else DMA_ASM("s_mov_b32 exec_hi, 0\n\t", "sc1 ", "\n\ts_mov_b32 exec_hi, -1");
    };
    auto ald4 = [&](u32x4& d, u32x4 rs, u32 voff) { asm volatile("buffer_load_dwordx4 %0, %1, %2, 0 offen" : "=v"(d) : "v"(voff), "s"(rs) : "memory"); };
    auto ald4s = [&](u32x4& d, u32 voff) { asm volatile("buffer_load_dwordx4 %0, %1, %2, 0 offen sc1" : "=v"(d) : "v"(voff), "s"(d_ws) : "memory"); };
    auto ald2s = [&](u32x2& d, u32 voff) { asm volatile("buffer_load_dwordx2 %0, %1, %2, 0 offen sc1" : "=v"(d) : "v"(voff), "s"(d_ws) : "memory"); };

    const int pr = lane >> 3, pc = lane & 7;
    const int xr0 = 8 * hd + pr, xr2 = 64 + 4 * hd + pr;
    const u32 xv0 = (u32)((T0 - 16 + xr0) * 1024 + ((pc ^ ((xr0 >> 1) & 7)) << 4));
    const u32 xv2 = (u32)((T0 - 16 + xr2) * 1024 + ((pc ^ ((xr2 >> 1) & 7)) << 4));
    const int wfw = ((xr0 >> 1) & 1) | (((xr0 >> 3) & 1) << 1);
    const u32 wv0 = (u32)(xr0 * 3072 + mem * 128 + ((((pc >> 1) ^ wfw) * 2 + (pc & 1)) << 4));
    auto issue_piece = [&](auto sc, auto ic) {
        constexpr int s = decltype(sc)::value, i = decltype(ic)::value;
        const u32 kp = (u32)((s + rot) & 7);
        constexpr int sb = (s & 3) * L_SLOT;
        if constexpr (i == 0) dma(IC<2>{}, IC<sb>{}, d_x, hb1k, xv0, kp * 128);
        else if constexpr (i == 1) dma(IC<0>{}, IC<sb + 4096>{}, d_x, hb1k, xv0 + 32768, kp * 128);
        else if constexpr (i == 2) dma(IC<1>{}, IC<sb + 8192>{}, d_x, hb512, xv2, kp * 128);
        else dma(IC<0>{}, IC<sb + 10240 + (i - 3) * 4096>{}, d_w, hb1k, wv0, kp * (64 * 3072) + ((i - 3) >> 1) * 1024 + ((i - 3) & 1) * (32 * 3072));
    };
    u32 wov = 0;
    auto issue_wo = [&](auto ic) {
        constexpr int i = decltype(ic)::value;
        if constexpr ((i & 7) == 0) {
            const int l2 = opq(tid) & 63, pr = l2 >> 3, pc = l2 & 7;
            wov = (u32)((8 * hd + pr) * 1024 + mem * 128 + ((((pc >> 1) ^ (((pr >> 1) & 1) | ((hd & 1) << 1))) * 2 + (pc & 1)) << 4));
            pin(wov);
        }
        const u32 j = (u32)((i + 2 * grp) & 7);
        dma(IC<0>{}, IC<L_WO + (i & 8) * 4096>{}, d_wo, hb1k + j * 4096, wov, ((i & 8) + j) * 32768);
    };
    auto issue_kv = [&](int i) {
        const int l2 = opq(tid) & 63, pr = l2 >> 3, pc = l2 & 7, p = hd + 4 * (i & 1);
        const u32 cb = (u32)(mem * 128);
        if (i < 2) dma(IC<0>{}, IC<L_K>{}, d_k, hb1k + (i & 1) * 4096, (u32)((T0 - 8 + 8 * p + pr) * 1024) + cb + ((pc ^ p) << 4), 0);
        else if (i < 4) dma(IC<0>{}, IC<L_V>{}, d_v, hb1k + (i & 1) * 4096, (u32)((T0 + 8 * p + pr) * 1024) + cb + ((pc ^ p) << 4), 0);
        else if (hd == 0) dma(IC<0>{}, IC<L_K + 8192>{}, d_k, ldsbase, (u32)((T0 - 8 + 64 + pr) * 1024) + cb + (pc << 4), 0);
        else dma(IC<0>{}, IC<L_SK>{}, d_sk, ldsbase, (u32)(mem * 512 + l2 * 16), 0);
    };

    const int srow = T0 + 8 * mem + 2 * hd;
    u32x4 xrow[2];
    sfor<0, 9>([&](auto ic) { if constexpr ((decltype(ic)::value & 1) != R) issue_piece(IC<0>{}, ic); });
    if constexpr (R == 0) {
#pragma unroll
        for (int i = 0; i < 5; ++i) issue_kv(i);
    }
    sfor<0, 9>([&](auto ic) { if constexpr ((decltype(ic)::value & 1) != R) issue_piece(IC<1>{}, ic); });
    asm volatile("s_waitcnt lgkmcnt(0)" : "+s"(epoch), "+s"(dw));
    const u64 tag = mix64(epoch);
    u32 tl = (u32)tag, th = (u32)(tag >> 32);
    asm volatile("" : "+s"(tl), "+s"(th));
    u32 xo = 0, s0o = 0, ka = 0, kb = 0, ska = 0, uto = 0, clo = 0;
    if constexpr (R == 0) {
        xo = (u32)(srow * 1024 + lane * 16);
        s0o = (lane & 31) == 0 ? WS_ST0 + (u32)((srow + (lane >> 5)) * 16) : 0xFFFFFF00u;
        const int tg = lane & 7, cp = 8 * hd + (lane >> 3);
        ska = L_SK + cp * 16;
        ka = L_K + tg * 1024 + ((((cp >> 2) ^ tg) & 7) << 4) + (cp & 3) * 4;
        kb = L_K + tg * 1024 + ((((cp >> 2) ^ (tg + 1)) & 7) << 4) + (cp & 3) * 4;
        uto = WS_UT + (u32)((mem * 64 + 2 * cp) * 4096 + (2040 - T0 - 8 * tg) * 2);
        clo = WS_O1 + (u32)((4 * grp + hd) * 16384 + mem * 2048 + lane * 16);
        pin(xo); pin(s0o); pin(ska); pin(ka); pin(kb); pin(uto); pin(clo);
    }
    if (R == 1 && wave == 7 && lane < 32) *(LAS u32x4*)(lds + (lane >> 3) * L_SLOT + (lane & 7) * 16) = (u32x4){0x3F803F80u, 0x3F803F80u, 0x3F803F80u, 0x3F803F80u};

    auto trr = [&](u32 off) { return __builtin_amdgcn_ds_read_tr16_b64_v4i16((LAS s16x4*)(lds + off)); };
    auto ldsA = [&](u32 off) { return *(LAS s16x8*)(lds + off); };
    auto flag_ok = [&](const u32x2& f) { return __all(f[0] == tl && f[1] == th); };
    auto spin_flags = [&](u32x2& f, u32 off) {
        _Pragma("unroll 1") for (int spin = 0; spin < SPIN_CAP; ++spin) {
            if (__builtin_expect(flag_ok(f), 1)) break;
            __builtin_amdgcn_s_sleep(2);
            asm volatile("buffer_load_dwordx2 %0, %1, %2, 0 offen sc1\n\ts_waitcnt vmcnt(0)" : "=&v"(f) : "v"(off), "s"(d_ws) : "memory");
        }
    };

    s16x8 Wq[16], Wm[16];
    f32x4 accM[5], accQ[5];
    const f32x4 zero4 = {0.f, 0.f, 0.f, 0.f};
    const s16x8 ones8 = {0x3F80, 0x3F80, 0x3F80, 0x3F80, 0x3F80, 0x3F80, 0x3F80, 0x3F80};
    s16x8 Ab[3][5];
    auto qkv_step = [&](auto lc, int st, const s16x8 (&A)[5], auto fill) {
        sfor<0, 5>([&](auto mc) {
            constexpr int mt = decltype(mc)::value;
            const f32x4 cm = st ? accM[mt] : zero4;
            accM[mt] = R ? mfma32(A[mt], Wm[st], cm) : mfma32(Wm[st], A[mt], cm);
            if constexpr (R == 0 && (mt > 0 || decltype(lc)::value == 0)) accQ[mt] = mfma32(Wq[st], A[mt], st ? accQ[mt] : zero4);
            fill(mc);
        });
    };

    float m1[5], r1[5]; f32x4 m4[5], r4[5], csK, csQ;
    auto load_tab = [&](bool cs) {
        const int l2 = opq(tid) & 63;
        u32 ta = L_TAB + (R ? (l2 >> 4) * 16 : (l2 & 15) * 4);
        pin(ta);
        if (R == 0 && cs) { csK = *(LAS f32x4*)(lds + L_CS + hd * 128 + (l2 >> 4) * 16); csQ = *(LAS f32x4*)(lds + L_CS + hd * 128 + 64 + (l2 >> 4) * 16); }
#pragma unroll
        for (int mt = 0; mt < 5; ++mt) {
            if (R == 0) { m1[mt] = *(LAS float*)(lds + ta + mt * 64); r1[mt] = *(LAS float*)(lds + ta + 320 + mt * 64); }
            else { m4[mt] = *(LAS f32x4*)(lds + ta + mt * 64); r4[mt] = *(LAS f32x4*)(lds + ta + 320 + mt * 64); }
        }
    };

    u32x4 hr[4];
    u32x2 flg;
    {
        const int li = lane & 15, lq = lane >> 4, tq = li >> 2, tp = lane & 3;
        const u32 xa0 = (u32)(li * 128 + ((lq ^ (li >> 1)) << 4));
        const u32 wa = (u32)((8 * lq + tq) * 128 + ((hd ^ (((tq >> 1) & 1) | ((lq & 1) << 1))) << 5) + tp * 8);
        u32 xa, wb, kw[11], vw[8];
        u32x4 s0v, skw;
        auto pf_addr = [&](int st) {
            const u32 sb = (u32)((st >> 1) & 3) * L_SLOT;
            xa = sb + (xa0 ^ ((st & 1) * 64)); wb = sb + 10240 + (st & 1) * 4096 + wa;
            asm volatile("" : "+v"(xa), "+v"(wb));
        };
        auto pf_part = [&](int st, int mt) {
            if (mt == 0) Wm[st] = cat8(trr(wb + (1 + R) * 8192), trr(wb + (1 + R) * 8192 + 512));
            Ab[st & 1][mt] = ldsA(xa + mt * 2048);
            if (R == 0 && mt == 1) Wq[st] = cat8(trr(wb), trr(wb + 512));
        };
        WAIT_VM(R ? 5 : 4);
        BAR();
        pf_addr(0);
#pragma unroll
        for (int mt = 0; mt < 5; ++mt) pf_part(0, mt);
        if constexpr (R == 0) {
            skw = *(LAS u32x4*)(lds + ska);
#pragma unroll
            for (int i = 0; i < 11; ++i) kw[i] = *(LAS u32*)(lds + ((i + 5 < 8) ? ka : kb) + (i + 5) * 128);
#pragma unroll
            for (int i = 0; i < 8; ++i) vw[i] = *(LAS u32*)(lds + ka + (L_V - L_K) + i * 128);
        }
        sfor<0, 16>([&](auto stc) {
            constexpr int st = decltype(stc)::value;
            if constexpr ((st & 1) && st < 15) {
                constexpr int s1 = (st >> 1) + 1;
                if constexpr (R == 0) {
                    if constexpr (s1 == 1) WAIT_VM(4);
                    if constexpr (s1 == 3) WAIT_VM(0);
                } else {
                    constexpr int WN[8] = {0, 9, 9, 9, 9, 9, 10, 8};
                    WAIT_VM(WN[s1]);
                    if constexpr (s1 == 7) {
                        pin(s0v);
                        const int l2 = opq(tid) & 63, tr = hd * 20 + min(l2, 19), row = T0 - 16 + tr;
                        _Pragma("unroll 1") for (int spin = 0; spin < SPIN_CAP; ++spin) {
                            if (__builtin_expect(__all(row < 0 || (s0v[1] == tl && s0v[3] == th)), 1)) break;
                            __builtin_amdgcn_s_sleep(2);
                            ald4s(s0v, WS_ST0 + (u32)(max(row, 0) * 16));
                            WAIT_VM(0);
                            pin(s0v);
                        }
                        *(LAS u32*)(lds + L_TAB + tr * 4) = row < 0 ? 0u : s0v[0];
                        *(LAS u32*)(lds + L_TAB + 320 + tr * 4) = row < 0 ? 0x3F800000u : s0v[2];
                        asm volatile("s_waitcnt lgkmcnt(0)" ::: "memory");
                    }
                }
                RBAR();
                if constexpr (s1 == 3 && R == 0) { if (tid == 0) __builtin_amdgcn_raw_buffer_store_b64((u32x2){tl, th}, r_ws, WS_F1 + (mem * 32 + grp) * 128, 0, 16); CFENCE(); }
                if constexpr (s1 == 5 && R == 1) { const int row = T0 - 16 + hd * 20 + min(opq(tid) & 63, 19); ald4s(s0v, WS_ST0 + (u32)(max(row, 0) * 16)); }
            }
            if (st < 15) pf_addr(st + 1);
            if constexpr (st == 14) load_tab(false);
            SB();
            qkv_step(IC<0>{}, st, Ab[st & 1], [&](auto mc) {
                constexpr int mt = decltype(mc)::value;
                if (st < 15) pf_part(st + 1, mt);
                if constexpr (R == 1) {
                    if constexpr (st == 0) { issue_piece(IC<2>{}, IC<2 * mt>{}); if constexpr (mt < 4) issue_piece(IC<2>{}, IC<2 * mt + 1>{}); }
                    if constexpr (st >= 1 && st <= 10 && ((st - 1) & 1) * 5 + mt < 9) issue_piece(IC<((st - 1) >> 1) + 3>{}, IC<((st - 1) & 1) * 5 + mt>{});
                    if constexpr (st == 11 && mt == 0) { const u32 ho = (u32)(b * 8192 + opq(tid) * 16); ald4(hr[2], d_h, ho); ald4(hr[3], d_h, ho + 16); }
                    if constexpr ((st == 11 && (mt == 2 || mt == 4)) || (st == 12 && mt == 1)) {
                        const int l2 = opq(tid) & 63;
                        const u32 co = st == 12 ? WS_Y2 + (u32)(b * 8192 + hd * 2048 + l2 * 16) : (mt == 2 ? WS_O2 : WS_X2) + (u32)((4 * grp + hd) * 16384 + mem * 2048 + l2 * 16);
                        __builtin_amdgcn_raw_buffer_store_b128((u32x4){SENT, SENT, SENT, SENT}, r_ws, co, 0, 16);
                        __builtin_amdgcn_raw_buffer_store_b128((u32x4){SENT, SENT, SENT, SENT}, r_ws, co + 1024, 0, 16);
                        CFENCE();
                    }
                    if constexpr (st == 13 && mt == 0) { if (wave == 7) { const int l2 = opq(tid) & 63; ald2s(flg, WS_F1 + (u32)((l2 < 32 ? (b >> 5) * 32 + l2 : (l2 & 7) * 32 + grp) * 128)); } }
                    if constexpr (st == 13 && mt >= 1) {
                        if constexpr (mt == 1) { const int l2 = opq(tid) & 63; wov = (u32)((l2 >> 3) * 1024 + (l2 & 7) * 16); pin(wov); }
                        dma(IC<0>{}, IC<L_DUMMY>{}, d_wo, ldsbase, wov, (u32)(((grp & 3) * 128 + hd * 32 + (mt - 1) * 8) * 1024 + mem * 128));
                    }
                } else {
                    if constexpr (st == 0) {
                        if constexpr (mt == 0) { ald4(xrow[0], d_x, xo); ald4(xrow[1], d_x, xo + 1024); }
                    }
                    if constexpr (st == 11 && mt < 2) { const u32 ho = (u32)(b * 8192 + mt * 4096 + opq(tid) * 16); ald4(hr[2 * mt], d_h, ho); ald4(hr[2 * mt + 1], d_h, ho + 16); }
                }
                SB();
            });
            if constexpr (R == 0 && st == 2) {
                WAIT_VM(4);
                pin(xrow[0]); pin(xrow[1]);
                float s[2] = {0.f, 0.f}, q[2] = {0.f, 0.f};
#pragma unroll
                for (int i = 0; i < 4; ++i)
#pragma unroll
                    for (int k = 0; k < 2; ++k) { s[k] = dot2(xrow[k][i], 0x3F803F80u, s[k]); q[k] = dot2(xrow[k][i], xrow[k][i], q[k]); }
                auto ss = __builtin_amdgcn_permlane32_swap(__float_as_uint(s[0]), __float_as_uint(s[1]), false, false);
                auto qq = __builtin_amdgcn_permlane32_swap(__float_as_uint(q[0]), __float_as_uint(q[1]), false, false);
                float sv1 = rowsum16(__uint_as_float(ss[0]) + __uint_as_float(ss[1])), qv1 = rowsum16(__uint_as_float(qq[0]) + __uint_as_float(qq[1]));
                auto s2 = __builtin_amdgcn_permlane16_swap(__float_as_uint(sv1), __float_as_uint(sv1), false, false);
                auto q2 = __builtin_amdgcn_permlane16_swap(__float_as_uint(qv1), __float_as_uint(qv1), false, false);
                const float m = (__uint_as_float(s2[0]) + __uint_as_float(s2[1])) * (1.f / 512.f);
                const float r = __builtin_amdgcn_rsqf(fmaxf((__uint_as_float(q2[0]) + __uint_as_float(q2[1])) * (1.f / 512.f) - m * m, 0.f) + 1e-5f);
                const u32x4 sv = {__float_as_uint(m), tl, __float_as_uint(r), th};
                __builtin_amdgcn_raw_buffer_store_b128(sv, r_ws, s0o, 0, 16);
                CFENCE();
                SB();
            }
            if constexpr (R == 0 && (st == 0 || st == 1)) {
                constexpr bool HI = st == 1;
                auto ex = [&](u32 w) { return HI ? bfhi(w) : bflo(w); };
                const float a0 = bflo(skw[2 * HI]), a1 = bfhi(skw[2 * HI]), a2 = bflo(skw[2 * HI + 1]), a3 = bfhi(skw[2 * HI + 1]);
                float ua[8];
#pragma unroll
                for (int i = 0; i < 8; ++i) ua[i] = (ex(kw[i]) * a3 + ex(kw[i + 1]) * a2 + ex(kw[i + 2]) * a1 + ex(kw[i + 3]) * a0) * ex(vw[i]);
                const u32x4 oa = {pk2(ua[7], ua[6]), pk2(ua[5], ua[4]), pk2(ua[3], ua[2]), pk2(ua[1], ua[0])};
                __builtin_amdgcn_raw_buffer_store_b128(oa, r_ws, uto, HI * 4096, 16);
                __builtin_amdgcn_raw_buffer_store_b128((u32x4){SENT, SENT, SENT, SENT}, r_ws, clo + HI * 1024, 0, 16);
                CFENCE();
                SB();
            }
        });
    }

    u32x4 uu;
    float csV;
    auto attention = [&](auto lc) {
        constexpr int LAYER = decltype(lc)::value;
        const int lane = opq(tid) & 63, li = lane & 15, lq = lane >> 4;
        u32 ex = L_EX + hd * 2048 + lane * 8;
        pin(ex);
        u32 f1ok = 1;
        if constexpr (R == 0) {
            if constexpr (LAYER == 0) {
#pragma unroll
                for (int i = 0; i < 4; ++i) { csK[i] = bcast_row0(accM[0][i]); csQ[i] = bcast_row0(accQ[0][i]); }
                if (li == 0) { *(LAS f32x4*)(lds + L_CS + hd * 128 + lq * 16) = csK; *(LAS f32x4*)(lds + L_CS + hd * 128 + 64 + lq * 16) = csQ; }
            }
            const int par = li & 1;
            const bool cur0 = (4 * lq + par) <= li, cur1 = (4 * lq + par + 2) <= li;
            s16x4 Kb[5], Qb[5];
#pragma unroll
            for (int mt = 0; mt < 5; ++mt) {
                Kb[mt] = pack4((accM[mt] - m1[mt] * csK) * r1[mt]);
                if (mt) Qb[mt] = pack4(accQ[mt] - m1[mt] * csQ);
            }
            f32x4 Sc[5], Sp[5];
#pragma unroll
            for (int mt = 1; mt < 5; ++mt) { Sc[mt] = mfma16(Kb[mt], Qb[mt], zero4); Sp[mt] = mfma16(Kb[mt - 1], Qb[mt], zero4); }
            float s0[5], s1[5], mx[5];
#pragma unroll
            for (int mt = 1; mt < 5; ++mt) {
                const float sp0 = Sp[mt][0], sp1 = Sp[mt][1], sp2 = Sp[mt][2], sp3 = Sp[mt][3], c0 = Sc[mt][0], c1 = Sc[mt][1], c2 = Sc[mt][2], c3 = Sc[mt][3];
                float p0 = par ? sp1 : sp0, p1 = par ? sp3 : sp2, q0 = par ? c1 : c0, q1 = par ? c3 : c2;
                asm volatile("" : "+v"(p0), "+v"(p1), "+v"(q0), "+v"(q1));
                if (mt == 1 && grp == 0) { p0 = -3.0e38f; p1 = -3.0e38f; }
                s0[mt] = cur0 ? q0 : p0; s1[mt] = cur1 ? q1 : p1;
                mx[mt] = vmax(s0[mt], s1[mt]);
            }
#pragma unroll
            for (int mt = 1; mt < 5; ++mt) { auto r = __builtin_amdgcn_permlane32_swap(__float_as_uint(mx[mt]), __float_as_uint(mx[mt]), false, false); mx[mt] = vmax(__uint_as_float(r[0]), __uint_as_float(r[1])); }
#pragma unroll
            for (int mt = 1; mt < 5; ++mt) { auto r = __builtin_amdgcn_permlane16_swap(__float_as_uint(mx[mt]), __float_as_uint(mx[mt]), false, false); mx[mt] = vmax(__uint_as_float(r[0]), __uint_as_float(r[1])); }
#pragma unroll
            for (int mt = 1; mt < 5; ++mt) {
                const float sc = r1[mt] * (0.25f * 1.44269504088896341f);
                const float e0 = __builtin_amdgcn_exp2f((s0[mt] - mx[mt]) * sc), e1 = __builtin_amdgcn_exp2f((s1[mt] - mx[mt]) * sc);
                *(LAS u32x2*)(lds + ex + (mt - 1) * 512) = (u32x2){pk2(e0, e0), pk2(e1, e1)};
            }
            BAR();
            if constexpr (LAYER == 0) f1ok = __builtin_amdgcn_readfirstlane(*(LAS u32*)(lds + L_FLG));
        } else {
            if constexpr (LAYER == 0) {
                auto s = __builtin_amdgcn_permlane16_swap(__float_as_uint(accM[0][0]), __float_as_uint(accM[0][0]), false, false);
                auto t = __builtin_amdgcn_permlane32_swap(s[0], s[0], false, false);
                csV = __uint_as_float(t[0]);
                if (wave == 7) { WAIT_VM(4); pin(flg); const u32 ok = flag_ok(flg); *(LAS u32*)(lds + L_FLG) = ok; }
            }
            const int par = li & 1;
            const bool cur0 = (4 * lq + par) <= li, cur1 = (4 * lq + par + 2) <= li;
            const u32 pm = par ? 0xffff0000u : 0x0000ffffu;
            u32 mC0 = cur0 ? pm : 0u, mP0 = cur0 ? 0u : pm, mC1 = cur1 ? pm : 0u, mP1 = cur1 ? 0u : pm;
            u32 oo = (LAYER ? WS_O2 : WS_O1) + (u32)((4 * grp + (lq & 1)) * 16384 + (4 * mem + hd) * 512 + (lq >> 1) * 256 + li * 16);
            asm volatile("" : "+v"(mC0), "+v"(mP0), "+v"(mC1), "+v"(mP1), "+v"(oo));
            s16x4 Vb[5];
#pragma unroll
            for (int mt = 0; mt < 5; ++mt) { Vb[mt] = pack4((accM[mt] - m4[mt] * csV) * r4[mt]); pin(Vb[mt]); }
            BAR();
            u32x2 pw[4];
#pragma unroll
            for (int j = 0; j < 4; ++j) pw[j] = *(LAS u32x2*)(lds + ex + j * 512);
            if constexpr (LAYER == 0) {
                f1ok = __builtin_amdgcn_readfirstlane(*(LAS u32*)(lds + L_FLG));
                if (f1ok) ald4s(uu, WS_UT + (u32)(b * 8192 + opq(tid) * 16));
            }
            SB();
            f32x4 O[4], L[4];
#pragma unroll
            for (int j = 0; j < 4; ++j) {
                const s16x8 P = __builtin_bit_cast(s16x8, (u32x4){pw[j][0] & mC0, pw[j][1] & mC1, pw[j][0] & mP0, pw[j][1] & mP1});
                L[j] = mfma32(ones8, P, zero4);
                O[j] = mfma32(cat8(Vb[j + 1], Vb[j]), P, zero4);
            }
            u32 ow[4][2];
#pragma unroll
            for (int j = 0; j < 4; ++j) {
                const float inv = __builtin_amdgcn_rcpf(L[j][0]);
                ow[j][0] = nosent(pk2(O[j][0] * inv, O[j][1] * inv)); ow[j][1] = nosent(pk2(O[j][2] * inv, O[j][3] * inv));
            }
#pragma unroll
            for (int j = 0; j < 4; j += 2) {
                auto r0 = __builtin_amdgcn_permlane16_swap(ow[j][0], ow[j + 1][0], false, false);
                auto r1 = __builtin_amdgcn_permlane16_swap(ow[j][1], ow[j + 1][1], false, false);
                __builtin_amdgcn_raw_buffer_store_b128((u32x4){r0[0], r1[0], r0[1], r1[1]}, r_ws, oo, j * 16384, 16);
            }
            CFENCE();
        }
        if constexpr (LAYER == 0 && R == 0) { if (f1ok) ald4s(uu, WS_UT + (u32)(b * 8192 + opq(tid) * 16)); }
        SB();
        return f1ok;
    };
    const u32 f1ok = attention(IC<0>{});

    const int TI = 4 * grp + hd;
    u32x4 of[8];
    auto load_o = [&](u32 ws_o) {
        const int lane = opq(tid) & 63;
#pragma unroll
        for (int i = 0; i < 8; ++i) of[i] = __builtin_amdgcn_raw_buffer_load_b128(r_ws, ws_o + (u32)(TI * 16384 + R * 8192 + lane * 16) + (i & 3) * 1024, (i >> 2) * 4096, 16);
    };
    u32 xw[8], yw[8];
    u32 woa[4];
    auto resid = [&](int n) { return R ? zero4 : (f32x4){bflo(xw[2 * n]), bfhi(xw[2 * n]), bflo(xw[2 * n + 1]), bfhi(xw[2 * n + 1])}; };
    s16x8 F[8][4];
    auto wo_addr = [&]() {
        const int l2 = opq(tid) & 63, lq = l2 >> 4, tq = (l2 & 15) >> 2, tp = l2 & 3;
#pragma unroll
        for (int n = 0; n < 4; ++n) woa[n] = L_WO + (u32)(R * 32768 + (8 * lq + tq) * 128 + ((n ^ (((lq & 1) << 1) | (tq >> 1))) << 5) + tp * 8);
    };
    auto load_F = [&](int ks) {
#pragma unroll
        for (int n = 0; n < 4; ++n) F[ks][n] = cat8(trr(woa[n] + ks * 4096), trr(woa[n] + ks * 4096 + 512));
    };

    {
        const int t2 = opq(tid), lane = t2 & 63, li = lane & 15, lq = lane >> 4;
        constexpr u32 OFFS = 0xEDA85410u;
        {
            const int j = t2 & 255;
            {
                if constexpr (R == 0) { if (f1ok) WAIT_VM(1); else WAIT_VM(0); }
                else WAIT_VM(2);
                u32 cb = L_HC + R * 34816 + (j + 2) * 16;
                pin(cb);
#pragma unroll
                for (int ch = R; ch < 2; ++ch) {
                    pin(hr[2 * ch]); pin(hr[2 * ch + 1]);
                    const u32 d[8] = {hr[2 * ch][0], hr[2 * ch][1], hr[2 * ch][2], hr[2 * ch][3], hr[2 * ch + 1][0], hr[2 * ch + 1][1], hr[2 * ch + 1][2], hr[2 * ch + 1][3]};
                    u32 e[7];
#pragma unroll
                    for (int i = 0; i < 7; ++i) e[i] = __builtin_amdgcn_alignbit(d[i + 1], d[i], 16);
                    if (ch > R) { cb += 34816; pin(cb); }
#pragma unroll
                    for (int p = (R ? 4 : 0); p < ((ch && !R) ? 4 : 8); ++p) {
                        const u32 po = (272 * p + ((OFFS >> (4 * p)) & 15)) * 16;
                        const int h = p >> 1;
                        const u32x4 w = (p & 1) ? (u32x4){e[h], e[h + 1], e[h + 2], e[h + 3]} : (u32x4){d[h], d[h + 1], d[h + 2], d[h + 3]};
                        *(LAS u32x4*)(lds + cb + po) = w;
                    }
                }
            }
            if constexpr (R == 0) {
                if (hd == 0) {
                    u32 hh[4];
#pragma unroll
                    for (int i = 0; i < 4; ++i) { const u32 a = __builtin_amdgcn_readfirstlane(hr[0][i]), c = __builtin_amdgcn_readfirstlane(hr[2][i]); hh[i] = (lane & 16) ? c : a; }
                    if (lane < 32) {
                        const int p = lane & 7;
                        const u32 po = (272 * p + ((OFFS >> (4 * p)) & 15)) * 16;
                        const unsigned __int128 hv = ((unsigned __int128)(((u64)hh[3] << 32) | hh[2]) << 64) | (((u64)hh[1] << 32) | hh[0]);
                        const unsigned __int128 sv = (p == 0 || !(lane & 8)) ? (unsigned __int128)0 : (hv << (16 * (8 - p)));
                        const u32x4 w = {(u32)sv, (u32)(sv >> 32), (u32)(sv >> 64), (u32)(sv >> 96)};
                        *(LAS u32x4*)(lds + L_HC + (lane >> 4) * 34816 + po + ((lane >> 3) & 1) * 16) = w;
                    }
                }
            }
            if (hd == 1 && lane < 30) *(LAS u32x4*)(lds + L_UR + R * 4576 + 4096 + lane * 16) = (u32x4){0, 0, 0, 0};
            if (!f1ok) {
                const u32 fo = WS_F1 + (u32)(((lane & 8) ? (lane & 7) * 32 + grp : (b >> 5) * 32 + 31 - hd * 8 - (lane & 7)) * 128);
                flg = (u32x2){0u, 0u};
                spin_flags(flg, fo);
                ald4s(uu, WS_UT + (u32)(b * 8192 + t2 * 16));
            }
            if (f1ok) WAIT_VM(R ? 2 : 0); else WAIT_VM(0);
            pin(uu);
            *(LAS u32x4*)(lds + L_UR + R * 4576 + j * 16) = uu;
        }
        BAR();
        if (R == 0 && tid == 0) __builtin_amdgcn_raw_buffer_store_b64((u32x2){tl, th}, r_ws, WS_F2 + (grp * 8 + mem) * 128, 0, 16);
        const int ch = hd >> 1, e0 = (hd & 1) * 4;
        const int p = (li + 1) & 7;
        u32 aA = L_HC + ch * 34816 + (272 * p + ((OFFS >> (4 * p)) & 15) + ((li + 1) >> 3) + lq + 4 * e0) * 16;
        u32 aB = L_UR + ch * 4576 + (254 - 224 - 2 * li + lq + 4 * e0) * 16;
        asm volatile("" : "+v"(aA), "+v"(aB));
        f32x4 y[4];
        u32 ooff = WS_O1 + (u32)(TI * 16384 + R * 8192 + lane * 16);
        {
            constexpr ConvSched S(R);
            s16x8 A[4][8], B[4][8];
            auto loads_at = [&](auto pc) {
                constexpr int p = decltype(pc)::value;
                sfor<0, 64>([&](auto ic) {
                    constexpr int i = decltype(ic)::value, e = i >> 4, f = i & 7;
                    if constexpr (S.slot[i] == p) {
                        if constexpr (i & 8) B[e][f] = ldsA(aB + 64 * e + 512 * (7 - f)); else A[e][f] = ldsA(aA + 64 * (8 * f + e));
                    }
                });
            };
            loads_at(IC<-1>{});
            SB();
            sfor<0, 72>([&](auto pc) {
                constexpr int p = decltype(pc)::value, e = p / 18, f = S.f[p % 18], i = S.i[p % 18], g = S.n[i] - f;
                y[i] = mfma32(A[e][f], B[e][g], (e == 0 && f == 0) ? zero4 : y[i]);
                loads_at(pc);
                if constexpr (p < 24 && (p % 3) == 1) issue_wo(IC<R * 8 + p / 3>{});
                if constexpr (p == 54) WAIT_VM(0);
                if constexpr (p >= 55 && p < 71 && (p & 1)) {
                    constexpr int i = (p - 55) >> 1;
                    ald4o<(i & 3) * 1024>(of[i], ooff + (i >> 2) * 4096, d_ws);
                }
                SB();
            });
        }
        RBAR();
        {
            constexpr int N0[4] = {0, 3, 4, 7};
            u32 ra = L_HC + hd * 8192 + lane * 16;
            asm volatile("" : "+v"(ra));
#pragma unroll
            for (int i = 0; i < 4; ++i) *(LAS f32x4*)(lds + ra + (N0[i] ^ R) * 1024) = y[i];
        }
        SB();
        if constexpr (R == 0) {
#pragma unroll
            for (int n = 0; n < 4; ++n) { const u32x2 w = __builtin_amdgcn_raw_buffer_load_b64(r_x, (u32)((TI * 16 + li) * 1024 + mem * 128 + lq * 8) + n * 32, 0, 0); xw[2 * n] = w[0]; xw[2 * n + 1] = w[1]; }
        }
        wo_addr();
        SB();
        BAR();
        {
            const float D0 = bflo(dw), D1 = bfhi(dw);
            u32 ra = L_HC + wave * 1024 + lane * 16;
            const int tb = 256 * wave + 16 * li + 4 * lq;
            u32 ru = L_UR + (2044 - tb) * 2;
            asm volatile("" : "+v"(ra), "+v"(ru));
            const f32x4 y0 = *(LAS f32x4*)(lds + ra), y1 = *(LAS f32x4*)(lds + ra + 8192), y2 = *(LAS f32x4*)(lds + ra + 16384), y3 = *(LAS f32x4*)(lds + ra + 24576);
            const u32x2 ua = *(LAS u32x2*)(lds + ru), ub = *(LAS u32x2*)(lds + ru + 4576);
            load_F(0); load_F(1);
            SB();
            const f32x4 ya = y0 + y1, yb = y2 + y3;
            const u32x4 w = {nosent(pk2(ya[0] + bfhi(ua[1]) * D0, yb[0] + bfhi(ub[1]) * D1)), nosent(pk2(ya[1] + bflo(ua[1]) * D0, yb[1] + bflo(ub[1]) * D1)),
                             nosent(pk2(ya[2] + bfhi(ua[0]) * D0, yb[2] + bfhi(ub[0]) * D1)), nosent(pk2(ya[3] + bflo(ua[0]) * D0, yb[3] + bflo(ub[0]) * D1))};
            __builtin_amdgcn_raw_buffer_store_b128(w, r_ws, WS_Y2 + (u32)(b * 8192 + tb * 4), 0, 16);
        }
        SB();
        WAIT_VM(1);

        u32 f2oth = 0;
        _Pragma("unroll 1") for (int attempt = 0; attempt < 4096; ++attempt) {
            sfor<0, 8>([&](auto ic) { pin(of[decltype(ic)::value]); });
            u32 m0 = 0, m1 = 0;
#pragma unroll
            for (int ks = 0; ks < 8; ++ks) {
                load_F((ks + 2) & 7);
                if (R == 1 && ks == 4 && wave == 7) {
                    const int pb = 32 * mem + (lane & 31);
                    f2oth = WS_F2 + (u32)((lane < 32 ? ((pb & 7) * 4 + (pb >> 6)) * 8 + ((pb >> 3) & 7) : max(grp * 8 - 8, 0) + (lane & 15)) * 128);
                    ald2s(flg, f2oth);
                }
                SB();
#pragma unroll
                for (int n = 0; n < 4; ++n) accM[n] = mfma32(F[ks][n], __builtin_bit_cast(s16x8, of[ks]), ks ? accM[n] : resid(n));
                m0 = max(max(m0, of[ks][0]), of[ks][1]); m1 = max(max(m1, of[ks][2]), of[ks][3]);
                SB();
            }
            if (__builtin_expect(__all(max(m0, m1) != SENT), 1)) break;
            __builtin_amdgcn_s_sleep(4);
            WAIT_VM(0);
            sfor<0, 8>([&](auto ic) { constexpr int i = decltype(ic)::value; ald4o<(i & 3) * 1024>(of[i], ooff + (i >> 2) * 4096, d_ws); });
            WAIT_VM(0);
        }
        if constexpr (R == 1) { if (wave == 7) { WAIT_VM(0); pin(flg); spin_flags(flg, f2oth); } }
    }

    auto load_y = [&]() {
        const int l2 = opq(tid) & 63;
#pragma unroll
        for (int i = 0; i < 8; ++i)
            yw[i] = __builtin_amdgcn_raw_buffer_load_b32(r_ws, WS_Y2 + (u32)((32 * mem + 2 * (l2 >> 4)) * 8192 + (TI * 16 + (l2 & 15)) * 4), (8 * (i >> 1) + (i & 1)) * 8192, 16);
    };
    auto pub_off = [&](u32 ws_st) {
        const int lane = opq(tid) & 63;
        return (lane & 16) ? 0xFFFFFF00u : ws_st + (u32)((TI * 8 + mem) * 256 + (lane & 15) * 16 + (lane >> 5) * 8);
    };
    auto pub_stat = [&](u32 off, u32 tg, float s, float q) {
        auto r = __builtin_amdgcn_permlane32_swap(__float_as_uint(s), __float_as_uint(q), false, false);
        const float v = __uint_as_float(r[0]) + __uint_as_float(r[1]);
        auto t = __builtin_amdgcn_permlane16_swap(__float_as_uint(v), __float_as_uint(v), false, false);
        __builtin_amdgcn_raw_buffer_store_b64((u32x2){__float_as_uint(__uint_as_float(t[0]) + __uint_as_float(t[1])), tg}, r_ws, off, 0, 16);
    };
    auto stat_off = [&](u32 ws_st, int ti) { const int l2 = opq(tid) & 63; return ws_st + (u32)(ti * 2048 + (l2 >> 4) * 512 + (l2 & 15) * 16); };
    auto fin_stat = [&](u32x4& a, u32x4& c, u32 off, float& mean, float& rstd) {
        _Pragma("unroll 1") for (int spin = 0; spin < SPIN_CAP; ++spin) {
            if (__builtin_expect(__all(((a[1] ^ tl) | (a[3] ^ th) | (c[1] ^ tl) | (c[3] ^ th)) == 0), 1)) break;
            __builtin_amdgcn_s_sleep(1);
            asm volatile("buffer_load_dwordx4 %0, %2, %3, 0 offen sc1\n\tbuffer_load_dwordx4 %1, %2, %3, 0 offen offset:256 sc1\n\ts_waitcnt vmcnt(0)"
                         : "=&v"(a), "=&v"(c) : "v"(off), "s"(d_ws) : "memory");
        }
        auto r = __builtin_amdgcn_permlane32_swap(__float_as_uint(__uint_as_float(a[0]) + __uint_as_float(c[0])), __float_as_uint(__uint_as_float(a[2]) + __uint_as_float(c[2])), false, false);
        const float v = __uint_as_float(r[0]) + __uint_as_float(r[1]);
        auto t = __builtin_amdgcn_permlane16_swap(__float_as_uint(v), __float_as_uint(v), false, false);
        const u32 v2 = __float_as_uint(__uint_as_float(t[0]) + __uint_as_float(t[1]));
        auto u = __builtin_amdgcn_permlane32_swap(v2, v2, false, false);
        const float ts = __uint_as_float(u[0]), tq = __uint_as_float(u[1]);
        mean = ts * (1.f / 512.f);
        rstd = __builtin_amdgcn_rsqf(fmaxf(tq * (1.f / 512.f) - mean * mean, 0.f) + 1e-5f);
    };
    auto regroup = [&](u32 (&w)[8]) {
#pragma unroll
        for (int j = 0; j < 2; ++j) {
            auto r0 = __builtin_amdgcn_permlane16_swap(w[4 * j], w[4 * j + 2], false, false);
            auto r1 = __builtin_amdgcn_permlane16_swap(w[4 * j + 1], w[4 * j + 3], false, false);
            w[4 * j] = r0[0]; w[4 * j + 1] = r1[0]; w[4 * j + 2] = r0[1]; w[4 * j + 3] = r1[1];
        }
    };
    auto wo_finish = [&](auto lc) {
        constexpr int LAYER = decltype(lc)::value;
        const int lane = opq(tid) & 63, li = lane & 15, lq = lane >> 4;
        u32 wx = L_WX + hd * 4096 + lane * 16;
        pin(wx);
        u32 po = 0, so = 0, tg = 0, oa = 0;
        if constexpr (R == 1) {
#pragma unroll
            for (int n = 0; n < 4; ++n) *(LAS f32x4*)(lds + wx + n * 1024) = accM[n];
        } else {
            po = pub_off(LAYER ? WS_ST3 : WS_ST1); so = stat_off(LAYER ? WS_ST3 : WS_ST1, TI); tg = lane < 32 ? tl : th;
            const int c = 2 * (lq & 1) + (lq >> 1);
            oa = LAYER ? (u32)((TI * 16 + li) * 1024 + mem * 128 + c * 16) : WS_X2 + (u32)((TI * 8 + mem) * 2048 + li * 128 + ((c ^ (li >> 1)) << 4));
            pin(po); pin(so); pin(tg); pin(oa);
        }
        BAR();
        if constexpr (R == 0) {
            f32x4 t[4];
#pragma unroll
            for (int n = 0; n < 4; ++n) t[n] = *(LAS f32x4*)(lds + wx + n * 1024);
            SB();
            float s = 0.f, q = 0.f, s2 = 0.f, q2 = 0.f;
#pragma unroll
            for (int n = 0; n < 4; ++n) {
                const f32x4 v = accM[n] + t[n];
                const u32 w0 = pk2(v[0], v[1]), w1 = pk2(v[2], v[3]);
                xw[2 * n] = w0; xw[2 * n + 1] = w1;
                s = dot2(w0, 0x3F803F80u, s); q = dot2(w0, w0, q); s2 = dot2(w1, 0x3F803F80u, s2); q2 = dot2(w1, w1, q2);
            }
            pub_stat(po, tg, s + s2, q + q2);
            CFENCE();
            if constexpr (!LAYER) load_y();
            regroup(xw);
            SB();
            WAIT_VM(0);
            if constexpr (!LAYER) {
#pragma unroll
                for (int i = 0; i < 8; ++i) pin(yw[i]);
            }
            u32x4 a, c;
            ald4s(a, so); ald4s(c, so + 256);
            if constexpr (!LAYER) {
                _Pragma("unroll 1") for (int spin = 0; spin < SPIN_CAP; ++spin) {
                    if (__builtin_expect(__all(max(max(max(yw[0], yw[1]), max(yw[2], yw[3])), max(max(yw[4], yw[5]), max(yw[6], yw[7]))) != SENT), 1)) break;
                    __builtin_amdgcn_s_sleep(1);
                    CFENCE();
                    load_y();
                }
                regroup(yw);
            }
            f32x2 xa[8], ya[8], xy[8];
#pragma unroll
            for (int i = 0; i < 8; ++i) {
                xa[i] = (f32x2){bflo(xw[i]), bfhi(xw[i])}; ya[i] = (f32x2){bflo(yw[i]), bfhi(yw[i])}; xy[i] = xa[i] * ya[i];
                pin(xa[i]); pin(ya[i]); pin(xy[i]);
            }
            WAIT_VM(0);
            pin(a); pin(c);
            float mean, rstd;
            fin_stat(a, c, so, mean, rstd);
            const float nm = -mean * rstd;
#pragma unroll
            for (int i = 0; i < 8; ++i) {
                const f32x2 o = xy[i] * rstd + (ya[i] * nm + xa[i]);
                xw[i] = pk2(o[0], o[1]);
                if (!LAYER) xw[i] = nosent(xw[i]);
            }
#pragma unroll
            for (int j = 0; j < 2; ++j) {
                const u32x4 o = {xw[4 * j], xw[4 * j + 1], xw[4 * j + 2], xw[4 * j + 3]};
                if constexpr (LAYER) __builtin_amdgcn_raw_buffer_store_b128(o, r_out, oa + j * 64, 0, 0);
                else __builtin_amdgcn_raw_buffer_store_b128(o, r_ws, oa ^ (j * 64), 0, 16);
            }
            if constexpr (!LAYER) {
                float s = 0.f, q = 0.f, s2 = 0.f, q2 = 0.f;
#pragma unroll
                for (int i = 0; i < 8; i += 2) { s = dot2(xw[i], 0x3F803F80u, s); q = dot2(xw[i], xw[i], q); s2 = dot2(xw[i + 1], 0x3F803F80u, s2); q2 = dot2(xw[i + 1], xw[i + 1], q2); }
                pub_stat(po + (WS_ST2 - WS_ST1) * !(lane & 16), tg, s + s2, q + q2);
                CFENCE();
                regroup(xw);
#pragma unroll
                for (int i = 0; i < 8; ++i) { pin(xw[i]); pin(yw[i]); }
                WAIT_VM(0);
            }
        }
    };
    wo_finish(IC<0>{});
    if constexpr (R == 1) {
        if (b == 0 && wave == 7) {
            const u32 fo = WS_F1 + (u32)((opq(tid) & 63) * 128);
            u32x2 ef[4];
#pragma unroll
            for (int i = 0; i < 4; ++i) ald2s(ef[i], fo + i * 8192);
            WAIT_VM(0);
#pragma unroll
            for (int i = 0; i < 4; ++i) { pin(ef[i]); spin_flags(ef[i], fo + i * 8192); }
            if (fo == WS_F1) __builtin_amdgcn_raw_buffer_store_b64(__builtin_bit_cast(u32x2, epoch + 1), r_ws, WS_EPOCH, 0, 16);
            WAIT_VM(0);
        }
    }
    RBAR();

    {
        const int l3 = opq(tid) & 63;
        const u32 g0 = (u32)((4 * grp - 1 + (hd >> 1)) * 16384 + (hd & 1) * 1024 + l3 * 16);
        const u32 g2 = (u32)((4 * grp + 3) * 16384 + hd * 512 + l3 * 16);
        auto x2_piece = [&](auto ic) {
            constexpr int i = decltype(ic)::value, s = i / 3, p = i % 3;
            const u32 kp = (u32)((s + rot) & 7);
            if constexpr (p == 0) dma(IC<4>{}, IC<L_CX + s * 10240>{}, d_x2, hb1k, g0, kp * 2048);
            else if constexpr (p == 1) dma(IC<4>{}, IC<L_CX + s * 10240 + 4096>{}, d_x2, hb1k, g0 + 32768, kp * 2048);
            else dma(IC<5>{}, IC<L_CX + s * 10240 + 8192>{}, d_x2, hb512, g2, kp * 2048);
        };
        u32 va = L_CX + hd * 1024 + l3 * 16, vq = L_CX + 8192 + hd * 512 + (l3 & 31) * 16 + (l3 >> 5) * 10240;
        pin(va); pin(vq);
        u32x4 vr[5];
        auto ver_read = [&](auto cc) {
            constexpr int c = decltype(cc)::value;
            u32 a = va + c * 20480, q = vq + c * 20480;
            pin(a); pin(q); CFENCE();
            vr[0] = *(LAS u32x4*)(lds + a); vr[1] = *(LAS u32x4*)(lds + a + 4096); vr[2] = *(LAS u32x4*)(lds + a + 10240); vr[3] = *(LAS u32x4*)(lds + a + 14336);
            vr[4] = *(LAS u32x4*)(lds + q);
        };
        auto ver_check = [&](auto cc) {
            constexpr int c = decltype(cc)::value;
            _Pragma("unroll 1") for (int spin = 0; spin < SPIN_CAP; ++spin) {
                u32 m0 = 0, m1 = 0;
#pragma unroll
                for (int i = 0; i < 5; ++i) { m0 = max(max(m0, vr[i][0]), vr[i][1]); m1 = max(max(m1, vr[i][2]), vr[i][3]); }
                if (__builtin_expect(__all(max(m0, m1) != SENT), 1)) break;
                __builtin_amdgcn_s_sleep(1);
                sfor<6 * c, 6 * c + 6>([&](auto ic) { x2_piece(ic); });
                WAIT_VM(0);
                ver_read(cc);
            }
        };
        const u32 xb0 = (u32)((l3 & 15) * 128 + (((l3 >> 4) ^ ((l3 & 15) >> 1)) << 4));
        if constexpr (R == 1) {
            sfor<0, 12>([&](auto ic) { x2_piece(ic); });
            WAIT_VM(6);
            ver_read(IC<0>{});
            ver_check(IC<0>{});
        } else {
            u32x4 sa, sc, ha, hc;
            const u32 so = stat_off(WS_ST2, 4 * grp + hd);
            const bool halo = hd == 0 && grp > 0;
            ald4s(sa, so); ald4s(sc, so + 256);
            if (halo) { ald4s(ha, so - 2048); ald4s(hc, so - 2048 + 256); }
            WAIT_VM(0);
            pin(sa); pin(sc);
            float mean, rstd;
            fin_stat(sa, sc, so, mean, rstd);
            if (l3 < 16) { *(LAS float*)(lds + L_TAB + 64 + hd * 64 + l3 * 4) = mean; *(LAS float*)(lds + L_TAB + 320 + 64 + hd * 64 + l3 * 4) = rstd; }
            if (halo) {
                pin(ha); pin(hc);
                fin_stat(ha, hc, so - 2048, mean, rstd);
                if (l3 < 16) { *(LAS float*)(lds + L_TAB + l3 * 4) = mean; *(LAS float*)(lds + L_TAB + 320 + l3 * 4) = rstd; }
            }
        }
        BAR();
        u32 xa;
        auto pf_addr = [&](int st) { xa = L_CX + (st >> 1) * 10240 + (xb0 ^ ((st & 1) * 64)); pin(xa); };
        pf_addr(0);
#pragma unroll
        for (int mt = 0; mt < 5; ++mt) Ab[0][mt] = ldsA(xa + mt * 2048);
        sfor<0, 16>([&](auto stc) {
            constexpr int st = decltype(stc)::value;
            if constexpr (R == 1) {
                if constexpr (st == 2) { WAIT_VM(10); ver_read(IC<1>{}); }
                if constexpr (st == 3) ver_check(IC<1>{});
                if constexpr (st == 5) { WAIT_VM(6); ver_read(IC<2>{}); }
                if constexpr (st == 6) { ver_check(IC<2>{}); WAIT_VM(0); ver_read(IC<3>{}); }
                if constexpr (st == 7) ver_check(IC<3>{});
            }
            if constexpr (st == 3) { RBAR(); }
            if constexpr (st == 7) { RBAR(); }
            if (st < 15) pf_addr(st + 1);
            if constexpr (st == 14) load_tab(true);
            SB();
            qkv_step(IC<1>{}, st, Ab[st & 1], [&](auto mc) {
                constexpr int mt = decltype(mc)::value;
                if (st < 15) Ab[(st + 1) & 1][mt] = ldsA(xa + mt * 2048);
                if constexpr (R == 1 && 12 + 5 * st + mt < 24) x2_piece(IC<12 + 5 * st + mt>{});
                SB();
            });
        });
    }
    attention(IC<1>{});

    {
        wo_addr();
#pragma unroll
        for (int ks = 0; ks < 8; ++ks) load_F(ks);
        SB();
        WAIT_VM(0);
        RBAR();
        load_o(WS_O2);
        SB();
        _Pragma("unroll 1") for (int spin = 0; spin < 64; ++spin) {
            u32 m0 = 0, m1 = 0;
#pragma unroll
            for (int ks = 0; ks < 8; ++ks) {
#pragma unroll
                for (int n = 0; n < 4; ++n) accM[n] = mfma32(F[ks][n], __builtin_bit_cast(s16x8, of[ks]), ks ? accM[n] : resid(n));
                m0 = max(max(m0, of[ks][0]), of[ks][1]); m1 = max(max(m1, of[ks][2]), of[ks][3]);
                SB();
            }
            if (__builtin_expect(__all(max(m0, m1) != SENT), 1)) break;
            _Pragma("unroll 1") for (int sp2 = 0; sp2 < SPIN_CAP; ++sp2) {
                CFENCE();
                __builtin_amdgcn_s_sleep(1);
                const u32 pv = __builtin_amdgcn_raw_buffer_load_b32(r_ws, WS_O2 + (u32)(TI * 16384 + R * 8192 + (opq(tid) & 15) * 512 + 508), 0, 16);
                if (__all(pv != SENT)) break;
            }
            CFENCE();
            load_o(WS_O2);
        }
        SB();
    }
    wo_finish(IC<1>{});
}

__global__ void __launch_bounds__(512, 2)
fused_kernel(const u16* __restrict__ gx, const u16* __restrict__ gwqkv, const u16* __restrict__ gwo,
             const u16* __restrict__ gbk, const u16* __restrict__ gbv, const u16* __restrict__ gsk,
             const u16* __restrict__ ghk, const u16* __restrict__ gdv, u16* __restrict__ gout, char* __restrict__ ws) {
    __shared__ __attribute__((aligned(16))) char lds_[L_TOTAL];
    typedef u32 u32x16 __attribute__((ext_vector_type(16)));
    u32x16 ka; u32x4 kb;
    {
        auto kp = __builtin_amdgcn_kernarg_segment_ptr();
        asm volatile("s_load_dwordx16 %0, %2, 0x0\n\ts_load_dwordx4 %1, %2, 0x40\n\ts_waitcnt lgkmcnt(0)" : "=&s"(ka), "=&s"(kb) : "s"(kp) : "memory");
    }
    auto mkp = [](u32 lo, u32 hi) { return (char*)(((u64)hi << 32) | lo); };
    const Args a = {(const u16*)mkp(ka[0], ka[1]), (const u16*)mkp(ka[2], ka[3]), (const u16*)mkp(ka[4], ka[5]), (const u16*)mkp(ka[6], ka[7]), (const u16*)mkp(ka[8], ka[9]),
                    (const u16*)mkp(ka[10], ka[11]), (const u16*)mkp(ka[12], ka[13]), (const u16*)mkp(ka[14], ka[15]), (u16*)mkp(kb[0], kb[1]), mkp(kb[2], kb[3])};
    const int wave = __builtin_amdgcn_readfirstlane(threadIdx.x >> 6);
    if (wave < 4) body<0>(a, (LAS char*)lds_, wave); else body<1>(a, (LAS char*)lds_, wave);
}

extern "C" void kernel_launch(void* const* d_in, const int* in_sizes, int n_in,
                              void* d_out, int out_size, void* d_ws, size_t ws_size,
                              hipStream_t stream) {
    fused_kernel<<<256, 512, 0, stream>>>((const u16*)d_in[0], (const u16*)d_in[1], (const u16*)d_in[2],
                                          (const u16*)d_in[4], (const u16*)d_in[5], (const u16*)d_in[6],
                                          (const u16*)d_in[7], (const u16*)d_in[8], (u16*)d_out, (char*)d_ws);
}
```

```cpp
#include <hip/hip_runtime.h>
#include <stdint.h>

typedef unsigned short u16;
typedef unsigned u32;
typedef unsigned long long u64;
typedef short s16x4 __attribute__((ext_vector_type(4)));
typedef short s16x8 __attribute__((ext_vector_type(8)));
typedef __bf16 bf16x2 __attribute__((ext_vector_type(2)));
typedef float f32x2 __attribute__((ext_vector_type(2)));
typedef float f32x4 __attribute__((ext_vector_type(4)));
typedef u32 u32x2 __attribute__((ext_vector_type(2)));
typedef u32 u32x4 __attribute__((ext_vector_type(4)));
#define LAS __attribute__((address_space(3)))
#define DEV __device__ __forceinline__
typedef __amdgpu_buffer_rsrc_t rsrc_t;

constexpr u32 WS_EPOCH = 0;
constexpr u32 WS_F1 = 32768;
constexpr u32 WS_F2 = 65536;
constexpr u32 WS_ST0 = 98304;
constexpr u32 WS_ST1 = 131072;
constexpr u32 WS_ST2 = WS_ST1 + 262144;
constexpr u32 WS_ST3 = WS_ST2 + 262144;
constexpr u32 WS_UT = 1u << 20;
constexpr u32 WS_O1 = WS_UT + (2u << 20);
constexpr u32 WS_O2 = WS_O1 + (2u << 20);
constexpr u32 WS_Y2 = WS_O2 + (2u << 20);
constexpr u32 WS_X2 = WS_Y2 + (2u << 20);
constexpr u32 WS_END = WS_X2 + (2u << 20);

constexpr u32 L_SLOT = 34816;
constexpr u32 L_KV = 4 * L_SLOT;
constexpr u32 L_K = L_KV, L_V = L_KV + 9216;
constexpr u32 L_SK = L_KV + 17408;
constexpr u32 L_WO = 0;
constexpr u32 L_CX = 65536;
constexpr u32 L_HC = L_CX;
constexpr u32 L_UR = L_CX + 69632;
constexpr u32 L_WX = L_CX + 40960;
constexpr u32 L_EX = 147456;
constexpr u32 L_TAB = L_EX + 12288;
constexpr u32 L_CS = L_TAB + 1024;
constexpr u32 L_FLG = L_TAB + 2048;
constexpr u32 L_DUMMY = L_FLG + 1024;
constexpr u32 L_TOTAL = 160 * 1024;

constexpr int SPIN_CAP = 1 << 14;
constexpr u32 SENT = 0xFFFFFFFFu;

#define WAIT_VM(n) asm volatile("s_waitcnt vmcnt(%0)" ::"i"(n) : "memory")
#define BAR() do { asm volatile("s_waitcnt lgkmcnt(0)" ::: "memory"); __builtin_amdgcn_s_barrier(); asm volatile("" ::: "memory"); } while (0)
#define RBAR() do { asm volatile("" ::: "memory"); __builtin_amdgcn_s_barrier(); asm volatile("" ::: "memory"); } while (0)
#define CFENCE() asm volatile("" ::: "memory")
#define SB() __builtin_amdgcn_sched_barrier(0)
template <int N> struct IC { static constexpr int value = N; };
template <int I, int N, class F> DEV void sfor(F&& f) { if constexpr (I < N) { f(IC<I>{}); sfor<I + 1, N>(f); } }

DEV f32x4 mfma32(s16x8 a, s16x8 b, f32x4 c) { return __builtin_amdgcn_mfma_f32_16x16x32_bf16(a, b, c, 0, 0, 0); }
DEV f32x4 mfma16(s16x4 a, s16x4 b, f32x4 c) { return __builtin_amdgcn_mfma_f32_16x16x16bf16_1k(a, b, c, 0, 0, 0); }
DEV float vmax(float a, float b) { float r; asm("v_max_f32 %0, %1, %2" : "=v"(r) : "v"(a), "v"(b)); return r; }
DEV u32 pk2(float lo, float hi) { f32x2 f = {lo, hi}; bf16x2 b = __builtin_convertvector(f, bf16x2); return __builtin_bit_cast(u32, b); }
DEV float bflo(u32 w) { return __uint_as_float(w << 16); }
DEV float bfhi(u32 w) { return __uint_as_float(w & 0xffff0000u); }
DEV float dot2(u32 a, u32 b, float c) { return __builtin_amdgcn_fdot2_f32_bf16(__builtin_bit_cast(bf16x2, a), __builtin_bit_cast(bf16x2, b), c, false); }
DEV u32 nosent(u32 w) { return min(w, 0xFFFFFFFEu); }
DEV float rowsum16(float v) {
    v += __builtin_bit_cast(float, __builtin_amdgcn_update_dpp(0, __builtin_bit_cast(int, v), 0xB1, 0xf, 0xf, true));
    v += __builtin_bit_cast(float, __builtin_amdgcn_update_dpp(0, __builtin_bit_cast(int, v), 0x4E, 0xf, 0xf, true));
    v += __builtin_bit_cast(float, __builtin_amdgcn_update_dpp(0, __builtin_bit_cast(int, v), 0x141, 0xf, 0xf, true));
    v += __builtin_bit_cast(float, __builtin_amdgcn_update_dpp(0, __builtin_bit_cast(int, v), 0x140, 0xf, 0xf, true));
    return v;
}
DEV float bcast_row0(float v) { return __builtin_bit_cast(float, __builtin_amdgcn_update_dpp(0, __builtin_bit_cast(int, v), 0x150, 0xf, 0xf, true)); }
template <int OFF> DEV void ald4o(u32x4& d, u32 voff, u32x4 rs) { asm volatile("buffer_load_dwordx4 %0, %1, %2, 0 offen offset:%3 sc1" : "=v"(d) : "v"(voff), "s"(rs), "i"(OFF) : "memory"); }
template <class T> DEV void pin(T& v) { asm volatile("" : "+v"(v)); }
DEV int opq(int v) { asm volatile("" : "+v"(v)); return v; }
DEV s16x8 cat8(s16x4 a, s16x4 b) { return __builtin_shufflevector(a, b, 0, 1, 2, 3, 4, 5, 6, 7); }
DEV s16x4 pack4(f32x4 v) { u32x2 w = {pk2(v[0], v[1]), pk2(v[2], v[3])}; return __builtin_bit_cast(s16x4, w); }
DEV u64 mix64(u64 z) { z += 0x9E3779B97F4A7C15ull; z = (z ^ (z >> 30)) * 0xBF58476D1CE4E5B9ull; z = (z ^ (z >> 27)) * 0x94D049BB133111EBull; return z ^ (z >> 31); }

struct ConvSched {
    int n[4], f[18], i[18], slot[64];
    constexpr ConvSched(int R) : n{}, f{}, i{}, slot{} {
        constexpr int LEAD = 9;
        const int N0[4] = {0, 3, 4, 7};
        for (int k = 0; k < 4; ++k) n[k] = N0[k] ^ R;
        int c = 0;
        for (int ff = 0; ff < 8; ++ff) for (int k = 3; k >= 0; --k) if (ff <= n[k]) { f[c] = ff; i[c] = k; ++c; }
        for (int k = 0; k < 64; ++k) slot[k] = 1000;
        for (int p = 71; p >= 0; --p) {
            const int e = p / 18, ff = f[p % 18], g = n[i[p % 18]] - ff, s = p - LEAD < -1 ? -1 : p - LEAD;
            slot[e * 16 + ff] = s; slot[e * 16 + 8 + g] = s;
        }
    }
};

struct Args { const u16 *gx, *gwqkv, *gwo, *gbk, *gbv, *gsk, *ghk, *gdv; u16* gout; char* ws; };

template <int R>
DEV void body(const Args& A_, LAS char* const lds, const int wave) {
    const u16* gx = A_.gx; char* ws = A_.ws;
    const int b = blockIdx.x, tid = threadIdx.x, lane = tid & 63;
    const int hd = wave & 3;
    const int grp = (b & 7) * 4 + (b >> 6), mem = (b >> 3) & 7;
    const int T0 = grp * 64;
    const int rot = (mem + 2 * (grp & 3)) & 7;

    u64 epoch;
    asm volatile("s_load_dwordx2 %0, %1, 0x0" : "=s"(epoch) : "s"(ws));
    u32 dw;
    asm volatile("s_load_dword %0, %1, 0x0" : "=s"(dw) : "s"(A_.gdv + 2 * b));
    const rsrc_t r_x = __builtin_amdgcn_make_buffer_rsrc((void*)gx, 0, 2048 * 512 * 2, 0x00020000);
    const rsrc_t r_ws = __builtin_amdgcn_make_buffer_rsrc((void*)ws, 0, WS_END, 0x00020000);
    const rsrc_t r_out = __builtin_amdgcn_make_buffer_rsrc((void*)A_.gout, 0, 2048 * 512 * 2, 0x00020000);
    const u32 ldsbase = (u32)(size_t)lds;
    auto mkdesc = [](const void* p, u32 bytes) { const u64 a = (u64)p; return (u32x4){(u32)a, (u32)(a >> 32) & 0xffffu, bytes, 0x00020000u}; };
    const u32x4 d_x = mkdesc(gx, 2048 * 512 * 2), d_w = mkdesc(A_.gwqkv, 512 * 1536 * 2), d_wo = mkdesc(A_.gwo, 512 * 512 * 2);
    const u32x4 d_k = mkdesc(A_.gbk, 2048 * 512 * 2), d_v = mkdesc(A_.gbv, 2048 * 512 * 2), d_sk = mkdesc(A_.gsk, 512 * 4 * 2);
    const u32x4 d_h = mkdesc(A_.ghk, 512 * 2048 * 2), d_x2 = mkdesc(ws + WS_X2, 2u << 20), d_ws = mkdesc(ws, WS_END);
    const u32 hb1k = ldsbase + hd * 1024, hb512 = ldsbase + hd * 512;
#define DMA_ASM(pre, mods, post) asm volatile("s_add_u32 m0, %0, %4\n\t" pre "buffer_load_dwordx4 %1, %2, %3 offen " mods "lds" post \
                     :: "s"(sbase), "v"(voff), "s"(rs), "s"(soff), "i"(IMM) : "memory", "scc")
    const u32 xmlo = hd == 0 ? ~0xFFu : ~0u;
    auto dma = [&](auto kind, auto imm, u32x4 rs, u32 sbase, u32 voff, u32 soff) {
        constexpr int K = decltype(kind)::value; constexpr int IMM = decltype(imm)::value;
        if constexpr (K == 0) DMA_ASM("s_nop 0\n\t", "", "");
        else if constexpr (K == 1) DMA_ASM("s_mov_b32 exec_hi, 0\n\t", "", "\n\ts_mov_b32 exec_hi, -1");
        else if constexpr (K == 2) {
            const u32 ml = xmlo;
            asm volatile("s_add_u32 m0, %0, %4\n\ts_mov_b32 exec_lo, %5\n\tbuffer_load_dwordx4 %1, %2, %3 offen lds\n\ts_mov_b32 exec_lo, -1"
                         :: "s"(sbase), "v"(voff), "s"(rs), "s"(soff), "i"(IMM), "s"(ml) : "memory", "scc");
        }
        else if constexpr (K == 4) DMA_ASM("s_nop 0\n\t", "sc1 ", "");
        else DMA_ASM("s_mov_b32 exec_hi, 0\n\t", "sc1 ", "\n\ts_mov_b32 exec_hi, -1");
    };
    auto ald4 = [&](u32x4& d, u32x4 rs, u32 voff) { asm volatile("buffer_load_dwordx4 %0, %1, %2, 0 offen" : "=v"(d) : "v"(voff), "s"(rs) : "memory"); };
    auto ald4s = [&](u32x4& d, u32 voff) { asm volatile("buffer_load_dwordx4 %0, %1, %2, 0 offen sc1" : "=v"(d) : "v"(voff), "s"(d_ws) : "memory"); };
    auto ald2s = [&](u32x2& d, u32 voff) { asm volatile("buffer_load_dwordx2 %0, %1, %2, 0 offen sc1" : "=v"(d) : "v"(voff), "s"(d_ws) : "memory"); };

    const int pr = lane >> 3, pc = lane & 7;
    const int xr0 = 8 * hd + pr, xr2 = 64 + 4 * hd + pr;
    const u32 xv0 = (u32)((T0 - 16 + xr0) * 1024 + ((pc ^ ((xr0 >> 1) & 7)) << 4));
    const u32 xv2 = (u32)((T0 - 16 + xr2) * 1024 + ((pc ^ ((xr2 >> 1) & 7)) << 4));
    const int wfw = ((xr0 >> 1) & 1) | (((xr0 >> 3) & 1) << 1);
    const u32 wv0 = (u32)(xr0 * 3072 + mem * 128 + ((((pc >> 1) ^ wfw) * 2 + (pc & 1)) << 4));
    auto issue_piece = [&](auto sc, auto ic) {
        constexpr int s = decltype(sc)::value, i = decltype(ic)::value;
        const u32 kp = (u32)((s + rot) & 7);
        constexpr int sb = (s & 3) * L_SLOT;
        if constexpr (i == 0) dma(IC<2>{}, IC<sb>{}, d_x, hb1k, xv0, kp * 128);
        else if constexpr (i == 1) dma(IC<0>{}, IC<sb + 4096>{}, d_x, hb1k, xv0 + 32768, kp * 128);
        else if constexpr (i == 2) dma(IC<1>{}, IC<sb + 8192>{}, d_x, hb512, xv2, kp * 128);
        else dma(IC<0>{}, IC<sb + 10240 + (i - 3) * 4096>{}, d_w, hb1k, wv0, kp * (64 * 3072) + ((i - 3) >> 1) * 1024 + ((i - 3) & 1) * (32 * 3072));
    };
    u32 wov = 0;
    auto issue_wo = [&](auto ic) {
        constexpr int i = decltype(ic)::value;
        if constexpr ((i & 7) == 0) {
            const int l2 = opq(tid) & 63, pr = l2 >> 3, pc = l2 & 7;
            wov = (u32)((8 * hd + pr) * 1024 + mem * 128 + ((((pc >> 1) ^ (((pr >> 1) & 1) | ((hd & 1) << 1))) * 2 + (pc & 1)) << 4));
            pin(wov);
        }
        const u32 j = (u32)((i + 2 * grp) & 7);
        dma(IC<0>{}, IC<L_WO + (i & 8) * 4096>{}, d_wo, hb1k + j * 4096, wov, ((i & 8) + j) * 32768);
    };
    auto issue_kv = [&](int i) {
        const int l2 = opq(tid) & 63, pr = l2 >> 3, pc = l2 & 7, p = hd + 4 * (i & 1);
        const u32 cb = (u32)(mem * 128);
        if (i < 2) dma(IC<0>{}, IC<L_K>{}, d_k, hb1k + (i & 1) * 4096, (u32)((T0 - 8 + 8 * p + pr) * 1024) + cb + ((pc ^ p) << 4), 0);
        else if (i < 4) dma(IC<0>{}, IC<L_V>{}, d_v, hb1k + (i & 1) * 4096, (u32)((T0 + 8 * p + pr) * 1024) + cb + ((pc ^ p) << 4), 0);
        else if (hd == 0) dma(IC<0>{}, IC<L_K + 8192>{}, d_k, ldsbase, (u32)((T0 - 8 + 64 + pr) * 1024) + cb + (pc << 4), 0);
        else dma(IC<0>{}, IC<L_SK>{}, d_sk, ldsbase, (u32)(mem * 512 + l2 * 16), 0);
    };

    const int srow = T0 + 8 * mem + 2 * hd;
    u32x4 xrow[2];
    sfor<0, 9>([&](auto ic) { if constexpr ((decltype(ic)::value & 1) != R) issue_piece(IC<0>{}, ic); });
    if constexpr (R == 0) {
#pragma unroll
        for (int i = 0; i < 5; ++i) issue_kv(i);
    }
    sfor<0, 9>([&](auto ic) { if constexpr ((decltype(ic)::value & 1) != R) issue_piece(IC<1>{}, ic); });
    asm volatile("s_waitcnt lgkmcnt(0)" : "+s"(epoch), "+s"(dw));
    const u64 tag = mix64(epoch);
    u32 tl = (u32)tag, th = (u32)(tag >> 32);
    asm volatile("" : "+s"(tl), "+s"(th));
    u32 xo = 0, s0o = 0, ka = 0, kb = 0, ska = 0, uto = 0, clo = 0;
    if constexpr (R == 0) {
        xo = (u32)(srow * 1024 + lane * 16);
        s0o = (lane & 31) == 0 ? WS_ST0 + (u32)((srow + (lane >> 5)) * 16) : 0xFFFFFF00u;
        const int tg = lane & 7, cp = 8 * hd + (lane >> 3);
        ska = L_SK + cp * 16;
        ka = L_K + tg * 1024 + ((((cp >> 2) ^ tg) & 7) << 4) + (cp & 3) * 4;
        kb = L_K + tg * 1024 + ((((cp >> 2) ^ (tg + 1)) & 7) << 4) + (cp & 3) * 4;
        uto = WS_UT + (u32)((mem * 64 + 2 * cp) * 4096 + (2040 - T0 - 8 * tg) * 2);
        clo = WS_O1 + (u32)((4 * grp + hd) * 16384 + mem * 2048 + lane * 16);
        pin(xo); pin(s0o); pin(ska); pin(ka); pin(kb); pin(uto); pin(clo);
    }
    if (R == 1 && wave == 7 && lane < 32) *(LAS u32x4*)(lds + (lane >> 3) * L_SLOT + (lane & 7) * 16) = (u32x4){0x3F803F80u, 0x3F803F80u, 0x3F803F80u, 0x3F803F80u};

    auto trr = [&](u32 off) { return __builtin_amdgcn_ds_read_tr16_b64_v4i16((LAS s16x4*)(lds + off)); };
    auto ldsA = [&](u32 off) { return *(LAS s16x8*)(lds + off); };
    auto flag_ok = [&](const u32x2& f) { return __all(f[0] == tl && f[1] == th); };
    auto spin_flags = [&](u32x2& f, u32 off) {
        _Pragma("unroll 1") for (int spin = 0; spin < SPIN_CAP; ++spin) {
            if (__builtin_expect(flag_ok(f), 1)) break;
            __builtin_amdgcn_s_sleep(2);
            asm volatile("buffer_load_dwordx2 %0, %1, %2, 0 offen sc1\n\ts_waitcnt vmcnt(0)" : "=&v"(f) : "v"(off), "s"(d_ws) : "memory");
        }
    };

    s16x8 Wq[16], Wm[16];
    f32x4 accM[5], accQ[5];
    const f32x4 zero4 = {0.f, 0.f, 0.f, 0.f};
    const s16x8 ones8 = {0x3F80, 0x3F80, 0x3F80, 0x3F80, 0x3F80, 0x3F80, 0x3F80, 0x3F80};
    s16x8 Ab[3][5];
    auto qkv_step = [&](auto lc, int st, const s16x8 (&A)[5], auto fill) {
        sfor<0, 5>([&](auto mc) {
            constexpr int mt = decltype(mc)::value;
            const f32x4 cm = st ? accM[mt] : zero4;
            accM[mt] = R ? mfma32(A[mt], Wm[st], cm) : mfma32(Wm[st], A[mt], cm);
            if constexpr (R == 0 && (mt > 0 || decltype(lc)::value == 0)) accQ[mt] = mfma32(Wq[st], A[mt], st ? accQ[mt] : zero4);
            fill(mc);
        });
    };

    float m1[5], r1[5], w0[5], w1[5]; f32x4 m4[5], r4[5], csK, csQ;
    auto load_tab = [&](bool cs) {
        const int l2 = opq(tid) & 63;
        u32 ta = L_TAB + (R ? (l2 >> 4) * 16 : (l2 & 15) * 4);
        pin(ta);
        const int j0 = 4 * (l2 >> 4) + (l2 & 1);
        u32 k0 = L_TAB + 320 + (j0 <= (l2 & 15) ? j0 : j0 - 16) * 4, k1 = L_TAB + 320 + (j0 + 2 <= (l2 & 15) ? j0 + 2 : j0 - 14) * 4;
        if (R == 0) { pin(k0); pin(k1); }
        if (R == 0 && cs) { csK = *(LAS f32x4*)(lds + L_CS + hd * 128 + (l2 >> 4) * 16); csQ = *(LAS f32x4*)(lds + L_CS + hd * 128 + 64 + (l2 >> 4) * 16); }
#pragma unroll
        for (int mt = 0; mt < 5; ++mt) {
            if (R == 0) {
                m1[mt] = *(LAS float*)(lds + ta + mt * 64);
                if (mt) { r1[mt] = *(LAS float*)(lds + ta + 320 + mt * 64); w0[mt] = *(LAS float*)(lds + k0 + mt * 64); w1[mt] = *(LAS float*)(lds + k1 + mt * 64); }
            }
            else { m4[mt] = *(LAS f32x4*)(lds + ta + mt * 64); r4[mt] = *(LAS f32x4*)(lds + ta + 320 + mt * 64); }
        }
    };

    auto fin_tab = [&]() {
        if constexpr (R == 0) {
#pragma unroll
            for (int mt = 1; mt < 5; ++mt) { const float c = r1[mt] * (0.25f * 1.44269504088896341f); w0[mt] *= c; w1[mt] *= c; pin(w0[mt]); pin(w1[mt]); }
        }
    };

    u32x4 hr[4];
    u32x2 flg;
    {
        const int li = lane & 15, lq = lane >> 4, tq = li >> 2, tp = lane & 3;
        const u32 xa0 = (u32)(li * 128 + ((lq ^ (li >> 1)) << 4));
        const u32 wa = (u32)((8 * lq + tq) * 128 + ((hd ^ (((tq >> 1) & 1) | ((lq & 1) << 1))) << 5) + tp * 8);
        u32 xa, wb, kw[11], vw[8];
        u32x4 s0v, skw;
        auto pf_addr = [&](int st) {
            const u32 sb = (u32)((st >> 1) & 3) * L_SLOT;
            xa = sb + (xa0 ^ ((st & 1) * 64)); wb = sb + 10240 + (st & 1) * 4096 + wa;
            asm volatile("" : "+v"(xa), "+v"(wb));
        };
        auto pf_part = [&](int st, int mt) {
            if (mt == 0) Wm[st] = cat8(trr(wb + (1 + R) * 8192), trr(wb + (1 + R) * 8192 + 512));
            Ab[st & 1][mt] = ldsA(xa + mt * 2048);
            if (R == 0 && mt == 1) Wq[st] = cat8(trr(wb), trr(wb + 512));
        };
        WAIT_VM(R ? 5 : 4);
        BAR();
        pf_addr(0);
#pragma unroll
        for (int mt = 0; mt < 5; ++mt) pf_part(0, mt);
        if constexpr (R == 0) {
            skw = *(LAS u32x4*)(lds + ska);
#pragma unroll
            for (int i = 0; i < 11; ++i) kw[i] = *(LAS u32*)(lds + ((i + 5 < 8) ? ka : kb) + (i + 5) * 128);
#pragma unroll
            for (int i = 0; i < 8; ++i) vw[i] = *(LAS u32*)(lds + ka + (L_V - L_K) + i * 128);
        }
        sfor<0, 16>([&](auto stc) {
            constexpr int st = decltype(stc)::value;
            if constexpr ((st & 1) && st < 15) {
                constexpr int s1 = (st >> 1) + 1;
                if constexpr (R == 0) {
                    if constexpr (s1 == 1) WAIT_VM(4);
                    if constexpr (s1 == 3) WAIT_VM(0);
                } else {
                    constexpr int WN[8] = {0, 9, 9, 9, 9, 9, 10, 8};
                    WAIT_VM(WN[s1]);
                    if constexpr (s1 == 7) {
                        pin(s0v);
                        const int l2 = opq(tid) & 63, tr = hd * 20 + min(l2, 19), row = T0 - 16 + tr;
                        _Pragma("unroll 1") for (int spin = 0; spin < SPIN_CAP; ++spin) {
                            if (__builtin_expect(__all(row < 0 || (s0v[1] == tl && s0v[3] == th)), 1)) break;
                            __builtin_amdgcn_s_sleep(2);
                            ald4s(s0v, WS_ST0 + (u32)(max(row, 0) * 16));
                            WAIT_VM(0);
                            pin(s0v);
                        }
                        *(LAS u32*)(lds + L_TAB + tr * 4) = row < 0 ? 0u : s0v[0];
                        *(LAS u32*)(lds + L_TAB + 320 + tr * 4) = row < 0 ? 0x3F800000u : s0v[2];
                        asm volatile("s_waitcnt lgkmcnt(0)" ::: "memory");
                    }
                }
                RBAR();
                if constexpr (s1 == 3 && R == 0) { if (tid == 0) __builtin_amdgcn_raw_buffer_store_b64((u32x2){tl, th}, r_ws, WS_F1 + (mem * 32 + grp) * 128, 0, 16); CFENCE(); }
                if constexpr (s1 == 5 && R == 1) { const int row = T0 - 16 + hd * 20 + min(opq(tid) & 63, 19); ald4s(s0v, WS_ST0 + (u32)(max(row, 0) * 16)); }
            }
            if (st < 15) pf_addr(st + 1);
            if constexpr (st == 14 + !R) load_tab(false);
            SB();
            qkv_step(IC<0>{}, st, Ab[st & 1], [&](auto mc) {
                constexpr int mt = decltype(mc)::value;
                if (st < 15) pf_part(st + 1, mt);
                if constexpr (R == 1) {
                    if constexpr (st == 0) { issue_piece(IC<2>{}, IC<2 * mt>{}); if constexpr (mt < 4) issue_piece(IC<2>{}, IC<2 * mt + 1>{}); }
                    if constexpr (st >= 1 && st <= 10 && ((st - 1) & 1) * 5 + mt < 9) issue_piece(IC<((st - 1) >> 1) + 3>{}, IC<((st - 1) & 1) * 5 + mt>{});
                    if constexpr (st == 11 && mt == 0) { const u32 ho = (u32)(b * 8192 + opq(tid) * 16); ald4(hr[2], d_h, ho); ald4(hr[3], d_h, ho + 16); }
                    if constexpr ((st == 11 && (mt == 2 || mt == 4)) || (st == 12 && mt == 1)) {
                        const int l2 = opq(tid) & 63;
                        const u32 co = st == 12 ? WS_Y2 + (u32)(b * 8192 + hd * 2048 + l2 * 16) : (mt == 2 ? WS_O2 : WS_X2) + (u32)((4 * grp + hd) * 16384 + mem * 2048 + l2 * 16);
                        __builtin_amdgcn_raw_buffer_store_b128((u32x4){SENT, SENT, SENT, SENT}, r_ws, co, 0, 16);
                        __builtin_amdgcn_raw_buffer_store_b128((u32x4){SENT, SENT, SENT, SENT}, r_ws, co + 1024, 0, 16);
                        CFENCE();
                    }
                    if constexpr (st == 13 && mt == 0) { if (wave == 7) { const int l2 = opq(tid) & 63; ald2s(flg, WS_F1 + (u32)((l2 < 32 ? (b >> 5) * 32 + l2 : (l2 & 7) * 32 + grp) * 128)); } }
                    if constexpr (st == 13 && mt >= 1) {
                        if constexpr (mt == 1) { const int l2 = opq(tid) & 63; wov = (u32)((l2 >> 3) * 1024 + (l2 & 7) * 16); pin(wov); }
                        dma(IC<0>{}, IC<L_DUMMY>{}, d_wo, ldsbase, wov, (u32)(((grp & 3) * 128 + hd * 32 + (mt - 1) * 8) * 1024 + mem * 128));
                    }
                } else {
                    if constexpr (st == 0) {
                        if constexpr (mt == 0) { ald4(xrow[0], d_x, xo); ald4(xrow[1], d_x, xo + 1024); }
                    }
                    if constexpr (st == 11 && mt < 2) { const u32 ho = (u32)(b * 8192 + mt * 4096 + opq(tid) * 16); ald4(hr[2 * mt], d_h, ho); ald4(hr[2 * mt + 1], d_h, ho + 16); }
                }
                SB();
            });
            if constexpr (R == 0 && st == 2) {
                WAIT_VM(4);
                pin(xrow[0]); pin(xrow[1]);
                float s[2] = {0.f, 0.f}, q[2] = {0.f, 0.f};
#pragma unroll
                for (int i = 0; i < 4; ++i)
#pragma unroll
                    for (int k = 0; k < 2; ++k) { s[k] = dot2(xrow[k][i], 0x3F803F80u, s[k]); q[k] = dot2(xrow[k][i], xrow[k][i], q[k]); }
                auto ss = __builtin_amdgcn_permlane32_swap(__float_as_uint(s[0]), __float_as_uint(s[1]), false, false);
                auto qq = __builtin_amdgcn_permlane32_swap(__float_as_uint(q[0]), __float_as_uint(q[1]), false, false);
                float sv1 = rowsum16(__uint_as_float(ss[0]) + __uint_as_float(ss[1])), qv1 = rowsum16(__uint_as_float(qq[0]) + __uint_as_float(qq[1]));
                auto s2 = __builtin_amdgcn_permlane16_swap(__float_as_uint(sv1), __float_as_uint(sv1), false, false);
                auto q2 = __builtin_amdgcn_permlane16_swap(__float_as_uint(qv1), __float_as_uint(qv1), false, false);
                const float m = (__uint_as_float(s2[0]) + __uint_as_float(s2[1])) * (1.f / 512.f);
                const float r = __builtin_amdgcn_rsqf(fmaxf((__uint_as_float(q2[0]) + __uint_as_float(q2[1])) * (1.f / 512.f) - m * m, 0.f) + 1e-5f);
                const u32x4 sv = {__float_as_uint(m), tl, __float_as_uint(r), th};
                __builtin_amdgcn_raw_buffer_store_b128(sv, r_ws, s0o, 0, 16);
                CFENCE();
                SB();
            }
            if constexpr (R == 0 && (st == 0 || st == 1)) {
                constexpr bool HI = st == 1;
                auto ex = [&](u32 w) { return HI ? bfhi(w) : bflo(w); };
                const float a0 = bflo(skw[2 * HI]), a1 = bfhi(skw[2 * HI]), a2 = bflo(skw[2 * HI + 1]), a3 = bfhi(skw[2 * HI + 1]);
                float ua[8];
#pragma unroll
                for (int i = 0; i < 8; ++i) ua[i] = (ex(kw[i]) * a3 + ex(kw[i + 1]) * a2 + ex(kw[i + 2]) * a1 + ex(kw[i + 3]) * a0) * ex(vw[i]);
                const u32x4 oa = {pk2(ua[7], ua[6]), pk2(ua[5], ua[4]), pk2(ua[3], ua[2]), pk2(ua[1], ua[0])};
                __builtin_amdgcn_raw_buffer_store_b128(oa, r_ws, uto, HI * 4096, 16);
                __builtin_amdgcn_raw_buffer_store_b128((u32x4){SENT, SENT, SENT, SENT}, r_ws, clo + HI * 1024, 0, 16);
                CFENCE();
                SB();
            }
        });
    }

    u32x4 uu;
    float csV;
    auto attention = [&](auto lc) {
        constexpr int LAYER = decltype(lc)::value;
        const int lane = opq(tid) & 63, li = lane & 15, lq = lane >> 4;
        u32 ex = L_EX + hd * 1024 + lane * 16;
        pin(ex);
        u32 f1ok = 1;
        if constexpr (R == 0) {
            if constexpr (LAYER == 0) {
                fin_tab();
#pragma unroll
                for (int i = 0; i < 4; ++i) { csK[i] = bcast_row0(accM[0][i]); csQ[i] = bcast_row0(accQ[0][i]); }
                if (li == 0) { *(LAS f32x4*)(lds + L_CS + hd * 128 + lq * 16) = csK; *(LAS f32x4*)(lds + L_CS + hd * 128 + 64 + lq * 16) = csQ; }
            }
            const int par = li & 1;
            const bool cur0 = (4 * lq + par) <= li, cur1 = (4 * lq + par + 2) <= li;
            s16x4 Kb[5], Qb[5];
#pragma unroll
            for (int mt = 0; mt < 5; ++mt) {
                Kb[mt] = pack4(accM[mt] - m1[mt] * csK);
                if (mt) Qb[mt] = pack4(accQ[mt] - m1[mt] * csQ);
            }
            f32x4 Sc[5], Sp[5];
#pragma unroll
            for (int mt = 1; mt < 5; ++mt) { Sc[mt] = mfma16(Kb[mt], Qb[mt], zero4); Sp[mt] = mfma16(Kb[mt - 1], Qb[mt], zero4); }
            float s0[5], s1[5], mx[5];
#pragma unroll
            for (int mt = 1; mt < 5; ++mt) {
                const float sp0 = Sp[mt][0], sp1 = Sp[mt][1], sp2 = Sp[mt][2], sp3 = Sp[mt][3], c0 = Sc[mt][0], c1 = Sc[mt][1], c2 = Sc[mt][2], c3 = Sc[mt][3];
                float p0 = par ? sp1 : sp0, p1 = par ? sp3 : sp2, q0 = par ? c1 : c0, q1 = par ? c3 : c2;
                asm volatile("" : "+v"(p0), "+v"(p1), "+v"(q0), "+v"(q1));
                s0[mt] = (cur0 ? q0 : p0) * w0[mt]; s1[mt] = (cur1 ? q1 : p1) * w1[mt];
                if (mt == 1 && grp == 0) { s0[mt] = cur0 ? s0[mt] : -3.0e38f; s1[mt] = cur1 ? s1[mt] : -3.0e38f; }
                mx[mt] = vmax(s0[mt], s1[mt]);
            }
            {
                asm volatile("s_nop 1" : "+v"(mx[1]), "+v"(mx[2]), "+v"(mx[3]), "+v"(mx[4]));
                auto a = __builtin_amdgcn_permlane32_swap(__float_as_uint(mx[1]), __float_as_uint(mx[2]), false, false);
                auto c = __builtin_amdgcn_permlane32_swap(__float_as_uint(mx[3]), __float_as_uint(mx[4]), false, false);
                float ma = vmax(__uint_as_float(a[0]), __uint_as_float(a[1])), mc = vmax(__uint_as_float(c[0]), __uint_as_float(c[1]));
                asm volatile("s_nop 1" : "+v"(ma), "+v"(mc));
                auto d = __builtin_amdgcn_permlane16_swap(__float_as_uint(ma), __float_as_uint(mc), false, false);
                const u32 md = __float_as_uint(vmax(__uint_as_float(d[0]), __uint_as_float(d[1])));
                auto e = __builtin_amdgcn_permlane16_swap(md, md, false, false);
                auto f = __builtin_amdgcn_permlane32_swap(e[0], e[0], false, false);
                auto g = __builtin_amdgcn_permlane32_swap(e[1], e[1], false, false);
                mx[1] = __uint_as_float(f[0]); mx[2] = __uint_as_float(f[1]); mx[3] = __uint_as_float(g[0]); mx[4] = __uint_as_float(g[1]);
            }
            u32x4 pe;
#pragma unroll
            for (int mt = 1; mt < 5; ++mt) pe[mt - 1] = pk2(__builtin_amdgcn_exp2f(s0[mt] - mx[mt]), __builtin_amdgcn_exp2f(s1[mt] - mx[mt]));
            *(LAS u32x4*)(lds + ex) = pe;
            BAR();
            if constexpr (LAYER == 0) f1ok = __builtin_amdgcn_readfirstlane(*(LAS u32*)(lds + L_FLG));
        } else {
            if constexpr (LAYER == 0) {
                auto s = __builtin_amdgcn_permlane16_swap(__float_as_uint(accM[0][0]), __float_as_uint(accM[0][0]), false, false);
                auto t = __builtin_amdgcn_permlane32_swap(s[0], s[0], false, false);
                csV = __uint_as_float(t[0]);
                if (wave == 7) { WAIT_VM(4); pin(flg); const u32 ok = flag_ok(flg); *(LAS u32*)(lds + L_FLG) = ok; }
            }
            const int par = li & 1;
            const bool cur0 = (4 * lq + par) <= li, cur1 = (4 * lq + par + 2) <= li;
            const u32 z = 0x0c0c0c0cu, e0s = par ? 0x01000c0cu : 0x0c0c0100u, e1s = par ? 0x03020c0cu : 0x0c0c0302u;
            u32 mC0 = cur0 ? e0s : z, mP0 = cur0 ? z : e0s, mC1 = cur1 ? e1s : z, mP1 = cur1 ? z : e1s;
            u32 oo = (LAYER ? WS_O2 : WS_O1) + (u32)((4 * grp + (lq & 1)) * 16384 + (4 * mem + hd) * 512 + (lq >> 1) * 256 + li * 16);
            asm volatile("" : "+v"(mC0), "+v"(mP0), "+v"(mC1), "+v"(mP1), "+v"(oo));
            s16x4 Vb[5];
#pragma unroll
            for (int mt = 0; mt < 5; ++mt) { Vb[mt] = pack4((accM[mt] - m4[mt] * csV) * r4[mt]); pin(Vb[mt]); }
            BAR();
            const u32x4 pw = *(LAS u32x4*)(lds + ex);
            if constexpr (LAYER == 0) {
                f1ok = __builtin_amdgcn_readfirstlane(*(LAS u32*)(lds + L_FLG));
                if (f1ok) ald4s(uu, WS_UT + (u32)(b * 8192 + opq(tid) * 16));
            }
            SB();
            f32x4 O[4], L[4];
#pragma unroll
            for (int j = 0; j < 4; ++j) {
                const s16x8 P = __builtin_bit_cast(s16x8, (u32x4){__builtin_amdgcn_perm(0u, pw[j], mC0), __builtin_amdgcn_perm(0u, pw[j], mC1), __builtin_amdgcn_perm(0u, pw[j], mP0), __builtin_amdgcn_perm(0u, pw[j], mP1)});
                L[j] = mfma32(ones8, P, zero4);
                O[j] = mfma32(cat8(Vb[j + 1], Vb[j]), P, zero4);
            }
            u32 ow[4][2];
#pragma unroll
            for (int j = 0; j < 4; ++j) {
                const float inv = __builtin_amdgcn_rcpf(L[j][0]);
                ow[j][0] = nosent(pk2(O[j][0] * inv, O[j][1] * inv)); ow[j][1] = nosent(pk2(O[j][2] * inv, O[j][3] * inv));
            }
#pragma unroll
            for (int j = 0; j < 4; j += 2) {
                auto r0 = __builtin_amdgcn_permlane16_swap(ow[j][0], ow[j + 1][0], false, false);
                auto r1 = __builtin_amdgcn_permlane16_swap(ow[j][1], ow[j + 1][1], false, false);
                __builtin_amdgcn_raw_buffer_store_b128((u32x4){r0[0], r1[0], r0[1], r1[1]}, r_ws, oo, j * 16384, 16);
            }
            CFENCE();
        }
        if constexpr (LAYER == 0 && R == 0) { if (f1ok) ald4s(uu, WS_UT + (u32)(b * 8192 + opq(tid) * 16)); }
        SB();
        return f1ok;
    };
    const u32 f1ok = attention(IC<0>{});

    const int TI = 4 * grp + hd;
    u32x4 of[8];
    auto load_o = [&](u32 ws_o) {
        const int lane = opq(tid) & 63;
#pragma unroll
        for (int i = 0; i < 8; ++i) of[i] = __builtin_amdgcn_raw_buffer_load_b128(r_ws, ws_o + (u32)(TI * 16384 + R * 8192 + lane * 16) + (i & 3) * 1024, (i >> 2) * 4096, 16);
    };
    u32 xw[8], yw[8];
    u32 woa[4];
    auto resid = [&](int n) { return R ? zero4 : (f32x4){bflo(xw[2 * n]), bfhi(xw[2 * n]), bflo(xw[2 * n + 1]), bfhi(xw[2 * n + 1])}; };
    s16x8 F[8][4];
    auto wo_addr = [&]() {
        const int l2 = opq(tid) & 63, lq = l2 >> 4, tq = (l2 & 15) >> 2, tp = l2 & 3;
#pragma unroll
        for (int n = 0; n < 4; ++n) woa[n] = L_WO + (u32)(R * 32768 + (8 * lq + tq) * 128 + ((n ^ (((lq & 1) << 1) | (tq >> 1))) << 5) + tp * 8);
    };
    auto load_F = [&](int ks) {
#pragma unroll
        for (int n = 0; n < 4; ++n) F[ks][n] = cat8(trr(woa[n] + ks * 4096), trr(woa[n] + ks * 4096 + 512));
    };

    {
        const int t2 = opq(tid), lane = t2 & 63, li = lane & 15, lq = lane >> 4;
        constexpr u32 OFFS = 0xEDA85410u;
        {
            const int j = t2 & 255;
            {
                if constexpr (R == 0) { if (f1ok) WAIT_VM(1); else WAIT_VM(0); }
                else WAIT_VM(2);
                u32 cb = L_HC + R * 34816 + (j + 2) * 16;
                pin(cb);
#pragma unroll
                for (int ch = R; ch < 2; ++ch) {
                    pin(hr[2 * ch]); pin(hr[2 * ch + 1]);
                    const u32 d[8] = {hr[2 * ch][0], hr[2 * ch][1], hr[2 * ch][2], hr[2 * ch][3], hr[2 * ch + 1][0], hr[2 * ch + 1][1], hr[2 * ch + 1][2], hr[2 * ch + 1][3]};
                    u32 e[7];
#pragma unroll
                    for (int i = 0; i < 7; ++i) e[i] = __builtin_amdgcn_alignbit(d[i + 1], d[i], 16);
                    if (ch > R) { cb += 34816; pin(cb); }
#pragma unroll
                    for (int p = (R ? 4 : 0); p < ((ch && !R) ? 4 : 8); ++p) {
                        const u32 po = (272 * p + ((OFFS >> (4 * p)) & 15)) * 16;
                        const int h = p >> 1;
                        const u32x4 w = (p & 1) ? (u32x4){e[h], e[h + 1], e[h + 2], e[h + 3]} : (u32x4){d[h], d[h + 1], d[h + 2], d[h + 3]};
                        *(LAS u32x4*)(lds + cb + po) = w;
                    }
                }
            }
            if constexpr (R == 0) {
                if (hd == 0) {
                    u32 hh[4];
#pragma unroll
                    for (int i = 0; i < 4; ++i) { const u32 a = __builtin_amdgcn_readfirstlane(hr[0][i]), c = __builtin_amdgcn_readfirstlane(hr[2][i]); hh[i] = (lane & 16) ? c : a; }
                    if (lane < 32) {
                        const int p = lane & 7;
                        const u32 po = (272 * p + ((OFFS >> (4 * p)) & 15)) * 16;
                        const unsigned __int128 hv = ((unsigned __int128)(((u64)hh[3] << 32) | hh[2]) << 64) | (((u64)hh[1] << 32) | hh[0]);
                        const unsigned __int128 sv = (p == 0 || !(lane & 8)) ? (unsigned __int128)0 : (hv << (16 * (8 - p)));
                        const u32x4 w = {(u32)sv, (u32)(sv >> 32), (u32)(sv >> 64), (u32)(sv >> 96)};
                        *(LAS u32x4*)(lds + L_HC + (lane >> 4) * 34816 + po + ((lane >> 3) & 1) * 16) = w;
                    }
                }
            }
            if (hd == 1 && lane < 30) *(LAS u32x4*)(lds + L_UR + R * 4576 + 4096 + lane * 16) = (u32x4){0, 0, 0, 0};
            if (!f1ok) {
                const u32 fo = WS_F1 + (u32)(((lane & 8) ? (lane & 7) * 32 + grp : (b >> 5) * 32 + 31 - hd * 8 - (lane & 7)) * 128);
                flg = (u32x2){0u, 0u};
                spin_flags(flg, fo);
                ald4s(uu, WS_UT + (u32)(b * 8192 + t2 * 16));
            }
            if (f1ok) WAIT_VM(R ? 2 : 0); else WAIT_VM(0);
            pin(uu);
            *(LAS u32x4*)(lds + L_UR + R * 4576 + j * 16) = uu;
        }
        BAR();
        if (R == 0 && tid == 0) __builtin_amdgcn_raw_buffer_store_b64((u32x2){tl, th}, r_ws, WS_F2 + (grp * 8 + mem) * 128, 0, 16);
        const int ch = hd >> 1, e0 = (hd & 1) * 4;
        const int p = (li + 1) & 7;
        u32 aA = L_HC + ch * 34816 + (272 * p + ((OFFS >> (4 * p)) & 15) + ((li + 1) >> 3) + lq + 4 * e0) * 16;
        u32 aB = L_UR + ch * 4576 + (254 - 224 - 2 * li + lq + 4 * e0) * 16;
        asm volatile("" : "+v"(aA), "+v"(aB));
        f32x4 y[4];
        u32 ooff = WS_O1 + (u32)(TI * 16384 + R * 8192 + lane * 16);
        {
            constexpr ConvSched S(R);
            s16x8 A[4][8], B[4][8];
            auto loads_at = [&](auto pc) {
                constexpr int p = decltype(pc)::value;
                sfor<0, 64>([&](auto ic) {
                    constexpr int i = decltype(ic)::value, e = i >> 4, f = i & 7;
                    if constexpr (S.slot[i] == p) {
                        if constexpr (i & 8) B[e][f] = ldsA(aB + 64 * e + 512 * (7 - f)); else A[e][f] = ldsA(aA + 64 * (8 * f + e));
                    }
                });
            };
            loads_at(IC<-1>{});
            SB();
            sfor<0, 72>([&](auto pc) {
                constexpr int p = decltype(pc)::value, e = p / 18, f = S.f[p % 18], i = S.i[p % 18], g = S.n[i] - f;
                y[i] = mfma32(A[e][f], B[e][g], (e == 0 && f == 0) ? zero4 : y[i]);
                loads_at(pc);
                if constexpr (p < 24 && (p % 3) == 1) issue_wo(IC<R * 8 + p / 3>{});
                if constexpr (p == 54) WAIT_VM(0);
                if constexpr (p >= 55 && p < 71 && (p & 1)) {
                    constexpr int i = (p - 55) >> 1;
                    ald4o<(i & 3) * 1024>(of[i], ooff + (i >> 2) * 4096, d_ws);
                }
                SB();
            });
        }
        RBAR();
        {
            constexpr int N0[4] = {0, 3, 4, 7};
            u32 ra = L_HC + hd * 8192 + lane * 16;
            asm volatile("" : "+v"(ra));
#pragma unroll
            for (int i = 0; i < 4; ++i) *(LAS f32x4*)(lds + ra + (N0[i] ^ R) * 1024) = y[i];
        }
        SB();
        if constexpr (R == 0) {
#pragma unroll
            for (int n = 0; n < 4; ++n) { const u32x2 w = __builtin_amdgcn_raw_buffer_load_b64(r_x, (u32)((TI * 16 + li) * 1024 + mem * 128 + lq * 8) + n * 32, 0, 0); xw[2 * n] = w[0]; xw[2 * n + 1] = w[1]; }
        }
        wo_addr();
        SB();
        BAR();
        {
            const float D0 = bflo(dw), D1 = bfhi(dw);
            u32 ra = L_HC + wave * 1024 + lane * 16;
            const int tb = 256 * wave + 16 * li + 4 * lq;
            u32 ru = L_UR + (2044 - tb) * 2;
            asm volatile("" : "+v"(ra), "+v"(ru));
            const f32x4 y0 = *(LAS f32x4*)(lds + ra), y1 = *(LAS f32x4*)(lds + ra + 8192), y2 = *(LAS f32x4*)(lds + ra + 16384), y3 = *(LAS f32x4*)(lds + ra + 24576);
            const u32x2 ua = *(LAS u32x2*)(lds + ru), ub = *(LAS u32x2*)(lds + ru + 4576);
            load_F(0); load_F(1);
            SB();
            const f32x4 ya = y0 + y1, yb = y2 + y3;
            const u32x4 w = {nosent(pk2(ya[0] + bfhi(ua[1]) * D0, yb[0] + bfhi(ub[1]) * D1)), nosent(pk2(ya[1] + bflo(ua[1]) * D0, yb[1] + bflo(ub[1]) * D1)),
                             nosent(pk2(ya[2] + bfhi(ua[0]) * D0, yb[2] + bfhi(ub[0]) * D1)), nosent(pk2(ya[3] + bflo(ua[0]) * D0, yb[3] + bflo(ub[0]) * D1))};
            __builtin_amdgcn_raw_buffer_store_b128(w, r_ws, WS_Y2 + (u32)(b * 8192 + tb * 4), 0, 16);
        }
        SB();
        WAIT_VM(1);

        u32 f2oth = 0;
        _Pragma("unroll 1") for (int attempt = 0; attempt < 4096; ++attempt) {
            sfor<0, 8>([&](auto ic) { pin(of[decltype(ic)::value]); });
            u32 m0 = 0, m1 = 0;
#pragma unroll
            for (int ks = 0; ks < 8; ++ks) {
                load_F((ks + 2) & 7);
                if (R == 1 && ks == 4 && wave == 7) {
                    const int pb = 32 * mem + (lane & 31);
                    f2oth = WS_F2 + (u32)((lane < 32 ? ((pb & 7) * 4 + (pb >> 6)) * 8 + ((pb >> 3) & 7) : max(grp * 8 - 8, 0) + (lane & 15)) * 128);
                    ald2s(flg, f2oth);
                }
                SB();
#pragma unroll
                for (int n = 0; n < 4; ++n) accM[n] = mfma32(F[ks][n], __builtin_bit_cast(s16x8, of[ks]), ks ? accM[n] : resid(n));
                m0 = max(max(m0, of[ks][0]), of[ks][1]); m1 = max(max(m1, of[ks][2]), of[ks][3]);
                SB();
            }
            if (__builtin_expect(__all(max(m0, m1) != SENT), 1)) break;
            __builtin_amdgcn_s_sleep(4);
            WAIT_VM(0);
            sfor<0, 8>([&](auto ic) { constexpr int i = decltype(ic)::value; ald4o<(i & 3) * 1024>(of[i], ooff + (i >> 2) * 4096, d_ws); });
            WAIT_VM(0);
        }
        if constexpr (R == 1) { if (wave == 7) { WAIT_VM(0); pin(flg); spin_flags(flg, f2oth); } }
    }

    auto load_y = [&]() {
        const int l2 = opq(tid) & 63;
#pragma unroll
        for (int i = 0; i < 8; ++i)
            yw[i] = __builtin_amdgcn_raw_buffer_load_b32(r_ws, WS_Y2 + (u32)((32 * mem + 2 * (l2 >> 4)) * 8192 + (TI * 16 + (l2 & 15)) * 4), (8 * (i >> 1) + (i & 1)) * 8192, 16);
    };
    auto pub_off = [&](u32 ws_st) {
        const int lane = opq(tid) & 63;
        return (lane & 16) ? 0xFFFFFF00u : ws_st + (u32)((TI * 8 + mem) * 256 + (lane & 15) * 16 + (lane >> 5) * 8);
    };
    auto pub_stat = [&](u32 off, u32 tg, float s, float q) {
        auto r = __builtin_amdgcn_permlane32_swap(__float_as_uint(s), __float_as_uint(q), false, false);
        const float v = __uint_as_float(r[0]) + __uint_as_float(r[1]);
        auto t = __builtin_amdgcn_permlane16_swap(__float_as_uint(v), __float_as_uint(v), false, false);
        __builtin_amdgcn_raw_buffer_store_b64((u32x2){__float_as_uint(__uint_as_float(t[0]) + __uint_as_float(t[1])), tg}, r_ws, off, 0, 16);
    };
    auto stat_off = [&](u32 ws_st, int ti) { const int l2 = opq(tid) & 63; return ws_st + (u32)(ti * 2048 + (l2 >> 4) * 512 + (l2 & 15) * 16); };
    auto fin_stat = [&](u32x4& a, u32x4& c, u32 off, float& mean, float& rstd) {
        _Pragma("unroll 1") for (int spin = 0; spin < SPIN_CAP; ++spin) {
            if (__builtin_expect(__all(((a[1] ^ tl) | (a[3] ^ th) | (c[1] ^ tl) | (c[3] ^ th)) == 0), 1)) break;
            __builtin_amdgcn_s_sleep(1);
            asm volatile("buffer_load_dwordx4 %0, %2, %3, 0 offen sc1\n\tbuffer_load_dwordx4 %1, %2, %3, 0 offen offset:256 sc1\n\ts_waitcnt vmcnt(0)"
                         : "=&v"(a), "=&v"(c) : "v"(off), "s"(d_ws) : "memory");
        }
        auto r = __builtin_amdgcn_permlane32_swap(__float_as_uint(__uint_as_float(a[0]) + __uint_as_float(c[0])), __float_as_uint(__uint_as_float(a[2]) + __uint_as_float(c[2])), false, false);
        const float v = __uint_as_float(r[0]) + __uint_as_float(r[1]);
        auto t = __builtin_amdgcn_permlane16_swap(__float_as_uint(v), __float_as_uint(v), false, false);
        const u32 v2 = __float_as_uint(__uint_as_float(t[0]) + __uint_as_float(t[1]));
        auto u = __builtin_amdgcn_permlane32_swap(v2, v2, false, false);
        const float ts = __uint_as_float(u[0]), tq = __uint_as_float(u[1]);
        mean = ts * (1.f / 512.f);
        rstd = __builtin_amdgcn_rsqf(fmaxf(tq * (1.f / 512.f) - mean * mean, 0.f) + 1e-5f);
    };
    auto regroup = [&](u32 (&w)[8]) {
#pragma unroll
        for (int j = 0; j < 2; ++j) {
            auto r0 = __builtin_amdgcn_permlane16_swap(w[4 * j], w[4 * j + 2], false, false);
            auto r1 = __builtin_amdgcn_permlane16_swap(w[4 * j + 1], w[4 * j + 3], false, false);
            w[4 * j] = r0[0]; w[4 * j + 1] = r1[0]; w[4 * j + 2] = r0[1]; w[4 * j + 3] = r1[1];
        }
    };
    auto wo_finish = [&](auto lc) {
        constexpr int LAYER = decltype(lc)::value;
        const int lane = opq(tid) & 63, li = lane & 15, lq = lane >> 4;
        u32 wx = L_WX + hd * 4096 + lane * 16;
        pin(wx);
        u32 po = 0, so = 0, tg = 0, oa = 0;
        if constexpr (R == 1) {
#pragma unroll
            for (int n = 0; n < 4; ++n) *(LAS f32x4*)(lds + wx + n * 1024) = accM[n];
        } else {
            po = pub_off(LAYER ? WS_ST3 : WS_ST1); so = stat_off(LAYER ? WS_ST3 : WS_ST1, TI); tg = lane < 32 ? tl : th;
            const int c = 2 * (lq & 1) + (lq >> 1);
            oa = LAYER ? (u32)((TI * 16 + li) * 1024 + mem * 128 + c * 16) : WS_X2 + (u32)((TI * 8 + mem) * 2048 + li * 128 + ((c ^ (li >> 1)) << 4));
            pin(po); pin(so); pin(tg); pin(oa);
        }
        BAR();
        if constexpr (R == 0) {
            f32x4 t[4];
#pragma unroll
            for (int n = 0; n < 4; ++n) t[n] = *(LAS f32x4*)(lds + wx + n * 1024);
            SB();
            float s = 0.f, q = 0.f, s2 = 0.f, q2 = 0.f;
#pragma unroll
            for (int n = 0; n < 4; ++n) {
                const f32x4 v = accM[n] + t[n];
                const u32 w0 = pk2(v[0], v[1]), w1 = pk2(v[2], v[3]);
                xw[2 * n] = w0; xw[2 * n + 1] = w1;
                s = dot2(w0, 0x3F803F80u, s); q = dot2(w0, w0, q); s2 = dot2(w1, 0x3F803F80u, s2); q2 = dot2(w1, w1, q2);
            }
            pub_stat(po, tg, s + s2, q + q2);
            CFENCE();
            if constexpr (!LAYER) load_y();
            regroup(xw);
            SB();
            WAIT_VM(0);
            if constexpr (!LAYER) {
#pragma unroll
                for (int i = 0; i < 8; ++i) pin(yw[i]);
            }
            u32x4 a, c;
            ald4s(a, so); ald4s(c, so + 256);
            if constexpr (!LAYER) {
                _Pragma("unroll 1") for (int spin = 0; spin < SPIN_CAP; ++spin) {
                    if (__builtin_expect(__all(max(max(max(yw[0], yw[1]), max(yw[2], yw[3])), max(max(yw[4], yw[5]), max(yw[6], yw[7]))) != SENT), 1)) break;
                    __builtin_amdgcn_s_sleep(1);
                    CFENCE();
                    load_y();
                }
                regroup(yw);
            }
            f32x2 xa[8], ya[8], xy[8];
#pragma unroll
            for (int i = 0; i < 8; ++i) {
                xa[i] = (f32x2){bflo(xw[i]), bfhi(xw[i])}; ya[i] = (f32x2){bflo(yw[i]), bfhi(yw[i])}; xy[i] = xa[i] * ya[i];
                pin(xa[i]); pin(ya[i]); pin(xy[i]);
            }
            WAIT_VM(0);
            pin(a); pin(c);
            float mean, rstd;
            fin_stat(a, c, so, mean, rstd);
            const float nm = -mean * rstd;
#pragma unroll
            for (int i = 0; i < 8; ++i) {
                const f32x2 o = xy[i] * rstd + (ya[i] * nm + xa[i]);
                xw[i] = pk2(o[0], o[1]);
                if (!LAYER) xw[i] = nosent(xw[i]);
            }
#pragma unroll
            for (int j = 0; j < 2; ++j) {
                const u32x4 o = {xw[4 * j], xw[4 * j + 1], xw[4 * j + 2], xw[4 * j + 3]};
                if constexpr (LAYER) __builtin_amdgcn_raw_buffer_store_b128(o, r_out, oa + j * 64, 0, 0);
                else __builtin_amdgcn_raw_buffer_store_b128(o, r_ws, oa ^ (j * 64), 0, 16);
            }
            if constexpr (!LAYER) {
                float s = 0.f, q = 0.f, s2 = 0.f, q2 = 0.f;
#pragma unroll
                for (int i = 0; i < 8; i += 2) { s = dot2(xw[i], 0x3F803F80u, s); q = dot2(xw[i], xw[i], q); s2 = dot2(xw[i + 1], 0x3F803F80u, s2); q2 = dot2(xw[i + 1], xw[i + 1], q2); }
                pub_stat(po + (WS_ST2 - WS_ST1) * !(lane & 16), tg, s + s2, q + q2);
                CFENCE();
                regroup(xw);
#pragma unroll
                for (int i = 0; i < 8; ++i) { pin(xw[i]); pin(yw[i]); }
                WAIT_VM(0);
            }
        }
    };
    wo_finish(IC<0>{});
    if constexpr (R == 1) {
        if (b == 0 && wave == 7) {
            const u32 fo = WS_F1 + (u32)((opq(tid) & 63) * 128);
            u32x2 ef[4];
#pragma unroll
            for (int i = 0; i < 4; ++i) ald2s(ef[i], fo + i * 8192);
            WAIT_VM(0);
#pragma unroll
            for (int i = 0; i < 4; ++i) { pin(ef[i]); spin_flags(ef[i], fo + i * 8192); }
            if (fo == WS_F1) __builtin_amdgcn_raw_buffer_store_b64(__builtin_bit_cast(u32x2, epoch + 1), r_ws, WS_EPOCH, 0, 16);
            WAIT_VM(0);
        }
    }
    RBAR();

    {
        const int l3 = opq(tid) & 63;
        const u32 g0 = (u32)((4 * grp - 1 + (hd >> 1)) * 16384 + (hd & 1) * 1024 + l3 * 16);
        const u32 g2 = (u32)((4 * grp + 3) * 16384 + hd * 512 + l3 * 16);
        auto x2_piece = [&](auto ic) {
            constexpr int i = decltype(ic)::value, s = i / 3, p = i % 3;
            const u32 kp = (u32)((s + rot) & 7);
            if constexpr (p == 0) dma(IC<4>{}, IC<L_CX + s * 10240>{}, d_x2, hb1k, g0, kp * 2048);
            else if constexpr (p == 1) dma(IC<4>{}, IC<L_CX + s * 10240 + 4096>{}, d_x2, hb1k, g0 + 32768, kp * 2048);
            else dma(IC<5>{}, IC<L_CX + s * 10240 + 8192>{}, d_x2, hb512, g2, kp * 2048);
        };
        u32 va = L_CX + hd * 1024 + l3 * 16, vq = L_CX + 8192 + hd * 512 + (l3 & 31) * 16 + (l3 >> 5) * 10240;
        pin(va); pin(vq);
        u32x4 vr[5];
        auto ver_read = [&](auto cc) {
            constexpr int c = decltype(cc)::value;
            u32 a = va + c * 20480, q = vq + c * 20480;
            pin(a); pin(q); CFENCE();
            vr[0] = *(LAS u32x4*)(lds + a); vr[1] = *(LAS u32x4*)(lds + a + 4096); vr[2] = *(LAS u32x4*)(lds + a + 10240); vr[3] = *(LAS u32x4*)(lds + a + 14336);
            vr[4] = *(LAS u32x4*)(lds + q);
        };
        auto ver_check = [&](auto cc) {
            constexpr int c = decltype(cc)::value;
            _Pragma("unroll 1") for (int spin = 0; spin < SPIN_CAP; ++spin) {
                u32 m0 = 0, m1 = 0;
#pragma unroll
                for (int i = 0; i < 5; ++i) { m0 = max(max(m0, vr[i][0]), vr[i][1]); m1 = max(max(m1, vr[i][2]), vr[i][3]); }
                if (__builtin_expect(__all(max(m0, m1) != SENT), 1)) break;
                __builtin_amdgcn_s_sleep(1);
                sfor<6 * c, 6 * c + 6>([&](auto ic) { x2_piece(ic); });
                WAIT_VM(0);
                ver_read(cc);
            }
        };
        const u32 xb0 = (u32)((l3 & 15) * 128 + (((l3 >> 4) ^ ((l3 & 15) >> 1)) << 4));
        if constexpr (R == 1) {
            {
                const int pp = l3 >> 3;
                const u32 po = (pp >= 2 ? (u32)((4 * grp + 3) * 16384 + hd * 512 + 508) : (u32)((4 * grp - 1 + (hd >> 1) + 2 * pp) * 16384 + (hd & 1) * 1024 + 1020)) + (l3 & 7) * 2048;
                _Pragma("unroll 1") for (int spin = 0; spin < SPIN_CAP; ++spin) {
                    u32 pv;
                    asm volatile("buffer_load_dword %0, %1, %2, 0 offen sc1\n\ts_waitcnt vmcnt(0)" : "=&v"(pv) : "v"(po), "s"(d_x2) : "memory");
                    if (__builtin_expect(__all(pv != SENT), 1)) break;
                    __builtin_amdgcn_s_sleep(1);
                }
            }
            sfor<0, 12>([&](auto ic) { x2_piece(ic); });
            WAIT_VM(6);
            ver_read(IC<0>{});
            ver_check(IC<0>{});
        } else {
            u32x4 sa, sc, ha, hc;
            const u32 so = stat_off(WS_ST2, 4 * grp + hd);
            const bool halo = hd == 0 && grp > 0;
            ald4s(sa, so); ald4s(sc, so + 256);
            if (halo) { ald4s(ha, so - 2048); ald4s(hc, so - 2048 + 256); }
            WAIT_VM(0);
            pin(sa); pin(sc);
            float mean, rstd;
            fin_stat(sa, sc, so, mean, rstd);
            if (l3 < 16) { *(LAS float*)(lds + L_TAB + 64 + hd * 64 + l3 * 4) = mean; *(LAS float*)(lds + L_TAB + 320 + 64 + hd * 64 + l3 * 4) = rstd; }
            if (halo) {
                pin(ha); pin(hc);
                fin_stat(ha, hc, so - 2048, mean, rstd);
                if (l3 < 16) { *(LAS float*)(lds + L_TAB + l3 * 4) = mean; *(LAS float*)(lds + L_TAB + 320 + l3 * 4) = rstd; }
            }
        }
        BAR();
        u32 xa;
        auto pf_addr = [&](int st) { xa = L_CX + (st >> 1) * 10240 + (xb0 ^ ((st & 1) * 64)); pin(xa); };
        pf_addr(0);
#pragma unroll
        for (int mt = 0; mt < 5; ++mt) Ab[0][mt] = ldsA(xa + mt * 2048);
        sfor<0, 16>([&](auto stc) {
            constexpr int st = decltype(stc)::value;
            if constexpr (R == 1) {
                if constexpr (st == 2) { WAIT_VM(10); ver_read(IC<1>{}); }
                if constexpr (st == 3) ver_check(IC<1>{});
                if constexpr (st == 5) { WAIT_VM(6); ver_read(IC<2>{}); }
                if constexpr (st == 6) { ver_check(IC<2>{}); WAIT_VM(0); ver_read(IC<3>{}); }
                if constexpr (st == 7) ver_check(IC<3>{});
            }
            if constexpr (st == 3) { RBAR(); }
            if constexpr (st == 7) { RBAR(); }
            if (st < 15) pf_addr(st + 1);
            if constexpr (st == (R ? 14 : 5)) load_tab(true);
            if constexpr (st == 6) fin_tab();
            SB();
            qkv_step(IC<1>{}, st, Ab[st & 1], [&](auto mc) {
                constexpr int mt = decltype(mc)::value;
                if (st < 15) Ab[(st + 1) & 1][mt] = ldsA(xa + mt * 2048);
                if constexpr (R == 1 && 12 + 5 * st + mt < 24) x2_piece(IC<12 + 5 * st + mt>{});
                SB();
            });
        });
    }
    attention(IC<1>{});

    {
        wo_addr();
#pragma unroll
        for (int ks = 0; ks < 8; ++ks) load_F(ks);
        SB();
        WAIT_VM(0);
        RBAR();
        load_o(WS_O2);
        SB();
        _Pragma("unroll 1") for (int spin = 0; spin < 64; ++spin) {
            u32 m0 = 0, m1 = 0;
#pragma unroll
            for (int ks = 0; ks < 8; ++ks) {
#pragma unroll
                for (int n = 0; n < 4; ++n) accM[n] = mfma32(F[ks][n], __builtin_bit_cast(s16x8, of[ks]), ks ? accM[n] : resid(n));
                m0 = max(max(m0, of[ks][0]), of[ks][1]); m1 = max(max(m1, of[ks][2]), of[ks][3]);
                SB();
            }
            if (__builtin_expect(__all(max(m0, m1) != SENT), 1)) break;
            _Pragma("unroll 1") for (int sp2 = 0; sp2 < SPIN_CAP; ++sp2) {
                CFENCE();
                __builtin_amdgcn_s_sleep(1);
                const u32 pv = __builtin_amdgcn_raw_buffer_load_b32(r_ws, WS_O2 + (u32)(TI * 16384 + R * 8192 + (opq(tid) & 15) * 512 + 508), 0, 16);
                if (__all(pv != SENT)) break;
            }
            CFENCE();
            load_o(WS_O2);
        }
        SB();
    }
    wo_finish(IC<1>{});
}

__global__ void __launch_bounds__(512, 2)
fused_kernel(const u16* __restrict__ gx, const u16* __restrict__ gwqkv, const u16* __restrict__ gwo,
             const u16* __restrict__ gbk, const u16* __restrict__ gbv, const u16* __restrict__ gsk,
             const u16* __restrict__ ghk, const u16* __restrict__ gdv, u16* __restrict__ gout, char* __restrict__ ws) {
    __shared__ __attribute__((aligned(16))) char lds_[L_TOTAL];
    typedef u32 u32x16 __attribute__((ext_vector_type(16)));
    u32x16 ka; u32x4 kb;
    {
        auto kp = __builtin_amdgcn_kernarg_segment_ptr();
        asm volatile("s_load_dwordx16 %0, %2, 0x0\n\ts_load_dwordx4 %1, %2, 0x40\n\ts_waitcnt lgkmcnt(0)" : "=&s"(ka), "=&s"(kb) : "s"(kp) : "memory");
    }
    auto mkp = [](u32 lo, u32 hi) { return (char*)(((u64)hi << 32) | lo); };
    const Args a = {(const u16*)mkp(ka[0], ka[1]), (const u16*)mkp(ka[2], ka[3]), (const u16*)mkp(ka[4], ka[5]), (const u16*)mkp(ka[6], ka[7]), (const u16*)mkp(ka[8], ka[9]),
                    (const u16*)mkp(ka[10], ka[11]), (const u16*)mkp(ka[12], ka[13]), (const u16*)mkp(ka[14], ka[15]), (u16*)mkp(kb[0], kb[1]), mkp(kb[2], kb[3])};
    const int wave = __builtin_amdgcn_readfirstlane(threadIdx.x >> 6);
    if (wave < 4) body<0>(a, (LAS char*)lds_, wave); else body<1>(a, (LAS char*)lds_, wave);
}

extern "C" void kernel_launch(void* const* d_in, const int* in_sizes, int n_in,
                              void* d_out, int out_size, void* d_ws, size_t ws_size,
                              hipStream_t stream) {
    fused_kernel<<<256, 512, 0, stream>>>((const u16*)d_in[0], (const u16*)d_in[1], (const u16*)d_in[2],
                                          (const u16*)d_in[4], (const u16*)d_in[5], (const u16*)d_in[6],
                                          (const u16*)d_in[7], (const u16*)d_in[8], (u16*)d_out, (char*)d_ws);
}
```
